```python
import torch

def _softmax(x, axis=-1):
    return torch.softmax(x, dim=axis)
def _gelu(x):
    return torch.nn.functional.gelu(x, approximate="tanh")
def _silu(x):
    return torch.nn.functional.silu(x)
def _sigmoid(x):
    return torch.sigmoid(x)
def _layernorm(x, axis=-1, eps=1e-5):
    m = x.mean(dim=axis, keepdim=True)
    v = x.var(dim=axis, keepdim=True, unbiased=False)
    return (x - m) / torch.sqrt(v + eps)
def _scatter_add(tgt, idx, src, axis=0):
    return tgt.clone().index_add_(axis, idx, src)
def _segment_sum(src, seg, n):
    out = torch.zeros((n,) + src.shape[1:], dtype=src.dtype)
    return out.index_add_(0, seg, src)
def _pad(x, pw):
    flat = tuple(v for pair in reversed(pw) for v in pair)
    return torch.nn.functional.pad(x, flat)

kernel_name = 'interleave_layers__sliding_local_dilated__softmax_full_mha__prompt'

def setup_inputs():

    _w = (lambda a: (lambda t: t.to(torch.bfloat16) if t.is_floating_point() else t)(torch.as_tensor(a)))
    def _A_setup():
        B, S, D = 1, 2048, 1024
        return {
            "x": _w((lambda *s: torch.randn(*s, dtype=torch.bfloat16) * 0.05)(B, S, D)),
            "wqkv": _w((lambda *s: torch.randn(*s, dtype=torch.bfloat16) * 0.05)(D, 3 * D)),
            "wo": _w((lambda *s: torch.randn(*s, dtype=torch.bfloat16) * 0.05)(D, D)),
        }
    _A_in = _A_setup()

    def _B_setup():
        B, S, D = 1, 2048, 1024
        return {
            "x": _w((lambda *s: torch.randn(*s, dtype=torch.bfloat16) * 0.05)(B, S, D)),
            "wqkv": _w((lambda *s: torch.randn(*s, dtype=torch.bfloat16) * 0.05)(D, 3 * D)),
            "wo": _w((lambda *s: torch.randn(*s, dtype=torch.bfloat16) * 0.05)(D, D)),
        }
    _B_in = _B_setup()

    return {
        "A_x": _A_in["x"],
        "A_wqkv": _A_in["wqkv"],
        "A_wo": _A_in["wo"],
        "B_x": _B_in["x"],
        "B_wqkv": _B_in["wqkv"],
        "B_wo": _B_in["wo"],
    }

def reference(A_x, A_wqkv, A_wo, B_x, B_wqkv, B_wo):
    def _A_ref(x, wqkv, wo):
        B, S, D = x.shape
        H = 32; Dh = D // H
        W, dil = 8, 2
        qkv = (lambda x, *d: x.permute(*d))((x @ wqkv).reshape(B, S, 3, H, Dh), 2, 0, 3, 1, 4)
        q, k, v = qkv[0], qkv[1], qkv[2]
        scale = Dh ** -0.5
        pos = torch.arange(S)
        off = torch.arange(W) * dil
        idx = pos[:, None] - off[None, :]
        mask = idx >= 0
        idx_c = torch.where(mask, idx, 0).reshape(-1)
        kg = k[:, :, idx_c, :].reshape(B, H, S, W, Dh)
        vg = v[:, :, idx_c, :].reshape(B, H, S, W, Dh)
        logits = torch.einsum("bhsd,bhswd->bhsw", q, kg) * scale
        logits = torch.where(mask[None, None, :, :], logits, -1e9)
        attn = _softmax(logits, axis=-1)
        o = torch.einsum("bhsw,bhswd->bhsd", attn, vg)
        return (lambda x, *d: x.permute(*d))(o, 0, 2, 1, 3).reshape(B, S, D) @ wo

    def _B_ref(x, wqkv, wo):
        B, S, D = x.shape
        H = 32; Dh = D // H
        qkv = (lambda x, *d: x.permute(*d))((x @ wqkv).reshape(B, S, 3, H, Dh), 2, 0, 3, 1, 4)
        q, k, v = qkv[0], qkv[1], qkv[2]
        scale = Dh ** -0.5
        scores = torch.einsum("bhqd,bhkd->bhqk", q, k) * scale
        p = _softmax(scores, axis=-1)
        o = torch.einsum("bhqk,bhkd->bhqd", p, v)
        return (lambda x, *d: x.permute(*d))(o, 0, 2, 1, 3).reshape(B, S, D) @ wo

    x = A_x
    x = x + _A_ref(_layernorm(x, axis=-1), A_wqkv, A_wo)
    x = x + _B_ref(_layernorm(x, axis=-1), B_wqkv, B_wo)
    x = x + _A_ref(_layernorm(x, axis=-1), A_wqkv, A_wo)
    x = x + _B_ref(_layernorm(x, axis=-1), B_wqkv, B_wo)
    return x
```

```cpp
#include <hip/hip_runtime.h>
#include <hip/hip_bf16.h>
#include <stdint.h>

typedef __attribute__((ext_vector_type(8))) short bf16x8;
typedef __attribute__((ext_vector_type(4))) short s16x4;
typedef __attribute__((ext_vector_type(4))) float f32x4;
typedef __attribute__((ext_vector_type(2))) float f32x2;
typedef __attribute__((ext_vector_type(16))) float f32x16;
typedef __attribute__((ext_vector_type(4))) unsigned u32x4;
typedef __attribute__((ext_vector_type(2))) unsigned u32x2;
typedef __attribute__((ext_vector_type(2))) __bf16 bf16x2_t;
typedef __attribute__((ext_vector_type(8))) __bf16 bf8_t;
typedef __attribute__((ext_vector_type(16))) __bf16 bf16v_t;
typedef __attribute__((address_space(3))) const char* lds_cptr;
typedef __attribute__((address_space(3))) char* lds_ptr;

#define DI __device__ __forceinline__
#define MFMA16(a, b, c) __builtin_amdgcn_mfma_f32_16x16x32_bf16((a), (b), (c), 0, 0, 0)
#define MFMA32(a, b, c) __builtin_amdgcn_mfma_f32_32x32x16_bf16((a), (b), (c), 0, 0, 0)
#define SBAR() __builtin_amdgcn_sched_barrier(0)
#define STAMP(i)
#define STAMPR(i)
#define STAMP_DECL
#define STAMP_FLUSH(it)
#define SP_PARAM
#define SP_ARG
typedef unsigned long long u64;

constexpr float QSCALE = 0.17677669529663687f * 1.4426950408889634f;
constexpr float ATT_SCALE = 0.17677669529663687f;
constexpr float LOG2E = 1.4426950408889634f;
constexpr float NEG_MASK = -1e9f / ATT_SCALE;

DI unsigned pack_bf16(float lo, float hi) {
    f32x2 v = {lo, hi};
    bf16x2_t r = __builtin_convertvector(v, bf16x2_t);
    return __builtin_bit_cast(unsigned, r);
}
DI float bf_lo(unsigned u) { return __uint_as_float(u << 16); }
DI float bf_hi(unsigned u) { return __uint_as_float(u & 0xffff0000u); }
DI float round_bf16(float x) { return bf_lo(pack_bf16(x, 0.f)); }
DI void ln_stats(float sum, float sumsq, float& mean_out, float& rstd_out) {
    const float mean = sum * (1.f / 1024.f);
    const float var = fmaxf(sumsq * (1.f / 1024.f) - mean * mean, 0.f);
    mean_out = round_bf16(mean);
    rstd_out = __builtin_amdgcn_rcpf(round_bf16(__builtin_amdgcn_sqrtf(round_bf16(round_bf16(var) + 1e-5f))));
}

DI void glds16(const void* sbase, unsigned voff, unsigned ldsaddr) {
    asm volatile("s_mov_b32 m0, %0\n\ts_nop 0\n\tglobal_load_lds_dwordx4 %1, %2"
                 :: "s"(ldsaddr), "v"(voff), "s"(sbase) : "memory");
}
DI void glds16c(const void* sbase, unsigned voff, unsigned ldsaddr) {
    asm volatile("s_mov_b32 m0, %0\n\ts_nop 0\n\tglobal_load_lds_dwordx4 %1, %2 sc1"
                 :: "s"(ldsaddr), "v"(voff), "s"(sbase) : "memory");
}
DI void st16(void* sbase, unsigned voff, u32x4 v) {
    asm volatile("global_store_dwordx4 %0, %1, %2 sc1\n\ts_nop 1" :: "v"(voff), "v"(v), "s"(sbase) : "memory");
}
template <int OFF> DI void st16o(void* sbase, unsigned voff, u32x4 v) {
    asm volatile("global_store_dwordx4 %0, %1, %2 offset:%3 sc1\n\ts_nop 1" :: "v"(voff), "v"(v), "s"(sbase), "n"(OFF) : "memory");
}
DI bf16x8 lds_b128(lds_cptr p) { return *(const __attribute__((address_space(3))) bf16x8*)p; }
DI s16x4 lds_tr(lds_cptr p) {
    return __builtin_amdgcn_ds_read_tr16_b64_v4i16((__attribute__((address_space(3))) s16x4*)p);
}
DI bf16x8 cat8(s16x4 a, s16x4 b) { return __builtin_shufflevector(a, b, 0, 1, 2, 3, 4, 5, 6, 7); }
DI float dot2bf(unsigned a, unsigned b, float c) {
    return __builtin_amdgcn_fdot2_f32_bf16(__builtin_bit_cast(bf16x2_t, a), __builtin_bit_cast(bf16x2_t, b), c, false);
}
DI float sum_g(float v) {
    auto a = __builtin_amdgcn_permlane16_swap(__float_as_uint(v), __float_as_uint(v), false, false);
    v = __uint_as_float(a[0]) + __uint_as_float(a[1]);
    auto b = __builtin_amdgcn_permlane32_swap(__float_as_uint(v), __float_as_uint(v), false, false);
    return __uint_as_float(b[0]) + __uint_as_float(b[1]);
}
DI float wave_sum(float v) {
    v += __builtin_bit_cast(float, __builtin_amdgcn_update_dpp(0, __builtin_bit_cast(int, v), 0xB1, 0xF, 0xF, true));
    v += __builtin_bit_cast(float, __builtin_amdgcn_update_dpp(0, __builtin_bit_cast(int, v), 0x4E, 0xF, 0xF, true));
    v += __builtin_bit_cast(float, __builtin_amdgcn_update_dpp(0, __builtin_bit_cast(int, v), 0x141, 0xF, 0xF, true));
    v += __builtin_bit_cast(float, __builtin_amdgcn_update_dpp(0, __builtin_bit_cast(int, v), 0x140, 0xF, 0xF, true));
    return sum_g(v);
}
DI float amax3(float a, float b, float c) { float r; asm("v_max3_f32 %0, %1, %2, %3" : "=v"(r) : "v"(a), "v"(b), "v"(c)); return r; }
DI float max2f(float a, float b) { return __builtin_amdgcn_fmed3f(a, b, INFINITY); }
DI float max_h(float v) {
    auto b = __builtin_amdgcn_permlane32_swap(__float_as_uint(v), __float_as_uint(v), false, false);
    return max2f(__uint_as_float(b[0]), __uint_as_float(b[1]));
}

template <int OFF> DI void st8o(void* sbase, unsigned voff, f32x2 v) {
    asm volatile("global_store_dwordx2 %0, %1, %2 offset:%3 sc1\n\ts_nop 1" :: "v"(voff), "v"(v), "s"(sbase), "n"(OFF) : "memory");
}
DI void code_touch(unsigned& x, int slice, int lane) {
    unsigned long long c0, c1;
    asm volatile("s_getpc_b64 s[96:97]\n\ts_add_u32 s96, s96, mega_kernel@rel32@lo+4\n\ts_addc_u32 s97, s97, mega_kernel@rel32@hi+12\n\ts_mov_b64 %0, s[96:97]\n\t"
                 "s_getpc_b64 s[96:97]\n\ts_add_u32 s96, s96, zz_code_end_kernel@rel32@lo+4\n\ts_addc_u32 s97, s97, zz_code_end_kernel@rel32@hi+12\n\ts_mov_b64 %1, s[96:97]"
                 : "=s"(c0), "=s"(c1) :: "s96", "s97", "scc");
    const unsigned off = min((unsigned)(slice * 64 + lane) * 128u, (unsigned)(c1 - c0));
    asm volatile("global_load_dword %0, %1, %2" : "+v"(x) : "v"(off), "s"(c0) : "memory");
}

#define BAR() asm volatile("s_barrier" ::: "memory")
#define WAIT_LGKM() do { SBAR(); __builtin_amdgcn_s_waitcnt(0xC07F); SBAR(); } while (0)
#define WAIT_LGKM_BAR() do { SBAR(); __builtin_amdgcn_s_waitcnt(0xC07F); asm volatile("s_barrier" ::: "memory"); SBAR(); } while (0)
#define VM_BAR(n) asm volatile("s_waitcnt vmcnt(" #n ")\n\ts_barrier" ::: "memory")
constexpr int CH_LDS = 163840;
constexpr int OFF_SYNC = CH_LDS - 64;
typedef __attribute__((address_space(3))) unsigned* lds_uptr;
typedef __attribute__((address_space(1))) u64* g_u64ptr;

constexpr size_t MBy = 1 << 20;
constexpr size_t WS_X3 = 0, WS_X2 = 4 * MBy, WS_X1 = 8 * MBy, WS_OF = 12 * MBy  , WS_KVF = 36 * MBy  ,
                 WS_ST = 52 * MBy  , WS_CSP = 54 * MBy  , WS_FLAG = 55 * MBy  ,
                 WS_EPOCH = WS_FLAG + 16 * 32768, WS_Y0 = 56 * MBy;
DI char* flag_line(char* ws, int set, int group, int consumer) { return ws + WS_FLAG + set * 32768 + group * 2048 + consumer * 128; }
DI void flag_store(char* ws, int set, int group, int producer, u64 tag, int lane) {
    asm volatile("" : "+s"(tag));
    if (lane < 16) __hip_atomic_store((g_u64ptr)(flag_line(ws, set, group, lane) + producer * 8), tag, __ATOMIC_RELAXED, __HIP_MEMORY_SCOPE_AGENT);
}
DI void flag_poll(const char* p, u64 tag) {
    asm volatile("" : "+s"(tag));
#pragma unroll 1
    for (int spin = 0; spin < (1 << 16); ++spin) {
        const u64 v = __hip_atomic_load((g_u64ptr)p, __ATOMIC_RELAXED, __HIP_MEMORY_SCOPE_AGENT);
        if (__all(v == tag)) break;
        __builtin_amdgcn_s_sleep(1);
    }
    asm volatile("" ::: "memory");
}
struct Ids { int pq, cq, po, co; };
DI Ids ids() {
    int bid = blockIdx.x; asm volatile("" : "+s"(bid));
    const int xcd = bid & 7, idx = bid >> 3;
    return Ids{(xcd & 1) * 8 + (idx & 7), (xcd >> 1) * 4 + (idx >> 3), (xcd & 3) * 4 + (idx & 3), (xcd >> 2) * 8 + (idx >> 2)};
}

template <int V> struct IC { static constexpr int value = V; };
template <bool LOCAL> struct QC {
    static constexpr int AROWS = LOCAL ? 144 : 128;
    static constexpr int ABYTES = AROWS * 128;
    static constexpr int STAGE = ABYTES + 24576;
    static constexpr int OFF_STATP = 129024;
    static constexpr int OFF_MEAN = OFF_STATP + 18432;
    static constexpr int OFF_RSTD = OFF_MEAN + 576;
    static constexpr int OFF_CS = OFF_RSTD + 576;
    static constexpr int OFF_CSP = OFF_CS + 768;
};
constexpr int OFF_EX = 131072;

DI void colsum_load(u32x4 (&c)[8], const void* W, int G, int lane) {
    const Ids I = ids();
    const unsigned off = (unsigned)(I.pq * 64 + (lane >> 3)) * 6144u + (unsigned)(G * 2048 + I.cq * 128 + (lane & 7) * 16);
#pragma unroll
    for (int i = 0; i < 8; ++i)
        asm volatile("global_load_dwordx4 %0, %1, %2" : "=&v"(c[i]) : "v"(off), "s"((const char*)W + i * 49152) : "memory");
}
DI void colsum_store(const u32x4 (&c)[8], char* dst, int G, int lane) {
    const Ids I = ids();
    const int m = lane & 15;
    const unsigned one = (m & 1) ? 0x3f800000u : 0x3f80u;
    const u32x4 sl = {(m >> 1) == 0 ? one : 0u, (m >> 1) == 1 ? one : 0u, (m >> 1) == 2 ? one : 0u, (m >> 1) == 3 ? one : 0u};
    f32x4 d = {0, 0, 0, 0};
#pragma unroll
    for (int i = 0; i < 8; ++i) d = MFMA16(__builtin_bit_cast(bf16x8, sl), __builtin_bit_cast(bf16x8, c[i]), d);
#pragma unroll
    for (int r = 0; r < 4; ++r)
        d[r] += __int_as_float(__builtin_amdgcn_update_dpp(0, __float_as_int(d[r]), 0x128, 0xF, 0xF, true));
    if (lane < 32 && (lane & 8) == 0)
        st16(dst, (unsigned)((I.cq * 16 + I.pq) * 768 + G * 256 + (lane & 7) * 32 + (lane >> 4) * 16), __builtin_bit_cast(u32x4, d));
}

template <bool LOCAL, class F>
DI void qkv_loader(char* smem, const __hip_bfloat16* __restrict__ X, const __hip_bfloat16* __restrict__ W,
                   const float* __restrict__ statp, const char* __restrict__ csp, bool ident, F early SP_PARAM) {
    typedef QC<LOCAL> C;
    constexpr int NT = 16;
    int tid = threadIdx.x; asm volatile("" : "+v"(tid));
    const int lane = tid & 63;
    const int w = __builtin_amdgcn_readfirstlane(tid >> 6) & 3;
    const int idx = blockIdx.x >> 3;
    const Ids I = ids();
    const int panel = I.pq, ct = I.cq;
    const int R0 = panel * 128;
    const unsigned lds0 = (unsigned)(uintptr_t)smem;
    const lds_cptr sm = (lds_cptr)smem;
    auto grow = [&](int i) -> int {
        if (!LOCAL) return R0 + i;
        if (i < 128) { const int t = (i >> 4) & 3, a = i & 15; return R0 + (i & 64) + 2 * (16 * (t & 1) + a) + (t >> 1); }
        const int a = i & 15; const int r = R0 - 16 + 2 * (a & 7) + (a >> 3);
        return r < 0 ? 0 : r;
    };
    unsigned voffA[5], voffB[6];
#pragma unroll
    for (int i = 0; i < 6; ++i) {
        const int j = w + 4 * i, G = j >> 3, k = 8 * (j & 7) + (lane >> 3);
        const int pos = lane & 7, f = ((k >> 1) & 1) | (((k >> 3) & 1) << 1);
        const int seg = (pos >> 1) ^ f;
        voffB[i] = (unsigned)k * 6144u + (unsigned)(G * 1024 + ct * 64 + seg * 16 + (pos & 1) * 8) * 2u;
    }
    auto issueW = [&](int t, int buf) {
        const char* wb = (const char*)W + (size_t)t * (64 * 6144);
        const unsigned sb = lds0 + buf * C::STAGE;
#pragma unroll
        for (int i = 0; i < 6; ++i) glds16(wb, voffB[i], sb + C::ABYTES + (w + 4 * i) * 1024);
    };
    issueW(0, 0); issueW(1, 1);
    early();
#pragma unroll
    for (int i = 0; i < 5; ++i) {
        const int j = (i < 4) ? (w + 4 * i) : (16 + (w & 1));
        const int row = 8 * j + (lane >> 3);
        const int c = (lane & 7) ^ ((row >> 1) & 7);
        voffA[i] = (unsigned)grow(row < C::AROWS ? row : 0) * 2048u + c * 16;
        asm volatile("" : "+v"(voffA[i]));
    }
    auto issueA = [&](int t, int buf) {
        const char* xa = (const char*)X + t * 128;
        const unsigned sb = lds0 + buf * C::STAGE;
        if (LOCAL && w < 2) glds16c(xa, voffA[4], sb + (16 + w) * 1024);
#pragma unroll
        for (int i = 0; i < 4; ++i) glds16c(xa, voffA[i], sb + (w + 4 * i) * 1024);
    };
    BAR(); BAR();
    STAMP(LOCAL ? 1 : 6);
    issueA(0, 0); issueA(1, 1);
    VM_BAR(4);
    STAMP(LOCAL ? 2 : 7);
    issueW(2, 2);
    unsigned junkc = 0;
    auto issueS = [&](int i) {
        if (i == 8 && idx < 3) code_touch(junkc, idx * 4 + w, lane);
        if (ident) return;
        if (i < 4) glds16c(statp, (unsigned)((panel * 16 + w + 4 * i) * 1024 + lane * 16), lds0 + C::OFF_STATP + (w + 4 * i) * 1024);
        else if (LOCAL && i == 4 && w < 2) {
            const unsigned o = panel ? (unsigned)(((panel - 1) * 16 + w * 8 + (lane >> 3)) * 1024 + 896 + (lane & 7) * 16)
                                     : (unsigned)((w * 8 + (lane >> 3)) * 1024 + (lane & 7) * 16);
            glds16c(statp, o, lds0 + C::OFF_STATP + (16 + w) * 1024);
        }
    };
    issueA(2, 2);
    VM_BAR(10);
    f32x4 cp[4];
    asm volatile("global_load_dwordx4 %0, %4, %5 sc1\n\tglobal_load_dwordx4 %1, %4, %5 offset:768 sc1\n\t"
                 "global_load_dwordx4 %2, %4, %5 offset:1536 sc1\n\tglobal_load_dwordx4 %3, %4, %5 offset:2304 sc1"
                 : "=&v"(cp[0]), "=&v"(cp[1]), "=&v"(cp[2]), "=&v"(cp[3])
                 : "v"((unsigned)((ct * 16 + w * 4) * 768 + min(lane, 47) * 16)), "s"(csp) : "memory");
    issueA(3, 0); issueW(3, 0);
    int buf = 1;
#pragma unroll 1
    for (int t = 1; t < NT - 3; ++t) {
        VM_BAR(10);
        issueA(t + 3, buf); issueW(t + 3, buf);
        if (t >= 2) issueS(t - 2);
        buf = (buf == 2) ? 0 : buf + 1;
    }
    const int lt = tid - 256;
    float sa = 0.f, sb = 0.f;
    asm volatile("" : "+v"(cp[0]), "+v"(cp[1]), "+v"(cp[2]), "+v"(cp[3]));
    if (!ident) {
        if (lane < 48) *(__attribute__((address_space(3))) f32x4*)((lds_ptr)smem + C::OFF_CSP + w * 768 + lane * 16) = (cp[0] + cp[1]) + (cp[2] + cp[3]);
        if (lt < C::AROWS) {
            const int gr = LOCAL ? grow(lt) - R0 : ((lt & 64) + (lt & 1) * 32 + ((lt & 63) >> 1));
            const int off = (lt < 128) ? gr * 8 : (16384 + (panel ? (gr + 16) : 0) * 8);
            const int str = (lt < 128) ? 1024 : 128;
#pragma unroll
            for (int i = 0; i < 16; ++i) {
                const f32x2 v = *(const __attribute__((address_space(3))) f32x2*)(sm + C::OFF_STATP + off + i * str);
                sa += v[0]; sb += v[1];
            }
        }
    }
    asm volatile("" : "+v"(sa), "+v"(sb));
    WAIT_LGKM();
    VM_BAR(10);
    asm volatile("" :: "v"(junkc));
    if (!ident) {
        __attribute__((address_space(3))) float* meanL = (__attribute__((address_space(3))) float*)((lds_ptr)smem + C::OFF_MEAN);
        __attribute__((address_space(3))) float* rstdL = (__attribute__((address_space(3))) float*)((lds_ptr)smem + C::OFF_RSTD);
        __attribute__((address_space(3))) float* csL = (__attribute__((address_space(3))) float*)((lds_ptr)smem + C::OFF_CS);
        if (lt < C::AROWS) {
            float mean, rstd;
            ln_stats(sa, sb, mean, rstd);
            meanL[lt] = mean; rstdL[lt] = rstd;
        }
        if (lt >= 64) {
            const __attribute__((address_space(3))) float* cq = (const __attribute__((address_space(3))) float*)(sm + C::OFF_CSP) + (lt - 64);
            csL[lt - 64] = -((cq[0] + cq[192]) + (cq[384] + cq[576]));
        }
    }
    WAIT_LGKM();
    VM_BAR(0);
    BAR();
}

template <bool LOCAL>
DI void qkv_compute(char* smem, u32x4 (&qo4)[4], __hip_bfloat16* __restrict__ kvf, __hip_bfloat16* __restrict__ ofrag, bool ident) {
    typedef QC<LOCAL> C;
    constexpr int NT = 16, ABYTES = C::ABYTES, STAGE = C::STAGE;
    int tz = threadIdx.x; asm volatile("" : "+v"(tz));
    const int lane = tz & 63;
    const int w = __builtin_amdgcn_readfirstlane(tz >> 6) & 3;
    const int wm = w & 1, wn = w >> 1;
    const Ids I = ids();
    const int panel = I.pq, ct = I.cq;
    const int l15 = lane & 15, g = lane >> 4;
    const lds_cptr sm = (lds_cptr)smem;
    lds_ptr smw = (lds_ptr)smem;

    f32x4 acc[3][4][2];
    f32x4 acch[2];
#pragma unroll
    for (int a = 0; a < 3; ++a)
#pragma unroll
        for (int t = 0; t < 4; ++t) { acc[a][t][0] = f32x4{0, 0, 0, 0}; acc[a][t][1] = f32x4{0, 0, 0, 0}; }
    acch[0] = f32x4{0, 0, 0, 0}; acch[1] = f32x4{0, 0, 0, 0};

    struct Frags { bf16x8 a[4]; bf16x8 h; s16x4 b[3][2][2]; };
    auto rd = [&](auto WM, Frags& F, lds_cptr pa, lds_cptr pb0, lds_cptr pb1, int n) {
        switch (n) {
            case 0: F.b[0][0][0] = lds_tr(pb0); break;
            case 1: F.b[0][0][1] = lds_tr(pb0 + 512); break;
            case 2: F.a[0] = lds_b128(pa); break;
            case 3: F.b[0][1][0] = lds_tr(pb1); break;
            case 4: F.b[0][1][1] = lds_tr(pb1 + 512); break;
            case 5: F.a[1] = lds_b128(pa + 2048); break;
            case 6: F.a[2] = lds_b128(pa + 4096); break;
            case 7: F.a[3] = lds_b128(pa + 6144); break;
            case 8: F.b[1][0][0] = lds_tr(pb0 + 8192); break;
            case 9: F.b[1][0][1] = lds_tr(pb0 + 8192 + 512); break;
            case 10: F.b[1][1][0] = lds_tr(pb1 + 8192); break;
            case 11: F.b[1][1][1] = lds_tr(pb1 + 8192 + 512); break;
            case 12: F.b[2][0][0] = lds_tr(pb0 + 16384); break;
            case 13: F.b[2][0][1] = lds_tr(pb0 + 16384 + 512); break;
            case 14: F.b[2][1][0] = lds_tr(pb1 + 16384); break;
            case 15: F.b[2][1][1] = lds_tr(pb1 + 16384 + 512); break;
            case 16: if (LOCAL) F.h = lds_b128(pa + 16384 - WM.value * 8192); break;
            default: break;
        }
    };
    auto half = [&](auto WM, const Frags& C, Frags& N, bool doread, lds_cptr pa, lds_cptr pb0, lds_cptr pb1, auto tail) {
#pragma unroll
        for (int G = 0; G < 3; ++G) {
            const bf16x8 b0 = cat8(C.b[G][0][0], C.b[G][0][1]), b1 = cat8(C.b[G][1][0], C.b[G][1][1]);
#pragma unroll
            for (int t = 0; t < 4; ++t) {
                const int n = G * 8 + t * 2;
                if (G < 2) acc[G][t][0] = MFMA16(b0, C.a[t], acc[G][t][0]); else acc[G][t][0] = MFMA16(C.a[t], b0, acc[G][t][0]);
                if (n == 0) SBAR();
                if (doread) rd(WM, N, pa, pb0, pb1, n);
                tail(n);
                SBAR();
                if (G < 2) acc[G][t][1] = MFMA16(b1, C.a[t], acc[G][t][1]); else acc[G][t][1] = MFMA16(C.a[t], b1, acc[G][t][1]);
                if (doread) rd(WM, N, pa, pb0, pb1, n + 1);
                tail(n + 1);
                SBAR();
            }
        }
        if (LOCAL) {
            if (WM.value == 0) { acch[0] = MFMA16(cat8(C.b[1][0][0], C.b[1][0][1]), C.h, acch[0]); acch[1] = MFMA16(cat8(C.b[1][1][0], C.b[1][1][1]), C.h, acch[1]); }
            else         { acch[0] = MFMA16(C.h, cat8(C.b[2][0][0], C.b[2][0][1]), acch[0]); acch[1] = MFMA16(C.h, cat8(C.b[2][1][0], C.b[2][1][1]), acch[1]); }
            SBAR();
        }
    };

    Frags F0, F1;
    f32x4 cq[2], ck[2], mu4[5], rs4[5]; float cv[2], mu[5], rs[5];
    u32x4 qfr[4], kfr[4];
    u32x4 vfr[4], cc;
    f32x4 mwin, mfirst;
    typedef __attribute__((address_space(3))) u32x4* lds_u4;
    volatile __attribute__((address_space(3))) unsigned* fl = (volatile __attribute__((address_space(3))) unsigned*)(smw + OFF_SYNC + 16);
    constexpr int OFF_CST = C::OFF_CSP + 3072;
    const bool negpos = (wm == 0) && (panel == 0);
    if (LOCAL) {
        const unsigned keepv = negpos ? __float_as_uint(NEG_MASK) : 0u;
        u32x4 mw, mf;
        bool cur[4];
#pragma unroll
        for (int r = 0; r < 4; ++r) {
            const int d = l15 - (4 * g + r);
            const unsigned out = (unsigned)(((d << 28) >> 31));
            mw[r] = out & 0xff800000u;
            mf[r] = (out & 0xff800000u) | (~out & keepv);
            cur[r] = d >= 0;
        }
        const unsigned e0 = (unsigned)(OFF_EX + wn * 6144);
        lds_u4 cst = (lds_u4)(smw + OFF_CST) + lane;
        fl[w] = 0u;
        cst[0] = mw; cst[64 + wm * 64] = mf;
        cst[192 + w * 64] = u32x4{(cur[0] ? 0xffffu : 0u) | (cur[1] ? 0xffff0000u : 0u), (cur[2] ? 0xffffu : 0u) | (cur[3] ? 0xffff0000u : 0u),
                                  e0 + (unsigned)(wm ? lane : 256 + (lane ^ 8)) * 16u, e0 + (unsigned)(wm ? 128 + lane : 320 + (lane ^ 32)) * 16u};
    }
    typedef const __attribute__((address_space(3))) f32x4* lds_f4; typedef const __attribute__((address_space(3))) float* lds_f1;
    auto mainloop = [&](auto WM) {
#pragma unroll
        for (int a = 0; a < 3; ++a)
            asm volatile("" : "+v"(acc[a][0][0]), "+v"(acc[a][0][1]), "+v"(acc[a][1][0]), "+v"(acc[a][1][1]), "+v"(acc[a][2][0]), "+v"(acc[a][2][1]),
                         "+v"(acc[a][3][0]), "+v"(acc[a][3][1]));
        int ty = threadIdx.x; asm volatile("" : "+v"(ty));
        const int l15 = ty & 15, g = (ty >> 4) & 3;
        const int swA = (l15 >> 1) & 7;
        const int q4 = l15 >> 2, p4 = l15 & 3, fk = ((q4 >> 1) & 1) | ((g & 1) << 1);
        lds_cptr pA0 = sm + wm * 8192 + l15 * 128 + ((0 + g) ^ swA) * 16, pA1 = sm + wm * 8192 + l15 * 128 + ((4 + g) ^ swA) * 16;
        lds_cptr pB0 = sm + ABYTES + (8 * g + q4) * 128 + (((2 * wn + 0) ^ fk) * 32) + p4 * 8;
        lds_cptr pB1 = sm + ABYTES + (8 * g + q4) * 128 + (((2 * wn + 1) ^ fk) * 32) + p4 * 8;
        asm volatile("" : "+v"(acch[0]), "+v"(acch[1]), "+v"(pA0), "+v"(pA1), "+v"(pB0), "+v"(pB1));
        BAR();
#pragma unroll
        for (int n = 0; n < 17; ++n) rd(WM, F0, pA0, pB0, pB1, n);
        WAIT_LGKM();
        int off = 0;
        lds_cptr a = pA1, b0 = pB0, b1 = pB1;
#pragma unroll 1
        for (int t = 0; t < NT - 1; ++t) {
            half(WM, F0, F1, true, a, b0 + 4096, b1 + 4096, [&](int n) {
                if (n == 17) { off = (off == 2 * STAGE) ? 0 : off + STAGE; asm volatile("" : "+s"(off)); }
                if (n == 19) { b0 = pB0 + off; asm volatile("" : "+v"(b0)); }
                if (n == 20) { a = pA0 + off; asm volatile("" : "+v"(a)); }
                if (n == 21) { b1 = pB1 + off; asm volatile("" : "+v"(b1)); }
            });
            WAIT_LGKM_BAR();
            half(WM, F1, F0, true, a, b0, b1, [&](int n) {
                if (n == 19) { a = pA1 + off; asm volatile("" : "+v"(a)); }
                if (n == 22) __builtin_amdgcn_s_waitcnt(0xC07F);
            });
        }
        lds_cptr s1 = sm, s4 = sm, c1 = sm, c4 = sm;
        half(WM, F0, F1, true, a, b0 + 4096, b1 + 4096, [&](int n) {
            if (n == 16) { ty = threadIdx.x; asm volatile("" : "+v"(ty)); }
            if (n == 17) { s1 = sm + C::OFF_MEAN + wm * 256 + (ty & 15) * 4; asm volatile("" : "+v"(s1)); }
            if (n == 18) { s4 = sm + C::OFF_MEAN + wm * 256 + (ty & 48); asm volatile("" : "+v"(s4)); }
            if (n == 19) { c1 = sm + C::OFF_CS + wn * 128 + (ty & 15) * 4; asm volatile("" : "+v"(c1)); }
            if (n == 20) { c4 = sm + C::OFF_CS + wn * 128 + (ty & 48); asm volatile("" : "+v"(c4)); }
        });
        WAIT_LGKM_BAR();
        half(WM, F1, F0, false, sm, sm, sm, [&](int n) {
            constexpr int RS = C::OFF_RSTD - C::OFF_MEAN, H = 512 - WM.value * 256;
            if (n < 2) { if (!LOCAL) cq[n] = *(lds_f4)(c4 + n * 64); ck[n] = *(lds_f4)(c4 + 256 + n * 64); }
            else if (n < 6) { mu[n - 2] = *(lds_f1)(s1 + (n - 2) * 64); rs[n - 2] = *(lds_f1)(s1 + RS + (n - 2) * 64); }
            else if (n < 10) { if (!LOCAL || (n & 1)) { mu4[n - 6] = *(lds_f4)(s4 + (n - 6) * 64); rs4[n - 6] = *(lds_f4)(s4 + RS + (n - 6) * 64); } }
            else if (n == 10) { cv[0] = *(lds_f1)(c1 + 512); cv[1] = *(lds_f1)(c1 + 512 + 64); }
            else if (LOCAL && n == 11) {
                if (WM.value == 0) { mu[4] = *(lds_f1)(s1 + H); rs[4] = *(lds_f1)(s1 + RS + H); }
                else { mu4[4] = *(lds_f4)(s4 + H); rs4[4] = *(lds_f4)(s4 + RS + H); }
            }
        });
        if (LOCAL) {
            auto fix = [&](int a, int t) {
#pragma unroll
                for (int f = 0; f < 2; ++f)
                    acc[a][t][f] = (a == 2) ? rs4[t] * (acc[2][t][f] + mu4[t] * cv[f]) : rs[t] * (acc[a][t][f] + mu[t] * (a ? ck[f] : cq[f]));
            };
            auto pk = [&](const f32x4 (&v)[2]) { return u32x4{pack_bf16(v[0][0], v[0][1]), pack_bf16(v[0][2], v[0][3]), pack_bf16(v[1][0], v[1][1]), pack_bf16(v[1][2], v[1][3])}; };
            cq[0] = *(lds_f4)c4; cq[1] = *(lds_f4)(c4 + 64);
            SBAR();
            if (!ident) {
                fix(1, 1); fix(1, 3); fix(2, 1); fix(2, 3);
#pragma unroll
                for (int f = 0; f < 2; ++f)
                    acch[f] = (WM.value == 0) ? rs[4] * (acch[f] + mu[4] * ck[f]) : rs4[4] * (acch[f] + mu4[4] * cv[f]);
            }
            kfr[1] = pk(acc[1][1]); kfr[3] = pk(acc[1][3]); vfr[1] = pk(acc[2][1]); vfr[3] = pk(acc[2][3]);
            lds_u4 ex = (lds_u4)(smw + OFF_EX + wn * 6144) + (ty & 63);
            if (WM.value == 0) { ex[0] = kfr[1]; ex[64] = kfr[3]; ex[128] = vfr[1]; ex[192] = vfr[3]; }
            ex[256 + WM.value * 64] = pk(acch);
#pragma unroll
            for (int t = 0; t < 4; t += 2) { mu4[t] = *(lds_f4)(s4 + t * 64); rs4[t] = *(lds_f4)(s4 + (C::OFF_RSTD - C::OFF_MEAN) + t * 64); }
            SBAR();
            if (!ident) { fix(0, 0); fix(0, 1); fix(0, 2); fix(0, 3); }
#pragma unroll
            for (int t = 0; t < 4; ++t) qfr[t] = pk(acc[0][t]);
            WAIT_LGKM();
            fl[w] = 1u;
            lds_u4 cst = (lds_u4)(smw + OFF_CST) + (ty & 63);
            mwin = __builtin_bit_cast(f32x4, cst[0]); mfirst = __builtin_bit_cast(f32x4, cst[64 + WM.value * 64]); cc = cst[192 + w * 64];
            SBAR();
            if (!ident) { fix(1, 0); fix(1, 2); fix(2, 0); fix(2, 2); }
            kfr[0] = pk(acc[1][0]); kfr[2] = pk(acc[1][2]); vfr[0] = pk(acc[2][0]); vfr[2] = pk(acc[2][2]);
        }
    };
    const int head = ct * 2 + wn;
    unsigned qo = 0, ko = 0, vo = 0;
    if (LOCAL) {
        qo = (unsigned)((((panel * 8 + wm * 4) * 32 + head) * 64 + lane) * 16);
        asm volatile("" : "+v"(qo));
    } else {
        ko = (unsigned)((wm * 8 + (g & 1)) * 1024 + ((l15 & 7) + ((l15 & 8) << 1) + 32 * (g >> 1)) * 16);
        vo = (unsigned)((wm * 8 + 2 + (g >> 1)) * 1024 + (l15 + 32 * (g & 1)) * 16);
        asm volatile("" : "+v"(ko), "+v"(vo));
    }
    if (LOCAL && wm) mainloop(IC<1>{}); else mainloop(IC<0>{});

    if (!LOCAL) {
        auto pk = [&](f32x4 a, f32x4 b) { return u32x4{pack_bf16(a[0], a[1]), pack_bf16(a[2], a[3]), pack_bf16(b[0], b[1]), pack_bf16(b[2], b[3])}; };
        char* kvp = (char*)kvf + (size_t)(head * 16 + panel) * 16384;
        lds_ptr own = smw + wn * 16384;
        SBAR();
#pragma unroll
        for (int t = 0; t < 4; ++t) {
            const u32x4 kk = pk(rs[t] * (acc[1][t][0] + mu[t] * ck[0]), rs[t] * (acc[1][t][1] + mu[t] * ck[1]));
            if (t & 1) st16o<128>(kvp + (t >> 1) * 4096, ko, kk); else st16o<0>(kvp + (t >> 1) * 4096, ko, kk);
            *(lds_u4)(own + ko + (t >> 1) * 4096 + (t & 1) * 128) = kk;
            SBAR();
            const int u = t >> 1, f = t & 1;
            const u32x4 vv = pk(rs4[2 * u] * (acc[2][2 * u][f] + mu4[2 * u] * cv[f]), rs4[2 * u + 1] * (acc[2][2 * u + 1][f] + mu4[2 * u + 1] * cv[f]));
            if (f) st16o<256>(kvp + u * 4096, vo, vv); else st16o<0>(kvp + u * 4096, vo, vv);
            *(lds_u4)(own + vo + u * 4096 + f * 256) = vv;
            SBAR();
        }
#pragma unroll
        for (int u = 0; u < 2; ++u) {
            const float r0 = rs[2 * u] * QSCALE, r1 = rs[2 * u + 1] * QSCALE;
            const u32x4 qa = pk(r0 * (acc[0][2 * u][0] + mu[2 * u] * cq[0]), r0 * (acc[0][2 * u][1] + mu[2 * u] * cq[1]));
            const u32x4 qb = pk(r1 * (acc[0][2 * u + 1][0] + mu[2 * u + 1] * cq[0]), r1 * (acc[0][2 * u + 1][1] + mu[2 * u + 1] * cq[1]));
#pragma unroll
            for (int e = 0; e < 4; ++e) {
                const auto x = __builtin_amdgcn_permlane16_swap(qa[e], qb[e], false, false);
                qo4[2 * u][e] = x[0]; qo4[2 * u + 1][e] = x[1];
            }
        }
        return;
    }
    {
        const f32x4 zero4 = {0, 0, 0, 0};
        const unsigned cm01 = cc[0], cm23 = cc[1];
#pragma unroll 1
        for (int spin = 0; spin < (1 << 22); ++spin)
            if (__builtin_amdgcn_readfirstlane(fl[w ^ 1]) != 0u) break;
        asm volatile("" ::: "memory");
        u32x4 kp[2], vp[2];
        kp[0] = *(lds_u4)(smw + cc[2]); kp[1] = *(lds_u4)(smw + (cc[2] ^ (wm ? 1024u : 128u)));
        vp[0] = *(lds_u4)(smw + cc[3]); vp[1] = *(lds_u4)(smw + (cc[3] ^ (wm ? 1024u : 512u)));
        if (negpos) { kp[0] = u32x4{0, 0, 0, 0}; kp[1] = kp[0]; }
        f32x4 sp[4], sc[4];
#pragma unroll
        for (int i = 0; i < 4; ++i) {
            const int t = (i == 0) ? 1 : (i == 1) ? 3 : (i == 2) ? 0 : 2;
            const bf16x8 qb = __builtin_bit_cast(bf16x8, qfr[t]);
            sc[t] = MFMA16(__builtin_bit_cast(bf16x8, kfr[t]), qb, mwin);
            sp[t] = MFMA16(__builtin_bit_cast(bf16x8, (t & 1) ? kfr[t - 1] : kp[t >> 1]), qb, (t & 1) ? mwin : mfirst);
        }
        SBAR();
        float x[4][4], m[4], inv[4], c2[4];
        unsigned a[4][2];
#pragma unroll
        for (int t = 0; t < 4; ++t)
#pragma unroll
            for (int h = 0; h < 2; ++h) {
                const unsigned uc = pack_bf16(sc[t][2 * h], sc[t][2 * h + 1]), up = pack_bf16(sp[t][2 * h], sp[t][2 * h + 1]);
                const unsigned cm = h ? cm23 : cm01;
                a[t][h] = (uc & cm) | (up & ~cm);
            }
        SBAR();
#pragma unroll
        for (int t = 0; t < 4; ++t)
#pragma unroll
            for (int h = 0; h < 2; ++h) { x[t][2 * h] = bf_lo(a[t][h]) * ATT_SCALE; x[t][2 * h + 1] = bf_hi(a[t][h]) * ATT_SCALE; }
        SBAR();
#pragma unroll
        for (int t = 0; t < 4; ++t)
#pragma unroll
            for (int h = 0; h < 2; ++h) a[t][h] = pack_bf16(x[t][2 * h], x[t][2 * h + 1]);
        SBAR();
#pragma unroll
        for (int t = 0; t < 4; ++t)
#pragma unroll
            for (int h = 0; h < 2; ++h) { x[t][2 * h] = bf_lo(a[t][h]); x[t][2 * h + 1] = bf_hi(a[t][h]); }
        SBAR();
#pragma unroll
        for (int t = 0; t < 4; ++t) m[t] = amax3(x[t][0], x[t][1], x[t][2]);
#pragma unroll
        for (int t = 0; t < 4; ++t) m[t] = amax3(m[t], x[t][3], x[t][3]);
        SBAR();
        asm volatile("s_nop 1");
        c2[0] = m[0]; c2[1] = m[1]; c2[2] = m[2]; c2[3] = m[3];
        asm volatile("" : "+v"(c2[0]), "+v"(c2[1]), "+v"(c2[2]), "+v"(c2[3]));
#pragma unroll
        for (int t = 0; t < 4; ++t) {
            const auto u = __builtin_amdgcn_permlane16_swap(__float_as_uint(m[t]), __float_as_uint(c2[t]), false, false);
            m[t] = amax3(__uint_as_float(u[0]), __uint_as_float(u[1]), __uint_as_float(u[1]));
        }
        SBAR();
        asm volatile("s_nop 1");
        c2[0] = m[0]; c2[1] = m[1]; c2[2] = m[2]; c2[3] = m[3];
        asm volatile("" : "+v"(c2[0]), "+v"(c2[1]), "+v"(c2[2]), "+v"(c2[3]));
#pragma unroll
        for (int t = 0; t < 4; ++t) {
            const auto u = __builtin_amdgcn_permlane32_swap(__float_as_uint(m[t]), __float_as_uint(c2[t]), false, false);
            m[t] = amax3(__uint_as_float(u[0]), __uint_as_float(u[1]), __uint_as_float(u[1]));
        }
        SBAR();
#pragma unroll
        for (int t = 0; t < 4; ++t)
#pragma unroll
            for (int r = 0; r < 4; ++r) x[t][r] = (x[t][r] - m[t]) * LOG2E;
        SBAR();
#pragma unroll
        for (int t = 0; t < 4; ++t)
#pragma unroll
            for (int r = 0; r < 4; ++r) x[t][r] = __builtin_amdgcn_exp2f(x[t][r]);
        SBAR();
#pragma unroll
        for (int t = 0; t < 4; ++t) inv[t] = (x[t][0] + x[t][1]) + (x[t][2] + x[t][3]);
        SBAR();
        c2[0] = inv[0]; c2[1] = inv[1]; c2[2] = inv[2]; c2[3] = inv[3];
        asm volatile("" : "+v"(c2[0]), "+v"(c2[1]), "+v"(c2[2]), "+v"(c2[3]));
#pragma unroll
        for (int t = 0; t < 4; ++t) {
            const auto u = __builtin_amdgcn_permlane16_swap(__float_as_uint(inv[t]), __float_as_uint(c2[t]), false, false);
            inv[t] = __uint_as_float(u[0]) + __uint_as_float(u[1]);
        }
        SBAR();
        c2[0] = inv[0]; c2[1] = inv[1]; c2[2] = inv[2]; c2[3] = inv[3];
        asm volatile("" : "+v"(c2[0]), "+v"(c2[1]), "+v"(c2[2]), "+v"(c2[3]));
#pragma unroll
        for (int t = 0; t < 4; ++t) {
            const auto u = __builtin_amdgcn_permlane32_swap(__float_as_uint(inv[t]), __float_as_uint(c2[t]), false, false);
            inv[t] = __builtin_amdgcn_rcpf(__uint_as_float(u[0]) + __uint_as_float(u[1]));
        }
        SBAR();
#pragma unroll
        for (int t = 0; t < 4; ++t)
#pragma unroll
            for (int h = 0; h < 2; ++h) a[t][h] = pack_bf16(x[t][2 * h] * inv[t], x[t][2 * h + 1] * inv[t]);
        SBAR();
        f32x4 o0[4], o1[4];
#pragma unroll
        for (int t = 0; t < 4; ++t) {
            const bf16x8 pb = __builtin_bit_cast(bf16x8, (u32x4{a[t][0] & ~cm01, a[t][1] & ~cm23, a[t][0] & cm01, a[t][1] & cm23}));
            const u32x4 vprev = (t & 1) ? vfr[t - 1] : vp[t >> 1];
            o0[t] = MFMA16(__builtin_bit_cast(bf16x8, (u32x4{vprev[0], vprev[1], vfr[t][0], vfr[t][1]})), pb, zero4);
            o1[t] = MFMA16(__builtin_bit_cast(bf16x8, (u32x4{vprev[2], vprev[3], vfr[t][2], vfr[t][3]})), pb, zero4);
        }
        SBAR();
#pragma unroll
        for (int t = 0; t < 4; ++t)
            st16(ofrag + t * 16384, qo, u32x4{pack_bf16(o0[t][0], o0[t][1]), pack_bf16(o0[t][2], o0[t][3]),
                                                 pack_bf16(o1[t][0], o1[t][1]), pack_bf16(o1[t][2], o1[t][3])});
    }
}

DI int out_row(int panel, bool localrows, int T, int a) {
    return localrows ? (panel * 128 + (T >> 2) * 64 + 2 * (16 * (T & 1) + a) + ((T >> 1) & 1)) : (panel * 128 + T * 16 + a);
}
DI int out_swz(int k) { return ((k >> 1) & 1) | (k & 4); }
DI void wo_quarter(const void* Wo, int q, unsigned ldsdst, int w, int lane) {
    const int kl = lane >> 3;
    const unsigned voff = (unsigned)(w * 8 + kl) * 2048u + (unsigned)(ids().co * 128 + ((lane & 7) ^ out_swz(kl)) * 16);
#pragma unroll
    for (int i = 0; i < 8; ++i) glds16((const char*)Wo + (size_t)(q * 8 + i) * 65536, voff, ldsdst + (w + 4 * i) * 1024);
}

template <bool AFTER_LOCAL>
DI void out_compute(char* smem, char* ws, int set, u64 tag, const __hip_bfloat16* __restrict__ ofrag, const __hip_bfloat16* __restrict__ Xin,
                    __hip_bfloat16* __restrict__ Xout, float* __restrict__ statp SP_PARAM) {
    int tz = threadIdx.x; asm volatile("" : "+v"(tz));
    const int lane = tz & 63;
    const int w = __builtin_amdgcn_readfirstlane(tz >> 6) & 3;
    const Ids I = ids();
    const int panel = I.po, ct = I.co;
    const int l15 = lane & 15, g = lane >> 4;
    auto orow = [&](int T, int a) -> int { return out_row(panel, AFTER_LOCAL, T, a); };
    const int q4 = l15 >> 2, p4 = l15 & 3;
    const int krow = 4 * g + q4;
    const int sw = out_swz(krow);
    const lds_cptr sm = (lds_cptr)smem;
    int bw[4];
#pragma unroll
    for (int t = 0; t < 4; ++t) bw[t] = krow * 128 + ((((t >> 1) * 4 + (p4 >> 1) * 2 + (t & 1)) ^ sw) * 16) + (p4 & 1) * 8;
    unsigned xo[2], so[2], sto, ao;
#pragma unroll
    for (int r2 = 0; r2 < 2; ++r2) {
        xo[r2] = (unsigned)orow(2 * w + r2, l15) * 2048u + (unsigned)(ct * 128 + (g >> 1) * 32 + (g & 1) * 8);
        so[r2] = (unsigned)orow(2 * w + r2, l15 & 7) * 2048u + (unsigned)(ct * 128 + (l15 >> 3) * 64 + g * 16);
    }
    sto = (unsigned)(((panel * 16 + ct) * 128 + 2 * w * 16 + l15) * 8);
    ao = (unsigned)((panel * 8 + 2 * w) * 32768 + lane * 16);
    asm volatile("" : "+v"(bw[0]), "+v"(bw[1]), "+v"(bw[2]), "+v"(bw[3]), "+v"(xo[0]), "+v"(xo[1]), "+v"(so[0]), "+v"(so[1]), "+v"(sto), "+v"(ao));
    const __amdgpu_buffer_rsrc_t rx = __builtin_amdgcn_make_buffer_rsrc((void*)Xin, 0, 4 << 20, 0x00020000);
    const __amdgpu_buffer_rsrc_t ra = __builtin_amdgcn_make_buffer_rsrc((void*)ofrag, 0, 4 << 20, 0x00020000);
    VM_BAR(0);
    STAMP(AFTER_LOCAL ? 3 : 10);
    if (w == 0) flag_store(ws, set, I.pq, I.cq, tag, lane);
    u32x2 xr[2][4];
#pragma unroll
    for (int r2 = 0; r2 < 2; ++r2)
#pragma unroll
        for (int c = 0; c < 4; ++c) xr[r2][c] = __builtin_amdgcn_raw_buffer_load_b64(rx, xo[r2] + (c >> 1) * 64 + (c & 1) * 16, 0, 16);
    if (w == 0) flag_poll(flag_line(ws, set, panel, ct) + l15 * 8, tag);
    BAR();
    STAMP(AFTER_LOCAL ? 4 : 11);
    u32x4 a[16][2];
    auto lda = [&](int j) {
        a[j & 15][0] = __builtin_amdgcn_raw_buffer_load_b128(ra, ao + (j & 3) * 1024, (j >> 2) * 4096, 16);
        a[j & 15][1] = __builtin_amdgcn_raw_buffer_load_b128(ra, ao + (j & 3) * 1024, 32768 + (j >> 2) * 4096, 16);
    };
#pragma unroll
    for (int j = 0; j < 16; ++j) lda(j);
    SBAR();
    auto boff = [&](int j) { return (j & 15) * 4096 - ((!AFTER_LOCAL && j >= 24) ? 32768 : 0); };
    s16x4 F[2][4][2];
#pragma unroll
    for (int h = 0; h < 2; ++h)
#pragma unroll
        for (int t = 0; t < 4; ++t) { F[h][t][0] = lds_tr(sm + bw[t] + boff(h)); F[h][t][1] = lds_tr(sm + bw[t] + boff(h) + 2048); }
    f32x4 acc[2][4];
#pragma unroll
    for (int r2 = 0; r2 < 2; ++r2)
#pragma unroll
        for (int c = 0; c < 4; ++c) acc[r2][c] = f32x4{bf_lo(xr[r2][c][0]), bf_hi(xr[r2][c][0]), bf_lo(xr[r2][c][1]), bf_hi(xr[r2][c][1])};
    WAIT_LGKM();
#pragma unroll
    for (int j = 0; j < 32; ++j) {
        s16x4 (&C)[4][2] = F[j & 1];
        const bf16x8 a0 = __builtin_bit_cast(bf16x8, a[j & 15][0]), a1 = __builtin_bit_cast(bf16x8, a[j & 15][1]);
        const int nb = boff(j + 2);
        if (j + 2 == 16 || (!AFTER_LOCAL && j + 2 == 24)) {
#pragma unroll
            for (int t = 0; t < 4; ++t) bw[t] += 65536;
            asm volatile("" : "+v"(bw[0]), "+v"(bw[1]), "+v"(bw[2]), "+v"(bw[3]));
        }
#pragma unroll
        for (int t = 0; t < 4; ++t) {
            const bf16x8 bb = cat8(C[t][0], C[t][1]);
            acc[0][t] = MFMA16(bb, a0, acc[0][t]);
            if (t > 0 && j < 30) C[t - 1][1] = lds_tr(sm + bw[t - 1] + nb + 2048);
            SBAR();
            acc[1][t] = MFMA16(bb, a1, acc[1][t]);
            if (j < 30) C[t][0] = lds_tr(sm + bw[t] + nb);
            if (t == 3 && j < 30) C[3][1] = lds_tr(sm + bw[3] + nb + 2048);
            if (t == 3 && j < 16) lda(j + 16);
            SBAR();
        }
    }
    STAMP(AFTER_LOCAL ? 5 : 12);
    u32x4 y[2][2];
#pragma unroll
    for (int r2 = 0; r2 < 2; ++r2)
#pragma unroll
        for (int u = 0; u < 2; ++u) {
            const f32x4 c0 = acc[r2][2 * u], c1 = acc[r2][2 * u + 1];
            const auto sa = __builtin_amdgcn_permlane16_swap(pack_bf16(c0[0], c0[1]), pack_bf16(c1[0], c1[1]), false, false);
            const auto sb = __builtin_amdgcn_permlane16_swap(pack_bf16(c0[2], c0[3]), pack_bf16(c1[2], c1[3]), false, false);
            y[r2][u] = u32x4{sa[0], sb[0], sa[1], sb[1]};
            if (u) {
                u32x4 za, zb;
#pragma unroll
                for (int e = 0; e < 4; ++e) {
                    za[e] = __builtin_amdgcn_update_dpp(y[r2][0][e], y[r2][1][e], 0x128, 0xF, 0xC, false);
                    zb[e] = __builtin_amdgcn_update_dpp(y[r2][1][e], y[r2][0][e], 0x128, 0xF, 0x3, false);
                }
                st16(Xout, so[r2], za);
                st16(Xout, so[r2] + (AFTER_LOCAL ? 16 : 8) * 2048, zb);
                SBAR();
            }
        }
    const bf16x8 ones = __builtin_bit_cast(bf16x8, (u32x4{0x3f803f80u, 0x3f803f80u, 0x3f803f80u, 0x3f803f80u}));
    const f32x4 zero4 = {0, 0, 0, 0};
    f32x4 d1[2], d2[2];
#pragma unroll
    for (int r2 = 0; r2 < 2; ++r2) {
        const bf16x8 y0 = __builtin_bit_cast(bf16x8, y[r2][0]), y1 = __builtin_bit_cast(bf16x8, y[r2][1]);
        d2[r2] = MFMA16(y0, y0, zero4); d1[r2] = MFMA16(ones, y0, zero4);
        d2[r2] = MFMA16(y1, y1, d2[r2]); d1[r2] = MFMA16(ones, y1, d1[r2]);
    }
    const bool b0 = p4 & 1, b1 = p4 & 2;
    if (g == q4) {
#pragma unroll
        for (int r2 = 0; r2 < 2; ++r2) {
            float e0 = d2[r2][0], e1 = d2[r2][1], e2 = d2[r2][2], e3 = d2[r2][3];
            asm("" : "+v"(e0), "+v"(e1), "+v"(e2), "+v"(e3));
            const float lo = b0 ? e1 : e0, hi = b0 ? e3 : e2;
            const f32x2 sv = {d1[r2][0], b1 ? hi : lo};
            if (r2) st8o<128>(statp, sto, sv); else st8o<0>(statp, sto, sv);
        }
    }
    BAR();
}

#define ROWSUM(p0, p1, c) __builtin_amdgcn_smfmac_f32_16x16x64_bf16(sel, __builtin_bit_cast(bf16v_t, __builtin_shufflevector(p0, p1, 0, 1, 2, 3, 4, 5, 6, 7)), c, sidx, 0, 0)
constexpr int ATT_STAGE = 32768;
typedef __attribute__((ext_vector_type(8))) unsigned u32x8;
typedef __attribute__((ext_vector_type(32))) unsigned u32x32;

DI void attn_loader(char* smem, char* ws, int set, u64 tag, const __hip_bfloat16* __restrict__ kvf, const void* Wo SP_PARAM) {
    int tid = threadIdx.x; asm volatile("" : "+v"(tid));
    const int lane = tid & 63;
    const int w = __builtin_amdgcn_readfirstlane(tid >> 6) & 3;
    const Ids I = ids();
    const int panel = I.pq, ct = I.cq;
    const unsigned lds0 = (unsigned)(uintptr_t)smem;
    volatile __attribute__((address_space(3))) unsigned* go = (volatile __attribute__((address_space(3))) unsigned*)((lds_ptr)smem + OFF_SYNC);
    const char* kvh = (const char*)kvf + (size_t)(ct * 2 + (w >> 1)) * 262144 + (w & 1) * 8192;
    const unsigned voff = (unsigned)(lane * 16);
    auto load = [&](int st) {
        const char* src = kvh + (size_t)((panel + st) & 15) * 16384;
        const unsigned dst = lds0 + w * 8192 + (st & 3) * ATT_STAGE;
#pragma unroll
        for (int u = 0; u < 2; ++u)
            asm volatile("s_mov_b32 m0, %0\n\ts_nop 0\n\tglobal_load_lds_dwordx4 %1, %2 sc1\n\tglobal_load_lds_dwordx4 %1, %2 offset:1024 sc1\n\t"
                         "global_load_lds_dwordx4 %1, %2 offset:2048 sc1\n\tglobal_load_lds_dwordx4 %1, %2 offset:3072 sc1"
                         :: "s"(dst + u * 4096), "v"(voff), "s"(src + u * 4096) : "memory");
    };
    if (w == 0) go[0] = 0u;
    WAIT_LGKM_BAR();
    BAR();
    STAMP(9);
    if (w == 0) {
        flag_store(ws, set, ct, panel, tag, lane);
        flag_poll(flag_line(ws, set, ct, panel) + (lane & 15) * 8, tag);
        go[0] = 1u;
    } else {
#pragma unroll 1
        for (int spin = 0; spin < (1 << 22); ++spin) {
            if (__builtin_amdgcn_readfirstlane(go[0]) != 0u) break;
            __builtin_amdgcn_s_sleep(1);
        }
        asm volatile("" ::: "memory");
    }
    STAMP(10);
    load(1); load(2); load(3);
    VM_BAR(16);
    STAMP(11);
#pragma unroll 1
    for (int st = 1; st < 13; ++st) {
        VM_BAR(8);
        if (st == 6) wo_quarter(Wo, 3, lds0 + 131072, w, lane);
        load(st + 3);
    }
    VM_BAR(8); wo_quarter(Wo, 0, lds0, w, lane);
    VM_BAR(8); wo_quarter(Wo, 1, lds0 + 32768, w, lane);
    BAR();     wo_quarter(Wo, 2, lds0 + 65536, w, lane);
}

DI void attn_compute(char* smem, const u32x4 (&q4)[4], const __hip_bfloat16* __restrict__ kvf, __hip_bfloat16* __restrict__ ofrag SP_PARAM) {
    int tz = threadIdx.x; asm volatile("" : "+v"(tz));
    const int lane = tz & 63;
    const int w = __builtin_amdgcn_readfirstlane(tz >> 6) & 3;
    const int wm = w & 1, wn = w >> 1;
    const Ids I = ids();
    const int panel = I.pq, head = I.cq * 2 + wn;
    const int l15 = lane & 15;
    const unsigned se = ((l15 & 2) == 0 && (l15 & 4) == 0) ? 0x3f803f80u : 0u, so = ((l15 & 2) == 0 && (l15 & 4) != 0) ? 0x3f803f80u : 0u;
    const bf8_t sel = __builtin_bit_cast(bf8_t, u32x4{se, se, so, so});
    const int sidx = (l15 & 1) ? (int)0xEEEEEEEEu : 0x44444444;
    const lds_cptr sm = (lds_cptr)smem + wn * 16384 + lane * 16;
    const f32x16 z16 = {0, 0, 0, 0, 0, 0, 0, 0, 0, 0, 0, 0, 0, 0, 0, 0};
    const bf16x8 qA0 = __builtin_bit_cast(bf16x8, q4[0]), qA1 = __builtin_bit_cast(bf16x8, q4[1]);
    const bf16x8 qB0 = __builtin_bit_cast(bf16x8, q4[2]), qB1 = __builtin_bit_cast(bf16x8, q4[3]);
    f32x16 oA, oB, nmA, nmB, sA, sB;
    f32x4 lsA, lsB;
    u32x8 pB;
    bf16x8 v11;
    asm volatile(".irp r,2,4,6,8,10,12,14,16,18,20,22,24,26,28,30,32,106,108,110,112,134,136,138,140,170,172\n\tv_mov_b64 v[\\r:\\r+1], 0\n\t.endr"
                 : "={v[2:17]}"(oA), "={v[18:33]}"(oB), "={v[106:113]}"(pB), "={v[134:137]}"(lsA), "={v[138:141]}"(lsB), "={v[170:173]}"(v11));
    WAIT_LGKM_BAR();
    STAMP(8);
    bf16x8 k00 = lds_b128(sm), k10 = lds_b128(sm + 1024), k01 = lds_b128(sm + 4096), k11 = lds_b128(sm + 4096 + 1024);
    bf16x8 v00 = lds_b128(sm + 2048), v10 = lds_b128(sm + 3072), v01 = lds_b128(sm + 4096 + 2048);
    {
        sA = MFMA32(k00, qA0, z16); sB = MFMA32(k00, qB0, z16);
        sA = MFMA32(k10, qA1, sA); sB = MFMA32(k10, qB1, sB);
        float mA = sA[0], mB = sB[0];
#pragma unroll
        for (int r = 1; r < 16; ++r) { mA = fmaxf(mA, sA[r]); mB = fmaxf(mB, sB[r]); }
        mA = max_h(mA); mB = max_h(mB);
#pragma unroll
        for (int r = 0; r < 16; ++r) { nmA[r] = -mA; nmB[r] = -mB; sA[r] -= mA; }
    }
    const unsigned a0 = (unsigned)(uintptr_t)sm;
    asm volatile(
        ".macro ATT_RD dst, y\n\t"
        "ds_read_b128 v[\\dst:\\dst+3], v[175+(((\\y)&0x1ffff)>>16)] offset:(\\y)&0xffff\n\t"
        ".endm\n\t"
        ".macro ATT_E4 c\n\t"
        "v_exp_f32 v[\\c], v[\\c]\n\tv_exp_f32 v[\\c+1], v[\\c+1]\n\tv_exp_f32 v[\\c+2], v[\\c+2]\n\tv_exp_f32 v[\\c+3], v[\\c+3]\n\t"
        ".endm\n\t"
        ".macro ATT_C4 p, c\n\t"
        "v_cvt_pk_bf16_f32 v[\\p], v[\\c], v[\\c+1]\n\tv_cvt_pk_bf16_f32 v[\\p+1], v[\\c+2], v[\\c+3]\n\t"
        "v_cvt_pk_bf16_f32 v[\\p+2], v[\\c+4], v[\\c+5]\n\tv_cvt_pk_bf16_f32 v[\\p+3], v[\\c+6], v[\\c+7]\n\t"
        ".endm\n\t"
        ".macro ATT_STEP cur, nxt, ka, kb, q, nm, oc, od, lsd, pc, pd, vd1, vc0, r0, y0, r1, y1, bar\n\t"
        "v_mfma_f32_32x32x16_bf16 v[\\nxt:\\nxt+15], v[\\ka:\\ka+3], v[\\q:\\q+3], v[\\nm:\\nm+15]\n\t"
        "ATT_E4 \\cur\n\t"
        "v_mfma_f32_32x32x16_bf16 v[\\nxt:\\nxt+15], v[\\kb:\\kb+3], v[\\q+4:\\q+7], v[\\nxt:\\nxt+15]\n\t"
        "ATT_E4 \\cur+4\n\t"
        "v_mfma_f32_32x32x16_bf16 v[\\od:\\od+15], v[\\vd1:\\vd1+3], v[\\pd+4:\\pd+7], v[\\od:\\od+15]\n\t"
        "s_waitcnt lgkmcnt(0)\n\t"
        "ATT_C4 \\pc, \\cur\n\t"
        "v_exp_f32 v[\\cur+8], v[\\cur+8]\n\tv_exp_f32 v[\\cur+9], v[\\cur+9]\n\t"
        "v_mfma_f32_32x32x16_bf16 v[\\oc:\\oc+15], v[\\vc0:\\vc0+3], v[\\pc:\\pc+3], v[\\oc:\\oc+15]\n\t"
        ".if \\bar\n\ts_barrier\n\t.endif\n\t"
        "ATT_E4 \\cur+10\n\t"
        "v_smfmac_f32_16x16x64_bf16 v[\\lsd:\\lsd+3], v[130:133], v[\\pd:\\pd+7], v174\n\t"
        "ATT_RD \\r0, \\y0\n\t"
        "ATT_RD \\r1, \\y1\n\t"
        "v_exp_f32 v[\\cur+14], v[\\cur+14]\n\tv_exp_f32 v[\\cur+15], v[\\cur+15]\n\t"
        "ATT_C4 \\pc+4, \\cur+8\n\t"
        ".endm\n\t"
        ".macro ATT_TILE p, y1, y2, bar\n\t"
        "ATT_STEP 66, 82, 142+\\p, 150+\\p, 122, 50, 2, 18, 138, 98, 106, 170-\\p, 158+\\p, 170-\\p, \\y1+3072, 142+\\p, \\y2, \\bar\n\t"
        "ATT_STEP 82, 66, 146-\\p, 154-\\p, 114, 34, 18, 2, 134, 106, 98, 166+\\p, 158+\\p, 158+\\p, \\y2+2048, 150+\\p, \\y2+1024, 0\n\t"
        ".endm\n\t"
        ".macro ATT_STAGE b\n\t"
        "ATT_TILE 0, \\b+4096, \\b+8192, 0\n\t"
        "ATT_TILE 4, \\b+8192, \\b+12288, 0\n\t"
        "ATT_TILE 0, \\b+12288, \\b+32768, 1\n\t"
        "ATT_TILE 4, \\b+32768, \\b+36864, 0\n\t"
        ".endm\n\t"
        "v_add_u32 v176, 0x10000, v175\n\t"
        "s_mov_b32 s96, 4\n\t"
        "s_waitcnt vmcnt(0) lgkmcnt(0)\n\t"
        "s_barrier\n\t"
        ".p2align 6\n"
        "att_loop_%=:\n\t"
        "ATT_STAGE 0\n\tATT_STAGE 32768\n\tATT_STAGE 65536\n\t"
        "s_sub_u32 s96, s96, 1\n\t"
        "ATT_STAGE 98304\n\t"
        "s_cmp_lg_u32 s96, 0\n\t"
        "s_cbranch_scc1 att_loop_%=\n\t"
        "s_nop 1\n\t"
        "v_mfma_f32_32x32x16_bf16 v[18:33], v[170:173], v[110:113], v[18:33]\n\t"
        "v_smfmac_f32_16x16x64_bf16 v[138:141], v[130:133], v[106:113], v174\n\t"
        "s_waitcnt lgkmcnt(0)\n\ts_nop 10\n\t"
        ".purgem ATT_RD\n\t.purgem ATT_E4\n\t.purgem ATT_C4\n\t.purgem ATT_STEP\n\t.purgem ATT_TILE\n\t.purgem ATT_STAGE"
        : "+{v[2:17]}"(oA), "+{v[18:33]}"(oB), "+{v[66:81]}"(sA), "+{v[106:113]}"(pB), "+{v[134:137]}"(lsA), "+{v[138:141]}"(lsB),
          "+{v[142:145]}"(k00), "+{v[146:149]}"(k01), "+{v[150:153]}"(k10), "+{v[154:157]}"(k11),
          "+{v[158:161]}"(v00), "+{v[162:165]}"(v01), "+{v[166:169]}"(v10), "+{v[170:173]}"(v11)
        : "{v[34:49]}"(nmA), "{v[50:65]}"(nmB), "{v[114:117]}"(qA0), "{v[118:121]}"(qA1), "{v[122:125]}"(qB0), "{v[126:129]}"(qB1),
          "{v[130:133]}"(sel), "{v174}"(sidx), "{v175}"(a0)
        : "memory", "scc", "s96", "v176", "v82", "v83", "v84", "v85", "v86", "v87", "v88", "v89", "v90", "v91", "v92", "v93", "v94", "v95",
          "v96", "v97", "v98", "v99", "v100", "v101", "v102", "v103", "v104", "v105");
    STAMP(9);
    float lA = lsA[0] + lsA[1], lB = lsB[0] + lsB[1];
    {
        const f32x16 os = oA + oB;
        const float osum = (((os[0] + os[1]) + (os[2] + os[3])) + ((os[4] + os[5]) + (os[6] + os[7]))) +
                           (((os[8] + os[9]) + (os[10] + os[11])) + ((os[12] + os[13]) + (os[14] + os[15])));
        const bool bad = !(lA < 1.0e24f) || !(lB < 1.0e24f) || !(fabsf(osum) < 3.0e38f);
        if (__any(bad)) {
            const __amdgpu_buffer_rsrc_t rk = __builtin_amdgcn_make_buffer_rsrc((char*)kvf + (size_t)head * 262144, 0, 262144, 0x00020000);
            oA = z16; oB = z16; lA = 0.f; lB = 0.f;
            float mA = -INFINITY, mB = -INFINITY;
#pragma unroll 1
            for (int t = 0; t < 64; ++t) {
                bf16x8 f[4];
#pragma unroll
                for (int i = 0; i < 4; ++i) f[i] = __builtin_bit_cast(bf16x8, __builtin_amdgcn_raw_buffer_load_b128(rk, lane * 16 + i * 1024, t * 4096, 16));
                auto upd = [&](bf16x8 q0, bf16x8 q1, f32x16& o, float& l, float& m) {
                    f32x16 s = MFMA32(f[0], q0, z16); s = MFMA32(f[1], q1, s);
                    float tm = s[0];
#pragma unroll
                    for (int r = 1; r < 16; ++r) tm = fmaxf(tm, s[r]);
                    tm = max_h(tm);
                    const float mn = fmaxf(m, tm);
                    const float alpha = __builtin_amdgcn_exp2f(m - mn);
                    m = mn;
#pragma unroll
                    for (int r = 0; r < 16; ++r) { s[r] = __builtin_amdgcn_exp2f(s[r] - mn); o[r] *= alpha; }
                    const u32x4 p0 = {pack_bf16(s[0], s[1]), pack_bf16(s[2], s[3]), pack_bf16(s[4], s[5]), pack_bf16(s[6], s[7])};
                    const u32x4 p1 = {pack_bf16(s[8], s[9]), pack_bf16(s[10], s[11]), pack_bf16(s[12], s[13]), pack_bf16(s[14], s[15])};
                    f32x4 t4 = {0, 0, 0, 0};
                    o = MFMA32(f[2], __builtin_bit_cast(bf16x8, p0), o);
                    o = MFMA32(f[3], __builtin_bit_cast(bf16x8, p1), o);
                    t4 = ROWSUM(p0, p1, t4);
                    l = l * alpha + (t4[0] + t4[1]);
                };
                upd(qA0, qA1, oA, lA, mA);
                upd(qB0, qB1, oB, lB, mB);
            }
        }
    }
    const unsigned dst = (unsigned)((((panel * 8 + wm * 4 + ((lane & 31) >> 4)) * 32 + head) * 64 + (lane & 15) + (lane >> 5) * 16) * 16);
#pragma unroll
    for (int u = 0; u < 2; ++u) {
        const f32x16 e = (u ? oB : oA) * __builtin_amdgcn_rcpf(u ? lB : lA);
        st16o<0>((char*)ofrag + u * 65536, dst, u32x4{pack_bf16(e[0], e[1]), pack_bf16(e[2], e[3]), pack_bf16(e[8], e[9]), pack_bf16(e[10], e[11])});
        st16o<512>((char*)ofrag + u * 65536, dst, u32x4{pack_bf16(e[4], e[5]), pack_bf16(e[6], e[7]), pack_bf16(e[12], e[13]), pack_bf16(e[14], e[15])});
    }
}

DI void ln_rows(const __hip_bfloat16* __restrict__ X, __hip_bfloat16* __restrict__ Y) {
    const int lane = threadIdx.x & 63;
    const int w = __builtin_amdgcn_readfirstlane(threadIdx.x >> 6);
    const int row = ids().po * 128 + ids().co * 8 + w * 2;
    const u32x4* p = (const u32x4*)X + (size_t)row * 128 + lane;
    u32x4 v[2][2];
    v[0][0] = p[0]; v[0][1] = p[64]; v[1][0] = p[128]; v[1][1] = p[192];
    float s1[2] = {0.f, 0.f}, s2[2] = {0.f, 0.f}, mean[2];
#pragma unroll
    for (int r = 0; r < 2; ++r)
#pragma unroll
        for (int e = 0; e < 8; ++e) s1[r] += bf_lo(v[r][e >> 2][e & 3]) + bf_hi(v[r][e >> 2][e & 3]);
#pragma unroll
    for (int r = 0; r < 2; ++r) mean[r] = wave_sum(s1[r]) * (1.f / 1024.f);
#pragma unroll
    for (int r = 0; r < 2; ++r)
#pragma unroll
        for (int e = 0; e < 8; ++e) {
            const float d0 = bf_lo(v[r][e >> 2][e & 3]) - mean[r], d1 = bf_hi(v[r][e >> 2][e & 3]) - mean[r];
            s2[r] += d0 * d0 + d1 * d1;
        }
#pragma unroll
    for (int r = 0; r < 2; ++r) {
        const float mb = round_bf16(mean[r]);
        const float rden = __builtin_amdgcn_rcpf(round_bf16(__builtin_amdgcn_sqrtf(round_bf16(round_bf16(wave_sum(s2[r]) * (1.f / 1024.f)) + 1e-5f))));
#pragma unroll
        for (int hh = 0; hh < 2; ++hh) {
            u32x4 y;
#pragma unroll
            for (int e = 0; e < 4; ++e) {
                const unsigned d = pack_bf16(bf_lo(v[r][hh][e]) - mb, bf_hi(v[r][hh][e]) - mb);
                y[e] = pack_bf16(bf_lo(d) * rden, bf_hi(d) * rden);
            }
            st16(Y, (unsigned)((row + r) * 2048 + hh * 1024 + lane * 16), y);
        }
    }
}

extern "C" __global__ __launch_bounds__(512, 2) void mega_kernel(const __hip_bfloat16* x, const __hip_bfloat16* wqkvA,
                                                            const __hip_bfloat16* woA, const __hip_bfloat16* wqkvB,
                                                            const __hip_bfloat16* woB, __hip_bfloat16* out,
                                                            char* ws) {
    extern __shared__ __attribute__((aligned(16))) char smem[];
    typedef __hip_bfloat16 bf;
    asm volatile("" :: "s"(x), "s"(wqkvA), "s"(woA), "s"(wqkvB), "s"(woB), "s"(out), "s"(ws));
    const int w8 = __builtin_amdgcn_readfirstlane(threadIdx.x >> 6);
    STAMP_DECL; STAMPR(14); STAMP(0);
    auto get_epoch = [&]() { return __hip_atomic_load((g_u64ptr)(ws + WS_EPOCH), __ATOMIC_RELAXED, __HIP_MEMORY_SCOPE_AGENT); };
    auto get_tag = [&](u64 epoch) {
        return (((u64)__builtin_amdgcn_readfirstlane((unsigned)(epoch >> 32)) << 32) | (unsigned)__builtin_amdgcn_readfirstlane((unsigned)epoch)) + 1;
    };
    if (w8 >= 4) {
        int w = w8 & 3;
        u64 tag = 0, epoch = 0;
        u32x4 ca[8], cb[8];
#pragma unroll 1
        for (int it = 0; it < 2; ++it) {
            int lane = threadIdx.x & 63; asm volatile("" : "+v"(lane));
            asm volatile("" : "+s"(wqkvA), "+s"(woA), "+s"(wqkvB), "+s"(woB), "+s"(ws), "+s"(w));
            const unsigned lds0 = (unsigned)(uintptr_t)smem;
            unsigned junk = 0;
            qkv_loader<true>(smem, (const bf*)(ws + (it ? WS_X2 : WS_Y0)), wqkvA, (const float*)(ws + WS_ST + 262144),
                             it ? ws + WS_CSP : (const char*)wqkvA, it == 0, [&] {
                if (it) return;
                if (w < 3) colsum_load(ca, wqkvA, w, lane);
                else {
                    if ((blockIdx.x >> 3) < 12) code_touch(junk, blockIdx.x >> 3, lane);
                    asm volatile("global_load_dword %0, %1, %2" : "+v"(junk) : "v"((blockIdx.x * 64 + lane) * 128), "s"(woA) : "memory");
                }
                asm volatile("global_load_dwordx2 %0, %1, %2 sc1" : "+v"(epoch) : "v"(0), "s"(ws + WS_EPOCH) : "memory");
            } SP_ARG);
            asm volatile("" : "+v"(epoch) : "v"(junk));
            tag = get_tag(epoch);
            STAMP(3);
#pragma unroll
            for (int q = 0; q < 4; ++q) wo_quarter(woA, q, lds0 + q * 32768, w, lane);
            const bool cs = (it == 0 && w < 3);
            if (cs) colsum_load(cb, wqkvB, w, lane);
            STAMP(4);
            BAR();
            if (cs) VM_BAR(8); else VM_BAR(0);
            if (cs) {
                asm volatile("s_waitcnt vmcnt(0)" : "+v"(cb[0]), "+v"(cb[1]), "+v"(cb[2]), "+v"(cb[3]), "+v"(cb[4]), "+v"(cb[5]), "+v"(cb[6]), "+v"(cb[7]) :: "memory");
                asm volatile("" : "+v"(ca[0]), "+v"(ca[1]), "+v"(ca[2]), "+v"(ca[3]), "+v"(ca[4]), "+v"(ca[5]), "+v"(ca[6]), "+v"(ca[7]));
                colsum_store(ca, ws + WS_CSP, w, lane);
                colsum_store(cb, ws + WS_CSP + 196608, w, lane);
                asm volatile("s_waitcnt vmcnt(0)" ::: "memory");
                flag_store(ws, 10 + w, ids().cq, ids().pq, tag, lane);
            }
            BAR();
            STAMP(5);
            qkv_loader<false>(smem, (const bf*)(ws + (it ? WS_X3 : WS_X1)), wqkvB, (const float*)(ws + WS_ST + it * 524288), ws + WS_CSP + 196608,
                              false, [] {} SP_ARG);
            STAMP(8);
            attn_loader(smem, ws, it * 5 + 3, tag, (const bf*)(ws + WS_KVF + it * 8 * MBy), woB SP_ARG);
            STAMP(12);
            BAR();
            VM_BAR(0);
            BAR();
            STAMP(13); STAMPR(15); STAMP_FLUSH(it);
        }
        if (blockIdx.x == 0 && threadIdx.x == 256) __hip_atomic_store((g_u64ptr)(ws + WS_EPOCH), tag, __ATOMIC_RELAXED, __HIP_MEMORY_SCOPE_AGENT);
    } else {
        __builtin_amdgcn_s_setprio(3);
        const u64 epoch = get_epoch();
        ln_rows(x, (bf*)(ws + WS_Y0));
        const u64 tag = get_tag(epoch);
        auto handoff = [&](int set, bool local) {
            VM_BAR(0);
            if (w8 == 0) {
                int lane = threadIdx.x; asm volatile("" : "+v"(lane));
                const Ids I = ids();
                flag_store(ws, set, I.po, I.co, tag, lane);
                const int r = lane >> 4;
                const char* p = local ? flag_line(ws, set, (r & 1) && I.pq ? I.pq - 1 : I.pq, I.cq)
                                      : (r ? flag_line(ws, 9 + r, I.cq, I.pq) : flag_line(ws, set, I.pq, I.cq));
                flag_poll(p + (lane & 15) * 8, tag);
            }
            BAR();
        };
#pragma unroll 1
        for (int it = 0; it < 2; ++it) {
            u32x4 q4[4];
            asm volatile("" : "+s"(ws));
            handoff(it * 5, true);
            STAMP(1);
            qkv_compute<true>(smem, q4, nullptr, (bf*)(ws + WS_OF + it * 8 * MBy), it == 0);
            STAMP(2);
            out_compute<true>(smem, ws, it * 5 + 1, tag, (const bf*)(ws + WS_OF + it * 8 * MBy), it ? (const bf*)(ws + WS_X2) : x,
                              (bf*)(ws + (it ? WS_X3 : WS_X1)), (float*)(ws + WS_ST + it * 524288) SP_ARG);
            STAMP(6);
            handoff(it * 5 + 2, false);
            STAMP(7);
            qkv_compute<false>(smem, q4, (bf*)(ws + WS_KVF + it * 8 * MBy), nullptr, false);
            attn_compute(smem, q4, (const bf*)(ws + WS_KVF + it * 8 * MBy), (bf*)(ws + WS_OF + (it * 8 + 4) * MBy) SP_ARG);
            out_compute<false>(smem, ws, it * 5 + 4, tag, (const bf*)(ws + WS_OF + (it * 8 + 4) * MBy), (const bf*)(ws + (it ? WS_X3 : WS_X1)),
                               it ? out : (bf*)(ws + WS_X2), (float*)(ws + WS_ST + 262144 + it * 524288) SP_ARG);
            STAMP(13); STAMPR(15); STAMP_FLUSH(it);
        }
    }
}

extern "C" __global__ void zz_code_end_kernel() {}

extern "C" void kernel_launch(void* const* d_in, const int* in_sizes, int n_in, void* d_out, int out_size, void* d_ws,
                              size_t ws_size, hipStream_t stream) {
    typedef __hip_bfloat16 bf;
    (void)hipFuncSetAttribute((const void*)mega_kernel, hipFuncAttributeMaxDynamicSharedMemorySize, CH_LDS);
    mega_kernel<<<256, 512, CH_LDS, stream>>>((const bf*)d_in[0], (const bf*)d_in[1], (const bf*)d_in[2], (const bf*)d_in[4], (const bf*)d_in[5],
                                              (bf*)d_out, (char*)d_ws);
}
```

```cpp
#include <hip/hip_runtime.h>
#include <hip/hip_bf16.h>
#include <stdint.h>

typedef __attribute__((ext_vector_type(8))) short bf16x8;
typedef __attribute__((ext_vector_type(4))) short s16x4;
typedef __attribute__((ext_vector_type(4))) float f32x4;
typedef __attribute__((ext_vector_type(2))) float f32x2;
typedef __attribute__((ext_vector_type(16))) float f32x16;
typedef __attribute__((ext_vector_type(4))) unsigned u32x4;
typedef __attribute__((ext_vector_type(2))) unsigned u32x2;
typedef __attribute__((ext_vector_type(2))) __bf16 bf16x2_t;
typedef __attribute__((ext_vector_type(8))) __bf16 bf8_t;
typedef __attribute__((ext_vector_type(16))) __bf16 bf16v_t;
typedef __attribute__((address_space(3))) const char* lds_cptr;
typedef __attribute__((address_space(3))) char* lds_ptr;

#define DI __device__ __forceinline__
#define MFMA16(a, b, c) __builtin_amdgcn_mfma_f32_16x16x32_bf16((a), (b), (c), 0, 0, 0)
#define MFMA32(a, b, c) __builtin_amdgcn_mfma_f32_32x32x16_bf16((a), (b), (c), 0, 0, 0)
#define SBAR() __builtin_amdgcn_sched_barrier(0)
#define STAMP(i)
#define STAMPR(i)
#define STAMP_DECL
#define STAMP_FLUSH(it)
#define SP_PARAM
#define SP_ARG
typedef unsigned long long u64;

constexpr float QSCALE = 0.17677669529663687f * 1.4426950408889634f;
constexpr float ATT_SCALE = 0.17677669529663687f;
constexpr float LOG2E = 1.4426950408889634f;
constexpr float NEG_MASK = -1e9f / ATT_SCALE;

DI unsigned pack_bf16(float lo, float hi) {
    f32x2 v = {lo, hi};
    bf16x2_t r = __builtin_convertvector(v, bf16x2_t);
    return __builtin_bit_cast(unsigned, r);
}
DI float bf_lo(unsigned u) { return __uint_as_float(u << 16); }
DI float bf_hi(unsigned u) { return __uint_as_float(u & 0xffff0000u); }
DI float round_bf16(float x) { return bf_lo(pack_bf16(x, 0.f)); }
DI void ln_stats(float sum, float sumsq, float& mean_out, float& rstd_out) {
    const float mean = sum * (1.f / 1024.f);
    const float var = fmaxf(sumsq * (1.f / 1024.f) - mean * mean, 0.f);
    mean_out = round_bf16(mean);
    rstd_out = __builtin_amdgcn_rcpf(round_bf16(__builtin_amdgcn_sqrtf(round_bf16(round_bf16(var) + 1e-5f))));
}

DI void glds16(const void* sbase, unsigned voff, unsigned ldsaddr) {
    asm volatile("s_mov_b32 m0, %0\n\ts_nop 0\n\tglobal_load_lds_dwordx4 %1, %2"
                 :: "s"(ldsaddr), "v"(voff), "s"(sbase) : "memory");
}
DI void glds16c(const void* sbase, unsigned voff, unsigned ldsaddr) {
    asm volatile("s_mov_b32 m0, %0\n\ts_nop 0\n\tglobal_load_lds_dwordx4 %1, %2 sc1"
                 :: "s"(ldsaddr), "v"(voff), "s"(sbase) : "memory");
}
DI void st16(void* sbase, unsigned voff, u32x4 v) {
    asm volatile("global_store_dwordx4 %0, %1, %2 sc1\n\ts_nop 1" :: "v"(voff), "v"(v), "s"(sbase) : "memory");
}
template <int OFF> DI void st16o(void* sbase, unsigned voff, u32x4 v) {
    asm volatile("global_store_dwordx4 %0, %1, %2 offset:%3 sc1\n\ts_nop 1" :: "v"(voff), "v"(v), "s"(sbase), "n"(OFF) : "memory");
}
DI bf16x8 lds_b128(lds_cptr p) { return *(const __attribute__((address_space(3))) bf16x8*)p; }
DI s16x4 lds_tr(lds_cptr p) {
    return __builtin_amdgcn_ds_read_tr16_b64_v4i16((__attribute__((address_space(3))) s16x4*)p);
}
DI bf16x8 cat8(s16x4 a, s16x4 b) { return __builtin_shufflevector(a, b, 0, 1, 2, 3, 4, 5, 6, 7); }
DI float dot2bf(unsigned a, unsigned b, float c) {
    return __builtin_amdgcn_fdot2_f32_bf16(__builtin_bit_cast(bf16x2_t, a), __builtin_bit_cast(bf16x2_t, b), c, false);
}
DI float sum_g(float v) {
    auto a = __builtin_amdgcn_permlane16_swap(__float_as_uint(v), __float_as_uint(v), false, false);
    v = __uint_as_float(a[0]) + __uint_as_float(a[1]);
    auto b = __builtin_amdgcn_permlane32_swap(__float_as_uint(v), __float_as_uint(v), false, false);
    return __uint_as_float(b[0]) + __uint_as_float(b[1]);
}
DI float wave_sum(float v) {
    v += __builtin_bit_cast(float, __builtin_amdgcn_update_dpp(0, __builtin_bit_cast(int, v), 0xB1, 0xF, 0xF, true));
    v += __builtin_bit_cast(float, __builtin_amdgcn_update_dpp(0, __builtin_bit_cast(int, v), 0x4E, 0xF, 0xF, true));
    v += __builtin_bit_cast(float, __builtin_amdgcn_update_dpp(0, __builtin_bit_cast(int, v), 0x141, 0xF, 0xF, true));
    v += __builtin_bit_cast(float, __builtin_amdgcn_update_dpp(0, __builtin_bit_cast(int, v), 0x140, 0xF, 0xF, true));
    return sum_g(v);
}
DI float amax3(float a, float b, float c) { float r; asm("v_max3_f32 %0, %1, %2, %3" : "=v"(r) : "v"(a), "v"(b), "v"(c)); return r; }
DI float max2f(float a, float b) { return __builtin_amdgcn_fmed3f(a, b, INFINITY); }
DI float max_h(float v) {
    auto b = __builtin_amdgcn_permlane32_swap(__float_as_uint(v), __float_as_uint(v), false, false);
    return max2f(__uint_as_float(b[0]), __uint_as_float(b[1]));
}

template <int OFF> DI void st8o(void* sbase, unsigned voff, f32x2 v) {
    asm volatile("global_store_dwordx2 %0, %1, %2 offset:%3 sc1\n\ts_nop 1" :: "v"(voff), "v"(v), "s"(sbase), "n"(OFF) : "memory");
}
DI void code_touch(unsigned& x, int slice, int lane) {
    unsigned long long c0, c1;
    asm volatile("s_getpc_b64 s[96:97]\n\ts_add_u32 s96, s96, mega_kernel@rel32@lo+4\n\ts_addc_u32 s97, s97, mega_kernel@rel32@hi+12\n\ts_mov_b64 %0, s[96:97]\n\t"
                 "s_getpc_b64 s[96:97]\n\ts_add_u32 s96, s96, zz_code_end_kernel@rel32@lo+4\n\ts_addc_u32 s97, s97, zz_code_end_kernel@rel32@hi+12\n\ts_mov_b64 %1, s[96:97]"
                 : "=s"(c0), "=s"(c1) :: "s96", "s97", "scc");
    const unsigned off = min((unsigned)(slice * 64 + lane) * 128u, (unsigned)(c1 - c0));
    asm volatile("global_load_dword %0, %1, %2" : "+v"(x) : "v"(off), "s"(c0) : "memory");
}

#define BAR() asm volatile("s_barrier" ::: "memory")
#define WAIT_LGKM() do { SBAR(); __builtin_amdgcn_s_waitcnt(0xC07F); SBAR(); } while (0)
#define WAIT_LGKM_BAR() do { SBAR(); __builtin_amdgcn_s_waitcnt(0xC07F); asm volatile("s_barrier" ::: "memory"); SBAR(); } while (0)
#define VM_BAR(n) asm volatile("s_waitcnt vmcnt(" #n ")\n\ts_barrier" ::: "memory")
constexpr int CH_LDS = 163840;
constexpr int OFF_SYNC = CH_LDS - 64;
typedef __attribute__((address_space(3))) unsigned* lds_uptr;
typedef __attribute__((address_space(1))) u64* g_u64ptr;

constexpr size_t MBy = 1 << 20;
constexpr size_t WS_X3 = 0, WS_X2 = 4 * MBy, WS_X1 = 8 * MBy, WS_OF = 12 * MBy  , WS_KVF = 36 * MBy  ,
                 WS_ST = 52 * MBy  , WS_CSP = 54 * MBy  , WS_FLAG = 55 * MBy  ,
                 WS_EPOCH = WS_FLAG + 15 * 65536, WS_Y0 = 56 * MBy;
DI unsigned flag_off(int set, int group, int consumer, int producer) { return (unsigned)(set * 65536 + group * 4096 + consumer * 256 + producer * 8); }
DI void flag_store(char* ws, int set, int group, int producer, u64 tag, int lane) {
    asm volatile("" : "+s"(tag));
    if (lane < 16) __hip_atomic_store((g_u64ptr)(ws + WS_FLAG + flag_off(set, group, lane, producer)), tag, __ATOMIC_RELAXED, __HIP_MEMORY_SCOPE_AGENT);
}
DI void flag_poll(const char* ws, unsigned off, u64 tag) {
    asm volatile("" : "+s"(tag));
#pragma unroll 1
    for (int spin = 0; spin < (1 << 16); ++spin) {
        const u64 v = __hip_atomic_load((g_u64ptr)(ws + WS_FLAG + off), __ATOMIC_RELAXED, __HIP_MEMORY_SCOPE_AGENT);
        if (__all(v == tag)) break;
        __builtin_amdgcn_s_sleep(1);
    }
    asm volatile("" ::: "memory");
}
#define POLL_INS "global_atomic_or_x2 %[land], %[po], %[zero], %[base] sc0 sc1"
template <bool DRAIN, bool ALLPOLL>
DI void signal_poll(int follower, const char* ws, unsigned soff, unsigned poff, u64 pmask, u64 tag, u64& land) {
    u64 tagv, zero;
    asm volatile("s_not_b64 s[96:97], %[ts]\n\tv_mov_b64 %[tag], %[ts]\n\tv_mov_b64 %[zero], 0\n\tv_mov_b64 %[land], s[96:97]\n\t"
                 ".if %[dr]\n\ts_waitcnt vmcnt(0)\n\t.endif\n\ts_barrier\n\t"
                 "s_cmp_lg_u32 %[fl], 0\n\ts_cbranch_scc1 sig_fol_%=\n\t"
                 "s_mov_b64 exec, 0xffff\n\t"
                 "global_store_dwordx2 %[so], %[tag], %[base] sc1\n"
                 ".if %[ap]\nsig_fol_%=:\n.endif\n\t"
                 "s_mov_b64 exec, %[pm]\n\t"
                 "s_movk_i32 s96, 0x7fff\n"
                 "sig_loop_%=:\n\t"
                 POLL_INS "\n\t"
                 "s_sleep 1\n\t"
                 "s_waitcnt vmcnt(3)\n\t"
                 "v_cmp_ne_u64 vcc, %[land], %[tag]\n\t"
                 "s_sub_u32 s96, s96, 1\n\t"
                 "s_cbranch_scc1 sig_out_%=\n\t"
                 "s_cbranch_vccnz sig_loop_%=\n"
                 "sig_out_%=:\n\t"
                 "s_mov_b64 exec, -1\n"
                 ".if !%[ap]\nsig_fol_%=:\n.endif"
                 : [land] "=&v"(land), [tag] "=&v"(tagv), [zero] "=&v"(zero)
                 : [fl] "s"(follower), [so] "v"(soff), [po] "v"(poff), [ts] "s"(tag), [base] "s"(ws + WS_FLAG), [pm] "s"(pmask), [ap] "n"(ALLPOLL ? 1 : 0), [dr] "n"(DRAIN ? 1 : 0)
                 : "memory", "scc", "vcc", "s96", "s97");
}
struct Ids { int pq, cq, po, co; };
DI Ids ids() {
    int bid = blockIdx.x; asm volatile("" : "+s"(bid));
    const int xcd = bid & 7, idx = bid >> 3;
    int pq, cq, po, co;
#define IDS_BODY pq = (xcd & 1) * 8 + (idx & 7); cq = (xcd >> 1) * 4 + (idx >> 3); po = (xcd & 3) * 4 + (idx & 3); co = (xcd >> 2) * 8 + (idx >> 2);
    IDS_BODY
    return Ids{pq, cq, po, co};
}

DI void slow_warm(unsigned dump, const void* W, int j0, int n, int lane) {
    int bid = blockIdx.x; asm volatile("" : "+s"(bid));
    const int xcd = bid & 7, idx = bid >> 3;
    const int sc = (3 - (int)((uintptr_t)W >> 7)) & 7;
    if ((sc >> 2) != ((xcd >> 1) & 1)) return;
    for (int j = j0; j < j0 + n; ++j) {
        const int pc = idx + 32 * j, rb = pc / 3, G = pc - rb * 3;
        glds16(W, (unsigned)(rb * 8 + (lane >> 3)) * 6144u + (unsigned)(G * 2048 + (sc + (xcd >> 2) * 8) * 128 + (lane & 7) * 16), dump);
    }
}

template <int V> struct IC { static constexpr int value = V; };
template <bool LOCAL> struct QC {
    static constexpr int AROWS = LOCAL ? 144 : 128;
    static constexpr int ABYTES = AROWS * 128;
    static constexpr int STAGE = ABYTES + 24576;
    static constexpr int OFF_STATP = 129024;
    static constexpr int OFF_MEAN = OFF_STATP + 18432;
    static constexpr int OFF_RSTD = OFF_MEAN + 576;
    static constexpr int OFF_CS = OFF_RSTD + 576;
    static constexpr int OFF_CSP = OFF_CS + 768;
};
constexpr int OFF_EX = 131072;

DI void colsum_load(u32x4 (&c)[8], const void* W, int G, int lane) {
    const Ids I = ids();
    const unsigned off = (unsigned)(I.pq * 64 + (lane >> 3)) * 6144u + (unsigned)(G * 2048 + I.cq * 128 + (lane & 7) * 16);
#pragma unroll
    for (int i = 0; i < 8; ++i)
        asm volatile("global_load_dwordx4 %0, %1, %2" : "=&v"(c[i]) : "v"(off), "s"((const char*)W + i * 49152) : "memory");
}
DI void colsum_store(const u32x4 (&c)[8], char* dst, int G, int lane) {
    const Ids I = ids();
    const int m = lane & 15;
    const unsigned one = (m & 1) ? 0x3f800000u : 0x3f80u;
    const u32x4 sl = {(m >> 1) == 0 ? one : 0u, (m >> 1) == 1 ? one : 0u, (m >> 1) == 2 ? one : 0u, (m >> 1) == 3 ? one : 0u};
    f32x4 d = {0, 0, 0, 0};
#pragma unroll
    for (int i = 0; i < 8; ++i) d = MFMA16(__builtin_bit_cast(bf16x8, sl), __builtin_bit_cast(bf16x8, c[i]), d);
#pragma unroll
    for (int r = 0; r < 4; ++r)
        d[r] += __int_as_float(__builtin_amdgcn_update_dpp(0, __float_as_int(d[r]), 0x128, 0xF, 0xF, true));
    if (lane < 32 && (lane & 8) == 0)
        st16(dst, (unsigned)((I.cq * 16 + I.pq) * 768 + G * 256 + (lane & 7) * 32 + (lane >> 4) * 16), __builtin_bit_cast(u32x4, d));
}

template <bool LOCAL>
DI void qkv_loader(char* smem, char* ws, int set, u64 tag, const __hip_bfloat16* __restrict__ X, const __hip_bfloat16* __restrict__ W,
                   const float* __restrict__ statp, const char* __restrict__ csp, bool ident, const void* nextW SP_PARAM) {
    typedef QC<LOCAL> C;
    constexpr int NT = 16;
    int tid = threadIdx.x; asm volatile("" : "+v"(tid));
    const int lane = tid & 63;
    const int w = __builtin_amdgcn_readfirstlane(tid >> 6) & 3;
    const int idx = blockIdx.x >> 3;
    const Ids I = ids();
    const int panel = I.pq, ct = I.cq;
    const int R0 = panel * 128;
    const unsigned lds0 = (unsigned)(uintptr_t)smem;
    const lds_cptr sm = (lds_cptr)smem;
    auto grow = [&](int i) -> int {
        if (!LOCAL) return R0 + i;
        if (i < 128) { const int t = (i >> 4) & 3, a = i & 15; return R0 + (i & 64) + 2 * (16 * (t & 1) + a) + (t >> 1); }
        const int a = i & 15; const int r = R0 - 16 + 2 * (a & 7) + (a >> 3);
        return r < 0 ? 0 : r;
    };
    unsigned voffA[5], voffB[6];
#pragma unroll
    for (int i = 0; i < 6; ++i) {
        const int j = w + 4 * i, G = j >> 3, k = 8 * (j & 7) + (lane >> 3);
        const int pos = lane & 7, f = ((k >> 1) & 1) | (((k >> 3) & 1) << 1);
        const int seg = (pos >> 1) ^ f;
        voffB[i] = (unsigned)k * 6144u + (unsigned)(G * 1024 + ct * 64 + seg * 16 + (pos & 1) * 8) * 2u;
    }
    auto issueW = [&](int t, int buf) {
        const char* wb = (const char*)W + (size_t)t * (64 * 6144);
        const unsigned sb = lds0 + buf * C::STAGE;
#pragma unroll
        for (int i = 0; i < 6; ++i) glds16(wb, voffB[i], sb + C::ABYTES + (w + 4 * i) * 1024);
    };
    unsigned voffP[2];
#pragma unroll
    for (int i = 0; i < 2; ++i) {
        const int k = 8 * i + (lane >> 3), pos = lane & 7;
        voffP[i] = (unsigned)k * 6144u + (unsigned)(w * 1024 + ct * 64 + ((pos >> 1) ^ (((k >> 1) & 1) | (i << 1))) * 16 + (pos & 1) * 8) * 2u;
        asm volatile("" : "+v"(voffP[i]), "+v"(voffB[3 * i]), "+v"(voffB[3 * i + 1]), "+v"(voffB[3 * i + 2]));
    }
#pragma unroll
    for (int i = 0; i < 5; ++i) {
        const int j = (i < 4) ? (w + 4 * i) : (16 + (w & 1));
        const int row = 8 * j + (lane >> 3);
        const int c = (lane & 7) ^ ((row >> 1) & 7);
        voffA[i] = (unsigned)grow(row < C::AROWS ? row : 0) * 2048u + c * 16;
        asm volatile("" : "+v"(voffA[i]));
    }
    auto issueA = [&](int t, int buf) {
        const char* xa = (const char*)X + t * 128;
        const unsigned sb = lds0 + buf * C::STAGE;
        if (LOCAL && w < 2) glds16c(xa, voffA[4], sb + (16 + w) * 1024);
#pragma unroll
        for (int i = 0; i < 4; ++i) glds16c(xa, voffA[i], sb + (w + 4 * i) * 1024);
    };
    u64 land;
    if (w == 3) __builtin_amdgcn_s_setprio(3);
    signal_poll<false, false>(w - 3, ws, flag_off(set, I.po, lane & 15, I.co),
                              LOCAL ? flag_off(set, (lane & 16) && panel ? panel - 1 : panel, ct, lane & 15)
                                    : (lane < 16 ? flag_off(set, panel, ct, lane) : flag_off(9 + (lane >> 4), ct, panel, lane & 15)),
                              LOCAL ? 0xffffffffull : ~0ull, tag, land);
    if (w < 3) {
#pragma unroll
        for (int i = 0; i < 16; ++i)
            glds16((const char*)W + (size_t)(i >> 1) * (16 * 6144), voffP[i & 1], lds0 + (i >> 3) * C::STAGE + C::ABYTES + (w * 8 + (i & 7)) * 1024);
    }
    BAR();
    STAMP(LOCAL ? 1 : 6);
    issueA(0, 0); issueA(1, 1);
    VM_BAR(4);
    asm volatile("" :: "v"(land));
    __builtin_amdgcn_s_setprio(0);
    STAMP(LOCAL ? 2 : 7);
    issueW(2, 2);
    unsigned junkc = 0;
    auto issueS = [&](int i) {
        if (i == 8 && idx < 3) code_touch(junkc, idx * 4 + w, lane);
        if (LOCAL && i == 9)
            glds16(nextW, (unsigned)((idx + 32 * w) * 8 + (lane >> 3)) * 2048u +
                   (unsigned)(((I.co & 8) + ((3 - (int)((uintptr_t)nextW >> 7)) & 7)) * 128 + (lane & 7) * 16), lds0 + 159744);
        if (ident) return;
        if (i < 4) glds16c(statp, (unsigned)((panel * 16 + w + 4 * i) * 1024 + lane * 16), lds0 + C::OFF_STATP + (w + 4 * i) * 1024);
        else if (LOCAL && i == 4 && w < 2) {
            const unsigned o = panel ? (unsigned)(((panel - 1) * 16 + w * 8 + (lane >> 3)) * 1024 + 896 + (lane & 7) * 16)
                                     : (unsigned)((w * 8 + (lane >> 3)) * 1024 + (lane & 7) * 16);
            glds16c(statp, o, lds0 + C::OFF_STATP + (16 + w) * 1024);
        }
    };
    issueA(2, 2);
    VM_BAR(10);
    f32x4 cp[4];
    asm volatile("global_load_dwordx4 %0, %4, %5 sc1\n\tglobal_load_dwordx4 %1, %4, %5 offset:768 sc1\n\t"
                 "global_load_dwordx4 %2, %4, %5 offset:1536 sc1\n\tglobal_load_dwordx4 %3, %4, %5 offset:2304 sc1"
                 : "=&v"(cp[0]), "=&v"(cp[1]), "=&v"(cp[2]), "=&v"(cp[3])
                 : "v"((unsigned)((ct * 16 + w * 4) * 768 + min(lane, 47) * 16)), "s"(csp) : "memory");
    issueA(3, 0); issueW(3, 0);
    int buf = 1;
#pragma unroll 1
    for (int t = 1; t < NT - 3; ++t) {
        VM_BAR(10);
        issueA(t + 3, buf); issueW(t + 3, buf);
        if (t >= 2) issueS(t - 2);
        buf = (buf == 2) ? 0 : buf + 1;
    }
    VM_BAR(10);
    asm volatile("" :: "v"(junkc));
    const int lt = tid - 256;
    float sa = 0.f, sb = 0.f;
    asm volatile("" : "+v"(cp[0]), "+v"(cp[1]), "+v"(cp[2]), "+v"(cp[3]));
    if (!ident) {
        if (lane < 48) *(__attribute__((address_space(3))) f32x4*)((lds_ptr)smem + C::OFF_CSP + w * 768 + lane * 16) = (cp[0] + cp[1]) + (cp[2] + cp[3]);
        if (lt < C::AROWS) {
            const int gr = LOCAL ? grow(lt) - R0 : ((lt & 64) + (lt & 1) * 32 + ((lt & 63) >> 1));
            const int off = (lt < 128) ? gr * 8 : (16384 + (panel ? (gr + 16) : 0) * 8);
            const int str = (lt < 128) ? 1024 : 128;
#pragma unroll
            for (int i = 0; i < 16; ++i) {
                const f32x2 v = *(const __attribute__((address_space(3))) f32x2*)(sm + C::OFF_STATP + off + i * str);
                sa += v[0]; sb += v[1];
            }
        }
    }
    asm volatile("" : "+v"(sa), "+v"(sb));
    WAIT_LGKM();
    VM_BAR(0);
    if (!ident) {
        __attribute__((address_space(3))) float* meanL = (__attribute__((address_space(3))) float*)((lds_ptr)smem + C::OFF_MEAN);
        __attribute__((address_space(3))) float* rstdL = (__attribute__((address_space(3))) float*)((lds_ptr)smem + C::OFF_RSTD);
        __attribute__((address_space(3))) float* csL = (__attribute__((address_space(3))) float*)((lds_ptr)smem + C::OFF_CS);
        if (lt < C::AROWS) {
            float mean, rstd;
            ln_stats(sa, sb, mean, rstd);
            meanL[lt] = mean; rstdL[lt] = rstd;
        }
        if (lt >= 64) {
            const __attribute__((address_space(3))) float* cq = (const __attribute__((address_space(3))) float*)(sm + C::OFF_CSP) + (lt - 64);
            csL[lt - 64] = -((cq[0] + cq[192]) + (cq[384] + cq[576]));
        }
    }
    __builtin_amdgcn_s_setprio(0);
    WAIT_LGKM_BAR();
}

template <bool LOCAL>
DI void qkv_compute(char* smem, const void* W, u32x4 (&qo4)[4], __hip_bfloat16* __restrict__ kvf, __hip_bfloat16* __restrict__ ofrag, bool ident SP_PARAM) {
    typedef QC<LOCAL> C;
    constexpr int NT = 16, ABYTES = C::ABYTES, STAGE = C::STAGE;
    int tz = threadIdx.x; asm volatile("" : "+v"(tz));
    const int lane = tz & 63;
    const int w = __builtin_amdgcn_readfirstlane(tz >> 6) & 3;
    const int wm = w & 1, wn = w >> 1;
    const Ids I = ids();
    const int panel = I.pq, ct = I.cq;
    const int l15 = lane & 15, g = lane >> 4;
    const lds_cptr sm = (lds_cptr)smem;
    lds_ptr smw = (lds_ptr)smem;

    VM_BAR(0);
    f32x4 acc[3][4][2];
    f32x4 acch[2];
    f32x2 z2 = {0, 0}; asm volatile("" : "+v"(z2));
    const f32x4 z4 = {z2[0], z2[1], z2[0], z2[1]};
#pragma unroll
    for (int a = 0; a < 3; ++a)
#pragma unroll
        for (int t = 0; t < 4; ++t) { acc[a][t][0] = z4; acc[a][t][1] = z4; }
    acch[0] = z4; acch[1] = z4;

    struct Frags { bf16x8 a[4]; bf16x8 h; s16x4 b[3][2][2]; };
    auto rd = [&](auto WM, Frags& F, lds_cptr pa, lds_cptr pb0, lds_cptr pb1, int n) {
        switch (n) {
            case 0: F.b[0][0][0] = lds_tr(pb0); break;
            case 1: F.b[0][0][1] = lds_tr(pb0 + 512); break;
            case 2: F.a[0] = lds_b128(pa); break;
            case 3: F.b[0][1][0] = lds_tr(pb1); break;
            case 4: F.b[0][1][1] = lds_tr(pb1 + 512); break;
            case 5: F.a[1] = lds_b128(pa + 2048); break;
            case 6: F.a[2] = lds_b128(pa + 4096); break;
            case 7: F.a[3] = lds_b128(pa + 6144); break;
            case 8: F.b[1][0][0] = lds_tr(pb0 + 8192); break;
            case 9: F.b[1][0][1] = lds_tr(pb0 + 8192 + 512); break;
            case 10: F.b[1][1][0] = lds_tr(pb1 + 8192); break;
            case 11: F.b[1][1][1] = lds_tr(pb1 + 8192 + 512); break;
            case 12: F.b[2][0][0] = lds_tr(pb0 + 16384); break;
            case 13: F.b[2][0][1] = lds_tr(pb0 + 16384 + 512); break;
            case 14: F.b[2][1][0] = lds_tr(pb1 + 16384); break;
            case 15: F.b[2][1][1] = lds_tr(pb1 + 16384 + 512); break;
            case 16: if (LOCAL) F.h = lds_b128(pa + 16384 - WM.value * 8192); break;
            default: break;
        }
    };
    auto half = [&](auto WM, const Frags& C, Frags& N, bool doread, lds_cptr pa, lds_cptr pb0, lds_cptr pb1, auto tail) {
#pragma unroll
        for (int G = 0; G < 3; ++G) {
            const bf16x8 b0 = cat8(C.b[G][0][0], C.b[G][0][1]), b1 = cat8(C.b[G][1][0], C.b[G][1][1]);
#pragma unroll
            for (int t = 0; t < 4; ++t) {
                const int n = G * 8 + t * 2;
                if (G < 2) acc[G][t][0] = MFMA16(b0, C.a[t], acc[G][t][0]); else acc[G][t][0] = MFMA16(C.a[t], b0, acc[G][t][0]);
                if (n == 0) SBAR();
                if (doread) rd(WM, N, pa, pb0, pb1, n);
                tail(n);
                SBAR();
                if (G < 2) acc[G][t][1] = MFMA16(b1, C.a[t], acc[G][t][1]); else acc[G][t][1] = MFMA16(C.a[t], b1, acc[G][t][1]);
                if (doread) rd(WM, N, pa, pb0, pb1, n + 1);
                tail(n + 1);
                SBAR();
            }
        }
        if (LOCAL) {
            if (WM.value == 0) { acch[0] = MFMA16(cat8(C.b[1][0][0], C.b[1][0][1]), C.h, acch[0]); acch[1] = MFMA16(cat8(C.b[1][1][0], C.b[1][1][1]), C.h, acch[1]); }
            else         { acch[0] = MFMA16(C.h, cat8(C.b[2][0][0], C.b[2][0][1]), acch[0]); acch[1] = MFMA16(C.h, cat8(C.b[2][1][0], C.b[2][1][1]), acch[1]); }
            SBAR();
        }
    };

    Frags F0, F1;
    f32x4 cq[2], ck[2], mu4[5], rs4[5]; float cv[2], mu[5], rs[5];
    u32x4 qfr[4], kfr[4];
    u32x4 vfr[4], cc;
    f32x4 mwin, mfirst;
    typedef __attribute__((address_space(3))) u32x4* lds_u4;
    volatile __attribute__((address_space(3))) unsigned* fl = (volatile __attribute__((address_space(3))) unsigned*)(smw + OFF_SYNC + 16);
    constexpr int OFF_CST = C::OFF_CSP + 3072;
    const bool negpos = (wm == 0) && (panel == 0);
    if (LOCAL) {
        const unsigned keepv = negpos ? __float_as_uint(NEG_MASK) : 0u;
        u32x4 mw, mf;
        bool cur[4];
#pragma unroll
        for (int r = 0; r < 4; ++r) {
            const int d = l15 - (4 * g + r);
            const unsigned out = (unsigned)(((d << 28) >> 31));
            mw[r] = out & 0xff800000u;
            mf[r] = (out & 0xff800000u) | (~out & keepv);
            cur[r] = d >= 0;
        }
        const unsigned e0 = (unsigned)(OFF_EX + wn * 6144);
        lds_u4 cst = (lds_u4)(smw + OFF_CST) + lane;
        fl[w] = 0u;
        cst[0] = mw; cst[64 + wm * 64] = mf;
        cst[192 + w * 64] = u32x4{(cur[0] ? 0xffffu : 0u) | (cur[1] ? 0xffff0000u : 0u), (cur[2] ? 0xffffu : 0u) | (cur[3] ? 0xffff0000u : 0u),
                                  e0 + (unsigned)(wm ? lane : 256 + (lane ^ 8)) * 16u, e0 + (unsigned)(wm ? 128 + lane : 320 + (lane ^ 32)) * 16u};
    }
    typedef const __attribute__((address_space(3))) f32x4* lds_f4; typedef const __attribute__((address_space(3))) float* lds_f1;
    auto mainloop = [&](auto WM) {
#pragma unroll
        for (int a = 0; a < 3; ++a)
            asm volatile("" : "+v"(acc[a][0][0]), "+v"(acc[a][0][1]), "+v"(acc[a][1][0]), "+v"(acc[a][1][1]), "+v"(acc[a][2][0]), "+v"(acc[a][2][1]),
                         "+v"(acc[a][3][0]), "+v"(acc[a][3][1]));
        int ty = threadIdx.x; asm volatile("" : "+v"(ty));
        const int l15 = ty & 15, g = (ty >> 4) & 3;
        const int swA = (l15 >> 1) & 7;
        const int q4 = l15 >> 2, p4 = l15 & 3, fk = ((q4 >> 1) & 1) | ((g & 1) << 1);
        lds_cptr pA0 = sm + wm * 8192 + l15 * 128 + ((0 + g) ^ swA) * 16, pA1 = sm + wm * 8192 + l15 * 128 + ((4 + g) ^ swA) * 16;
        lds_cptr pB0 = sm + ABYTES + (8 * g + q4) * 128 + (((2 * wn + 0) ^ fk) * 32) + p4 * 8;
        lds_cptr pB1 = sm + ABYTES + (8 * g + q4) * 128 + (((2 * wn + 1) ^ fk) * 32) + p4 * 8;
        asm volatile("" : "+v"(acch[0]), "+v"(acch[1]), "+v"(pA0), "+v"(pA1), "+v"(pB0), "+v"(pB1));
        slow_warm((unsigned)(uintptr_t)smem + 159744, W, w * 3, 3, ty & 63);
        BAR();
        STAMP(LOCAL ? 1 : 7);
        BAR();
#pragma unroll
        for (int n = 0; n < 17; ++n) rd(WM, F0, pA0, pB0, pB1, n);
        WAIT_LGKM();
        int off = 0;
        lds_cptr a = pA1, b0 = pB0, b1 = pB1;
#pragma unroll 1
        for (int t = 0; t < NT - 1; ++t) {
            half(WM, F0, F1, true, a, b0 + 4096, b1 + 4096, [&](int n) {
                if (n == 17) { off = (off == 2 * STAGE) ? 0 : off + STAGE; asm volatile("" : "+s"(off)); }
                if (n == 19) { b0 = pB0 + off; asm volatile("" : "+v"(b0)); }
                if (n == 20) { a = pA0 + off; asm volatile("" : "+v"(a)); }
                if (n == 21) { b1 = pB1 + off; asm volatile("" : "+v"(b1)); }
            });
            WAIT_LGKM_BAR();
            half(WM, F1, F0, true, a, b0, b1, [&](int n) {
                if (n == 19) { a = pA1 + off; asm volatile("" : "+v"(a)); }
                if (n == 22) __builtin_amdgcn_s_waitcnt(0xC07F);
            });
        }
        lds_cptr s1 = sm, s4 = sm, c1 = sm, c4 = sm;
        half(WM, F0, F1, true, a, b0 + 4096, b1 + 4096, [&](int n) {
            if (n == 16) { ty = threadIdx.x; asm volatile("" : "+v"(ty)); }
            if (n == 17) { s1 = sm + C::OFF_MEAN + wm * 256 + (ty & 15) * 4; asm volatile("" : "+v"(s1)); }
            if (n == 18) { s4 = sm + C::OFF_MEAN + wm * 256 + (ty & 48); asm volatile("" : "+v"(s4)); }
            if (n == 19) { c1 = sm + C::OFF_CS + wn * 128 + (ty & 15) * 4; asm volatile("" : "+v"(c1)); }
            if (n == 20) { c4 = sm + C::OFF_CS + wn * 128 + (ty & 48); asm volatile("" : "+v"(c4)); }
        });
        WAIT_LGKM_BAR();
        half(WM, F1, F0, false, sm, sm, sm, [&](int n) {
            constexpr int RS = C::OFF_RSTD - C::OFF_MEAN, H = 512 - WM.value * 256;
            if (n < 2) { if (!LOCAL) cq[n] = *(lds_f4)(c4 + n * 64); ck[n] = *(lds_f4)(c4 + 256 + n * 64); }
            else if (n < 6) { mu[n - 2] = *(lds_f1)(s1 + (n - 2) * 64); rs[n - 2] = *(lds_f1)(s1 + RS + (n - 2) * 64); }
            else if (n < 10) { if (!LOCAL || (n & 1)) { mu4[n - 6] = *(lds_f4)(s4 + (n - 6) * 64); rs4[n - 6] = *(lds_f4)(s4 + RS + (n - 6) * 64); } }
            else if (n == 10) { cv[0] = *(lds_f1)(c1 + 512); cv[1] = *(lds_f1)(c1 + 512 + 64); }
            else if (LOCAL && n == 11) {
                if (WM.value == 0) { mu[4] = *(lds_f1)(s1 + H); rs[4] = *(lds_f1)(s1 + RS + H); }
                else { mu4[4] = *(lds_f4)(s4 + H); rs4[4] = *(lds_f4)(s4 + RS + H); }
            }
        });
        if (LOCAL) {
            auto fix = [&](int a, int t) {
#pragma unroll
                for (int f = 0; f < 2; ++f)
                    acc[a][t][f] = (a == 2) ? rs4[t] * (acc[2][t][f] + mu4[t] * cv[f]) : rs[t] * (acc[a][t][f] + mu[t] * (a ? ck[f] : cq[f]));
            };
            auto pk = [&](const f32x4 (&v)[2]) { return u32x4{pack_bf16(v[0][0], v[0][1]), pack_bf16(v[0][2], v[0][3]), pack_bf16(v[1][0], v[1][1]), pack_bf16(v[1][2], v[1][3])}; };
            cq[0] = *(lds_f4)c4; cq[1] = *(lds_f4)(c4 + 64);
            SBAR();
            if (!ident) {
                fix(1, 1); fix(1, 3); fix(2, 1); fix(2, 3);
#pragma unroll
                for (int f = 0; f < 2; ++f)
                    acch[f] = (WM.value == 0) ? rs[4] * (acch[f] + mu[4] * ck[f]) : rs4[4] * (acch[f] + mu4[4] * cv[f]);
            }
            kfr[1] = pk(acc[1][1]); kfr[3] = pk(acc[1][3]); vfr[1] = pk(acc[2][1]); vfr[3] = pk(acc[2][3]);
            lds_u4 ex = (lds_u4)(smw + OFF_EX + wn * 6144) + (ty & 63);
            if (WM.value == 0) { ex[0] = kfr[1]; ex[64] = kfr[3]; ex[128] = vfr[1]; ex[192] = vfr[3]; }
            ex[256 + WM.value * 64] = pk(acch);
#pragma unroll
            for (int t = 0; t < 4; t += 2) { mu4[t] = *(lds_f4)(s4 + t * 64); rs4[t] = *(lds_f4)(s4 + (C::OFF_RSTD - C::OFF_MEAN) + t * 64); }
            SBAR();
            if (!ident) { fix(0, 0); fix(0, 1); fix(0, 2); fix(0, 3); }
#pragma unroll
            for (int t = 0; t < 4; ++t) qfr[t] = pk(acc[0][t]);
            WAIT_LGKM();
            fl[w] = 1u;
            lds_u4 cst = (lds_u4)(smw + OFF_CST) + (ty & 63);
            mwin = __builtin_bit_cast(f32x4, cst[0]); mfirst = __builtin_bit_cast(f32x4, cst[64 + WM.value * 64]); cc = cst[192 + w * 64];
            SBAR();
            if (!ident) { fix(1, 0); fix(1, 2); fix(2, 0); fix(2, 2); }
            kfr[0] = pk(acc[1][0]); kfr[2] = pk(acc[1][2]); vfr[0] = pk(acc[2][0]); vfr[2] = pk(acc[2][2]);
        }
    };
    const int head = ct * 2 + wn;
    unsigned qo = 0, ko = 0, vo = 0;
    if (LOCAL) {
        qo = (unsigned)((((panel * 8 + wm * 4) * 32 + head) * 64 + lane) * 16);
        asm volatile("" : "+v"(qo));
    } else {
        ko = (unsigned)((wm * 8 + (g & 1)) * 1024 + ((l15 & 7) + ((l15 & 8) << 1) + 32 * (g >> 1)) * 16);
        vo = (unsigned)((wm * 8 + 2 + (g >> 1)) * 1024 + (l15 + 32 * (g & 1)) * 16);
        asm volatile("" : "+v"(ko), "+v"(vo));
    }
    if (LOCAL && wm) mainloop(IC<1>{}); else mainloop(IC<0>{});

    if (!LOCAL) {
        auto pk = [&](f32x4 a, f32x4 b) { return u32x4{pack_bf16(a[0], a[1]), pack_bf16(a[2], a[3]), pack_bf16(b[0], b[1]), pack_bf16(b[2], b[3])}; };
        char* kvp = (char*)kvf + (size_t)(head * 16 + panel) * 16384;
        lds_ptr own = smw + wn * 16384;
        SBAR();
#pragma unroll
        for (int t = 0; t < 4; ++t) {
            const u32x4 kk = pk(rs[t] * (acc[1][t][0] + mu[t] * ck[0]), rs[t] * (acc[1][t][1] + mu[t] * ck[1]));
            if (t & 1) st16o<128>(kvp + (t >> 1) * 4096, ko, kk); else st16o<0>(kvp + (t >> 1) * 4096, ko, kk);
            *(lds_u4)(own + ko + (t >> 1) * 4096 + (t & 1) * 128) = kk;
            SBAR();
            const int u = t >> 1, f = t & 1;
            const u32x4 vv = pk(rs4[2 * u] * (acc[2][2 * u][f] + mu4[2 * u] * cv[f]), rs4[2 * u + 1] * (acc[2][2 * u + 1][f] + mu4[2 * u + 1] * cv[f]));
            if (f) st16o<256>(kvp + u * 4096, vo, vv); else st16o<0>(kvp + u * 4096, vo, vv);
            *(lds_u4)(own + vo + u * 4096 + f * 256) = vv;
            SBAR();
        }
#pragma unroll
        for (int u = 0; u < 2; ++u) {
            const float r0 = rs[2 * u] * QSCALE, r1 = rs[2 * u + 1] * QSCALE;
            const u32x4 qa = pk(r0 * (acc[0][2 * u][0] + mu[2 * u] * cq[0]), r0 * (acc[0][2 * u][1] + mu[2 * u] * cq[1]));
            const u32x4 qb = pk(r1 * (acc[0][2 * u + 1][0] + mu[2 * u + 1] * cq[0]), r1 * (acc[0][2 * u + 1][1] + mu[2 * u + 1] * cq[1]));
#pragma unroll
            for (int e = 0; e < 4; ++e) {
                const auto x = __builtin_amdgcn_permlane16_swap(qa[e], qb[e], false, false);
                qo4[2 * u][e] = x[0]; qo4[2 * u + 1][e] = x[1];
            }
        }
        return;
    }
    {
        const f32x4 zero4 = {0, 0, 0, 0};
        const unsigned cm01 = cc[0], cm23 = cc[1];
#pragma unroll 1
        for (int spin = 0; spin < (1 << 22); ++spin)
            if (__builtin_amdgcn_readfirstlane(fl[w ^ 1]) != 0u) break;
        asm volatile("" ::: "memory");
        u32x4 kp[2], vp[2];
        kp[0] = *(lds_u4)(smw + cc[2]); kp[1] = *(lds_u4)(smw + (cc[2] ^ (wm ? 1024u : 128u)));
        vp[0] = *(lds_u4)(smw + cc[3]); vp[1] = *(lds_u4)(smw + (cc[3] ^ (wm ? 1024u : 512u)));
        if (negpos) { kp[0] = u32x4{0, 0, 0, 0}; kp[1] = kp[0]; }
        f32x4 sp[4], sc[4];
#pragma unroll
        for (int i = 0; i < 4; ++i) {
            const int t = (i == 0) ? 1 : (i == 1) ? 3 : (i == 2) ? 0 : 2;
            const bf16x8 qb = __builtin_bit_cast(bf16x8, qfr[t]);
            sc[t] = MFMA16(__builtin_bit_cast(bf16x8, kfr[t]), qb, mwin);
            sp[t] = MFMA16(__builtin_bit_cast(bf16x8, (t & 1) ? kfr[t - 1] : kp[t >> 1]), qb, (t & 1) ? mwin : mfirst);
        }
        SBAR();
        float x[4][4], m[4], inv[4], c2[4];
        unsigned a[4][2];
#pragma unroll
        for (int t = 0; t < 4; ++t)
#pragma unroll
            for (int h = 0; h < 2; ++h) {
                const unsigned uc = pack_bf16(sc[t][2 * h], sc[t][2 * h + 1]), up = pack_bf16(sp[t][2 * h], sp[t][2 * h + 1]);
                const unsigned cm = h ? cm23 : cm01;
                a[t][h] = (uc & cm) | (up & ~cm);
            }
        SBAR();
#pragma unroll
        for (int t = 0; t < 4; ++t)
#pragma unroll
            for (int h = 0; h < 2; ++h) { x[t][2 * h] = bf_lo(a[t][h]) * ATT_SCALE; x[t][2 * h + 1] = bf_hi(a[t][h]) * ATT_SCALE; }
        SBAR();
#pragma unroll
        for (int t = 0; t < 4; ++t)
#pragma unroll
            for (int h = 0; h < 2; ++h) a[t][h] = pack_bf16(x[t][2 * h], x[t][2 * h + 1]);
        SBAR();
#pragma unroll
        for (int t = 0; t < 4; ++t)
#pragma unroll
            for (int h = 0; h < 2; ++h) { x[t][2 * h] = bf_lo(a[t][h]); x[t][2 * h + 1] = bf_hi(a[t][h]); }
        SBAR();
#pragma unroll
        for (int t = 0; t < 4; ++t) m[t] = amax3(x[t][0], x[t][1], x[t][2]);
#pragma unroll
        for (int t = 0; t < 4; ++t) m[t] = amax3(m[t], x[t][3], x[t][3]);
        SBAR();
        asm volatile("s_nop 1");
        c2[0] = m[0]; c2[1] = m[1]; c2[2] = m[2]; c2[3] = m[3];
        asm volatile("" : "+v"(c2[0]), "+v"(c2[1]), "+v"(c2[2]), "+v"(c2[3]));
#pragma unroll
        for (int t = 0; t < 4; ++t) {
            const auto u = __builtin_amdgcn_permlane16_swap(__float_as_uint(m[t]), __float_as_uint(c2[t]), false, false);
            m[t] = amax3(__uint_as_float(u[0]), __uint_as_float(u[1]), __uint_as_float(u[1]));
        }
        SBAR();
        asm volatile("s_nop 1");
        c2[0] = m[0]; c2[1] = m[1]; c2[2] = m[2]; c2[3] = m[3];
        asm volatile("" : "+v"(c2[0]), "+v"(c2[1]), "+v"(c2[2]), "+v"(c2[3]));
#pragma unroll
        for (int t = 0; t < 4; ++t) {
            const auto u = __builtin_amdgcn_permlane32_swap(__float_as_uint(m[t]), __float_as_uint(c2[t]), false, false);
            m[t] = amax3(__uint_as_float(u[0]), __uint_as_float(u[1]), __uint_as_float(u[1]));
        }
        SBAR();
#pragma unroll
        for (int t = 0; t < 4; ++t)
#pragma unroll
            for (int r = 0; r < 4; ++r) x[t][r] = (x[t][r] - m[t]) * LOG2E;
        SBAR();
#pragma unroll
        for (int t = 0; t < 4; ++t)
#pragma unroll
            for (int r = 0; r < 4; ++r) x[t][r] = __builtin_amdgcn_exp2f(x[t][r]);
        SBAR();
#pragma unroll
        for (int t = 0; t < 4; ++t) inv[t] = (x[t][0] + x[t][1]) + (x[t][2] + x[t][3]);
        SBAR();
        c2[0] = inv[0]; c2[1] = inv[1]; c2[2] = inv[2]; c2[3] = inv[3];
        asm volatile("" : "+v"(c2[0]), "+v"(c2[1]), "+v"(c2[2]), "+v"(c2[3]));
#pragma unroll
        for (int t = 0; t < 4; ++t) {
            const auto u = __builtin_amdgcn_permlane16_swap(__float_as_uint(inv[t]), __float_as_uint(c2[t]), false, false);
            inv[t] = __uint_as_float(u[0]) + __uint_as_float(u[1]);
        }
        SBAR();
        c2[0] = inv[0]; c2[1] = inv[1]; c2[2] = inv[2]; c2[3] = inv[3];
        asm volatile("" : "+v"(c2[0]), "+v"(c2[1]), "+v"(c2[2]), "+v"(c2[3]));
#pragma unroll
        for (int t = 0; t < 4; ++t) {
            const auto u = __builtin_amdgcn_permlane32_swap(__float_as_uint(inv[t]), __float_as_uint(c2[t]), false, false);
            inv[t] = __builtin_amdgcn_rcpf(__uint_as_float(u[0]) + __uint_as_float(u[1]));
        }
        SBAR();
#pragma unroll
        for (int t = 0; t < 4; ++t)
#pragma unroll
            for (int h = 0; h < 2; ++h) a[t][h] = pack_bf16(x[t][2 * h] * inv[t], x[t][2 * h + 1] * inv[t]);
        SBAR();
        f32x4 o0[4], o1[4];
#pragma unroll
        for (int t = 0; t < 4; ++t) {
            const bf16x8 pb = __builtin_bit_cast(bf16x8, (u32x4{a[t][0] & ~cm01, a[t][1] & ~cm23, a[t][0] & cm01, a[t][1] & cm23}));
            const u32x4 vprev = (t & 1) ? vfr[t - 1] : vp[t >> 1];
            o0[t] = MFMA16(__builtin_bit_cast(bf16x8, (u32x4{vprev[0], vprev[1], vfr[t][0], vfr[t][1]})), pb, zero4);
            o1[t] = MFMA16(__builtin_bit_cast(bf16x8, (u32x4{vprev[2], vprev[3], vfr[t][2], vfr[t][3]})), pb, zero4);
        }
        SBAR();
#pragma unroll
        for (int t = 0; t < 4; ++t)
            st16(ofrag + t * 16384, qo, u32x4{pack_bf16(o0[t][0], o0[t][1]), pack_bf16(o0[t][2], o0[t][3]),
                                                 pack_bf16(o1[t][0], o1[t][1]), pack_bf16(o1[t][2], o1[t][3])});
    }
}

DI int out_row(int panel, bool localrows, int T, int a) {
    return localrows ? (panel * 128 + (T >> 2) * 64 + 2 * (16 * (T & 1) + a) + ((T >> 1) & 1)) : (panel * 128 + T * 16 + a);
}
DI int out_swz(int k) { return ((k >> 1) & 1) | (k & 4); }
DI void wo_quarter(const void* Wo, int q, unsigned ldsdst, int w, int lane) {
    const int kl = lane >> 3, co = ids().co;
    const unsigned voff = (unsigned)(w * 8 + kl) * 2048u + (unsigned)(co * 128 + ((lane & 7) ^ out_swz(kl)) * 16);
    const char* src = (const char*)Wo + (size_t)q * 524288;
#pragma unroll
    for (int i = 0; i < 8; ++i) glds16(src + i * 65536, voff, ldsdst + (w + 4 * i) * 1024);
}
DI void res_load(u32x4 (&r)[4], const void* Xin, bool localrows, int w, int lane) {
    const Ids I = ids();
#pragma unroll
    for (int i = 0; i < 4; ++i) {
        const int row = (w + 4 * i) * 8 + (lane >> 3);
        asm volatile("global_load_dwordx4 %0, %1, %2 sc1" : "=&v"(r[i])
                     : "v"((unsigned)out_row(I.po, localrows, row >> 4, row & 15) * 2048u + (unsigned)(I.co * 128 + (((lane & 7) ^ (lane >> 3)) * 16))),
                       "s"(Xin) : "memory");
    }
}
DI void res_write(u32x4 (&r)[4], char* smem, int off, int w, int lane) {
    asm volatile("" : "+v"(r[0]), "+v"(r[1]), "+v"(r[2]), "+v"(r[3]));
#pragma unroll
    for (int i = 0; i < 4; ++i) *(__attribute__((address_space(3))) u32x4*)((lds_ptr)smem + off + (w + 4 * i) * 1024 + lane * 16) = r[i];
}

template <bool AFTER_LOCAL>
DI void out_compute(char* smem, char* ws, int set, u64 tag, const __hip_bfloat16* __restrict__ ofrag,
                    __hip_bfloat16* __restrict__ Xout, float* __restrict__ statp SP_PARAM) {
    constexpr int XOFF = AFTER_LOCAL ? 131072 : 98304;
    int tz = threadIdx.x; asm volatile("" : "+v"(tz));
    const int lane = tz & 63;
    const int w = __builtin_amdgcn_readfirstlane(tz >> 6) & 3;
    const Ids I = ids();
    const int panel = I.po, ct = I.co;
    const int l15 = lane & 15, g = lane >> 4;
    auto orow = [&](int T, int a) -> int { return out_row(panel, AFTER_LOCAL, T, a); };
    const int q4 = l15 >> 2, p4 = l15 & 3;
    const int krow = 4 * g + q4;
    const int sw = out_swz(krow);
    const lds_cptr sm = (lds_cptr)smem;
    lds_cptr bw[4], xo[4];
#pragma unroll
    for (int t = 0; t < 4; ++t) {
        bw[t] = sm + krow * 128 + ((((t >> 1) * 4 + (p4 >> 1) * 2 + (t & 1)) ^ sw) * 16) + (p4 & 1) * 8;
        xo[t] = sm + XOFF + (w * 32 + l15) * 128 + ((((t >> 1) * 4 + (g >> 1) * 2 + (t & 1)) ^ (l15 & 7)) * 16) + (g & 1) * 8;
    }
    unsigned so[2], sto, ao[2];
#pragma unroll
    for (int r2 = 0; r2 < 2; ++r2) {
        so[r2] = (unsigned)orow(2 * w + r2, l15 & 7) * 2048u + (unsigned)(ct * 128 + (l15 >> 3) * 64 + g * 16);
        ao[r2] = (unsigned)((panel * 8 + 2 * w + r2) * 32768 + lane * 16);
    }
    sto = (unsigned)(((panel * 16 + ct) * 128 + 2 * w * 16 + l15) * 8);
    asm volatile("" : "+v"(bw[0]), "+v"(bw[1]), "+v"(bw[2]), "+v"(bw[3]), "+v"(xo[0]), "+v"(xo[1]), "+v"(xo[2]), "+v"(xo[3]),
                 "+v"(so[0]), "+v"(so[1]), "+v"(sto), "+v"(ao[0]), "+v"(ao[1]));
    const __amdgpu_buffer_rsrc_t ra = __builtin_amdgcn_make_buffer_rsrc((void*)ofrag, 0, 4 << 20, 0x00020000);
    asm volatile("" :: "s"(ra));
    u64 land;
    STAMP(AFTER_LOCAL ? 3 : 10);
    signal_poll<true, false>(w, ws, flag_off(set, I.pq, l15, I.cq), flag_off(set, panel, ct, l15), 0xffffull, tag, land);
    BAR();
    STAMP(AFTER_LOCAL ? 4 : 11);
#define AWIN 6
    u32x4 a[AWIN][2];
    auto lda = [&](int j) {
        a[j % AWIN][0] = __builtin_amdgcn_raw_buffer_load_b128(ra, ao[0] + (j & 3) * 1024, (j >> 2) * 4096, 16);
        a[j % AWIN][1] = __builtin_amdgcn_raw_buffer_load_b128(ra, ao[1] + (j & 3) * 1024, (j >> 2) * 4096, 16);
    };
#pragma unroll
    for (int j = 0; j < AWIN; ++j) { lda(j); SBAR(); }
    auto boff = [&](int j) { return (j & 15) * 4096 - ((!AFTER_LOCAL && j >= 24) ? 32768 : 0); };
    s16x4 F[2][4][2];
#pragma unroll
    for (int h = 0; h < 2; ++h)
#pragma unroll
        for (int t = 0; t < 4; ++t) { F[h][t][0] = lds_tr(bw[t] + boff(h)); F[h][t][1] = lds_tr(bw[t] + boff(h) + 2048); }
    f32x4 acc[2][4];
#pragma unroll
    for (int r2 = 0; r2 < 2; ++r2)
#pragma unroll
        for (int c = 0; c < 4; ++c) {
            const u32x2 xr = *(const __attribute__((address_space(3))) u32x2*)(xo[c] + r2 * 2048);
            acc[r2][c] = f32x4{bf_lo(xr[0]), bf_hi(xr[0]), bf_lo(xr[1]), bf_hi(xr[1])};
        }
    WAIT_LGKM();
#pragma unroll
    for (int j = 0; j < 32; ++j) {
        s16x4 (&C)[4][2] = F[j & 1];
        const bf16x8 a0 = __builtin_bit_cast(bf16x8, a[j % AWIN][0]), a1 = __builtin_bit_cast(bf16x8, a[j % AWIN][1]);
        const int nb = boff(j + 2);
        if (j + 2 == 16 || (!AFTER_LOCAL && j + 2 == 24)) {
#pragma unroll
            for (int t = 0; t < 4; ++t) bw[t] += 65536;
            asm volatile("" : "+v"(bw[0]), "+v"(bw[1]), "+v"(bw[2]), "+v"(bw[3]));
        }
#pragma unroll
        for (int t = 0; t < 4; ++t) {
            const bf16x8 bb = cat8(C[t][0], C[t][1]);
            acc[0][t] = MFMA16(bb, a0, acc[0][t]);
            if (t > 0 && j < 30) C[t - 1][1] = lds_tr(bw[t - 1] + nb + 2048);
            SBAR();
            acc[1][t] = MFMA16(bb, a1, acc[1][t]);
            if (j < 30) C[t][0] = lds_tr(bw[t] + nb);
            if (t == 3 && j < 30) C[3][1] = lds_tr(bw[3] + nb + 2048);
            if (t == 3 && j + AWIN < 32) lda(j + AWIN);
            SBAR();
        }
    }
    asm volatile("" :: "v"(land));
    STAMP(AFTER_LOCAL ? 5 : 12);
    u32x4 y[2][2];
#pragma unroll
    for (int r2 = 0; r2 < 2; ++r2)
#pragma unroll
        for (int u = 0; u < 2; ++u) {
            const f32x4 c0 = acc[r2][2 * u], c1 = acc[r2][2 * u + 1];
            const auto sa = __builtin_amdgcn_permlane16_swap(pack_bf16(c0[0], c0[1]), pack_bf16(c1[0], c1[1]), false, false);
            const auto sb = __builtin_amdgcn_permlane16_swap(pack_bf16(c0[2], c0[3]), pack_bf16(c1[2], c1[3]), false, false);
            y[r2][u] = u32x4{sa[0], sb[0], sa[1], sb[1]};
            if (u) {
                u32x4 za, zb;
#pragma unroll
                for (int e = 0; e < 4; ++e) {
                    za[e] = __builtin_amdgcn_update_dpp(y[r2][0][e], y[r2][1][e], 0x128, 0xF, 0xC, false);
                    zb[e] = __builtin_amdgcn_update_dpp(y[r2][1][e], y[r2][0][e], 0x128, 0xF, 0x3, false);
                }
                st16(Xout, so[r2], za);
                st16(Xout, so[r2] + (AFTER_LOCAL ? 16 : 8) * 2048, zb);
                SBAR();
            }
        }
    const bf16x8 ones = __builtin_bit_cast(bf16x8, (u32x4{0x3f803f80u, 0x3f803f80u, 0x3f803f80u, 0x3f803f80u}));
    const f32x4 zero4 = {0, 0, 0, 0};
    f32x4 d1[2], d2[2];
#pragma unroll
    for (int r2 = 0; r2 < 2; ++r2) {
        const bf16x8 y0 = __builtin_bit_cast(bf16x8, y[r2][0]), y1 = __builtin_bit_cast(bf16x8, y[r2][1]);
        d2[r2] = MFMA16(y0, y0, zero4); d1[r2] = MFMA16(ones, y0, zero4);
        d2[r2] = MFMA16(y1, y1, d2[r2]); d1[r2] = MFMA16(ones, y1, d1[r2]);
    }
    const bool b0 = p4 & 1, b1 = p4 & 2;
    if (g == q4) {
#pragma unroll
        for (int r2 = 0; r2 < 2; ++r2) {
            float e0 = d2[r2][0], e1 = d2[r2][1], e2 = d2[r2][2], e3 = d2[r2][3];
            asm("" : "+v"(e0), "+v"(e1), "+v"(e2), "+v"(e3));
            const float lo = b0 ? e1 : e0, hi = b0 ? e3 : e2;
            const f32x2 sv = {d1[r2][0], b1 ? hi : lo};
            if (r2) st8o<128>(statp, sto, sv); else st8o<0>(statp, sto, sv);
        }
    }
}

#define ROWSUM(p0, p1, c) __builtin_amdgcn_smfmac_f32_16x16x64_bf16(sel, __builtin_bit_cast(bf16v_t, __builtin_shufflevector(p0, p1, 0, 1, 2, 3, 4, 5, 6, 7)), c, sidx, 0, 0)
constexpr int ATT_STAGE = 32768;
typedef __attribute__((ext_vector_type(8))) unsigned u32x8;
typedef __attribute__((ext_vector_type(32))) unsigned u32x32;

DI void attn_loader(char* smem, char* ws, int set, u64 tag, const __hip_bfloat16* __restrict__ kvf, const void* Wo, const void* Xin SP_PARAM) {
    int tid = threadIdx.x; asm volatile("" : "+v"(tid));
    const int lane = tid & 63;
    const int w = __builtin_amdgcn_readfirstlane(tid >> 6) & 3;
    const Ids I = ids();
    const int panel = I.pq, ct = I.cq;
    const unsigned lds0 = (unsigned)(uintptr_t)smem;
    const char* kvh = (const char*)kvf + (size_t)(ct * 2 + (w >> 1)) * 262144 + (w & 1) * 8192;
    const unsigned voff = (unsigned)(lane * 16);
    auto order = [&](int st) { return ((panel & 8) + st - ((st - 1 < (panel & 7)) ? 1 : 0)) & 15; };
    auto load = [&](int st) {
        const char* src = kvh + (size_t)order(st) * 16384;
        const unsigned dst = lds0 + w * 8192 + (st & 3) * ATT_STAGE;
#pragma unroll
        for (int u = 0; u < 2; ++u)
            asm volatile("s_mov_b32 m0, %0\n\ts_nop 0\n\tglobal_load_lds_dwordx4 %1, %2 sc1\n\tglobal_load_lds_dwordx4 %1, %2 offset:1024 sc1\n\t"
                         "global_load_lds_dwordx4 %1, %2 offset:2048 sc1\n\tglobal_load_lds_dwordx4 %1, %2 offset:3072 sc1"
                         :: "s"(dst + u * 4096), "v"(voff), "s"(src + u * 4096) : "memory");
    };
    const unsigned poff = flag_off(set, ct, panel, lane & 15);
    u64 land;
    __builtin_amdgcn_s_setprio(3);
    BAR();
    signal_poll<false, true>(w, ws, flag_off(set, ct, lane & 15, panel), poff, 1ull << order(1), tag, land);
    STAMP(9);
    load(1);
    asm volatile("global_load_dwordx2 %0, %1, %2 sc1\n\ts_waitcnt vmcnt(0)" : "+v"(land) : "v"(poff), "s"(ws + WS_FLAG) : "memory");
    STAMP(10);
    BAR();
    if (!__all(land == tag)) flag_poll(ws, poff, tag);
    load(2); load(3);
    __builtin_amdgcn_s_setprio(0);
    STAMP(11);
#pragma unroll 1
    for (int st = 1; st < 13; ++st) {
        VM_BAR(8);
        if (st == 6) wo_quarter(Wo, 3, lds0 + 131072, w, lane);
        load(st + 3);
    }
    VM_BAR(8); wo_quarter(Wo, 0, lds0, w, lane);
    VM_BAR(8); wo_quarter(Wo, 1, lds0 + 32768, w, lane);
    u32x4 res[4];
    res_load(res, Xin, false, w, lane);
    BAR();     wo_quarter(Wo, 2, lds0 + 65536, w, lane);
    STAMP(12);
    BAR();
    asm volatile("s_waitcnt vmcnt(0)" ::: "memory");
    res_write(res, smem, 98304, w, lane);
    WAIT_LGKM_BAR();
}

DI void attn_compute(char* smem, const u32x4 (&q4)[4], const __hip_bfloat16* __restrict__ kvf, __hip_bfloat16* __restrict__ ofrag SP_PARAM) {
    int tz = threadIdx.x; asm volatile("" : "+v"(tz));
    const int lane = tz & 63;
    const int w = __builtin_amdgcn_readfirstlane(tz >> 6) & 3;
    const int wm = w & 1, wn = w >> 1;
    const Ids I = ids();
    const int panel = I.pq, head = I.cq * 2 + wn;
    const int l15 = lane & 15;
    const unsigned se = ((l15 & 2) == 0 && (l15 & 4) == 0) ? 0x3f803f80u : 0u, so = ((l15 & 2) == 0 && (l15 & 4) != 0) ? 0x3f803f80u : 0u;
    const bf8_t sel = __builtin_bit_cast(bf8_t, u32x4{se, se, so, so});
    const int sidx = (l15 & 1) ? (int)0xEEEEEEEEu : 0x44444444;
    const lds_cptr sm = (lds_cptr)smem + wn * 16384 + lane * 16;
    const f32x16 z16 = {0, 0, 0, 0, 0, 0, 0, 0, 0, 0, 0, 0, 0, 0, 0, 0};
    const bf16x8 qA0 = __builtin_bit_cast(bf16x8, q4[0]), qA1 = __builtin_bit_cast(bf16x8, q4[1]);
    const bf16x8 qB0 = __builtin_bit_cast(bf16x8, q4[2]), qB1 = __builtin_bit_cast(bf16x8, q4[3]);
    f32x16 oA, oB, nmA, nmB, sA, sB;
    f32x4 lsA, lsB;
    u32x8 pB;
    bf16x8 v11;
    asm volatile(".irp r,2,4,6,8,10,12,14,16,18,20,22,24,26,28,30,32,106,108,110,112,134,136,138,140,170,172\n\tv_mov_b64 v[\\r:\\r+1], 0\n\t.endr"
                 : "={v[2:17]}"(oA), "={v[18:33]}"(oB), "={v[106:113]}"(pB), "={v[134:137]}"(lsA), "={v[138:141]}"(lsB), "={v[170:173]}"(v11));
    WAIT_LGKM_BAR();
    STAMP(8);
    bf16x8 k00 = lds_b128(sm), k10 = lds_b128(sm + 1024), k01 = lds_b128(sm + 4096), k11 = lds_b128(sm + 4096 + 1024);
    bf16x8 v00 = lds_b128(sm + 2048), v10 = lds_b128(sm + 3072), v01 = lds_b128(sm + 4096 + 2048);
    {
        sA = MFMA32(k00, qA0, z16); sB = MFMA32(k00, qB0, z16);
        sA = MFMA32(k10, qA1, sA); sB = MFMA32(k10, qB1, sB);
        float mA = sA[0], mB = sB[0];
#pragma unroll
        for (int r = 1; r < 16; ++r) { mA = fmaxf(mA, sA[r]); mB = fmaxf(mB, sB[r]); }
        mA = max_h(mA); mB = max_h(mB);
#pragma unroll
        for (int r = 0; r < 16; ++r) { nmA[r] = -mA; nmB[r] = -mB; sA[r] -= mA; }
    }
    const unsigned a0 = (unsigned)(uintptr_t)sm;
    asm volatile(
        ".macro ATT_RD dst, y\n\t"
        "ds_read_b128 v[\\dst:\\dst+3], v[175+(((\\y)&0x1ffff)>>16)] offset:(\\y)&0xffff\n\t"
        ".endm\n\t"
        ".macro ATT_E4 c\n\t"
        "v_exp_f32 v[\\c], v[\\c]\n\tv_exp_f32 v[\\c+1], v[\\c+1]\n\tv_exp_f32 v[\\c+2], v[\\c+2]\n\tv_exp_f32 v[\\c+3], v[\\c+3]\n\t"
        ".endm\n\t"
        ".macro ATT_C4 p, c\n\t"
        "v_cvt_pk_bf16_f32 v[\\p], v[\\c], v[\\c+1]\n\tv_cvt_pk_bf16_f32 v[\\p+1], v[\\c+2], v[\\c+3]\n\t"
        "v_cvt_pk_bf16_f32 v[\\p+2], v[\\c+4], v[\\c+5]\n\tv_cvt_pk_bf16_f32 v[\\p+3], v[\\c+6], v[\\c+7]\n\t"
        ".endm\n\t"
        ".macro ATT_STEP cur, nxt, ka, kb, q, nm, oc, od, lsd, pc, pd, vd1, vc0, r0, y0, r1, y1, bar, rd\n\t"
        "v_mfma_f32_32x32x16_bf16 v[\\nxt:\\nxt+15], v[\\ka:\\ka+3], v[\\q:\\q+3], v[\\nm:\\nm+15]\n\t"
        "ATT_E4 \\cur\n\t"
        "v_mfma_f32_32x32x16_bf16 v[\\nxt:\\nxt+15], v[\\kb:\\kb+3], v[\\q+4:\\q+7], v[\\nxt:\\nxt+15]\n\t"
        "ATT_E4 \\cur+4\n\t"
        "v_mfma_f32_32x32x16_bf16 v[\\od:\\od+15], v[\\vd1:\\vd1+3], v[\\pd+4:\\pd+7], v[\\od:\\od+15]\n\t"
        "s_waitcnt lgkmcnt(0)\n\t"
        "ATT_C4 \\pc, \\cur\n\t"
        "v_exp_f32 v[\\cur+8], v[\\cur+8]\n\tv_exp_f32 v[\\cur+9], v[\\cur+9]\n\t"
        "v_mfma_f32_32x32x16_bf16 v[\\oc:\\oc+15], v[\\vc0:\\vc0+3], v[\\pc:\\pc+3], v[\\oc:\\oc+15]\n\t"
        ".if \\bar\n\ts_barrier\n\t.endif\n\t"
        "ATT_E4 \\cur+10\n\t"
        "v_smfmac_f32_16x16x64_bf16 v[\\lsd:\\lsd+3], v[130:133], v[\\pd:\\pd+7], v174\n\t"
        ".if \\rd & 4\n\tATT_RD 142, 32768\n\tATT_RD 150, 33792\n\tATT_RD 158, 34816\n\t.endif\n\t"
        ".if \\rd & 1\n\tATT_RD \\r0, \\y0\n\t.endif\n\t"
        ".if \\rd & 2\n\tATT_RD \\r1, \\y1\n\t.endif\n\t"
        "v_exp_f32 v[\\cur+14], v[\\cur+14]\n\tv_exp_f32 v[\\cur+15], v[\\cur+15]\n\t"
        "ATT_C4 \\pc+4, \\cur+8\n\t"
        ".if \\rd & 4\n\ts_waitcnt lgkmcnt(3)\n\t.endif\n\t"
        ".endm\n\t"
        ".macro ATT_TILE p, y1, y2, bar, ra=3, rb=3\n\t"
        "ATT_STEP 66, 82, 142+\\p, 150+\\p, 122, 50, 2, 18, 138, 98, 106, 170-\\p, 158+\\p, 170-\\p, \\y1+3072, 142+\\p, \\y2, \\bar, \\ra\n\t"
        "ATT_STEP 82, 66, 146-\\p, 154-\\p, 114, 34, 18, 2, 134, 106, 98, 166+\\p, 158+\\p, 158+\\p, \\y2+2048, 150+\\p, \\y2+1024, 0, \\rb\n\t"
        ".endm\n\t"
        ".macro ATT_STAGE b\n\t"
        "ATT_TILE 0, \\b+4096, \\b+8192, 0\n\t"
        "ATT_TILE 4, \\b+8192, \\b+12288, 0\n\t"
        "ATT_TILE 0, \\b+12288, \\b+32768, 1\n\t"
        "ATT_TILE 4, \\b+32768, \\b+36864, 0\n\t"
        ".endm\n\t"
        "v_add_u32 v176, 0x10000, v175\n\t"
        "s_mov_b32 s96, 4\n\t"
        "s_waitcnt vmcnt(0) lgkmcnt(0)\n\t"
        "s_barrier\n\t"
        "ATT_TILE 0, 4096, 8192, 0\n\tATT_TILE 4, 8192, 12288, 0\n\tATT_TILE 0, 12288, 0, 0, 1, 0\n\tATT_TILE 4, 32768, 36864, 1, 7, 3\n\t"
        "s_branch att_entry_%=\n\t"
        ".p2align 6\n"
        "att_loop_%=:\n\t"
        "ATT_STAGE 0\n"
        "att_entry_%=:\n\t"
        "ATT_STAGE 32768\n\tATT_STAGE 65536\n\t"
        "s_sub_u32 s96, s96, 1\n\t"
        "ATT_STAGE 98304\n\t"
        "s_cmp_lg_u32 s96, 0\n\t"
        "s_cbranch_scc1 att_loop_%=\n\t"
        "s_nop 1\n\t"
        "v_mfma_f32_32x32x16_bf16 v[18:33], v[170:173], v[110:113], v[18:33]\n\t"
        "v_smfmac_f32_16x16x64_bf16 v[138:141], v[130:133], v[106:113], v174\n\t"
        "s_waitcnt lgkmcnt(0)\n\ts_nop 10\n\t"
        ".purgem ATT_RD\n\t.purgem ATT_E4\n\t.purgem ATT_C4\n\t.purgem ATT_STEP\n\t.purgem ATT_TILE\n\t.purgem ATT_STAGE"
        : "+{v[2:17]}"(oA), "+{v[18:33]}"(oB), "+{v[66:81]}"(sA), "+{v[106:113]}"(pB), "+{v[134:137]}"(lsA), "+{v[138:141]}"(lsB),
          "+{v[142:145]}"(k00), "+{v[146:149]}"(k01), "+{v[150:153]}"(k10), "+{v[154:157]}"(k11),
          "+{v[158:161]}"(v00), "+{v[162:165]}"(v01), "+{v[166:169]}"(v10), "+{v[170:173]}"(v11)
        : "{v[34:49]}"(nmA), "{v[50:65]}"(nmB), "{v[114:117]}"(qA0), "{v[118:121]}"(qA1), "{v[122:125]}"(qB0), "{v[126:129]}"(qB1),
          "{v[130:133]}"(sel), "{v174}"(sidx), "{v175}"(a0)
        : "memory", "scc", "s96", "v176", "v82", "v83", "v84", "v85", "v86", "v87", "v88", "v89", "v90", "v91", "v92", "v93", "v94", "v95",
          "v96", "v97", "v98", "v99", "v100", "v101", "v102", "v103", "v104", "v105");
    STAMP(9);
    float lA = lsA[0] + lsA[1], lB = lsB[0] + lsB[1];
    const unsigned dst = (unsigned)((((panel * 8 + wm * 4 + ((lane & 31) >> 4)) * 32 + head) * 64 + (lane & 15) + (lane >> 5) * 16) * 16);
    auto store = [&] {
#pragma unroll
    for (int u = 0; u < 2; ++u) {
        const f32x16 e = (u ? oB : oA) * __builtin_amdgcn_rcpf(u ? lB : lA);
        st16o<0>((char*)ofrag + u * 65536, dst, u32x4{pack_bf16(e[0], e[1]), pack_bf16(e[2], e[3]), pack_bf16(e[8], e[9]), pack_bf16(e[10], e[11])});
        st16o<512>((char*)ofrag + u * 65536, dst, u32x4{pack_bf16(e[4], e[5]), pack_bf16(e[6], e[7]), pack_bf16(e[12], e[13]), pack_bf16(e[14], e[15])});
    }
    };
    store();
    {
        const f32x16 os = oA + oB;
        const float osum = (((os[0] + os[1]) + (os[2] + os[3])) + ((os[4] + os[5]) + (os[6] + os[7]))) +
                           (((os[8] + os[9]) + (os[10] + os[11])) + ((os[12] + os[13]) + (os[14] + os[15])));
        const bool bad = !(lA < 1.0e24f) || !(lB < 1.0e24f) || !(fabsf(osum) < 3.0e38f);
        if (__any(bad)) {
            const __amdgpu_buffer_rsrc_t rk = __builtin_amdgcn_make_buffer_rsrc((char*)kvf + (size_t)head * 262144, 0, 262144, 0x00020000);
            oA = z16; oB = z16; lA = 0.f; lB = 0.f;
            float mA = -INFINITY, mB = -INFINITY;
#pragma unroll 1
            for (int t = 0; t < 64; ++t) {
                bf16x8 f[4];
#pragma unroll
                for (int i = 0; i < 4; ++i) f[i] = __builtin_bit_cast(bf16x8, __builtin_amdgcn_raw_buffer_load_b128(rk, lane * 16 + i * 1024, t * 4096, 16));
                auto upd = [&](bf16x8 q0, bf16x8 q1, f32x16& o, float& l, float& m) {
                    f32x16 s = MFMA32(f[0], q0, z16); s = MFMA32(f[1], q1, s);
                    float tm = s[0];
#pragma unroll
                    for (int r = 1; r < 16; ++r) tm = fmaxf(tm, s[r]);
                    tm = max_h(tm);
                    const float mn = fmaxf(m, tm);
                    const float alpha = __builtin_amdgcn_exp2f(m - mn);
                    m = mn;
#pragma unroll
                    for (int r = 0; r < 16; ++r) { s[r] = __builtin_amdgcn_exp2f(s[r] - mn); o[r] *= alpha; }
                    const u32x4 p0 = {pack_bf16(s[0], s[1]), pack_bf16(s[2], s[3]), pack_bf16(s[4], s[5]), pack_bf16(s[6], s[7])};
                    const u32x4 p1 = {pack_bf16(s[8], s[9]), pack_bf16(s[10], s[11]), pack_bf16(s[12], s[13]), pack_bf16(s[14], s[15])};
                    f32x4 t4 = {0, 0, 0, 0};
                    o = MFMA32(f[2], __builtin_bit_cast(bf16x8, p0), o);
                    o = MFMA32(f[3], __builtin_bit_cast(bf16x8, p1), o);
                    t4 = ROWSUM(p0, p1, t4);
                    l = l * alpha + (t4[0] + t4[1]);
                };
                upd(qA0, qA1, oA, lA, mA);
                upd(qB0, qB1, oB, lB, mB);
            }
            store();
        }
    }
}

DI void ln_rows(const __hip_bfloat16* __restrict__ X, __hip_bfloat16* __restrict__ Y) {
    const int lane = threadIdx.x & 63;
    const int w = __builtin_amdgcn_readfirstlane(threadIdx.x >> 6);
    const int row = ids().po * 128 + ids().co * 8 + w * 2;
    const u32x4* p = (const u32x4*)X + (size_t)row * 128 + lane;
    u32x4 v[2][2];
    v[0][0] = p[0]; v[0][1] = p[64]; v[1][0] = p[128]; v[1][1] = p[192];
    float s1[2] = {0.f, 0.f}, s2[2] = {0.f, 0.f}, mean[2];
#pragma unroll
    for (int r = 0; r < 2; ++r)
#pragma unroll
        for (int e = 0; e < 8; ++e) s1[r] += bf_lo(v[r][e >> 2][e & 3]) + bf_hi(v[r][e >> 2][e & 3]);
#pragma unroll
    for (int r = 0; r < 2; ++r) mean[r] = wave_sum(s1[r]) * (1.f / 1024.f);
#pragma unroll
    for (int r = 0; r < 2; ++r)
#pragma unroll
        for (int e = 0; e < 8; ++e) {
            const float d0 = bf_lo(v[r][e >> 2][e & 3]) - mean[r], d1 = bf_hi(v[r][e >> 2][e & 3]) - mean[r];
            s2[r] += d0 * d0 + d1 * d1;
        }
#pragma unroll
    for (int r = 0; r < 2; ++r) {
        const float mb = round_bf16(mean[r]);
        const float rden = __builtin_amdgcn_rcpf(round_bf16(__builtin_amdgcn_sqrtf(round_bf16(round_bf16(wave_sum(s2[r]) * (1.f / 1024.f)) + 1e-5f))));
#pragma unroll
        for (int hh = 0; hh < 2; ++hh) {
            u32x4 y;
#pragma unroll
            for (int e = 0; e < 4; ++e) {
                const unsigned d = pack_bf16(bf_lo(v[r][hh][e]) - mb, bf_hi(v[r][hh][e]) - mb);
                y[e] = pack_bf16(bf_lo(d) * rden, bf_hi(d) * rden);
            }
            st16(Y, (unsigned)((row + r) * 2048 + hh * 1024 + lane * 16), y);
        }
    }
}

extern "C" __global__ __launch_bounds__(512, 2) void mega_kernel(const __hip_bfloat16* x, const __hip_bfloat16* wqkvA,
                                                            const __hip_bfloat16* woA, const __hip_bfloat16* wqkvB,
                                                            const __hip_bfloat16* woB, __hip_bfloat16* out,
                                                            char* ws) {
    extern __shared__ __attribute__((aligned(16))) char smem[];
    typedef __hip_bfloat16 bf;
    asm volatile("" :: "s"(x), "s"(wqkvA), "s"(woA), "s"(wqkvB), "s"(woB), "s"(out), "s"(ws));
    const int w8 = __builtin_amdgcn_readfirstlane(threadIdx.x >> 6);
    STAMP_DECL; STAMPR(14); STAMP(0);
#define EPOCH_LOAD(e) asm volatile("s_load_dwordx2 %0, %1, 0x0" : "=s"(e) : "s"(ws + WS_EPOCH) : "memory")
#define EPOCH_WAIT(e) asm volatile("s_waitcnt lgkmcnt(0)" : "+s"(e) :: "memory")
    if (w8 >= 4) {
        int w = w8 & 3;
        u64 tag;
        EPOCH_LOAD(tag);
        u32x4 ca[8], cb[8];
        unsigned junk = 0;
        if (w < 3) colsum_load(ca, wqkvA, w, threadIdx.x & 63);
        else {
            if ((blockIdx.x >> 3) < 12) code_touch(junk, blockIdx.x >> 3, threadIdx.x & 63);
            asm volatile("global_load_dword %0, %1, %2" : "+v"(junk) : "v"((blockIdx.x * 64 + (threadIdx.x & 63)) * 128), "s"(woA) : "memory");
        }
        EPOCH_WAIT(tag);
        tag += 1;
#pragma unroll 1
        for (int it = 0; it < 2; ++it) {
            int lane = threadIdx.x & 63; asm volatile("" : "+v"(lane));
            asm volatile("" : "+s"(x), "+s"(wqkvA), "+s"(woA), "+s"(wqkvB), "+s"(woB), "+s"(ws), "+s"(w));
            const unsigned lds0 = (unsigned)(uintptr_t)smem;
            qkv_loader<true>(smem, ws, it * 5, tag, (const bf*)(ws + (it ? WS_X2 : WS_Y0)), wqkvA, (const float*)(ws + WS_ST + 262144),
                             it ? ws + WS_CSP : (const char*)wqkvA, it == 0, woA SP_ARG);
            asm volatile("" :: "v"(junk));
            STAMP(3);
#pragma unroll
            for (int q = 0; q < 4; ++q) wo_quarter(woA, q, lds0 + q * 32768, w, lane);
            u32x4 res[4];
            res_load(res, it ? (const void*)(ws + WS_X2) : x, true, w, lane);
            const bool cs = (it == 0 && w < 3);
            if (cs) colsum_load(cb, wqkvB, w, lane);
            STAMP(4);
            BAR();
            if (cs) asm volatile("s_waitcnt vmcnt(8)" ::: "memory"); else asm volatile("s_waitcnt vmcnt(0)" ::: "memory");
            res_write(res, smem, 131072, w, lane);
            WAIT_LGKM_BAR();
            if (cs) {
                asm volatile("s_waitcnt vmcnt(0)" : "+v"(cb[0]), "+v"(cb[1]), "+v"(cb[2]), "+v"(cb[3]), "+v"(cb[4]), "+v"(cb[5]), "+v"(cb[6]), "+v"(cb[7]) :: "memory");
                asm volatile("" : "+v"(ca[0]), "+v"(ca[1]), "+v"(ca[2]), "+v"(ca[3]), "+v"(ca[4]), "+v"(ca[5]), "+v"(ca[6]), "+v"(ca[7]));
                colsum_store(ca, ws + WS_CSP, w, lane);
                colsum_store(cb, ws + WS_CSP + 196608, w, lane);
                asm volatile("s_waitcnt vmcnt(0)" ::: "memory");
                flag_store(ws, 10 + w, ids().cq, ids().pq, tag, lane);
            }
            STAMP(5);
            qkv_loader<false>(smem, ws, it * 5 + 2, tag, (const bf*)(ws + (it ? WS_X3 : WS_X1)), wqkvB, (const float*)(ws + WS_ST + it * 524288), ws + WS_CSP + 196608,
                              false, nullptr SP_ARG);
            STAMP(8);
            attn_loader(smem, ws, it * 5 + 3, tag, (const bf*)(ws + WS_KVF + it * 8 * MBy), woB, ws + (it ? WS_X3 : WS_X1) SP_ARG);
            STAMP(13); STAMPR(15); STAMP_FLUSH(it);
        }
        if (blockIdx.x == 0 && threadIdx.x == 256) __hip_atomic_store((g_u64ptr)(ws + WS_EPOCH), tag, __ATOMIC_RELAXED, __HIP_MEMORY_SCOPE_AGENT);
    } else {
        __builtin_amdgcn_s_setprio(2);
        u64 tag;
        EPOCH_LOAD(tag);
        ln_rows(x, (bf*)(ws + WS_Y0));
        EPOCH_WAIT(tag);
        tag += 1;
#pragma unroll 1
        for (int it = 0; it < 2; ++it) {
            u32x4 q4[4];
            asm volatile("" : "+s"(ws), "+s"(wqkvA), "+s"(wqkvB));
            qkv_compute<true>(smem, wqkvA, q4, nullptr, (bf*)(ws + WS_OF + it * 8 * MBy), it == 0 SP_ARG);
            STAMP(2);
            out_compute<true>(smem, ws, it * 5 + 1, tag, (const bf*)(ws + WS_OF + it * 8 * MBy),
                              (bf*)(ws + (it ? WS_X3 : WS_X1)), (float*)(ws + WS_ST + it * 524288) SP_ARG);
            STAMP(6);
            qkv_compute<false>(smem, wqkvB, q4, (bf*)(ws + WS_KVF + it * 8 * MBy), nullptr, false SP_ARG);
            attn_compute(smem, q4, (const bf*)(ws + WS_KVF + it * 8 * MBy), (bf*)(ws + WS_OF + (it * 8 + 4) * MBy) SP_ARG);
            out_compute<false>(smem, ws, it * 5 + 4, tag, (const bf*)(ws + WS_OF + (it * 8 + 4) * MBy),
                               it ? out : (bf*)(ws + WS_X2), (float*)(ws + WS_ST + 262144 + it * 524288) SP_ARG);
            STAMP(13); STAMPR(15); STAMP_FLUSH(it);
        }
    }
}

extern "C" __global__ void zz_code_end_kernel() {}

extern "C" void kernel_launch(void* const* d_in, const int* in_sizes, int n_in, void* d_out, int out_size, void* d_ws,
                              size_t ws_size, hipStream_t stream) {
    typedef __hip_bfloat16 bf;
    (void)hipFuncSetAttribute((const void*)mega_kernel, hipFuncAttributeMaxDynamicSharedMemorySize, CH_LDS);
    mega_kernel<<<256, 512, CH_LDS, stream>>>((const bf*)d_in[0], (const bf*)d_in[1], (const bf*)d_in[2], (const bf*)d_in[4], (const bf*)d_in[5],
                                              (bf*)d_out, (char*)d_ws);
}
```

```cpp
#include <hip/hip_runtime.h>
#include <hip/hip_bf16.h>
#include <stdint.h>

typedef __attribute__((ext_vector_type(8))) short bf16x8;
typedef __attribute__((ext_vector_type(4))) short s16x4;
typedef __attribute__((ext_vector_type(4))) float f32x4;
typedef __attribute__((ext_vector_type(2))) float f32x2;
typedef __attribute__((ext_vector_type(16))) float f32x16;
typedef __attribute__((ext_vector_type(4))) unsigned u32x4;
typedef __attribute__((ext_vector_type(2))) unsigned u32x2;
typedef __attribute__((ext_vector_type(2))) __bf16 bf16x2_t;
typedef __attribute__((ext_vector_type(8))) __bf16 bf8_t;
typedef __attribute__((ext_vector_type(16))) __bf16 bf16v_t;
typedef __attribute__((address_space(3))) const char* lds_cptr;
typedef __attribute__((address_space(3))) char* lds_ptr;

#define DI __device__ __forceinline__
#define MFMA16(a, b, c) __builtin_amdgcn_mfma_f32_16x16x32_bf16((a), (b), (c), 0, 0, 0)
#define MFMA32(a, b, c) __builtin_amdgcn_mfma_f32_32x32x16_bf16((a), (b), (c), 0, 0, 0)
#define SBAR() __builtin_amdgcn_sched_barrier(0)
#define STAMP(i)
#define STAMPR(i)
#define STAMP_DECL
#define STAMP_FLUSH(it)
#define SP_PARAM
#define SP_ARG
typedef unsigned long long u64;
#define NTOUCH 3

constexpr float QSCALE = 0.17677669529663687f * 1.4426950408889634f;
constexpr float ATT_SCALE = 0.17677669529663687f;
constexpr float LOG2E = 1.4426950408889634f;
constexpr float NEG_MASK = -1e9f / ATT_SCALE;

DI unsigned pack_bf16(float lo, float hi) {
    f32x2 v = {lo, hi};
    bf16x2_t r = __builtin_convertvector(v, bf16x2_t);
    return __builtin_bit_cast(unsigned, r);
}
DI float bf_lo(unsigned u) { return __uint_as_float(u << 16); }
DI float bf_hi(unsigned u) { return __uint_as_float(u & 0xffff0000u); }
DI float round_bf16(float x) { return bf_lo(pack_bf16(x, 0.f)); }
DI void ln_stats(float sum, float sumsq, float& mean_out, float& rstd_out) {
    float mean = sum * (1.f / 1024.f);
    asm("" : "+v"(mean));
    const float var = fmaxf(sumsq * (1.f / 1024.f) - mean * mean, 0.f);
    mean_out = round_bf16(mean);
    rstd_out = __builtin_amdgcn_rcpf(round_bf16(__builtin_amdgcn_sqrtf(round_bf16(round_bf16(var) + 1e-5f))));
}

DI void glds16(const void* sbase, unsigned voff, unsigned ldsaddr) {
    asm volatile("s_mov_b32 m0, %0\n\ts_nop 0\n\tglobal_load_lds_dwordx4 %1, %2"
                 :: "s"(ldsaddr), "v"(voff), "s"(sbase) : "memory");
}
DI void glds16c(const void* sbase, unsigned voff, unsigned ldsaddr) {
    asm volatile("s_mov_b32 m0, %0\n\ts_nop 0\n\tglobal_load_lds_dwordx4 %1, %2 sc1"
                 :: "s"(ldsaddr), "v"(voff), "s"(sbase) : "memory");
}
DI void st16(void* sbase, unsigned voff, u32x4 v) {
    asm volatile("global_store_dwordx4 %0, %1, %2 sc1\n\ts_nop 1" :: "v"(voff), "v"(v), "s"(sbase) : "memory");
}
template <int OFF> DI void st16o(void* sbase, unsigned voff, u32x4 v) {
    asm volatile("global_store_dwordx4 %0, %1, %2 offset:%3 sc1\n\ts_nop 1" :: "v"(voff), "v"(v), "s"(sbase), "n"(OFF) : "memory");
}
DI bf16x8 lds_b128(lds_cptr p) { return *(const __attribute__((address_space(3))) bf16x8*)p; }
DI s16x4 lds_tr(lds_cptr p) {
    return __builtin_amdgcn_ds_read_tr16_b64_v4i16((__attribute__((address_space(3))) s16x4*)p);
}
DI bf16x8 cat8(s16x4 a, s16x4 b) { return __builtin_shufflevector(a, b, 0, 1, 2, 3, 4, 5, 6, 7); }
DI float dot2bf(unsigned a, unsigned b, float c) {
    return __builtin_amdgcn_fdot2_f32_bf16(__builtin_bit_cast(bf16x2_t, a), __builtin_bit_cast(bf16x2_t, b), c, false);
}
DI float sum_g(float v) {
    auto a = __builtin_amdgcn_permlane16_swap(__float_as_uint(v), __float_as_uint(v), false, false);
    v = __uint_as_float(a[0]) + __uint_as_float(a[1]);
    auto b = __builtin_amdgcn_permlane32_swap(__float_as_uint(v), __float_as_uint(v), false, false);
    return __uint_as_float(b[0]) + __uint_as_float(b[1]);
}
DI float wave_sum(float v) {
    v += __builtin_bit_cast(float, __builtin_amdgcn_update_dpp(0, __builtin_bit_cast(int, v), 0xB1, 0xF, 0xF, true));
    v += __builtin_bit_cast(float, __builtin_amdgcn_update_dpp(0, __builtin_bit_cast(int, v), 0x4E, 0xF, 0xF, true));
    v += __builtin_bit_cast(float, __builtin_amdgcn_update_dpp(0, __builtin_bit_cast(int, v), 0x141, 0xF, 0xF, true));
    v += __builtin_bit_cast(float, __builtin_amdgcn_update_dpp(0, __builtin_bit_cast(int, v), 0x140, 0xF, 0xF, true));
    return sum_g(v);
}
DI float amax3(float a, float b, float c) { float r; asm("v_max3_f32 %0, %1, %2, %3" : "=v"(r) : "v"(a), "v"(b), "v"(c)); return r; }
DI float addf(float a, float b) { float r; asm("v_add_f32 %0, %1, %2" : "=v"(r) : "v"(a), "v"(b)); return r; }
DI float max2f(float a, float b) { return __builtin_amdgcn_fmed3f(a, b, INFINITY); }
DI float max_h(float v) {
    auto b = __builtin_amdgcn_permlane32_swap(__float_as_uint(v), __float_as_uint(v), false, false);
    return max2f(__uint_as_float(b[0]), __uint_as_float(b[1]));
}

template <int OFF> DI void st8o(void* sbase, unsigned voff, f32x2 v) {
    asm volatile("global_store_dwordx2 %0, %1, %2 offset:%3 sc1\n\ts_nop 1" :: "v"(voff), "v"(v), "s"(sbase), "n"(OFF) : "memory");
}
constexpr unsigned OFF_DUMP = 159744;
extern __shared__ __attribute__((aligned(16))) char smem_base[];
DI void touch_at(const void* p, unsigned off) {
    asm volatile("s_mov_b32 m0, %0\n\ts_nop 0\n\tglobal_load_lds_dword %1, %2"
                 :: "s"((unsigned)(uintptr_t)smem_base + OFF_DUMP), "v"(off), "s"(p) : "memory");
}
DI void touch(const void* p, int slice, int lane) { touch_at(p, (unsigned)(slice * 64 + lane) * 128u); }
DI void code_touch(int slice, int lane) {
    unsigned long long c0, c1;
    asm volatile("s_getpc_b64 s[96:97]\n\ts_add_u32 s96, s96, mega_kernel@rel32@lo+4\n\ts_addc_u32 s97, s97, mega_kernel@rel32@hi+12\n\ts_mov_b64 %0, s[96:97]\n\t"
                 "s_getpc_b64 s[96:97]\n\ts_add_u32 s96, s96, zz_code_end_kernel@rel32@lo+4\n\ts_addc_u32 s97, s97, zz_code_end_kernel@rel32@hi+12\n\ts_mov_b64 %1, s[96:97]"
                 : "=s"(c0), "=s"(c1) :: "s96", "s97", "scc");
    touch_at((const void*)c0, min((unsigned)(slice * 64 + lane) * 128u, (unsigned)(c1 - c0)));
}

#define BAR() asm volatile("s_barrier" ::: "memory")
#define WAIT_LGKM() do { SBAR(); __builtin_amdgcn_s_waitcnt(0xC07F); SBAR(); } while (0)
#define WAIT_LGKM_BAR() do { SBAR(); __builtin_amdgcn_s_waitcnt(0xC07F); asm volatile("s_barrier" ::: "memory"); SBAR(); } while (0)
#define VM_BAR(n) asm volatile("s_waitcnt vmcnt(" #n ")\n\ts_barrier" ::: "memory")
constexpr int CH_LDS = 163840;
constexpr int OFF_SYNC = CH_LDS - 64;
typedef __attribute__((address_space(3))) unsigned* lds_uptr;
typedef __attribute__((address_space(1))) u64* g_u64ptr;

constexpr size_t MBy = 1 << 20;
constexpr size_t WS_X3 = 0, WS_X2 = 4 * MBy, WS_X1 = 8 * MBy, WS_OF = 12 * MBy  , WS_KVF = 36 * MBy  ,
                 WS_ST = 52 * MBy  , WS_CSP = 54 * MBy  , WS_FLAG = 55 * MBy  ,
                 WS_EPOCH = WS_FLAG + 15 * 65536, WS_Y0 = 56 * MBy;
DI unsigned flag_off(int set, int group, int consumer, int producer) { return (unsigned)(set * 65536 + group * 4096 + consumer * 256 + producer * 8); }
DI void flag_store(char* ws, int set, int group, int producer, u64 tag, int lane) {
    asm volatile("" : "+s"(tag));
    if (lane < 16) __hip_atomic_store((g_u64ptr)(ws + WS_FLAG + flag_off(set, group, lane, producer)), tag, __ATOMIC_RELAXED, __HIP_MEMORY_SCOPE_AGENT);
}
DI void flag_poll(const char* ws, unsigned off, u64 tag) {
    asm volatile("" : "+s"(tag));
#pragma unroll 1
    for (int spin = 0; spin < (1 << 16); ++spin) {
        const u64 v = __hip_atomic_load((g_u64ptr)(ws + WS_FLAG + off), __ATOMIC_RELAXED, __HIP_MEMORY_SCOPE_AGENT);
        if (__all(v == tag)) break;
        __builtin_amdgcn_s_sleep(1);
    }
    asm volatile("" ::: "memory");
}
#define POLL_INS "global_atomic_or_x2 %[land], %[po], %[zero], %[base] sc0 sc1"
template <bool DRAIN, bool ALLPOLL>
DI void signal_poll(int follower, const char* ws, unsigned soff, unsigned poff, u64 pmask, u64 tag, u64& land) {
    u64 tagv, zero;
    asm volatile("s_not_b64 s[96:97], %[ts]\n\tv_mov_b64 %[tag], %[ts]\n\tv_mov_b64 %[zero], 0\n\tv_mov_b64 %[land], s[96:97]\n\t"
                 ".if %[dr]\n\ts_waitcnt vmcnt(0)\n\t.endif\n\ts_barrier\n\t"
                 "s_cmp_lg_u32 %[fl], 0\n\ts_cbranch_scc1 sig_fol_%=\n\t"
                 "s_mov_b64 exec, 0xffff\n\t"
                 "global_store_dwordx2 %[so], %[tag], %[base] sc1\n"
                 ".if %[ap]\nsig_fol_%=:\n.endif\n\t"
                 "s_mov_b64 exec, %[pm]\n\t"
                 "s_movk_i32 s96, 0x7fff\n"
                 "sig_loop_%=:\n\t"
                 POLL_INS "\n\t"
                 "s_sleep 1\n\t"
                 "s_waitcnt vmcnt(3)\n\t"
                 "v_cmp_ne_u64 vcc, %[land], %[tag]\n\t"
                 "s_sub_u32 s96, s96, 1\n\t"
                 "s_cbranch_scc1 sig_out_%=\n\t"
                 "s_cbranch_vccnz sig_loop_%=\n"
                 "sig_out_%=:\n\t"
                 "s_mov_b64 exec, -1\n"
                 ".if !%[ap]\nsig_fol_%=:\n.endif"
                 : [land] "=&v"(land), [tag] "=&v"(tagv), [zero] "=&v"(zero)
                 : [fl] "s"(follower), [so] "v"(soff), [po] "v"(poff), [ts] "s"(tag), [base] "s"(ws + WS_FLAG), [pm] "s"(pmask), [ap] "n"(ALLPOLL ? 1 : 0), [dr] "n"(DRAIN ? 1 : 0)
                 : "memory", "scc", "vcc", "s96", "s97");
}
struct Ids { int pq, cq, po, co; };
DI Ids ids() {
    int bid = blockIdx.x; asm volatile("" : "+s"(bid));
    const int xcd = bid & 7, idx = bid >> 3;
    int pq, cq, po, co;
#define IDS_BODY pq = (xcd & 1) * 8 + (idx & 7); cq = (xcd >> 1) * 4 + (idx >> 3); po = (xcd & 3) * 4 + (idx & 3); co = (xcd >> 2) * 8 + (idx >> 2);
    IDS_BODY
    return Ids{pq, cq, po, co};
}

DI void slow_warm(unsigned dump, const void* W, int w, int lane) {
    int bid = blockIdx.x; asm volatile("" : "+s"(bid));
    const int xcd = bid & 7, idx = bid >> 3;
    const int sc = (3 - (int)((uintptr_t)W >> 7)) & 7;
    if ((sc >> 2) != ((xcd >> 1) & 1)) return;
    const char* src = (const char*)W + (idx + 32 * w) * 49152 + (sc + (xcd >> 2) * 8) * 128;
#pragma unroll
    for (int G = 0; G < 3; ++G) glds16(src + G * 2048, (unsigned)((lane >> 3) * 6144 + (lane & 7) * 16), dump);
}

template <int V> struct IC { static constexpr int value = V; };
template <bool LOCAL> struct QC {
    static constexpr int AROWS = LOCAL ? 144 : 128;
    static constexpr int ABYTES = AROWS * 128;
    static constexpr int STAGE = ABYTES + 24576;
    static constexpr int OFF_STATP = 129024;
    static constexpr int OFF_MEAN = OFF_STATP + 18432;
    static constexpr int OFF_RSTD = OFF_MEAN + 576;
    static constexpr int OFF_CS = OFF_RSTD + 576;
};
constexpr int OFF_EX = 131072;

DI void colsum_load(u32x4 (&c)[8], const void* W, int G, int lane) {
    const Ids I = ids();
    const unsigned off = (unsigned)(I.pq * 64 + (lane >> 3)) * 6144u + (unsigned)(G * 2048 + I.cq * 128 + (lane & 7) * 16);
#pragma unroll
    for (int i = 0; i < 8; ++i)
        asm volatile("global_load_dwordx4 %0, %1, %2" : "=&v"(c[i]) : "v"(off), "s"((const char*)W + i * 49152) : "memory");
}
DI void colsum_store(const u32x4 (&c)[8], char* dst, int G, int lane) {
    const Ids I = ids();
    const int m = lane & 15;
    const unsigned one = (m & 1) ? 0xbf800000u : 0xbf80u;
    const u32x4 sl = {(m >> 1) == 0 ? one : 0u, (m >> 1) == 1 ? one : 0u, (m >> 1) == 2 ? one : 0u, (m >> 1) == 3 ? one : 0u};
    f32x4 d = {0, 0, 0, 0};
#pragma unroll
    for (int i = 0; i < 8; ++i) d = MFMA16(__builtin_bit_cast(bf16x8, sl), __builtin_bit_cast(bf16x8, c[i]), d);
#pragma unroll
    for (int r = 0; r < 4; ++r)
        d[r] += __int_as_float(__builtin_amdgcn_update_dpp(0, __float_as_int(d[r]), 0x128, 0xF, 0xF, true));
    if (lane < 32 && (lane & 8) == 0)
        st16(dst, (unsigned)((I.cq * 16 + I.pq) * 768 + G * 256 + (lane & 7) * 32 + (lane >> 4) * 16), __builtin_bit_cast(u32x4, d));
}

template <bool LOCAL>
struct WFirst {
    unsigned v[2];
    DI WFirst(int w, int ct, int lane) {
#pragma unroll
        for (int i = 0; i < 2; ++i) {
            const int k = 8 * i + (lane >> 3), pos = lane & 7;
            v[i] = (unsigned)k * 6144u + (unsigned)(w * 1024 + ct * 64 + ((pos >> 1) ^ (((k >> 1) & 1) | (i << 1))) * 16 + (pos & 1) * 8) * 2u;
        }
        asm volatile("" : "+v"(v[0]), "+v"(v[1]));
    }
    DI void issue(const void* W, unsigned lds0, int w) const {
#pragma unroll
        for (int i = 0; i < 16; ++i)
            glds16((const char*)W + (size_t)(i >> 1) * (16 * 6144), v[i & 1], lds0 + (i >> 3) * QC<LOCAL>::STAGE + QC<LOCAL>::ABYTES + (w * 8 + (i & 7)) * 1024);
    }
};

template <bool LOCAL, class F>
DI void qkv_loader(char* smem, char* ws, int set, u64 tag, const __hip_bfloat16* __restrict__ X, const __hip_bfloat16* __restrict__ W,
                   const float* __restrict__ statp, const char* __restrict__ csp, bool ident, const void* nextW, F tail SP_PARAM) {
    typedef QC<LOCAL> C;
    constexpr int NT = 16;
    int tid = threadIdx.x; asm volatile("" : "+v"(tid));
    const int lane = tid & 63;
    const int w = __builtin_amdgcn_readfirstlane(tid >> 6) & 3;
    const int idx = blockIdx.x >> 3;
    const Ids I = ids();
    const int panel = I.pq, ct = I.cq;
    const int R0 = panel * 128;
    const unsigned lds0 = (unsigned)(uintptr_t)smem;
    const lds_cptr sm = (lds_cptr)smem;
    auto grow = [&](int i) -> int {
        if (!LOCAL) return R0 + i;
        if (i < 128) { const int t = (i >> 4) & 3, a = i & 15; return R0 + (i & 64) + 2 * (16 * (t & 1) + a) + (t >> 1); }
        const int a = i & 15; const int r = R0 - 16 + 2 * (a & 7) + (a >> 3);
        return r < 0 ? 0 : r;
    };
    unsigned voffA[5], voffB[6];
#pragma unroll
    for (int i = 0; i < 6; ++i) {
        const int j = w + 4 * i, G = j >> 3, k = 8 * (j & 7) + (lane >> 3);
        const int pos = lane & 7, f = ((k >> 1) & 1) | (((k >> 3) & 1) << 1);
        const int seg = (pos >> 1) ^ f;
        voffB[i] = (unsigned)k * 6144u + (unsigned)(G * 1024 + ct * 64 + seg * 16 + (pos & 1) * 8) * 2u;
    }
    auto issueW = [&](int t, int buf) {
        const char* wb = (const char*)W + (size_t)t * (64 * 6144);
        const unsigned sb = lds0 + buf * C::STAGE;
#pragma unroll
        for (int i = 0; i < 6; ++i) glds16(wb, voffB[i], sb + C::ABYTES + (w + 4 * i) * 1024);
    };
    asm volatile("" : "+v"(voffB[0]), "+v"(voffB[1]), "+v"(voffB[2]), "+v"(voffB[3]), "+v"(voffB[4]), "+v"(voffB[5]));
    const WFirst<LOCAL> first(w, ct, lane);
    const int lt = min(tid - 256, C::AROWS - 1);
    const int gr = LOCAL ? grow(lt) - R0 : ((lt & 64) + (lt & 1) * 32 + ((lt & 63) >> 1));
    lds_cptr sp = sm + C::OFF_STATP + ((lt < 128) ? gr * 8 : (16384 + (panel ? (gr + 16) : 0) * 8));
    lds_ptr mp = (lds_ptr)smem + C::OFF_MEAN + lt * 4, cp = (lds_ptr)smem + C::OFF_CS + lane * 12;
    asm volatile("" : "+v"(sp), "+v"(mp), "+v"(cp));
#pragma unroll
    for (int i = 0; i < 5; ++i) {
        const int j = (i < 4) ? (w + 4 * i) : (16 + (w & 1));
        const int row = 8 * j + (lane >> 3);
        const int c = (lane & 7) ^ ((row >> 1) & 7);
        voffA[i] = (unsigned)grow(row < C::AROWS ? row : 0) * 2048u + c * 16;
        asm volatile("" : "+v"(voffA[i]));
    }
    auto issueA = [&](int t, int buf) {
        const char* xa = (const char*)X + t * 128;
        const unsigned sb = lds0 + buf * C::STAGE;
        if (LOCAL && w < 2) glds16c(xa, voffA[4], sb + (16 + w) * 1024);
#pragma unroll
        for (int i = 0; i < 4; ++i) glds16c(xa, voffA[i], sb + (w + 4 * i) * 1024);
    };
    u64 land;
    if (w == 3) __builtin_amdgcn_s_setprio(3);
    signal_poll<LOCAL, false>(w - 3, ws, flag_off(set, I.po, lane & 15, I.co),
                              LOCAL ? flag_off(set, (lane & 16) && panel ? panel - 1 : panel, ct, lane & 15)
                                    : (lane < 16 ? flag_off(set, panel, ct, lane) : flag_off(9 + (lane >> 4), ct, panel, lane & 15)),
                              LOCAL ? 0xffffffffull : ~0ull, tag, land);
    if (w < 3) first.issue(W, lds0, w);
    BAR();
    STAMP(LOCAL ? 1 : 6);
    issueA(0, 0); issueA(1, 1);
    VM_BAR(4);
    asm volatile("" :: "v"(land));
    __builtin_amdgcn_s_setprio(0);
    STAMP(LOCAL ? 2 : 7);
    issueW(2, 2);
    auto issueS = [&](int i) {
        if (i == 8 && idx < 3) code_touch(idx * 4 + w, lane);
        if (LOCAL && i == 9)
            glds16(nextW, (unsigned)((idx + 32 * w) * 8 + (lane >> 3)) * 2048u +
                   (unsigned)(((I.co & 8) + ((3 - (int)((uintptr_t)nextW >> 7)) & 7)) * 128 + (lane & 7) * 16), lds0 + OFF_DUMP);
        if (ident) return;
        if (i < 4) glds16c(statp, (unsigned)((panel * 16 + w + 4 * i) * 1024 + lane * 16), lds0 + C::OFF_STATP + (w + 4 * i) * 1024);
        else if (LOCAL && i == 4 && w < 2) {
            const unsigned o = panel ? (unsigned)(((panel - 1) * 16 + w * 8 + (lane >> 3)) * 1024 + 896 + (lane & 7) * 16)
                                     : (unsigned)((w * 8 + (lane >> 3)) * 1024 + (lane & 7) * 16);
            glds16c(statp, o, lds0 + C::OFF_STATP + (16 + w) * 1024);
        }
    };
    issueA(2, 2);
    VM_BAR(10);
    typedef __attribute__((ext_vector_type(3))) float f32x3;
    f32x3 cs[16];
    if (w == 3 && !ident) {
#pragma unroll
        for (int i = 0; i < 16; i += 4)
            asm volatile("global_load_dwordx3 %0, %4, %5 sc1\n\tglobal_load_dwordx3 %1, %4, %5 offset:768 sc1\n\t"
                         "global_load_dwordx3 %2, %4, %5 offset:1536 sc1\n\tglobal_load_dwordx3 %3, %4, %5 offset:2304 sc1"
                         : "=&v"(cs[i]), "=&v"(cs[i + 1]), "=&v"(cs[i + 2]), "=&v"(cs[i + 3])
                         : "v"(lane * 12), "s"(csp + (ct * 16 + i) * 768) : "memory");
    }
    issueA(3, 0); issueW(3, 0);
    int buf = 1;
#pragma unroll 1
    for (int t = 1; t < NT - 3; ++t) {
        VM_BAR(10);
        issueA(t + 3, buf); issueW(t + 3, buf);
        if (t >= 2) issueS(t - 2);
        buf = (buf == 2) ? 0 : buf + 1;
    }
    VM_BAR(10);
    float sa = 0.f, sb = 0.f;
    auto fold = [&](int n) {
#pragma unroll
        for (int i = 0; i < n; ++i)
#pragma unroll
            for (int e = 0; e < 3; ++e) cs[i][e] = addf(cs[i][e], cs[i + n][e]);
    };
    auto rows = [&](auto STR) {
        f32x2 v[16];
#pragma unroll
        for (int i = 0; i < 16; ++i) v[i] = *(const __attribute__((address_space(3))) f32x2*)(sp + i * STR.value);
        sa = v[0][0]; sb = v[0][1];
#pragma unroll
        for (int i = 1; i < 16; ++i) { sa = addf(sa, v[i][0]); sb = addf(sb, v[i][1]); }
    };
    if (!ident) {
        if (w == 3) {
#pragma unroll
            for (int i = 0; i < 16; ++i) asm volatile("" : "+v"(cs[i]));
            fold(8);
#pragma unroll
            for (int i = 0; i < 8; ++i) asm volatile("" : "+v"(cs[i]));
        } else if (w < 2) rows(IC<1024>{});
        else if (LOCAL) rows(IC<128>{});
    }
    asm volatile("" : "+v"(sa), "+v"(sb));
    if (ident) tail(); else asm volatile("s_waitcnt vmcnt(0)" ::: "memory");
    BAR();
    if (!ident) {
        typedef __attribute__((address_space(3))) float* lds_f;
        if (w == 3) {
            fold(4); fold(2); fold(1);
#pragma unroll
            for (int e = 0; e < 3; ++e) ((lds_f)cp)[e] = cs[0][e];
        } else if (LOCAL || w < 2) {
            float mean, rstd;
            ln_stats(sa, sb, mean, rstd);
            *(lds_f)mp = mean; *(lds_f)(mp + (C::OFF_RSTD - C::OFF_MEAN)) = rstd;
        }
    }
    WAIT_LGKM_BAR();
}

template <bool LOCAL>
DI void qkv_compute(char* smem, const void* W, u32x4 (&qo4)[4], __hip_bfloat16* __restrict__ kvf, __hip_bfloat16* __restrict__ ofrag, bool ident SP_PARAM) {
    typedef QC<LOCAL> C;
    constexpr int NT = 16, ABYTES = C::ABYTES, STAGE = C::STAGE;
    int tz = threadIdx.x; asm volatile("" : "+v"(tz));
    const int lane = tz & 63;
    const int w = __builtin_amdgcn_readfirstlane(tz >> 6) & 3;
    const int wm = w & 1, wn = w >> 1;
    const Ids I = ids();
    const int panel = I.pq, ct = I.cq;
    const int l15 = lane & 15, g = lane >> 4;
    const lds_cptr sm = (lds_cptr)smem;
    lds_ptr smw = (lds_ptr)smem;

    VM_BAR(0);
    f32x4 acc[3][4][2];
    f32x4 acch[2];
    f32x2 z2 = {0, 0}; asm volatile("" : "+v"(z2));
    const f32x4 z4 = {z2[0], z2[1], z2[0], z2[1]};
#pragma unroll
    for (int a = 0; a < 3; ++a)
#pragma unroll
        for (int t = 0; t < 4; ++t) { acc[a][t][0] = z4; acc[a][t][1] = z4; }
    acch[0] = z4; acch[1] = z4;

    struct Frags { bf16x8 a[4]; bf16x8 h; s16x4 b[3][2][2]; };
    auto rd = [&](auto WM, Frags& F, lds_cptr pa, lds_cptr pb0, lds_cptr pb1, int n) {
        switch (n) {
            case 0: F.b[0][0][0] = lds_tr(pb0); break;
            case 1: F.b[0][0][1] = lds_tr(pb0 + 512); break;
            case 2: F.a[0] = lds_b128(pa); break;
            case 3: F.b[0][1][0] = lds_tr(pb1); break;
            case 4: F.b[0][1][1] = lds_tr(pb1 + 512); break;
            case 5: F.a[1] = lds_b128(pa + 2048); break;
            case 6: F.a[2] = lds_b128(pa + 4096); break;
            case 7: F.a[3] = lds_b128(pa + 6144); break;
            case 8: F.b[1][0][0] = lds_tr(pb0 + 8192); break;
            case 9: F.b[1][0][1] = lds_tr(pb0 + 8192 + 512); break;
            case 10: F.b[1][1][0] = lds_tr(pb1 + 8192); break;
            case 11: F.b[1][1][1] = lds_tr(pb1 + 8192 + 512); break;
            case 12: F.b[2][0][0] = lds_tr(pb0 + 16384); break;
            case 13: F.b[2][0][1] = lds_tr(pb0 + 16384 + 512); break;
            case 14: F.b[2][1][0] = lds_tr(pb1 + 16384); break;
            case 15: F.b[2][1][1] = lds_tr(pb1 + 16384 + 512); break;
            case 16: if (LOCAL) F.h = lds_b128(pa + 16384 - WM.value * 8192); break;
            default: break;
        }
    };
    auto half = [&](auto WM, const Frags& C, Frags& N, bool doread, lds_cptr pa, lds_cptr pb0, lds_cptr pb1, auto tail) {
#pragma unroll
        for (int G = 0; G < 3; ++G) {
            const bf16x8 b0 = cat8(C.b[G][0][0], C.b[G][0][1]), b1 = cat8(C.b[G][1][0], C.b[G][1][1]);
#pragma unroll
            for (int t = 0; t < 4; ++t) {
                const int n = G * 8 + t * 2;
                if (G < 2) acc[G][t][0] = MFMA16(b0, C.a[t], acc[G][t][0]); else acc[G][t][0] = MFMA16(C.a[t], b0, acc[G][t][0]);
                if (n == 0) SBAR();
                if (doread) rd(WM, N, pa, pb0, pb1, n);
                tail(n);
                SBAR();
                if (G < 2) acc[G][t][1] = MFMA16(b1, C.a[t], acc[G][t][1]); else acc[G][t][1] = MFMA16(C.a[t], b1, acc[G][t][1]);
                if (doread) rd(WM, N, pa, pb0, pb1, n + 1);
                tail(n + 1);
                SBAR();
            }
        }
        if (LOCAL) {
            if (WM.value == 0) { acch[0] = MFMA16(cat8(C.b[1][0][0], C.b[1][0][1]), C.h, acch[0]); acch[1] = MFMA16(cat8(C.b[1][1][0], C.b[1][1][1]), C.h, acch[1]); }
            else         { acch[0] = MFMA16(C.h, cat8(C.b[2][0][0], C.b[2][0][1]), acch[0]); acch[1] = MFMA16(C.h, cat8(C.b[2][1][0], C.b[2][1][1]), acch[1]); }
            SBAR();
        }
    };

    Frags F0, F1;
    f32x4 cq[2], ck[2], mu4[5], rs4[5]; float cv[2], mu[5], rs[5];
    u32x4 qfr[4], kfr[4];
    u32x4 vfr[4], cc;
    f32x4 mwin, mfirst;
    typedef __attribute__((address_space(3))) u32x4* lds_u4;
    volatile __attribute__((address_space(3))) unsigned* fl = (volatile __attribute__((address_space(3))) unsigned*)(smw + OFF_SYNC + 16);
    constexpr int OFF_CST = C::OFF_CS + 768;
    const bool negpos = (wm == 0) && (panel == 0);
    if (LOCAL) {
        const unsigned keepv = negpos ? __float_as_uint(NEG_MASK) : 0u;
        u32x4 mw, mf;
        bool cur[4];
#pragma unroll
        for (int r = 0; r < 4; ++r) {
            const int d = l15 - (4 * g + r);
            const unsigned out = (unsigned)(((d << 28) >> 31));
            mw[r] = out & 0xff800000u;
            mf[r] = (out & 0xff800000u) | (~out & keepv);
            cur[r] = d >= 0;
        }
        const unsigned e0 = (unsigned)(OFF_EX + wn * 6144);
        lds_u4 cst = (lds_u4)(smw + OFF_CST) + lane;
        fl[w] = 0u;
        cst[0] = mw; cst[64 + wm * 64] = mf;
        cst[192 + w * 64] = u32x4{(cur[0] ? 0xffffu : 0u) | (cur[1] ? 0xffff0000u : 0u), (cur[2] ? 0xffffu : 0u) | (cur[3] ? 0xffff0000u : 0u),
                                  e0 + (unsigned)(wm ? lane : 256 + (lane ^ 8)) * 16u, e0 + (unsigned)(wm ? 128 + lane : 320 + (lane ^ 32)) * 16u};
    }
    typedef const __attribute__((address_space(3))) f32x4* lds_f4; typedef const __attribute__((address_space(3))) float* lds_f1;
    auto mainloop = [&](auto WM) {
#pragma unroll
        for (int a = 0; a < 3; ++a)
            asm volatile("" : "+v"(acc[a][0][0]), "+v"(acc[a][0][1]), "+v"(acc[a][1][0]), "+v"(acc[a][1][1]), "+v"(acc[a][2][0]), "+v"(acc[a][2][1]),
                         "+v"(acc[a][3][0]), "+v"(acc[a][3][1]));
        int ty = threadIdx.x; asm volatile("" : "+v"(ty));
        const int l15 = ty & 15, g = (ty >> 4) & 3;
        const int swA = (l15 >> 1) & 7;
        const int q4 = l15 >> 2, p4 = l15 & 3, fk = ((q4 >> 1) & 1) | ((g & 1) << 1);
        lds_cptr pA0 = sm + wm * 8192 + l15 * 128 + ((0 + g) ^ swA) * 16, pA1 = sm + wm * 8192 + l15 * 128 + ((4 + g) ^ swA) * 16;
        lds_cptr pB0 = sm + ABYTES + (8 * g + q4) * 128 + (((2 * wn + 0) ^ fk) * 32) + p4 * 8;
        lds_cptr pB1 = sm + ABYTES + (8 * g + q4) * 128 + (((2 * wn + 1) ^ fk) * 32) + p4 * 8;
        asm volatile("" : "+v"(acch[0]), "+v"(acch[1]), "+v"(pA0), "+v"(pA1), "+v"(pB0), "+v"(pB1));
        if (LOCAL && ident && w >= NTOUCH && w < 3) touch(W, w * 256 + blockIdx.x, ty & 63);
        slow_warm((unsigned)(uintptr_t)smem + OFF_DUMP, W, w, ty & 63);
        BAR();
        STAMP(LOCAL ? 1 : 7);
        BAR();
#pragma unroll
        for (int n = 0; n < 17; ++n) rd(WM, F0, pA0, pB0, pB1, n);
        WAIT_LGKM();
        int off = 0;
        lds_cptr a = pA1, b0 = pB0, b1 = pB1;
#pragma unroll 1
        for (int t = 0; t < NT - 1; ++t) {
            half(WM, F0, F1, true, a, b0 + 4096, b1 + 4096, [&](int n) {
                if (n == 17) { off = (off == 2 * STAGE) ? 0 : off + STAGE; asm volatile("" : "+s"(off)); }
                if (n == 19) { b0 = pB0 + off; asm volatile("" : "+v"(b0)); }
                if (n == 20) { a = pA0 + off; asm volatile("" : "+v"(a)); }
                if (n == 21) { b1 = pB1 + off; asm volatile("" : "+v"(b1)); }
            });
            WAIT_LGKM_BAR();
            half(WM, F1, F0, true, a, b0, b1, [&](int n) {
                if (n == 19) { a = pA1 + off; asm volatile("" : "+v"(a)); }
                if (n == 22) __builtin_amdgcn_s_waitcnt(0xC07F);
            });
        }
        lds_cptr s1 = sm, s4 = sm, c1 = sm, c4 = sm;
        half(WM, F0, F1, true, a, b0 + 4096, b1 + 4096, [&](int n) {
            if (n == 16) { ty = threadIdx.x; asm volatile("" : "+v"(ty)); }
            if (n == 17) { s1 = sm + C::OFF_MEAN + wm * 256 + (ty & 15) * 4; asm volatile("" : "+v"(s1)); }
            if (n == 18) { s4 = sm + C::OFF_MEAN + wm * 256 + (ty & 48); asm volatile("" : "+v"(s4)); }
            if (n == 19) { c1 = sm + C::OFF_CS + wn * 128 + (ty & 15) * 4; asm volatile("" : "+v"(c1)); }
            if (n == 20) { c4 = sm + C::OFF_CS + wn * 128 + (ty & 48); asm volatile("" : "+v"(c4)); }
        });
        WAIT_LGKM_BAR();
        half(WM, F1, F0, false, sm, sm, sm, [&](int n) {
            constexpr int RS = C::OFF_RSTD - C::OFF_MEAN, H = 512 - WM.value * 256;
            if (n < 2) { if (!LOCAL) cq[n] = *(lds_f4)(c4 + n * 64); ck[n] = *(lds_f4)(c4 + 256 + n * 64); }
            else if (n < 6) { mu[n - 2] = *(lds_f1)(s1 + (n - 2) * 64); rs[n - 2] = *(lds_f1)(s1 + RS + (n - 2) * 64); }
            else if (n < 10) { if (!LOCAL || (n & 1)) { mu4[n - 6] = *(lds_f4)(s4 + (n - 6) * 64); rs4[n - 6] = *(lds_f4)(s4 + RS + (n - 6) * 64); } }
            else if (n == 10) { cv[0] = *(lds_f1)(c1 + 512); cv[1] = *(lds_f1)(c1 + 512 + 64); }
            else if (LOCAL && n == 11) {
                if (WM.value == 0) { mu[4] = *(lds_f1)(s1 + H); rs[4] = *(lds_f1)(s1 + RS + H); }
                else { mu4[4] = *(lds_f4)(s4 + H); rs4[4] = *(lds_f4)(s4 + RS + H); }
            }
        });
        if (LOCAL) {
            auto fix = [&](int a, int t) {
#pragma unroll
                for (int f = 0; f < 2; ++f)
                    acc[a][t][f] = (a == 2) ? rs4[t] * (acc[2][t][f] + mu4[t] * cv[f]) : rs[t] * (acc[a][t][f] + mu[t] * (a ? ck[f] : cq[f]));
            };
            auto pk = [&](const f32x4 (&v)[2]) { return u32x4{pack_bf16(v[0][0], v[0][1]), pack_bf16(v[0][2], v[0][3]), pack_bf16(v[1][0], v[1][1]), pack_bf16(v[1][2], v[1][3])}; };
            cq[0] = *(lds_f4)c4; cq[1] = *(lds_f4)(c4 + 64);
            SBAR();
            if (!ident) {
                fix(1, 1); fix(1, 3); fix(2, 1); fix(2, 3);
#pragma unroll
                for (int f = 0; f < 2; ++f)
                    acch[f] = (WM.value == 0) ? rs[4] * (acch[f] + mu[4] * ck[f]) : rs4[4] * (acch[f] + mu4[4] * cv[f]);
            }
            kfr[1] = pk(acc[1][1]); kfr[3] = pk(acc[1][3]); vfr[1] = pk(acc[2][1]); vfr[3] = pk(acc[2][3]);
            lds_u4 ex = (lds_u4)(smw + OFF_EX + wn * 6144) + (ty & 63);
            if (WM.value == 0) { ex[0] = kfr[1]; ex[64] = kfr[3]; ex[128] = vfr[1]; ex[192] = vfr[3]; }
            ex[256 + WM.value * 64] = pk(acch);
#pragma unroll
            for (int t = 0; t < 4; t += 2) { mu4[t] = *(lds_f4)(s4 + t * 64); rs4[t] = *(lds_f4)(s4 + (C::OFF_RSTD - C::OFF_MEAN) + t * 64); }
            SBAR();
            if (!ident) { fix(0, 0); fix(0, 1); fix(0, 2); fix(0, 3); }
#pragma unroll
            for (int t = 0; t < 4; ++t) qfr[t] = pk(acc[0][t]);
            WAIT_LGKM();
            fl[w] = 1u;
            lds_u4 cst = (lds_u4)(smw + OFF_CST) + (ty & 63);
            mwin = __builtin_bit_cast(f32x4, cst[0]); mfirst = __builtin_bit_cast(f32x4, cst[64 + WM.value * 64]); cc = cst[192 + w * 64];
            SBAR();
            if (!ident) { fix(1, 0); fix(1, 2); fix(2, 0); fix(2, 2); }
            kfr[0] = pk(acc[1][0]); kfr[2] = pk(acc[1][2]); vfr[0] = pk(acc[2][0]); vfr[2] = pk(acc[2][2]);
        }
    };
    const int head = ct * 2 + wn;
    unsigned qo = 0, ko = 0, vo = 0;
    if (LOCAL) {
        qo = (unsigned)((((panel * 8 + wm * 4) * 32 + head) * 64 + lane) * 16);
        asm volatile("" : "+v"(qo));
    } else {
        ko = (unsigned)((wm * 8 + (g & 1)) * 1024 + ((l15 & 7) + ((l15 & 8) << 1) + 32 * (g >> 1)) * 16);
        vo = (unsigned)((wm * 8 + 2 + (g >> 1)) * 1024 + (l15 + 32 * (g & 1)) * 16);
        asm volatile("" : "+v"(ko), "+v"(vo));
    }
    if (LOCAL && wm) mainloop(IC<1>{}); else mainloop(IC<0>{});

    if (!LOCAL) {
        auto pk = [&](f32x4 a, f32x4 b) { return u32x4{pack_bf16(a[0], a[1]), pack_bf16(a[2], a[3]), pack_bf16(b[0], b[1]), pack_bf16(b[2], b[3])}; };
        char* kvp = (char*)kvf + (size_t)(head * 16 + panel) * 16384;
        lds_ptr own = smw + wn * 16384;
        SBAR();
#pragma unroll
        for (int t = 0; t < 4; ++t) {
            const u32x4 kk = pk(rs[t] * (acc[1][t][0] + mu[t] * ck[0]), rs[t] * (acc[1][t][1] + mu[t] * ck[1]));
            if (t & 1) st16o<128>(kvp + (t >> 1) * 4096, ko, kk); else st16o<0>(kvp + (t >> 1) * 4096, ko, kk);
            *(lds_u4)(own + ko + (t >> 1) * 4096 + (t & 1) * 128) = kk;
            SBAR();
            const int u = t >> 1, f = t & 1;
            const u32x4 vv = pk(rs4[2 * u] * (acc[2][2 * u][f] + mu4[2 * u] * cv[f]), rs4[2 * u + 1] * (acc[2][2 * u + 1][f] + mu4[2 * u + 1] * cv[f]));
            if (f) st16o<256>(kvp + u * 4096, vo, vv); else st16o<0>(kvp + u * 4096, vo, vv);
            *(lds_u4)(own + vo + u * 4096 + f * 256) = vv;
            SBAR();
        }
#pragma unroll
        for (int u = 0; u < 2; ++u) {
            const float r0 = rs[2 * u] * QSCALE, r1 = rs[2 * u + 1] * QSCALE;
            const u32x4 qa = pk(r0 * (acc[0][2 * u][0] + mu[2 * u] * cq[0]), r0 * (acc[0][2 * u][1] + mu[2 * u] * cq[1]));
            const u32x4 qb = pk(r1 * (acc[0][2 * u + 1][0] + mu[2 * u + 1] * cq[0]), r1 * (acc[0][2 * u + 1][1] + mu[2 * u + 1] * cq[1]));
#pragma unroll
            for (int e = 0; e < 4; ++e) {
                const auto x = __builtin_amdgcn_permlane16_swap(qa[e], qb[e], false, false);
                qo4[2 * u][e] = x[0]; qo4[2 * u + 1][e] = x[1];
            }
        }
        return;
    }
    {
        const f32x4 zero4 = {0, 0, 0, 0};
        const unsigned cm01 = cc[0], cm23 = cc[1];
#pragma unroll 1
        for (int spin = 0; spin < (1 << 22); ++spin)
            if (__builtin_amdgcn_readfirstlane(fl[w ^ 1]) != 0u) break;
        asm volatile("" ::: "memory");
        u32x4 kp[2], vp[2];
        kp[0] = *(lds_u4)(smw + cc[2]); kp[1] = *(lds_u4)(smw + (cc[2] ^ (wm ? 1024u : 128u)));
        vp[0] = *(lds_u4)(smw + cc[3]); vp[1] = *(lds_u4)(smw + (cc[3] ^ (wm ? 1024u : 512u)));
        if (negpos) { kp[0] = u32x4{0, 0, 0, 0}; kp[1] = kp[0]; }
        f32x4 sp[4], sc[4];
#pragma unroll
        for (int i = 0; i < 4; ++i) {
            const int t = (i == 0) ? 1 : (i == 1) ? 3 : (i == 2) ? 0 : 2;
            const bf16x8 qb = __builtin_bit_cast(bf16x8, qfr[t]);
            sc[t] = MFMA16(__builtin_bit_cast(bf16x8, kfr[t]), qb, mwin);
            sp[t] = MFMA16(__builtin_bit_cast(bf16x8, (t & 1) ? kfr[t - 1] : kp[t >> 1]), qb, (t & 1) ? mwin : mfirst);
        }
        SBAR();
        float x[4][4], m[4], inv[4], c2[4];
        unsigned a[4][2];
#pragma unroll
        for (int t = 0; t < 4; ++t)
#pragma unroll
            for (int h = 0; h < 2; ++h) {
                const unsigned uc = pack_bf16(sc[t][2 * h], sc[t][2 * h + 1]), up = pack_bf16(sp[t][2 * h], sp[t][2 * h + 1]);
                const unsigned cm = h ? cm23 : cm01;
                a[t][h] = (uc & cm) | (up & ~cm);
            }
        SBAR();
#pragma unroll
        for (int t = 0; t < 4; ++t)
#pragma unroll
            for (int h = 0; h < 2; ++h) { x[t][2 * h] = bf_lo(a[t][h]) * ATT_SCALE; x[t][2 * h + 1] = bf_hi(a[t][h]) * ATT_SCALE; }
        SBAR();
#pragma unroll
        for (int t = 0; t < 4; ++t)
#pragma unroll
            for (int h = 0; h < 2; ++h) a[t][h] = pack_bf16(x[t][2 * h], x[t][2 * h + 1]);
        SBAR();
#pragma unroll
        for (int t = 0; t < 4; ++t)
#pragma unroll
            for (int h = 0; h < 2; ++h) { x[t][2 * h] = bf_lo(a[t][h]); x[t][2 * h + 1] = bf_hi(a[t][h]); }
        SBAR();
#pragma unroll
        for (int t = 0; t < 4; ++t) m[t] = amax3(x[t][0], x[t][1], x[t][2]);
#pragma unroll
        for (int t = 0; t < 4; ++t) m[t] = amax3(m[t], x[t][3], x[t][3]);
        SBAR();
        asm volatile("s_nop 1");
        c2[0] = m[0]; c2[1] = m[1]; c2[2] = m[2]; c2[3] = m[3];
        asm volatile("" : "+v"(c2[0]), "+v"(c2[1]), "+v"(c2[2]), "+v"(c2[3]));
#pragma unroll
        for (int t = 0; t < 4; ++t) {
            const auto u = __builtin_amdgcn_permlane16_swap(__float_as_uint(m[t]), __float_as_uint(c2[t]), false, false);
            m[t] = amax3(__uint_as_float(u[0]), __uint_as_float(u[1]), __uint_as_float(u[1]));
        }
        SBAR();
        asm volatile("s_nop 1");
        c2[0] = m[0]; c2[1] = m[1]; c2[2] = m[2]; c2[3] = m[3];
        asm volatile("" : "+v"(c2[0]), "+v"(c2[1]), "+v"(c2[2]), "+v"(c2[3]));
#pragma unroll
        for (int t = 0; t < 4; ++t) {
            const auto u = __builtin_amdgcn_permlane32_swap(__float_as_uint(m[t]), __float_as_uint(c2[t]), false, false);
            m[t] = amax3(__uint_as_float(u[0]), __uint_as_float(u[1]), __uint_as_float(u[1]));
        }
        SBAR();
#pragma unroll
        for (int t = 0; t < 4; ++t)
#pragma unroll
            for (int r = 0; r < 4; ++r) x[t][r] = (x[t][r] - m[t]) * LOG2E;
        SBAR();
#pragma unroll
        for (int t = 0; t < 4; ++t)
#pragma unroll
            for (int r = 0; r < 4; ++r) x[t][r] = __builtin_amdgcn_exp2f(x[t][r]);
        SBAR();
#pragma unroll
        for (int t = 0; t < 4; ++t) inv[t] = (x[t][0] + x[t][1]) + (x[t][2] + x[t][3]);
        SBAR();
        c2[0] = inv[0]; c2[1] = inv[1]; c2[2] = inv[2]; c2[3] = inv[3];
        asm volatile("" : "+v"(c2[0]), "+v"(c2[1]), "+v"(c2[2]), "+v"(c2[3]));
#pragma unroll
        for (int t = 0; t < 4; ++t) {
            const auto u = __builtin_amdgcn_permlane16_swap(__float_as_uint(inv[t]), __float_as_uint(c2[t]), false, false);
            inv[t] = __uint_as_float(u[0]) + __uint_as_float(u[1]);
        }
        SBAR();
        c2[0] = inv[0]; c2[1] = inv[1]; c2[2] = inv[2]; c2[3] = inv[3];
        asm volatile("" : "+v"(c2[0]), "+v"(c2[1]), "+v"(c2[2]), "+v"(c2[3]));
#pragma unroll
        for (int t = 0; t < 4; ++t) {
            const auto u = __builtin_amdgcn_permlane32_swap(__float_as_uint(inv[t]), __float_as_uint(c2[t]), false, false);
            inv[t] = __builtin_amdgcn_rcpf(__uint_as_float(u[0]) + __uint_as_float(u[1]));
        }
        SBAR();
#pragma unroll
        for (int t = 0; t < 4; ++t)
#pragma unroll
            for (int h = 0; h < 2; ++h) a[t][h] = pack_bf16(x[t][2 * h] * inv[t], x[t][2 * h + 1] * inv[t]);
        SBAR();
        f32x4 o0[4], o1[4];
#pragma unroll
        for (int t = 0; t < 4; ++t) {
            const bf16x8 pb = __builtin_bit_cast(bf16x8, (u32x4{a[t][0] & ~cm01, a[t][1] & ~cm23, a[t][0] & cm01, a[t][1] & cm23}));
            const u32x4 vprev = (t & 1) ? vfr[t - 1] : vp[t >> 1];
            o0[t] = MFMA16(__builtin_bit_cast(bf16x8, (u32x4{vprev[0], vprev[1], vfr[t][0], vfr[t][1]})), pb, zero4);
            o1[t] = MFMA16(__builtin_bit_cast(bf16x8, (u32x4{vprev[2], vprev[3], vfr[t][2], vfr[t][3]})), pb, zero4);
        }
        SBAR();
#pragma unroll
        for (int t = 0; t < 4; ++t)
            st16(ofrag + t * 16384, qo, u32x4{pack_bf16(o0[t][0], o0[t][1]), pack_bf16(o0[t][2], o0[t][3]),
                                                 pack_bf16(o1[t][0], o1[t][1]), pack_bf16(o1[t][2], o1[t][3])});
    }
}

DI int out_row(int panel, bool localrows, int T, int a) {
    return localrows ? (panel * 128 + (T >> 2) * 64 + 2 * (16 * (T & 1) + a) + ((T >> 1) & 1)) : (panel * 128 + T * 16 + a);
}
DI int out_swz(int k) { return ((k >> 1) & 1) | (k & 4); }
DI void wo_quarter(const void* Wo, int q, unsigned ldsdst, int w, int lane) {
    const int kl = lane >> 3, co = ids().co;
    const unsigned voff = (unsigned)(w * 8 + kl) * 2048u + (unsigned)(co * 128 + ((lane & 7) ^ out_swz(kl)) * 16);
    const char* src = (const char*)Wo + (size_t)q * 524288;
#pragma unroll
    for (int i = 0; i < 8; ++i) glds16(src + i * 65536, voff, ldsdst + (w + 4 * i) * 1024);
}
DI void res_load(u32x4 (&r)[4], const void* Xin, bool localrows, int w, int lane) {
    const Ids I = ids();
#pragma unroll
    for (int i = 0; i < 4; ++i) {
        const int row = (w + 4 * i) * 8 + (lane >> 3);
        asm volatile("global_load_dwordx4 %0, %1, %2 sc1" : "=&v"(r[i])
                     : "v"((unsigned)out_row(I.po, localrows, row >> 4, row & 15) * 2048u + (unsigned)(I.co * 128 + (((lane & 7) ^ (lane >> 3)) * 16))),
                       "s"(Xin) : "memory");
    }
}
DI void res_write(u32x4 (&r)[4], char* smem, int off, int w, int lane) {
    asm volatile("" : "+v"(r[0]), "+v"(r[1]), "+v"(r[2]), "+v"(r[3]));
#pragma unroll
    for (int i = 0; i < 4; ++i) *(__attribute__((address_space(3))) u32x4*)((lds_ptr)smem + off + (w + 4 * i) * 1024 + lane * 16) = r[i];
}

template <bool AFTER_LOCAL>
DI void out_compute(char* smem, char* ws, int set, u64 tag, const __hip_bfloat16* __restrict__ ofrag,
                    __hip_bfloat16* __restrict__ Xout, float* __restrict__ statp SP_PARAM) {
    constexpr int XOFF = AFTER_LOCAL ? 131072 : 98304;
    int tz = threadIdx.x; asm volatile("" : "+v"(tz));
    const int lane = tz & 63;
    const int w = __builtin_amdgcn_readfirstlane(tz >> 6) & 3;
    const Ids I = ids();
    const int panel = I.po, ct = I.co;
    const int l15 = lane & 15, g = lane >> 4;
    auto orow = [&](int T, int a) -> int { return out_row(panel, AFTER_LOCAL, T, a); };
    const int q4 = l15 >> 2, p4 = l15 & 3;
    const int krow = 4 * g + q4;
    const int sw = out_swz(krow);
    const lds_cptr sm = (lds_cptr)smem;
    lds_cptr bw[4], xo[4];
#pragma unroll
    for (int t = 0; t < 4; ++t) {
        bw[t] = sm + krow * 128 + ((((t >> 1) * 4 + (p4 >> 1) * 2 + (t & 1)) ^ sw) * 16) + (p4 & 1) * 8;
        xo[t] = sm + XOFF + (w * 32 + l15) * 128 + ((((t >> 1) * 4 + (g >> 1) * 2 + (t & 1)) ^ (l15 & 7)) * 16) + (g & 1) * 8;
    }
    unsigned so[2], sto, ao[2];
#pragma unroll
    for (int r2 = 0; r2 < 2; ++r2) {
        so[r2] = (unsigned)orow(2 * w + r2, l15 & 7) * 2048u + (unsigned)(ct * 128 + (l15 >> 3) * 64 + g * 16);
        ao[r2] = (unsigned)((panel * 8 + 2 * w + r2) * 32768 + lane * 16);
    }
    sto = (unsigned)(((panel * 16 + ct) * 128 + 2 * w * 16 + l15) * 8);
    asm volatile("" : "+v"(bw[0]), "+v"(bw[1]), "+v"(bw[2]), "+v"(bw[3]), "+v"(xo[0]), "+v"(xo[1]), "+v"(xo[2]), "+v"(xo[3]),
                 "+v"(so[0]), "+v"(so[1]), "+v"(sto), "+v"(ao[0]), "+v"(ao[1]));
    const __amdgpu_buffer_rsrc_t ra = __builtin_amdgcn_make_buffer_rsrc((void*)ofrag, 0, 4 << 20, 0x00020000);
    asm volatile("" :: "s"(ra));
    u64 land;
    STAMP(AFTER_LOCAL ? 3 : 10);
    signal_poll<true, false>(w, ws, flag_off(set, I.pq, l15, I.cq), flag_off(set, panel, ct, l15), 0xffffull, tag, land);
    BAR();
    STAMP(AFTER_LOCAL ? 4 : 11);
#define AWIN 6
    u32x4 a[AWIN][2];
    auto lda = [&](int j) {
        a[j % AWIN][0] = __builtin_amdgcn_raw_buffer_load_b128(ra, ao[0] + (j & 3) * 1024, (j >> 2) * 4096, 16);
        a[j % AWIN][1] = __builtin_amdgcn_raw_buffer_load_b128(ra, ao[1] + (j & 3) * 1024, (j >> 2) * 4096, 16);
    };
#pragma unroll
    for (int j = 0; j < AWIN; ++j) { lda(j); SBAR(); }
    auto boff = [&](int j) { return (j & 15) * 4096 - ((!AFTER_LOCAL && j >= 24) ? 32768 : 0); };
    s16x4 F[2][4][2];
#pragma unroll
    for (int h = 0; h < 2; ++h)
#pragma unroll
        for (int t = 0; t < 4; ++t) { F[h][t][0] = lds_tr(bw[t] + boff(h)); F[h][t][1] = lds_tr(bw[t] + boff(h) + 2048); }
    f32x4 acc[2][4];
#pragma unroll
    for (int r2 = 0; r2 < 2; ++r2)
#pragma unroll
        for (int c = 0; c < 4; ++c) {
            const u32x2 xr = *(const __attribute__((address_space(3))) u32x2*)(xo[c] + r2 * 2048);
            acc[r2][c] = f32x4{bf_lo(xr[0]), bf_hi(xr[0]), bf_lo(xr[1]), bf_hi(xr[1])};
        }
    WAIT_LGKM();
#pragma unroll
    for (int j = 0; j < 32; ++j) {
        s16x4 (&C)[4][2] = F[j & 1];
        const bf16x8 a0 = __builtin_bit_cast(bf16x8, a[j % AWIN][0]), a1 = __builtin_bit_cast(bf16x8, a[j % AWIN][1]);
        const int nb = boff(j + 2);
        if (j + 2 == 16 || (!AFTER_LOCAL && j + 2 == 24)) {
#pragma unroll
            for (int t = 0; t < 4; ++t) bw[t] += 65536;
            asm volatile("" : "+v"(bw[0]), "+v"(bw[1]), "+v"(bw[2]), "+v"(bw[3]));
        }
#pragma unroll
        for (int t = 0; t < 4; ++t) {
            const bf16x8 bb = cat8(C[t][0], C[t][1]);
            acc[0][t] = MFMA16(bb, a0, acc[0][t]);
            if (t > 0 && j < 30) C[t - 1][1] = lds_tr(bw[t - 1] + nb + 2048);
            SBAR();
            acc[1][t] = MFMA16(bb, a1, acc[1][t]);
            if (j < 30) C[t][0] = lds_tr(bw[t] + nb);
            if (t == 3 && j < 30) C[3][1] = lds_tr(bw[3] + nb + 2048);
            if (t == 3 && j + AWIN < 32) lda(j + AWIN);
            SBAR();
        }
    }
    asm volatile("" :: "v"(land));
    STAMP(AFTER_LOCAL ? 5 : 12);
    u32x4 y[2][2];
#pragma unroll
    for (int r2 = 0; r2 < 2; ++r2)
#pragma unroll
        for (int u = 0; u < 2; ++u) {
            const f32x4 c0 = acc[r2][2 * u], c1 = acc[r2][2 * u + 1];
            const auto sa = __builtin_amdgcn_permlane16_swap(pack_bf16(c0[0], c0[1]), pack_bf16(c1[0], c1[1]), false, false);
            const auto sb = __builtin_amdgcn_permlane16_swap(pack_bf16(c0[2], c0[3]), pack_bf16(c1[2], c1[3]), false, false);
            y[r2][u] = u32x4{sa[0], sb[0], sa[1], sb[1]};
            if (u) {
                u32x4 za, zb;
#pragma unroll
                for (int e = 0; e < 4; ++e) {
                    za[e] = __builtin_amdgcn_update_dpp(y[r2][0][e], y[r2][1][e], 0x128, 0xF, 0xC, false);
                    zb[e] = __builtin_amdgcn_update_dpp(y[r2][1][e], y[r2][0][e], 0x128, 0xF, 0x3, false);
                }
                st16(Xout, so[r2], za);
                st16(Xout, so[r2] + (AFTER_LOCAL ? 16 : 8) * 2048, zb);
                SBAR();
            }
        }
    const bf16x8 ones = __builtin_bit_cast(bf16x8, (u32x4{0x3f803f80u, 0x3f803f80u, 0x3f803f80u, 0x3f803f80u}));
    const f32x4 zero4 = {0, 0, 0, 0};
    f32x4 d1[2], d2[2];
#pragma unroll
    for (int r2 = 0; r2 < 2; ++r2) {
        const bf16x8 y0 = __builtin_bit_cast(bf16x8, y[r2][0]), y1 = __builtin_bit_cast(bf16x8, y[r2][1]);
        d2[r2] = MFMA16(y0, y0, zero4); d1[r2] = MFMA16(ones, y0, zero4);
        d2[r2] = MFMA16(y1, y1, d2[r2]); d1[r2] = MFMA16(ones, y1, d1[r2]);
    }
    const bool b0 = p4 & 1, b1 = p4 & 2;
    if (g == q4) {
#pragma unroll
        for (int r2 = 0; r2 < 2; ++r2) {
            float e0 = d2[r2][0], e1 = d2[r2][1], e2 = d2[r2][2], e3 = d2[r2][3];
            asm("" : "+v"(e0), "+v"(e1), "+v"(e2), "+v"(e3));
            const float lo = b0 ? e1 : e0, hi = b0 ? e3 : e2;
            const f32x2 sv = {d1[r2][0], b1 ? hi : lo};
            if (r2) st8o<128>(statp, sto, sv); else st8o<0>(statp, sto, sv);
        }
    }
}

#define ROWSUM(p0, p1, c) __builtin_amdgcn_smfmac_f32_16x16x64_bf16(sel, __builtin_bit_cast(bf16v_t, __builtin_shufflevector(p0, p1, 0, 1, 2, 3, 4, 5, 6, 7)), c, sidx, 0, 0)
constexpr int ATT_STAGE = 32768;
typedef __attribute__((ext_vector_type(8))) unsigned u32x8;
typedef __attribute__((ext_vector_type(32))) unsigned u32x32;

DI void attn_loader(char* smem, char* ws, int set, u64 tag, const __hip_bfloat16* __restrict__ kvf, const void* Wo, const void* Xin SP_PARAM) {
    int tid = threadIdx.x; asm volatile("" : "+v"(tid));
    const int lane = tid & 63;
    const int w = __builtin_amdgcn_readfirstlane(tid >> 6) & 3;
    const Ids I = ids();
    const int panel = I.pq, ct = I.cq;
    const unsigned lds0 = (unsigned)(uintptr_t)smem;
    const char* kvh = (const char*)kvf + (size_t)(ct * 2 + (w >> 1)) * 262144 + (w & 1) * 8192;
    const unsigned voff = (unsigned)(lane * 16);
    auto order = [&](int st) { return ((panel & 8) + st - ((st - 1 < (panel & 7)) ? 1 : 0)) & 15; };
    auto load = [&](int st) {
        const char* src = kvh + (size_t)order(st) * 16384;
        const unsigned dst = lds0 + w * 8192 + (st & 3) * ATT_STAGE;
#pragma unroll
        for (int u = 0; u < 2; ++u)
            asm volatile("s_mov_b32 m0, %0\n\ts_nop 0\n\tglobal_load_lds_dwordx4 %1, %2 sc1\n\tglobal_load_lds_dwordx4 %1, %2 offset:1024 sc1\n\t"
                         "global_load_lds_dwordx4 %1, %2 offset:2048 sc1\n\tglobal_load_lds_dwordx4 %1, %2 offset:3072 sc1"
                         :: "s"(dst + u * 4096), "v"(voff), "s"(src + u * 4096) : "memory");
    };
    const unsigned poff = flag_off(set, ct, panel, lane & 15);
    u64 land;
    __builtin_amdgcn_s_setprio(3);
    BAR();
    signal_poll<false, true>(w, ws, flag_off(set, ct, lane & 15, panel), poff, 1ull << order(1), tag, land);
    STAMP(9);
    load(1);
    asm volatile("global_load_dwordx2 %0, %1, %2 sc1\n\ts_waitcnt vmcnt(0)" : "+v"(land) : "v"(poff), "s"(ws + WS_FLAG) : "memory");
    STAMP(10);
    BAR();
    if (!__all(land == tag)) flag_poll(ws, poff, tag);
    load(2); load(3);
    __builtin_amdgcn_s_setprio(0);
    STAMP(11);
#pragma unroll 1
    for (int st = 1; st < 13; ++st) {
        VM_BAR(8);
        if (st == 6) wo_quarter(Wo, 3, lds0 + 131072, w, lane);
        load(st + 3);
    }
    VM_BAR(8); wo_quarter(Wo, 0, lds0, w, lane);
    VM_BAR(8); wo_quarter(Wo, 1, lds0 + 32768, w, lane);
    u32x4 res[4];
    res_load(res, Xin, false, w, lane);
    BAR();     wo_quarter(Wo, 2, lds0 + 65536, w, lane);
    STAMP(12);
    BAR();
    asm volatile("s_waitcnt vmcnt(0)" ::: "memory");
    res_write(res, smem, 98304, w, lane);
    WAIT_LGKM_BAR();
}

DI void attn_compute(char* smem, const u32x4 (&q4)[4], const __hip_bfloat16* __restrict__ kvf, __hip_bfloat16* __restrict__ ofrag SP_PARAM) {
    int tz = threadIdx.x; asm volatile("" : "+v"(tz));
    const int lane = tz & 63;
    const int w = __builtin_amdgcn_readfirstlane(tz >> 6) & 3;
    const int wm = w & 1, wn = w >> 1;
    const Ids I = ids();
    const int panel = I.pq, head = I.cq * 2 + wn;
    const int l15 = lane & 15;
    const unsigned se = ((l15 & 2) == 0 && (l15 & 4) == 0) ? 0x3f803f80u : 0u, so = ((l15 & 2) == 0 && (l15 & 4) != 0) ? 0x3f803f80u : 0u;
    const bf8_t sel = __builtin_bit_cast(bf8_t, u32x4{se, se, so, so});
    const int sidx = (l15 & 1) ? (int)0xEEEEEEEEu : 0x44444444;
    const lds_cptr sm = (lds_cptr)smem + wn * 16384 + lane * 16;
    const f32x16 z16 = {0, 0, 0, 0, 0, 0, 0, 0, 0, 0, 0, 0, 0, 0, 0, 0};
    const bf16x8 qA0 = __builtin_bit_cast(bf16x8, q4[0]), qA1 = __builtin_bit_cast(bf16x8, q4[1]);
    const bf16x8 qB0 = __builtin_bit_cast(bf16x8, q4[2]), qB1 = __builtin_bit_cast(bf16x8, q4[3]);
    f32x16 oA, oB, nmA, nmB, sA, sB;
    f32x4 lsA, lsB;
    u32x8 pB;
    bf16x8 v11;
    asm volatile(".irp r,2,4,6,8,10,12,14,16,18,20,22,24,26,28,30,32,106,108,110,112,134,136,138,140,170,172\n\tv_mov_b64 v[\\r:\\r+1], 0\n\t.endr"
                 : "={v[2:17]}"(oA), "={v[18:33]}"(oB), "={v[106:113]}"(pB), "={v[134:137]}"(lsA), "={v[138:141]}"(lsB), "={v[170:173]}"(v11));
    WAIT_LGKM_BAR();
    STAMP(8);
    bf16x8 k00 = lds_b128(sm), k10 = lds_b128(sm + 1024), k01 = lds_b128(sm + 4096), k11 = lds_b128(sm + 4096 + 1024);
    bf16x8 v00 = lds_b128(sm + 2048), v10 = lds_b128(sm + 3072), v01 = lds_b128(sm + 4096 + 2048);
    {
        sA = MFMA32(k00, qA0, z16); sB = MFMA32(k00, qB0, z16);
        sA = MFMA32(k10, qA1, sA); sB = MFMA32(k10, qB1, sB);
        float mA = sA[0], mB = sB[0];
#pragma unroll
        for (int r = 1; r < 16; ++r) { mA = fmaxf(mA, sA[r]); mB = fmaxf(mB, sB[r]); }
        mA = max_h(mA); mB = max_h(mB);
#pragma unroll
        for (int r = 0; r < 16; ++r) { nmA[r] = -mA; nmB[r] = -mB; sA[r] -= mA; }
    }
    const unsigned a0 = (unsigned)(uintptr_t)sm;
    asm volatile(
        ".macro ATT_RD dst, y\n\t"
        "ds_read_b128 v[\\dst:\\dst+3], v[175+(((\\y)&0x1ffff)>>16)] offset:(\\y)&0xffff\n\t"
        ".endm\n\t"
        ".macro ATT_E4 c\n\t"
        "v_exp_f32 v[\\c], v[\\c]\n\tv_exp_f32 v[\\c+1], v[\\c+1]\n\tv_exp_f32 v[\\c+2], v[\\c+2]\n\tv_exp_f32 v[\\c+3], v[\\c+3]\n\t"
        ".endm\n\t"
        ".macro ATT_C4 p, c\n\t"
        "v_cvt_pk_bf16_f32 v[\\p], v[\\c], v[\\c+1]\n\tv_cvt_pk_bf16_f32 v[\\p+1], v[\\c+2], v[\\c+3]\n\t"
        "v_cvt_pk_bf16_f32 v[\\p+2], v[\\c+4], v[\\c+5]\n\tv_cvt_pk_bf16_f32 v[\\p+3], v[\\c+6], v[\\c+7]\n\t"
        ".endm\n\t"
        ".macro ATT_STEP cur, nxt, ka, kb, q, nm, oc, od, lsd, pc, pd, vd1, vc0, r0, y0, r1, y1, bar, rd\n\t"
        "v_mfma_f32_32x32x16_bf16 v[\\nxt:\\nxt+15], v[\\ka:\\ka+3], v[\\q:\\q+3], v[\\nm:\\nm+15]\n\t"
        "ATT_E4 \\cur\n\t"
        "v_mfma_f32_32x32x16_bf16 v[\\nxt:\\nxt+15], v[\\kb:\\kb+3], v[\\q+4:\\q+7], v[\\nxt:\\nxt+15]\n\t"
        "ATT_E4 \\cur+4\n\t"
        "v_mfma_f32_32x32x16_bf16 v[\\od:\\od+15], v[\\vd1:\\vd1+3], v[\\pd+4:\\pd+7], v[\\od:\\od+15]\n\t"
        "s_waitcnt lgkmcnt(0)\n\t"
        "ATT_C4 \\pc, \\cur\n\t"
        "v_exp_f32 v[\\cur+8], v[\\cur+8]\n\tv_exp_f32 v[\\cur+9], v[\\cur+9]\n\t"
        "v_mfma_f32_32x32x16_bf16 v[\\oc:\\oc+15], v[\\vc0:\\vc0+3], v[\\pc:\\pc+3], v[\\oc:\\oc+15]\n\t"
        ".if \\bar\n\ts_barrier\n\t.endif\n\t"
        "ATT_E4 \\cur+10\n\t"
        "v_smfmac_f32_16x16x64_bf16 v[\\lsd:\\lsd+3], v[130:133], v[\\pd:\\pd+7], v174\n\t"
        ".if \\rd & 4\n\tATT_RD 142, 32768\n\tATT_RD 150, 33792\n\tATT_RD 158, 34816\n\t.endif\n\t"
        ".if \\rd & 1\n\tATT_RD \\r0, \\y0\n\t.endif\n\t"
        ".if \\rd & 2\n\tATT_RD \\r1, \\y1\n\t.endif\n\t"
        "v_exp_f32 v[\\cur+14], v[\\cur+14]\n\tv_exp_f32 v[\\cur+15], v[\\cur+15]\n\t"
        "ATT_C4 \\pc+4, \\cur+8\n\t"
        ".if \\rd & 4\n\ts_waitcnt lgkmcnt(3)\n\t.endif\n\t"
        ".endm\n\t"
        ".macro ATT_TILE p, y1, y2, bar, ra=3, rb=3\n\t"
        "ATT_STEP 66, 82, 142+\\p, 150+\\p, 122, 50, 2, 18, 138, 98, 106, 170-\\p, 158+\\p, 170-\\p, \\y1+3072, 142+\\p, \\y2, \\bar, \\ra\n\t"
        "ATT_STEP 82, 66, 146-\\p, 154-\\p, 114, 34, 18, 2, 134, 106, 98, 166+\\p, 158+\\p, 158+\\p, \\y2+2048, 150+\\p, \\y2+1024, 0, \\rb\n\t"
        ".endm\n\t"
        ".macro ATT_STAGE b\n\t"
        "ATT_TILE 0, \\b+4096, \\b+8192, 0\n\t"
        "ATT_TILE 4, \\b+8192, \\b+12288, 0\n\t"
        "ATT_TILE 0, \\b+12288, \\b+32768, 1\n\t"
        "ATT_TILE 4, \\b+32768, \\b+36864, 0\n\t"
        ".endm\n\t"
        "v_add_u32 v176, 0x10000, v175\n\t"
        "s_mov_b32 s96, 4\n\t"
        "s_waitcnt vmcnt(0) lgkmcnt(0)\n\t"
        "s_barrier\n\t"
        "ATT_TILE 0, 4096, 8192, 0\n\tATT_TILE 4, 8192, 12288, 0\n\tATT_TILE 0, 12288, 0, 0, 1, 0\n\tATT_TILE 4, 32768, 36864, 1, 7, 3\n\t"
        "s_branch att_entry_%=\n\t"
        ".p2align 6\n"
        "att_loop_%=:\n\t"
        "ATT_STAGE 0\n"
        "att_entry_%=:\n\t"
        "ATT_STAGE 32768\n\tATT_STAGE 65536\n\t"
        "s_sub_u32 s96, s96, 1\n\t"
        "ATT_STAGE 98304\n\t"
        "s_cmp_lg_u32 s96, 0\n\t"
        "s_cbranch_scc1 att_loop_%=\n\t"
        "s_nop 1\n\t"
        "v_mfma_f32_32x32x16_bf16 v[18:33], v[170:173], v[110:113], v[18:33]\n\t"
        "v_smfmac_f32_16x16x64_bf16 v[138:141], v[130:133], v[106:113], v174\n\t"
        "s_waitcnt lgkmcnt(0)\n\ts_nop 10\n\t"
        ".purgem ATT_RD\n\t.purgem ATT_E4\n\t.purgem ATT_C4\n\t.purgem ATT_STEP\n\t.purgem ATT_TILE\n\t.purgem ATT_STAGE"
        : "+{v[2:17]}"(oA), "+{v[18:33]}"(oB), "+{v[66:81]}"(sA), "+{v[106:113]}"(pB), "+{v[134:137]}"(lsA), "+{v[138:141]}"(lsB),
          "+{v[142:145]}"(k00), "+{v[146:149]}"(k01), "+{v[150:153]}"(k10), "+{v[154:157]}"(k11),
          "+{v[158:161]}"(v00), "+{v[162:165]}"(v01), "+{v[166:169]}"(v10), "+{v[170:173]}"(v11)
        : "{v[34:49]}"(nmA), "{v[50:65]}"(nmB), "{v[114:117]}"(qA0), "{v[118:121]}"(qA1), "{v[122:125]}"(qB0), "{v[126:129]}"(qB1),
          "{v[130:133]}"(sel), "{v174}"(sidx), "{v175}"(a0)
        : "memory", "scc", "s96", "v176", "v82", "v83", "v84", "v85", "v86", "v87", "v88", "v89", "v90", "v91", "v92", "v93", "v94", "v95",
          "v96", "v97", "v98", "v99", "v100", "v101", "v102", "v103", "v104", "v105");
    STAMP(9);
    float lA = lsA[0] + lsA[1], lB = lsB[0] + lsB[1];
    const unsigned dst = (unsigned)((((panel * 8 + wm * 4 + ((lane & 31) >> 4)) * 32 + head) * 64 + (lane & 15) + (lane >> 5) * 16) * 16);
    auto store = [&] {
#pragma unroll
    for (int u = 0; u < 2; ++u) {
        const f32x16 e = (u ? oB : oA) * __builtin_amdgcn_rcpf(u ? lB : lA);
        st16o<0>((char*)ofrag + u * 65536, dst, u32x4{pack_bf16(e[0], e[1]), pack_bf16(e[2], e[3]), pack_bf16(e[8], e[9]), pack_bf16(e[10], e[11])});
        st16o<512>((char*)ofrag + u * 65536, dst, u32x4{pack_bf16(e[4], e[5]), pack_bf16(e[6], e[7]), pack_bf16(e[12], e[13]), pack_bf16(e[14], e[15])});
    }
    };
    store();
    {
        const f32x16 os = oA + oB;
        const float osum = (((os[0] + os[1]) + (os[2] + os[3])) + ((os[4] + os[5]) + (os[6] + os[7]))) +
                           (((os[8] + os[9]) + (os[10] + os[11])) + ((os[12] + os[13]) + (os[14] + os[15])));
        const bool bad = !(lA < 1.0e24f) || !(lB < 1.0e24f) || !(fabsf(osum) < 3.0e38f);
        if (__any(bad)) {
            const __amdgpu_buffer_rsrc_t rk = __builtin_amdgcn_make_buffer_rsrc((char*)kvf + (size_t)head * 262144, 0, 262144, 0x00020000);
            oA = z16; oB = z16; lA = 0.f; lB = 0.f;
            float mA = -INFINITY, mB = -INFINITY;
#pragma unroll 1
            for (int t = 0; t < 64; ++t) {
                bf16x8 f[4];
#pragma unroll
                for (int i = 0; i < 4; ++i) f[i] = __builtin_bit_cast(bf16x8, __builtin_amdgcn_raw_buffer_load_b128(rk, lane * 16 + i * 1024, t * 4096, 16));
                auto upd = [&](bf16x8 q0, bf16x8 q1, f32x16& o, float& l, float& m) {
                    f32x16 s = MFMA32(f[0], q0, z16); s = MFMA32(f[1], q1, s);
                    float tm = s[0];
#pragma unroll
                    for (int r = 1; r < 16; ++r) tm = fmaxf(tm, s[r]);
                    tm = max_h(tm);
                    const float mn = fmaxf(m, tm);
                    const float alpha = __builtin_amdgcn_exp2f(m - mn);
                    m = mn;
#pragma unroll
                    for (int r = 0; r < 16; ++r) { s[r] = __builtin_amdgcn_exp2f(s[r] - mn); o[r] *= alpha; }
                    const u32x4 p0 = {pack_bf16(s[0], s[1]), pack_bf16(s[2], s[3]), pack_bf16(s[4], s[5]), pack_bf16(s[6], s[7])};
                    const u32x4 p1 = {pack_bf16(s[8], s[9]), pack_bf16(s[10], s[11]), pack_bf16(s[12], s[13]), pack_bf16(s[14], s[15])};
                    f32x4 t4 = {0, 0, 0, 0};
                    o = MFMA32(f[2], __builtin_bit_cast(bf16x8, p0), o);
                    o = MFMA32(f[3], __builtin_bit_cast(bf16x8, p1), o);
                    t4 = ROWSUM(p0, p1, t4);
                    l = l * alpha + (t4[0] + t4[1]);
                };
                upd(qA0, qA1, oA, lA, mA);
                upd(qB0, qB1, oB, lB, mB);
            }
            store();
        }
    }
}

DI void ln_row(const u32x4 (&v)[2], __hip_bfloat16* __restrict__ Y, unsigned off) {
    float s1 = 0.f, s2 = 0.f;
#pragma unroll
    for (int e = 0; e < 8; ++e) s1 = dot2bf(v[e >> 2][e & 3], 0x3f803f80u, s1);
    const float mean = wave_sum(s1) * (1.f / 1024.f);
#pragma unroll
    for (int e = 0; e < 8; ++e) {
        const float d0 = bf_lo(v[e >> 2][e & 3]) - mean, d1 = bf_hi(v[e >> 2][e & 3]) - mean;
        s2 += d0 * d0 + d1 * d1;
    }
    const float mb = round_bf16(mean);
    const float rden = __builtin_amdgcn_rcpf(round_bf16(__builtin_amdgcn_sqrtf(round_bf16(round_bf16(wave_sum(s2) * (1.f / 1024.f)) + 1e-5f))));
#pragma unroll
    for (int hh = 0; hh < 2; ++hh) {
        u32x4 y;
#pragma unroll
        for (int e = 0; e < 4; ++e) {
            const unsigned d = pack_bf16(bf_lo(v[hh][e]) - mb, bf_hi(v[hh][e]) - mb);
            y[e] = pack_bf16(bf_lo(d) * rden, bf_hi(d) * rden);
        }
        if (hh) st16o<1024>(Y, off, y); else st16o<0>(Y, off, y);
    }
}

extern "C" __global__ __launch_bounds__(512, 2) void mega_kernel(const __hip_bfloat16* x, const __hip_bfloat16* wqkvA,
                                                            const __hip_bfloat16* woA, const __hip_bfloat16* wqkvB,
                                                            const __hip_bfloat16* woB, __hip_bfloat16* out,
                                                            char* ws) {
    char* const smem = smem_base;
    typedef __hip_bfloat16 bf;
    const int w8 = __builtin_amdgcn_readfirstlane(threadIdx.x >> 6);
    if (w8 == 7 && (blockIdx.x >> 3) < 10) code_touch(blockIdx.x >> 3, threadIdx.x & 63);
    asm volatile("" :: "s"(x), "s"(wqkvA), "s"(woA), "s"(wqkvB), "s"(woB), "s"(out), "s"(ws));
    STAMP_DECL; STAMPR(14); STAMP(0);
    u64 tag;
    {
        const int lane = threadIdx.x & 63;
        const Ids I = ids();
        const unsigned off = (unsigned)((I.po * 128 + I.co * 8 + w8) * 2048 + lane * 16);
        u32x4 v[2];
        asm volatile("global_load_dwordx4 %0, %2, %3\n\tglobal_load_dwordx4 %1, %2, %3 offset:1024" : "=&v"(v[0]), "=&v"(v[1]) : "v"(off), "s"(x) : "memory");
        asm volatile("s_load_dwordx2 %0, %1, 0x0" : "=s"(tag) : "s"(ws + WS_EPOCH) : "memory");
        if (w8 == 7) {
            const int l = min(lane, 12), k = l % 5;
            const unsigned o = (l >= 10 || k == 3) ? flag_off(l, I.cq, I.pq, 0) : (k == 0 || k == 2) ? flag_off(l, I.pq, I.cq, 0) : flag_off(l, I.po, I.co, 0);
            touch_at(ws + WS_FLAG, o);
        } else if (w8 >= 4 && w8 - 4 < NTOUCH) touch(wqkvA, (w8 - 4) * 256 + blockIdx.x, lane);
        if (w8 == 7 || (w8 >= 4 && w8 - 4 < NTOUCH)) asm volatile("s_waitcnt vmcnt(1)" ::: "memory"); else asm volatile("s_waitcnt vmcnt(0)" ::: "memory");
        asm volatile("" : "+v"(v[0]), "+v"(v[1]));
        ln_row(v, (bf*)(ws + WS_Y0), off);
        asm volatile("s_waitcnt lgkmcnt(0)" : "+s"(tag) :: "memory");
        tag += 1;
    }
    if (w8 >= 4) {
        int w = w8 & 3;
#pragma unroll 1
        for (int it = 0; it < 2; ++it) {
            int lane = threadIdx.x; asm volatile("" : "+v"(lane)); lane &= 63;
            asm volatile("" : "+s"(x), "+s"(wqkvA), "+s"(woA), "+s"(wqkvB), "+s"(woB), "+s"(ws), "+s"(w));
            const unsigned lds0 = (unsigned)(uintptr_t)smem;
            u32x4 ca[8], cb[8];
            qkv_loader<true>(smem, ws, it * 5, tag, (const bf*)(ws + (it ? WS_X2 : WS_Y0)), wqkvA, (const float*)(ws + WS_ST + 262144),
                             ws + WS_CSP, it == 0, woA, [&] {
                if (w < 3) {
                    colsum_load(ca, wqkvA, w, lane); colsum_load(cb, wqkvB, w, lane);
                    asm volatile("s_waitcnt vmcnt(16)" ::: "memory");
                } else {
                    touch(woA, blockIdx.x, lane);
                    asm volatile("s_waitcnt vmcnt(1)" ::: "memory");
                }
            } SP_ARG);
            STAMP(3);
#pragma unroll
            for (int q = 0; q < 4; ++q) wo_quarter(woA, q, lds0 + q * 32768, w, lane);
            u32x4 res[4];
            res_load(res, it ? (const void*)(ws + WS_X2) : x, true, w, lane);
            const bool cs = (it == 0 && w < 3);
            STAMP(4);
            BAR();
            asm volatile("s_waitcnt vmcnt(0)" ::: "memory");
            res_write(res, smem, 131072, w, lane);
            WAIT_LGKM_BAR();
            if (cs) {
                asm volatile("" : "+v"(cb[0]), "+v"(cb[1]), "+v"(cb[2]), "+v"(cb[3]), "+v"(cb[4]), "+v"(cb[5]), "+v"(cb[6]), "+v"(cb[7]));
                asm volatile("" : "+v"(ca[0]), "+v"(ca[1]), "+v"(ca[2]), "+v"(ca[3]), "+v"(ca[4]), "+v"(ca[5]), "+v"(ca[6]), "+v"(ca[7]));
                colsum_store(ca, ws + WS_CSP, w, lane);
                colsum_store(cb, ws + WS_CSP + 196608, w, lane);
                asm volatile("s_waitcnt vmcnt(0)" ::: "memory");
                flag_store(ws, 10 + w, ids().cq, ids().pq, tag, lane);
            }
            STAMP(5);
            qkv_loader<false>(smem, ws, it * 5 + 2, tag, (const bf*)(ws + (it ? WS_X3 : WS_X1)), wqkvB, (const float*)(ws + WS_ST + it * 524288), ws + WS_CSP + 196608,
                              false, nullptr, [] {} SP_ARG);
            STAMP(8);
            attn_loader(smem, ws, it * 5 + 3, tag, (const bf*)(ws + WS_KVF + it * 8 * MBy), woB, ws + (it ? WS_X3 : WS_X1) SP_ARG);
            STAMP(13); STAMPR(15); STAMP_FLUSH(it);
        }
        if (blockIdx.x == 0 && threadIdx.x == 256) __hip_atomic_store((g_u64ptr)(ws + WS_EPOCH), tag, __ATOMIC_RELAXED, __HIP_MEMORY_SCOPE_AGENT);
    } else {
        __builtin_amdgcn_s_setprio(2);
#pragma unroll 1
        for (int it = 0; it < 2; ++it) {
            u32x4 q4[4];
            asm volatile("" : "+s"(ws), "+s"(wqkvA), "+s"(wqkvB));
            qkv_compute<true>(smem, wqkvA, q4, nullptr, (bf*)(ws + WS_OF + it * 8 * MBy), it == 0 SP_ARG);
            STAMP(2);
            out_compute<true>(smem, ws, it * 5 + 1, tag, (const bf*)(ws + WS_OF + it * 8 * MBy),
                              (bf*)(ws + (it ? WS_X3 : WS_X1)), (float*)(ws + WS_ST + it * 524288) SP_ARG);
            STAMP(6);
            qkv_compute<false>(smem, wqkvB, q4, (bf*)(ws + WS_KVF + it * 8 * MBy), nullptr, false SP_ARG);
            attn_compute(smem, q4, (const bf*)(ws + WS_KVF + it * 8 * MBy), (bf*)(ws + WS_OF + (it * 8 + 4) * MBy) SP_ARG);
            out_compute<false>(smem, ws, it * 5 + 4, tag, (const bf*)(ws + WS_OF + (it * 8 + 4) * MBy),
                               it ? out : (bf*)(ws + WS_X2), (float*)(ws + WS_ST + 262144 + it * 524288) SP_ARG);
            STAMP(13); STAMPR(15); STAMP_FLUSH(it);
        }
    }
}

extern "C" __global__ void zz_code_end_kernel() {}

extern "C" void kernel_launch(void* const* d_in, const int* in_sizes, int n_in, void* d_out, int out_size, void* d_ws,
                              size_t ws_size, hipStream_t stream) {
    typedef __hip_bfloat16 bf;
    (void)hipFuncSetAttribute((const void*)mega_kernel, hipFuncAttributeMaxDynamicSharedMemorySize, CH_LDS);
    mega_kernel<<<256, 512, CH_LDS, stream>>>((const bf*)d_in[0], (const bf*)d_in[1], (const bf*)d_in[2], (const bf*)d_in[4], (const bf*)d_in[5],
                                              (bf*)d_out, (char*)d_ws);
}
```

```cpp
#include <hip/hip_runtime.h>
#include <hip/hip_bf16.h>
#include <stdint.h>

typedef __attribute__((ext_vector_type(8))) short bf16x8;
typedef __attribute__((ext_vector_type(4))) short s16x4;
typedef __attribute__((ext_vector_type(4))) float f32x4;
typedef __attribute__((ext_vector_type(2))) float f32x2;
typedef __attribute__((ext_vector_type(16))) float f32x16;
typedef __attribute__((ext_vector_type(4))) unsigned u32x4;
typedef __attribute__((ext_vector_type(2))) unsigned u32x2;
typedef __attribute__((ext_vector_type(2))) __bf16 bf16x2_t;
typedef __attribute__((ext_vector_type(8))) __bf16 bf8_t;
typedef __attribute__((ext_vector_type(16))) __bf16 bf16v_t;
typedef __attribute__((address_space(3))) const char* lds_cptr;
typedef __attribute__((address_space(3))) char* lds_ptr;

#define DI __device__ __forceinline__
#define MFMA16(a, b, c) __builtin_amdgcn_mfma_f32_16x16x32_bf16((a), (b), (c), 0, 0, 0)
#define MFMA32(a, b, c) __builtin_amdgcn_mfma_f32_32x32x16_bf16((a), (b), (c), 0, 0, 0)
#define SBAR() __builtin_amdgcn_sched_barrier(0)
#define STAMP(i)
#define STAMPR(i)
#define STAMP_DECL
#define STAMP_FLUSH(it)
#define SP_PARAM
#define SP_ARG
typedef unsigned long long u64;
#define NTOUCH 2

constexpr float QSCALE = 0.17677669529663687f * 1.4426950408889634f;
constexpr float ATT_SCALE = 0.17677669529663687f;
constexpr float LOG2E = 1.4426950408889634f;
constexpr float NEG_MASK = -1e9f / ATT_SCALE;

DI unsigned pack_bf16(float lo, float hi) {
    f32x2 v = {lo, hi};
    bf16x2_t r = __builtin_convertvector(v, bf16x2_t);
    return __builtin_bit_cast(unsigned, r);
}
DI float bf_lo(unsigned u) { return __uint_as_float(u << 16); }
DI float bf_hi(unsigned u) { return __uint_as_float(u & 0xffff0000u); }
DI float round_bf16(float x) { return bf_lo(pack_bf16(x, 0.f)); }
DI void ln_stats(float sum, float sumsq, float& mean_out, float& rstd_out) {
    float mean = sum * (1.f / 1024.f);
    asm("" : "+v"(mean));
    const float var = fmaxf(sumsq * (1.f / 1024.f) - mean * mean, 0.f);
    mean_out = round_bf16(mean);
    rstd_out = __builtin_amdgcn_rcpf(round_bf16(__builtin_amdgcn_sqrtf(round_bf16(round_bf16(var) + 1e-5f))));
}

DI void glds16(const void* sbase, unsigned voff, unsigned ldsaddr) {
    asm volatile("s_mov_b32 m0, %0\n\ts_nop 0\n\tglobal_load_lds_dwordx4 %1, %2"
                 :: "s"(ldsaddr), "v"(voff), "s"(sbase) : "memory");
}
DI void glds16c(const void* sbase, unsigned voff, unsigned ldsaddr) {
    asm volatile("s_mov_b32 m0, %0\n\ts_nop 0\n\tglobal_load_lds_dwordx4 %1, %2 sc1"
                 :: "s"(ldsaddr), "v"(voff), "s"(sbase) : "memory");
}
DI void st16(void* sbase, unsigned voff, u32x4 v) {
    asm volatile("global_store_dwordx4 %0, %1, %2 sc1\n\ts_nop 1" :: "v"(voff), "v"(v), "s"(sbase) : "memory");
}
template <int OFF> DI void st16o(void* sbase, unsigned voff, u32x4 v) {
    asm volatile("global_store_dwordx4 %0, %1, %2 offset:%3 sc1\n\ts_nop 1" :: "v"(voff), "v"(v), "s"(sbase), "n"(OFF) : "memory");
}
DI bf16x8 lds_b128(lds_cptr p) { return *(const __attribute__((address_space(3))) bf16x8*)p; }
DI s16x4 lds_tr(lds_cptr p) {
    return __builtin_amdgcn_ds_read_tr16_b64_v4i16((__attribute__((address_space(3))) s16x4*)p);
}
DI bf16x8 cat8(s16x4 a, s16x4 b) { return __builtin_shufflevector(a, b, 0, 1, 2, 3, 4, 5, 6, 7); }
DI float dot2bf(unsigned a, unsigned b, float c) {
    return __builtin_amdgcn_fdot2_f32_bf16(__builtin_bit_cast(bf16x2_t, a), __builtin_bit_cast(bf16x2_t, b), c, false);
}
DI float sum_g(float v) {
    auto a = __builtin_amdgcn_permlane16_swap(__float_as_uint(v), __float_as_uint(v), false, false);
    v = __uint_as_float(a[0]) + __uint_as_float(a[1]);
    auto b = __builtin_amdgcn_permlane32_swap(__float_as_uint(v), __float_as_uint(v), false, false);
    return __uint_as_float(b[0]) + __uint_as_float(b[1]);
}
DI float wave_sum(float v) {
    v += __builtin_bit_cast(float, __builtin_amdgcn_update_dpp(0, __builtin_bit_cast(int, v), 0xB1, 0xF, 0xF, true));
    v += __builtin_bit_cast(float, __builtin_amdgcn_update_dpp(0, __builtin_bit_cast(int, v), 0x4E, 0xF, 0xF, true));
    v += __builtin_bit_cast(float, __builtin_amdgcn_update_dpp(0, __builtin_bit_cast(int, v), 0x141, 0xF, 0xF, true));
    v += __builtin_bit_cast(float, __builtin_amdgcn_update_dpp(0, __builtin_bit_cast(int, v), 0x140, 0xF, 0xF, true));
    return sum_g(v);
}
DI float amax3(float a, float b, float c) { float r; asm("v_max3_f32 %0, %1, %2, %3" : "=v"(r) : "v"(a), "v"(b), "v"(c)); return r; }
DI float addf(float a, float b) { float r; asm("v_add_f32 %0, %1, %2" : "=v"(r) : "v"(a), "v"(b)); return r; }
DI float max2f(float a, float b) { return __builtin_amdgcn_fmed3f(a, b, INFINITY); }
DI float max_h(float v) {
    auto b = __builtin_amdgcn_permlane32_swap(__float_as_uint(v), __float_as_uint(v), false, false);
    return max2f(__uint_as_float(b[0]), __uint_as_float(b[1]));
}

template <int OFF> DI void st8o(void* sbase, unsigned voff, f32x2 v) {
    asm volatile("global_store_dwordx2 %0, %1, %2 offset:%3 sc1\n\ts_nop 1" :: "v"(voff), "v"(v), "s"(sbase), "n"(OFF) : "memory");
}
constexpr unsigned OFF_DUMP = 159744;
extern __shared__ __attribute__((aligned(16))) char smem_base[];
DI void touch_at(const void* p, unsigned off) {
    asm volatile("s_mov_b32 m0, %0\n\ts_nop 0\n\tglobal_load_lds_dword %1, %2"
                 :: "s"((unsigned)(uintptr_t)smem_base + OFF_DUMP), "v"(off), "s"(p) : "memory");
}
DI void touch(const void* p, int slice, int lane) { touch_at(p, (unsigned)(slice * 64 + lane) * 128u); }
DI void code_touch(int slice, int lane) {
    unsigned long long c0, c1;
    asm volatile("s_getpc_b64 s[96:97]\n\ts_add_u32 s96, s96, mega_kernel@rel32@lo+4\n\ts_addc_u32 s97, s97, mega_kernel@rel32@hi+12\n\ts_mov_b64 %0, s[96:97]\n\t"
                 "s_getpc_b64 s[96:97]\n\ts_add_u32 s96, s96, zz_code_end_kernel@rel32@lo+4\n\ts_addc_u32 s97, s97, zz_code_end_kernel@rel32@hi+12\n\ts_mov_b64 %1, s[96:97]"
                 : "=s"(c0), "=s"(c1) :: "s96", "s97", "scc");
    touch_at((const void*)c0, min((unsigned)(slice * 64 + lane) * 128u, (unsigned)(c1 - c0)));
}

#define BAR() asm volatile("s_barrier" ::: "memory")
#define WAIT_LGKM() do { SBAR(); __builtin_amdgcn_s_waitcnt(0xC07F); SBAR(); } while (0)
#define WAIT_LGKM_BAR() do { SBAR(); __builtin_amdgcn_s_waitcnt(0xC07F); asm volatile("s_barrier" ::: "memory"); SBAR(); } while (0)
#define VM_BAR(n) asm volatile("s_waitcnt vmcnt(" #n ")\n\ts_barrier" ::: "memory")
constexpr int CH_LDS = 163840;
constexpr int OFF_SYNC = CH_LDS - 64;
typedef __attribute__((address_space(3))) unsigned* lds_uptr;
typedef __attribute__((address_space(1))) u64* g_u64ptr;

constexpr size_t MBy = 1 << 20;
constexpr size_t WS_X3 = 0, WS_X2 = 4 * MBy, WS_X1 = 8 * MBy, WS_OF = 12 * MBy  , WS_KVF = 36 * MBy  ,
                 WS_ST = 52 * MBy  , WS_CSP = 54 * MBy  , WS_FLAG = 55 * MBy  ,
                 WS_EPOCH = WS_FLAG + 15 * 65536, WS_Y0 = 56 * MBy;
DI unsigned flag_off(int set, int group, int consumer, int producer) { return (unsigned)(set * 65536 + group * 4096 + consumer * 256 + producer * 8); }
DI void flag_store(char* ws, int set, int group, int producer, u64 tag, int lane) {
    asm volatile("" : "+s"(tag));
    if (lane < 16) __hip_atomic_store((g_u64ptr)(ws + WS_FLAG + flag_off(set, group, lane, producer)), tag, __ATOMIC_RELAXED, __HIP_MEMORY_SCOPE_AGENT);
}
DI void flag_poll(const char* ws, unsigned off, u64 tag) {
    asm volatile("" : "+s"(tag));
#pragma unroll 1
    for (int spin = 0; spin < (1 << 16); ++spin) {
        const u64 v = __hip_atomic_load((g_u64ptr)(ws + WS_FLAG + off), __ATOMIC_RELAXED, __HIP_MEMORY_SCOPE_AGENT);
        if (__all(v == tag)) break;
        __builtin_amdgcn_s_sleep(1);
    }
    asm volatile("" ::: "memory");
}
#define POLL_INS "global_atomic_or_x2 %[land], %[po], %[zero], %[base] sc0 sc1"
template <bool DRAIN, bool ALLPOLL>
DI void signal_poll(int follower, const char* ws, unsigned soff, unsigned poff, u64 pmask, u64 tag, u64& land) {
    u64 tagv, zero;
    asm volatile("s_not_b64 s[96:97], %[ts]\n\tv_mov_b64 %[tag], %[ts]\n\tv_mov_b64 %[zero], 0\n\tv_mov_b64 %[land], s[96:97]\n\t"
                 ".if %[dr]\n\ts_waitcnt vmcnt(0)\n\t.endif\n\ts_barrier\n\t"
                 "s_cmp_lg_u32 %[fl], 0\n\ts_cbranch_scc1 sig_fol_%=\n\t"
                 "s_mov_b64 exec, 0xffff\n\t"
                 "global_store_dwordx2 %[so], %[tag], %[base] sc1\n"
                 ".if %[ap]\nsig_fol_%=:\n.endif\n\t"
                 "s_mov_b64 exec, %[pm]\n\t"
                 "s_movk_i32 s96, 0x7fff\n"
                 "sig_loop_%=:\n\t"
                 POLL_INS "\n\t"
                 "s_sleep 1\n\t"
                 "s_waitcnt vmcnt(3)\n\t"
                 "v_cmp_ne_u64 vcc, %[land], %[tag]\n\t"
                 "s_sub_u32 s96, s96, 1\n\t"
                 "s_cbranch_scc1 sig_out_%=\n\t"
                 "s_cbranch_vccnz sig_loop_%=\n"
                 "sig_out_%=:\n\t"
                 "s_mov_b64 exec, -1\n"
                 ".if !%[ap]\nsig_fol_%=:\n.endif"
                 : [land] "=&v"(land), [tag] "=&v"(tagv), [zero] "=&v"(zero)
                 : [fl] "s"(follower), [so] "v"(soff), [po] "v"(poff), [ts] "s"(tag), [base] "s"(ws + WS_FLAG), [pm] "s"(pmask), [ap] "n"(ALLPOLL ? 1 : 0), [dr] "n"(DRAIN ? 1 : 0)
                 : "memory", "scc", "vcc", "s96", "s97");
}
struct Ids { int pq, cq, po, co; };
DI Ids ids() {
    int bid = blockIdx.x; asm volatile("" : "+s"(bid));
    const int xcd = bid & 7, idx = bid >> 3;
    int pq, cq, po, co;
#define IDS_BODY pq = (xcd & 1) * 8 + (idx & 7); cq = (xcd >> 1) * 4 + (idx >> 3); po = (xcd & 3) * 4 + (idx & 3); co = (xcd >> 2) * 8 + (idx >> 2);
    IDS_BODY
    return Ids{pq, cq, po, co};
}

DI void slow_warm(unsigned dump, const void* W, int w, int lane) {
    int bid = blockIdx.x; asm volatile("" : "+s"(bid));
    const int xcd = bid & 7, idx = bid >> 3;
    const int sc = (3 - (int)((uintptr_t)W >> 7)) & 7;
    if ((sc >> 2) != ((xcd >> 1) & 1)) return;
    const char* src = (const char*)W + (idx + 32 * w) * 49152 + (sc + (xcd >> 2) * 8) * 128;
#pragma unroll
    for (int G = 0; G < 3; ++G) glds16(src + G * 2048, (unsigned)((lane >> 3) * 6144 + (lane & 7) * 16), dump);
}

template <int V> struct IC { static constexpr int value = V; };
template <bool LOCAL> struct QC {
    static constexpr int AROWS = LOCAL ? 144 : 128;
    static constexpr int ABYTES = AROWS * 128;
    static constexpr int STAGE = ABYTES + 24576;
    static constexpr int OFF_STATP = 129024;
    static constexpr int OFF_MEAN = OFF_STATP + 18432;
    static constexpr int OFF_RSTD = OFF_MEAN + 576;
    static constexpr int OFF_CS = OFF_RSTD + 576;
};
constexpr int OFF_EX = 131072;

DI void colsum_load(u32x4 (&c)[8], const void* W, int G, int lane) {
    const Ids I = ids();
    const unsigned off = (unsigned)(I.pq * 64 + (lane >> 3)) * 6144u + (unsigned)(G * 2048 + I.cq * 128 + (lane & 7) * 16);
#pragma unroll
    for (int i = 0; i < 8; ++i)
        asm volatile("global_load_dwordx4 %0, %1, %2" : "=&v"(c[i]) : "v"(off), "s"((const char*)W + i * 49152) : "memory");
}
DI void colsum_store(const u32x4 (&c)[8], char* dst, int G, int lane) {
    const Ids I = ids();
    const int m = lane & 15;
    const unsigned one = (m & 1) ? 0xbf800000u : 0xbf80u;
    const u32x4 sl = {(m >> 1) == 0 ? one : 0u, (m >> 1) == 1 ? one : 0u, (m >> 1) == 2 ? one : 0u, (m >> 1) == 3 ? one : 0u};
    f32x4 d = {0, 0, 0, 0};
#pragma unroll
    for (int i = 0; i < 8; ++i) d = MFMA16(__builtin_bit_cast(bf16x8, sl), __builtin_bit_cast(bf16x8, c[i]), d);
#pragma unroll
    for (int r = 0; r < 4; ++r)
        d[r] += __int_as_float(__builtin_amdgcn_update_dpp(0, __float_as_int(d[r]), 0x128, 0xF, 0xF, true));
    if (lane < 32 && (lane & 8) == 0)
        st16(dst, (unsigned)((I.cq * 16 + I.pq) * 768 + G * 256 + (lane & 7) * 32 + (lane >> 4) * 16), __builtin_bit_cast(u32x4, d));
}

template <bool LOCAL>
struct WFirst {
    unsigned v[2];
    DI WFirst(int w, int ct, int lane) {
#pragma unroll
        for (int i = 0; i < 2; ++i) {
            const int k = 8 * i + (lane >> 3), pos = lane & 7;
            v[i] = (unsigned)k * 6144u + (unsigned)(w * 1024 + ct * 64 + ((pos >> 1) ^ (((k >> 1) & 1) | (i << 1))) * 16 + (pos & 1) * 8) * 2u;
        }
        asm volatile("" : "+v"(v[0]), "+v"(v[1]));
    }
    DI void issue(const void* W, unsigned lds0, int w) const {
#pragma unroll
        for (int i = 0; i < 16; ++i)
            glds16((const char*)W + (size_t)(i >> 1) * (16 * 6144), v[i & 1], lds0 + (i >> 3) * QC<LOCAL>::STAGE + QC<LOCAL>::ABYTES + (w * 8 + (i & 7)) * 1024);
    }
};

template <bool LOCAL, class F>
DI void qkv_loader(char* smem, char* ws, int set, u64 tag, const __hip_bfloat16* __restrict__ X, const __hip_bfloat16* __restrict__ W,
                   const float* __restrict__ statp, const char* __restrict__ csp, bool ident, const void* nextW, F tail SP_PARAM) {
    typedef QC<LOCAL> C;
    constexpr int NT = 16;
    int tid = threadIdx.x; asm volatile("" : "+v"(tid));
    const int lane = tid & 63;
    const int w = __builtin_amdgcn_readfirstlane(tid >> 6) & 3;
    const int idx = blockIdx.x >> 3;
    const Ids I = ids();
    const int panel = I.pq, ct = I.cq;
    const int R0 = panel * 128;
    const unsigned lds0 = (unsigned)(uintptr_t)smem;
    const lds_cptr sm = (lds_cptr)smem;
    auto grow = [&](int i) -> int {
        if (!LOCAL) return R0 + i;
        if (i < 128) { const int t = (i >> 4) & 3, a = i & 15; return R0 + (i & 64) + 2 * (16 * (t & 1) + a) + (t >> 1); }
        const int a = i & 15; const int r = R0 - 16 + 2 * (a & 7) + (a >> 3);
        return r < 0 ? 0 : r;
    };
    unsigned voffA[5], voffB[6];
#pragma unroll
    for (int i = 0; i < 6; ++i) {
        const int j = w + 4 * i, G = j >> 3, k = 8 * (j & 7) + (lane >> 3);
        const int pos = lane & 7, f = ((k >> 1) & 1) | (((k >> 3) & 1) << 1);
        const int seg = (pos >> 1) ^ f;
        voffB[i] = (unsigned)k * 6144u + (unsigned)(G * 1024 + ct * 64 + seg * 16 + (pos & 1) * 8) * 2u;
    }
    auto issueW = [&](int t, int buf) {
        const char* wb = (const char*)W + (size_t)t * (64 * 6144);
        const unsigned sb = lds0 + buf * C::STAGE;
#pragma unroll
        for (int i = 0; i < 6; ++i) glds16(wb, voffB[i], sb + C::ABYTES + (w + 4 * i) * 1024);
    };
    asm volatile("" : "+v"(voffB[0]), "+v"(voffB[1]), "+v"(voffB[2]), "+v"(voffB[3]), "+v"(voffB[4]), "+v"(voffB[5]));
    const WFirst<LOCAL> first(w, ct, lane);
    const int lt = min(tid - 256, C::AROWS - 1);
    const int gr = LOCAL ? grow(lt) - R0 : ((lt & 64) + (lt & 1) * 32 + ((lt & 63) >> 1));
    lds_cptr sp = sm + C::OFF_STATP + ((lt < 128) ? gr * 8 : (16384 + (panel ? (gr + 16) : 0) * 8));
    lds_ptr mp = (lds_ptr)smem + C::OFF_MEAN + lt * 4, cp = (lds_ptr)smem + C::OFF_CS + lane * 12;
    asm volatile("" : "+v"(sp), "+v"(mp), "+v"(cp));
#pragma unroll
    for (int i = 0; i < 5; ++i) {
        const int j = (i < 4) ? (w + 4 * i) : (16 + (w & 1));
        const int row = 8 * j + (lane >> 3);
        const int c = (lane & 7) ^ ((row >> 1) & 7);
        voffA[i] = (unsigned)grow(row < C::AROWS ? row : 0) * 2048u + c * 16;
        asm volatile("" : "+v"(voffA[i]));
    }
    auto issueA = [&](int t, int buf) {
        const char* xa = (const char*)X + t * 128;
        const unsigned sb = lds0 + buf * C::STAGE;
        if (LOCAL && w < 2) glds16c(xa, voffA[4], sb + (16 + w) * 1024);
#pragma unroll
        for (int i = 0; i < 4; ++i) glds16c(xa, voffA[i], sb + (w + 4 * i) * 1024);
    };
    u64 land;
    if (w == 3) __builtin_amdgcn_s_setprio(3);
    signal_poll<LOCAL, false>(w - 3, ws, flag_off(set, I.po, lane & 15, I.co),
                              LOCAL ? flag_off(set, (lane & 16) && panel ? panel - 1 : panel, ct, lane & 15)
                                    : (lane < 16 ? flag_off(set, panel, ct, lane) : flag_off(9 + (lane >> 4), ct, panel, lane & 15)),
                              LOCAL ? 0xffffffffull : ~0ull, tag, land);
    if (w < 3) first.issue(W, lds0, w);
    BAR();
    STAMP(LOCAL ? 1 : 6);
    issueA(0, 0);
    issueA(1, 1);
    VM_BAR(4);
    asm volatile("" :: "v"(land));
    __builtin_amdgcn_s_setprio(0);
    STAMP(LOCAL ? 2 : 7);
    issueW(2, 2);
    auto issueS = [&](int i) {
        if (i == 8 && idx < 3) code_touch(idx * 4 + w, lane);
        if (LOCAL && i == 9)
            glds16(nextW, (unsigned)((idx + 32 * w) * 8 + (lane >> 3)) * 2048u +
                   (unsigned)(((I.co & 8) + ((3 - (int)((uintptr_t)nextW >> 7)) & 7)) * 128 + (lane & 7) * 16), lds0 + OFF_DUMP);
        if (ident) return;
        if (i < 4) glds16c(statp, (unsigned)((panel * 16 + w + 4 * i) * 1024 + lane * 16), lds0 + C::OFF_STATP + (w + 4 * i) * 1024);
        else if (LOCAL && i == 4 && w < 2) {
            const unsigned o = panel ? (unsigned)(((panel - 1) * 16 + w * 8 + (lane >> 3)) * 1024 + 896 + (lane & 7) * 16)
                                     : (unsigned)((w * 8 + (lane >> 3)) * 1024 + (lane & 7) * 16);
            glds16c(statp, o, lds0 + C::OFF_STATP + (16 + w) * 1024);
        }
    };
    issueA(2, 2);
    VM_BAR(10);
    typedef __attribute__((ext_vector_type(3))) float f32x3;
    f32x3 cs[16];
    if (w == 3 && !ident) {
#pragma unroll
        for (int i = 0; i < 16; i += 4)
            asm volatile("global_load_dwordx3 %0, %4, %5 sc1\n\tglobal_load_dwordx3 %1, %4, %5 offset:768 sc1\n\t"
                         "global_load_dwordx3 %2, %4, %5 offset:1536 sc1\n\tglobal_load_dwordx3 %3, %4, %5 offset:2304 sc1"
                         : "=&v"(cs[i]), "=&v"(cs[i + 1]), "=&v"(cs[i + 2]), "=&v"(cs[i + 3])
                         : "v"(lane * 12), "s"(csp + (ct * 16 + i) * 768) : "memory");
    }
    issueA(3, 0); issueW(3, 0);
    int buf = 1;
#pragma unroll 1
    for (int t = 1; t < NT - 3; ++t) {
        VM_BAR(10);
        issueA(t + 3, buf); issueW(t + 3, buf);
        if (t >= 2) issueS(t - 2);
        buf = (buf == 2) ? 0 : buf + 1;
    }
    VM_BAR(10);
    float sa = 0.f, sb = 0.f;
    auto fold = [&](int n) {
#pragma unroll
        for (int i = 0; i < n; ++i)
#pragma unroll
            for (int e = 0; e < 3; ++e) cs[i][e] = addf(cs[i][e], cs[i + n][e]);
    };
    auto rows = [&](auto STR) {
        f32x2 v[16];
#pragma unroll
        for (int i = 0; i < 16; ++i) v[i] = *(const __attribute__((address_space(3))) f32x2*)(sp + i * STR.value);
        sa = v[0][0]; sb = v[0][1];
#pragma unroll
        for (int i = 1; i < 16; ++i) { sa = addf(sa, v[i][0]); sb = addf(sb, v[i][1]); }
    };
    if (!ident) {
        if (w == 3) {
#pragma unroll
            for (int i = 0; i < 16; ++i) asm volatile("" : "+v"(cs[i]));
            fold(8);
#pragma unroll
            for (int i = 0; i < 8; ++i) asm volatile("" : "+v"(cs[i]));
        } else if (w < 2) rows(IC<1024>{});
        else if (LOCAL) rows(IC<128>{});
    }
    asm volatile("" : "+v"(sa), "+v"(sb));
    if (ident) tail(); else asm volatile("s_waitcnt vmcnt(0)" ::: "memory");
    BAR();
    if (!ident) {
        typedef __attribute__((address_space(3))) float* lds_f;
        if (w == 3) {
            fold(4); fold(2); fold(1);
#pragma unroll
            for (int e = 0; e < 3; ++e) ((lds_f)cp)[e] = cs[0][e];
        } else if (LOCAL || w < 2) {
            float mean, rstd;
            ln_stats(sa, sb, mean, rstd);
            *(lds_f)mp = mean; *(lds_f)(mp + (C::OFF_RSTD - C::OFF_MEAN)) = rstd;
        }
    }
    WAIT_LGKM_BAR();
}

template <bool LOCAL>
DI void qkv_compute(char* smem, const void* W, u32x4 (&qo4)[4], __hip_bfloat16* __restrict__ kvf, __hip_bfloat16* __restrict__ ofrag, bool ident SP_PARAM) {
    typedef QC<LOCAL> C;
    constexpr int NT = 16, ABYTES = C::ABYTES, STAGE = C::STAGE;
    int tz = threadIdx.x; asm volatile("" : "+v"(tz));
    const int lane = tz & 63;
    const int w = __builtin_amdgcn_readfirstlane(tz >> 6) & 3;
    const int wm = w & 1, wn = w >> 1;
    const Ids I = ids();
    const int panel = I.pq, ct = I.cq;
    const int l15 = lane & 15, g = lane >> 4;
    const lds_cptr sm = (lds_cptr)smem;
    lds_ptr smw = (lds_ptr)smem;

    VM_BAR(0);
    f32x4 acc[3][4][2];
    f32x4 acch[2];
    f32x2 z2 = {0, 0}; asm volatile("" : "+v"(z2));
    const f32x4 z4 = {z2[0], z2[1], z2[0], z2[1]};
#pragma unroll
    for (int a = 0; a < 3; ++a)
#pragma unroll
        for (int t = 0; t < 4; ++t) { acc[a][t][0] = z4; acc[a][t][1] = z4; }
    acch[0] = z4; acch[1] = z4;

    struct Frags { bf16x8 a[4]; bf16x8 h; s16x4 b[3][2][2]; };
    auto rd = [&](auto WM, Frags& F, lds_cptr pa, lds_cptr pb0, lds_cptr pb1, int n) {
        switch (n) {
            case 0: F.b[0][0][0] = lds_tr(pb0); break;
            case 1: F.b[0][0][1] = lds_tr(pb0 + 512); break;
            case 2: F.a[0] = lds_b128(pa); break;
            case 3: F.b[0][1][0] = lds_tr(pb1); break;
            case 4: F.b[0][1][1] = lds_tr(pb1 + 512); break;
            case 5: F.a[1] = lds_b128(pa + 2048); break;
            case 6: F.a[2] = lds_b128(pa + 4096); break;
            case 7: F.a[3] = lds_b128(pa + 6144); break;
            case 8: F.b[1][0][0] = lds_tr(pb0 + 8192); break;
            case 9: F.b[1][0][1] = lds_tr(pb0 + 8192 + 512); break;
            case 10: F.b[1][1][0] = lds_tr(pb1 + 8192); break;
            case 11: F.b[1][1][1] = lds_tr(pb1 + 8192 + 512); break;
            case 12: F.b[2][0][0] = lds_tr(pb0 + 16384); break;
            case 13: F.b[2][0][1] = lds_tr(pb0 + 16384 + 512); break;
            case 14: F.b[2][1][0] = lds_tr(pb1 + 16384); break;
            case 15: F.b[2][1][1] = lds_tr(pb1 + 16384 + 512); break;
            case 16: if (LOCAL) F.h = lds_b128(pa + 16384 - WM.value * 8192); break;
            default: break;
        }
    };
    auto half = [&](auto WM, const Frags& C, Frags& N, bool doread, lds_cptr pa, lds_cptr pb0, lds_cptr pb1, auto tail) {
#pragma unroll
        for (int G = 0; G < 3; ++G) {
            const bf16x8 b0 = cat8(C.b[G][0][0], C.b[G][0][1]), b1 = cat8(C.b[G][1][0], C.b[G][1][1]);
#pragma unroll
            for (int t = 0; t < 4; ++t) {
                const int n = G * 8 + t * 2;
                if (G < 2) acc[G][t][0] = MFMA16(b0, C.a[t], acc[G][t][0]); else acc[G][t][0] = MFMA16(C.a[t], b0, acc[G][t][0]);
                if (n == 0) SBAR();
                if (doread) rd(WM, N, pa, pb0, pb1, n);
                tail(n);
                SBAR();
                if (G < 2) acc[G][t][1] = MFMA16(b1, C.a[t], acc[G][t][1]); else acc[G][t][1] = MFMA16(C.a[t], b1, acc[G][t][1]);
                if (doread) rd(WM, N, pa, pb0, pb1, n + 1);
                tail(n + 1);
                SBAR();
            }
        }
        if (LOCAL) {
            if (WM.value == 0) { acch[0] = MFMA16(cat8(C.b[1][0][0], C.b[1][0][1]), C.h, acch[0]); acch[1] = MFMA16(cat8(C.b[1][1][0], C.b[1][1][1]), C.h, acch[1]); }
            else         { acch[0] = MFMA16(C.h, cat8(C.b[2][0][0], C.b[2][0][1]), acch[0]); acch[1] = MFMA16(C.h, cat8(C.b[2][1][0], C.b[2][1][1]), acch[1]); }
            SBAR();
        }
    };

    Frags F0, F1;
    f32x4 cq[2], ck[2], mu4[5], rs4[5]; float cv[2], mu[5], rs[5];
    u32x4 qfr[4], kfr[4];
    u32x4 vfr[4], cc;
    f32x4 mwin, mfirst;
    typedef __attribute__((address_space(3))) u32x4* lds_u4;
    volatile __attribute__((address_space(3))) unsigned* fl = (volatile __attribute__((address_space(3))) unsigned*)(smw + OFF_SYNC + 16);
    constexpr int OFF_CST = C::OFF_CS + 768;
    const bool negpos = (wm == 0) && (panel == 0);
    if (LOCAL) {
        const unsigned keepv = negpos ? __float_as_uint(NEG_MASK) : 0u;
        u32x4 mw, mf;
        bool cur[4];
#pragma unroll
        for (int r = 0; r < 4; ++r) {
            const int d = l15 - (4 * g + r);
            const unsigned out = (unsigned)(((d << 28) >> 31));
            mw[r] = out & 0xff800000u;
            mf[r] = (out & 0xff800000u) | (~out & keepv);
            cur[r] = d >= 0;
        }
        const unsigned e0 = (unsigned)(OFF_EX + wn * 6144);
        lds_u4 cst = (lds_u4)(smw + OFF_CST) + lane;
        fl[w] = 0u;
        cst[0] = mw; cst[64 + wm * 64] = mf;
        cst[192 + w * 64] = u32x4{(cur[0] ? 0xffffu : 0u) | (cur[1] ? 0xffff0000u : 0u), (cur[2] ? 0xffffu : 0u) | (cur[3] ? 0xffff0000u : 0u),
                                  e0 + (unsigned)(wm ? lane : 256 + (lane ^ 8)) * 16u, e0 + (unsigned)(wm ? 128 + lane : 320 + (lane ^ 32)) * 16u};
    }
    typedef const __attribute__((address_space(3))) f32x4* lds_f4; typedef const __attribute__((address_space(3))) float* lds_f1;
    auto mainloop = [&](auto WM) {
#pragma unroll
        for (int a = 0; a < 3; ++a)
            asm volatile("" : "+v"(acc[a][0][0]), "+v"(acc[a][0][1]), "+v"(acc[a][1][0]), "+v"(acc[a][1][1]), "+v"(acc[a][2][0]), "+v"(acc[a][2][1]),
                         "+v"(acc[a][3][0]), "+v"(acc[a][3][1]));
        int ty = threadIdx.x; asm volatile("" : "+v"(ty));
        const int l15 = ty & 15, g = (ty >> 4) & 3;
        const int swA = (l15 >> 1) & 7;
        const int q4 = l15 >> 2, p4 = l15 & 3, fk = ((q4 >> 1) & 1) | ((g & 1) << 1);
        lds_cptr pA0 = sm + wm * 8192 + l15 * 128 + ((0 + g) ^ swA) * 16, pA1 = sm + wm * 8192 + l15 * 128 + ((4 + g) ^ swA) * 16;
        lds_cptr pB0 = sm + ABYTES + (8 * g + q4) * 128 + (((2 * wn + 0) ^ fk) * 32) + p4 * 8;
        lds_cptr pB1 = sm + ABYTES + (8 * g + q4) * 128 + (((2 * wn + 1) ^ fk) * 32) + p4 * 8;
        asm volatile("" : "+v"(acch[0]), "+v"(acch[1]), "+v"(pA0), "+v"(pA1), "+v"(pB0), "+v"(pB1));
        slow_warm((unsigned)(uintptr_t)smem + OFF_DUMP, W, w, ty & 63);
        BAR();
        STAMP(LOCAL ? 1 : 7);
        BAR();
#pragma unroll
        for (int n = 0; n < 17; ++n) rd(WM, F0, pA0, pB0, pB1, n);
        WAIT_LGKM();
        int off = 0;
        lds_cptr a = pA1, b0 = pB0, b1 = pB1;
#pragma unroll 1
        for (int t = 0; t < NT - 1; ++t) {
            half(WM, F0, F1, true, a, b0 + 4096, b1 + 4096, [&](int n) {
                if (n == 17) { off = (off == 2 * STAGE) ? 0 : off + STAGE; asm volatile("" : "+s"(off)); }
                if (n == 19) { b0 = pB0 + off; asm volatile("" : "+v"(b0)); }
                if (n == 20) { a = pA0 + off; asm volatile("" : "+v"(a)); }
                if (n == 21) { b1 = pB1 + off; asm volatile("" : "+v"(b1)); }
            });
            WAIT_LGKM_BAR();
            half(WM, F1, F0, true, a, b0, b1, [&](int n) {
                if (n == 19) { a = pA1 + off; asm volatile("" : "+v"(a)); }
                if (n == 22) __builtin_amdgcn_s_waitcnt(0xC07F);
            });
        }
        lds_cptr s1 = sm, s4 = sm, c1 = sm, c4 = sm;
        half(WM, F0, F1, true, a, b0 + 4096, b1 + 4096, [&](int n) {
            if (n == 16) { ty = threadIdx.x; asm volatile("" : "+v"(ty)); }
            if (n == 17) { s1 = sm + C::OFF_MEAN + wm * 256 + (ty & 15) * 4; asm volatile("" : "+v"(s1)); }
            if (n == 18) { s4 = sm + C::OFF_MEAN + wm * 256 + (ty & 48); asm volatile("" : "+v"(s4)); }
            if (n == 19) { c1 = sm + C::OFF_CS + wn * 128 + (ty & 15) * 4; asm volatile("" : "+v"(c1)); }
            if (n == 20) { c4 = sm + C::OFF_CS + wn * 128 + (ty & 48); asm volatile("" : "+v"(c4)); }
        });
        WAIT_LGKM_BAR();
        half(WM, F1, F0, false, sm, sm, sm, [&](int n) {
            constexpr int RS = C::OFF_RSTD - C::OFF_MEAN, H = 512 - WM.value * 256;
            if (n < 2) { if (!LOCAL) cq[n] = *(lds_f4)(c4 + n * 64); ck[n] = *(lds_f4)(c4 + 256 + n * 64); }
            else if (n < 6) { mu[n - 2] = *(lds_f1)(s1 + (n - 2) * 64); rs[n - 2] = *(lds_f1)(s1 + RS + (n - 2) * 64); }
            else if (n < 10) { if (!LOCAL || (n & 1)) { mu4[n - 6] = *(lds_f4)(s4 + (n - 6) * 64); rs4[n - 6] = *(lds_f4)(s4 + RS + (n - 6) * 64); } }
            else if (n == 10) { cv[0] = *(lds_f1)(c1 + 512); cv[1] = *(lds_f1)(c1 + 512 + 64); }
            else if (LOCAL && n == 11) {
                if (WM.value == 0) { mu[4] = *(lds_f1)(s1 + H); rs[4] = *(lds_f1)(s1 + RS + H); }
                else { mu4[4] = *(lds_f4)(s4 + H); rs4[4] = *(lds_f4)(s4 + RS + H); }
            }
        });
        if (LOCAL) {
            auto fix = [&](int a, int t) {
#pragma unroll
                for (int f = 0; f < 2; ++f)
                    acc[a][t][f] = (a == 2) ? rs4[t] * (acc[2][t][f] + mu4[t] * cv[f]) : rs[t] * (acc[a][t][f] + mu[t] * (a ? ck[f] : cq[f]));
            };
            auto pk = [&](const f32x4 (&v)[2]) { return u32x4{pack_bf16(v[0][0], v[0][1]), pack_bf16(v[0][2], v[0][3]), pack_bf16(v[1][0], v[1][1]), pack_bf16(v[1][2], v[1][3])}; };
            cq[0] = *(lds_f4)c4; cq[1] = *(lds_f4)(c4 + 64);
            SBAR();
            if (!ident) {
                fix(1, 1); fix(1, 3); fix(2, 1); fix(2, 3);
#pragma unroll
                for (int f = 0; f < 2; ++f)
                    acch[f] = (WM.value == 0) ? rs[4] * (acch[f] + mu[4] * ck[f]) : rs4[4] * (acch[f] + mu4[4] * cv[f]);
            }
            kfr[1] = pk(acc[1][1]); kfr[3] = pk(acc[1][3]); vfr[1] = pk(acc[2][1]); vfr[3] = pk(acc[2][3]);
            lds_u4 ex = (lds_u4)(smw + OFF_EX + wn * 6144) + (ty & 63);
            if (WM.value == 0) { ex[0] = kfr[1]; ex[64] = kfr[3]; ex[128] = vfr[1]; ex[192] = vfr[3]; }
            ex[256 + WM.value * 64] = pk(acch);
#pragma unroll
            for (int t = 0; t < 4; t += 2) { mu4[t] = *(lds_f4)(s4 + t * 64); rs4[t] = *(lds_f4)(s4 + (C::OFF_RSTD - C::OFF_MEAN) + t * 64); }
            SBAR();
            if (!ident) { fix(0, 0); fix(0, 1); fix(0, 2); fix(0, 3); }
#pragma unroll
            for (int t = 0; t < 4; ++t) qfr[t] = pk(acc[0][t]);
            WAIT_LGKM();
            fl[w] = 1u;
            lds_u4 cst = (lds_u4)(smw + OFF_CST) + (ty & 63);
            mwin = __builtin_bit_cast(f32x4, cst[0]); mfirst = __builtin_bit_cast(f32x4, cst[64 + WM.value * 64]); cc = cst[192 + w * 64];
            SBAR();
            if (!ident) { fix(1, 0); fix(1, 2); fix(2, 0); fix(2, 2); }
            kfr[0] = pk(acc[1][0]); kfr[2] = pk(acc[1][2]); vfr[0] = pk(acc[2][0]); vfr[2] = pk(acc[2][2]);
        }
    };
    const int head = ct * 2 + wn;
    unsigned qo = 0, ko = 0, vo = 0;
    if (LOCAL) {
        qo = (unsigned)((((panel * 8 + wm * 4) * 32 + head) * 64 + lane) * 16);
        asm volatile("" : "+v"(qo));
    } else {
        ko = (unsigned)((wm * 8 + (g & 1)) * 1024 + ((l15 & 7) + ((l15 & 8) << 1) + 32 * (g >> 1)) * 16);
        vo = (unsigned)((wm * 8 + 2 + (g >> 1)) * 1024 + (l15 + 32 * (g & 1)) * 16);
        asm volatile("" : "+v"(ko), "+v"(vo));
    }
    if (LOCAL && wm) mainloop(IC<1>{}); else mainloop(IC<0>{});

    if (!LOCAL) {
        auto pk = [&](f32x4 a, f32x4 b) { return u32x4{pack_bf16(a[0], a[1]), pack_bf16(a[2], a[3]), pack_bf16(b[0], b[1]), pack_bf16(b[2], b[3])}; };
        char* kvp = (char*)kvf + (size_t)(head * 16 + panel) * 16384;
        lds_ptr own = smw + wn * 16384;
        SBAR();
#pragma unroll
        for (int t = 0; t < 4; ++t) {
            const u32x4 kk = pk(rs[t] * (acc[1][t][0] + mu[t] * ck[0]), rs[t] * (acc[1][t][1] + mu[t] * ck[1]));
            if (t & 1) st16o<128>(kvp + (t >> 1) * 4096, ko, kk); else st16o<0>(kvp + (t >> 1) * 4096, ko, kk);
            *(lds_u4)(own + ko + (t >> 1) * 4096 + (t & 1) * 128) = kk;
            SBAR();
            const int u = t >> 1, f = t & 1;
            const u32x4 vv = pk(rs4[2 * u] * (acc[2][2 * u][f] + mu4[2 * u] * cv[f]), rs4[2 * u + 1] * (acc[2][2 * u + 1][f] + mu4[2 * u + 1] * cv[f]));
            if (f) st16o<256>(kvp + u * 4096, vo, vv); else st16o<0>(kvp + u * 4096, vo, vv);
            *(lds_u4)(own + vo + u * 4096 + f * 256) = vv;
            SBAR();
        }
#pragma unroll
        for (int u = 0; u < 2; ++u) {
            const float r0 = rs[2 * u] * QSCALE, r1 = rs[2 * u + 1] * QSCALE;
            const u32x4 qa = pk(r0 * (acc[0][2 * u][0] + mu[2 * u] * cq[0]), r0 * (acc[0][2 * u][1] + mu[2 * u] * cq[1]));
            const u32x4 qb = pk(r1 * (acc[0][2 * u + 1][0] + mu[2 * u + 1] * cq[0]), r1 * (acc[0][2 * u + 1][1] + mu[2 * u + 1] * cq[1]));
#pragma unroll
            for (int e = 0; e < 4; ++e) {
                const auto x = __builtin_amdgcn_permlane16_swap(qa[e], qb[e], false, false);
                qo4[2 * u][e] = x[0]; qo4[2 * u + 1][e] = x[1];
            }
        }
        return;
    }
    {
        const f32x4 zero4 = {0, 0, 0, 0};
        const unsigned cm01 = cc[0], cm23 = cc[1];
#pragma unroll 1
        for (int spin = 0; spin < (1 << 22); ++spin)
            if (__builtin_amdgcn_readfirstlane(fl[w ^ 1]) != 0u) break;
        asm volatile("" ::: "memory");
        u32x4 kp[2], vp[2];
        kp[0] = *(lds_u4)(smw + cc[2]); kp[1] = *(lds_u4)(smw + (cc[2] ^ (wm ? 1024u : 128u)));
        vp[0] = *(lds_u4)(smw + cc[3]); vp[1] = *(lds_u4)(smw + (cc[3] ^ (wm ? 1024u : 512u)));
        if (negpos) { kp[0] = u32x4{0, 0, 0, 0}; kp[1] = kp[0]; }
        f32x4 sp[4], sc[4];
#pragma unroll
        for (int i = 0; i < 4; ++i) {
            const int t = (i == 0) ? 1 : (i == 1) ? 3 : (i == 2) ? 0 : 2;
            const bf16x8 qb = __builtin_bit_cast(bf16x8, qfr[t]);
            sc[t] = MFMA16(__builtin_bit_cast(bf16x8, kfr[t]), qb, mwin);
            sp[t] = MFMA16(__builtin_bit_cast(bf16x8, (t & 1) ? kfr[t - 1] : kp[t >> 1]), qb, (t & 1) ? mwin : mfirst);
        }
        SBAR();
        float x[4][4], m[4], inv[4], c2[4];
        unsigned a[4][2];
#pragma unroll
        for (int t = 0; t < 4; ++t)
#pragma unroll
            for (int h = 0; h < 2; ++h) {
                const unsigned uc = pack_bf16(sc[t][2 * h], sc[t][2 * h + 1]), up = pack_bf16(sp[t][2 * h], sp[t][2 * h + 1]);
                const unsigned cm = h ? cm23 : cm01;
                a[t][h] = (uc & cm) | (up & ~cm);
            }
        SBAR();
#pragma unroll
        for (int t = 0; t < 4; ++t)
#pragma unroll
            for (int h = 0; h < 2; ++h) { x[t][2 * h] = bf_lo(a[t][h]) * ATT_SCALE; x[t][2 * h + 1] = bf_hi(a[t][h]) * ATT_SCALE; }
        SBAR();
#pragma unroll
        for (int t = 0; t < 4; ++t)
#pragma unroll
            for (int h = 0; h < 2; ++h) a[t][h] = pack_bf16(x[t][2 * h], x[t][2 * h + 1]);
        SBAR();
#pragma unroll
        for (int t = 0; t < 4; ++t)
#pragma unroll
            for (int h = 0; h < 2; ++h) { x[t][2 * h] = bf_lo(a[t][h]); x[t][2 * h + 1] = bf_hi(a[t][h]); }
        SBAR();
#pragma unroll
        for (int t = 0; t < 4; ++t) m[t] = amax3(x[t][0], x[t][1], x[t][2]);
#pragma unroll
        for (int t = 0; t < 4; ++t) m[t] = amax3(m[t], x[t][3], x[t][3]);
        SBAR();
        asm volatile("s_nop 1");
        c2[0] = m[0]; c2[1] = m[1]; c2[2] = m[2]; c2[3] = m[3];
        asm volatile("" : "+v"(c2[0]), "+v"(c2[1]), "+v"(c2[2]), "+v"(c2[3]));
#pragma unroll
        for (int t = 0; t < 4; ++t) {
            const auto u = __builtin_amdgcn_permlane16_swap(__float_as_uint(m[t]), __float_as_uint(c2[t]), false, false);
            m[t] = amax3(__uint_as_float(u[0]), __uint_as_float(u[1]), __uint_as_float(u[1]));
        }
        SBAR();
        asm volatile("s_nop 1");
        c2[0] = m[0]; c2[1] = m[1]; c2[2] = m[2]; c2[3] = m[3];
        asm volatile("" : "+v"(c2[0]), "+v"(c2[1]), "+v"(c2[2]), "+v"(c2[3]));
#pragma unroll
        for (int t = 0; t < 4; ++t) {
            const auto u = __builtin_amdgcn_permlane32_swap(__float_as_uint(m[t]), __float_as_uint(c2[t]), false, false);
            m[t] = amax3(__uint_as_float(u[0]), __uint_as_float(u[1]), __uint_as_float(u[1]));
        }
        SBAR();
#pragma unroll
        for (int t = 0; t < 4; ++t)
#pragma unroll
            for (int r = 0; r < 4; ++r) x[t][r] = (x[t][r] - m[t]) * LOG2E;
        SBAR();
#pragma unroll
        for (int t = 0; t < 4; ++t)
#pragma unroll
            for (int r = 0; r < 4; ++r) x[t][r] = __builtin_amdgcn_exp2f(x[t][r]);
        SBAR();
#pragma unroll
        for (int t = 0; t < 4; ++t) inv[t] = (x[t][0] + x[t][1]) + (x[t][2] + x[t][3]);
        SBAR();
        c2[0] = inv[0]; c2[1] = inv[1]; c2[2] = inv[2]; c2[3] = inv[3];
        asm volatile("" : "+v"(c2[0]), "+v"(c2[1]), "+v"(c2[2]), "+v"(c2[3]));
#pragma unroll
        for (int t = 0; t < 4; ++t) {
            const auto u = __builtin_amdgcn_permlane16_swap(__float_as_uint(inv[t]), __float_as_uint(c2[t]), false, false);
            inv[t] = __uint_as_float(u[0]) + __uint_as_float(u[1]);
        }
        SBAR();
        c2[0] = inv[0]; c2[1] = inv[1]; c2[2] = inv[2]; c2[3] = inv[3];
        asm volatile("" : "+v"(c2[0]), "+v"(c2[1]), "+v"(c2[2]), "+v"(c2[3]));
#pragma unroll
        for (int t = 0; t < 4; ++t) {
            const auto u = __builtin_amdgcn_permlane32_swap(__float_as_uint(inv[t]), __float_as_uint(c2[t]), false, false);
            inv[t] = __builtin_amdgcn_rcpf(__uint_as_float(u[0]) + __uint_as_float(u[1]));
        }
        SBAR();
#pragma unroll
        for (int t = 0; t < 4; ++t)
#pragma unroll
            for (int h = 0; h < 2; ++h) a[t][h] = pack_bf16(x[t][2 * h] * inv[t], x[t][2 * h + 1] * inv[t]);
        SBAR();
        f32x4 o0[4], o1[4];
#pragma unroll
        for (int t = 0; t < 4; ++t) {
            const bf16x8 pb = __builtin_bit_cast(bf16x8, (u32x4{a[t][0] & ~cm01, a[t][1] & ~cm23, a[t][0] & cm01, a[t][1] & cm23}));
            const u32x4 vprev = (t & 1) ? vfr[t - 1] : vp[t >> 1];
            o0[t] = MFMA16(__builtin_bit_cast(bf16x8, (u32x4{vprev[0], vprev[1], vfr[t][0], vfr[t][1]})), pb, zero4);
            o1[t] = MFMA16(__builtin_bit_cast(bf16x8, (u32x4{vprev[2], vprev[3], vfr[t][2], vfr[t][3]})), pb, zero4);
        }
        SBAR();
#pragma unroll
        for (int t = 0; t < 4; ++t)
            st16(ofrag + t * 16384, qo, u32x4{pack_bf16(o0[t][0], o0[t][1]), pack_bf16(o0[t][2], o0[t][3]),
                                                 pack_bf16(o1[t][0], o1[t][1]), pack_bf16(o1[t][2], o1[t][3])});
    }
}

DI int out_row(int panel, bool localrows, int T, int a) {
    return localrows ? (panel * 128 + (T >> 2) * 64 + 2 * (16 * (T & 1) + a) + ((T >> 1) & 1)) : (panel * 128 + T * 16 + a);
}
DI int out_swz(int k) { return ((k >> 1) & 1) | (k & 4); }
DI void wo_quarter(const void* Wo, int q, unsigned ldsdst, int w, int lane) {
    const int kl = lane >> 3, co = ids().co;
    const unsigned voff = (unsigned)(w * 8 + kl) * 2048u + (unsigned)(co * 128 + ((lane & 7) ^ out_swz(kl)) * 16);
    const char* src = (const char*)Wo + (size_t)q * 524288;
#pragma unroll
    for (int i = 0; i < 8; ++i) glds16(src + i * 65536, voff, ldsdst + (w + 4 * i) * 1024);
}
DI void res_load(u32x4 (&r)[4], const void* Xin, bool localrows, int w, int lane) {
    const Ids I = ids();
#pragma unroll
    for (int i = 0; i < 4; ++i) {
        const int row = (w + 4 * i) * 8 + (lane >> 3);
        asm volatile("global_load_dwordx4 %0, %1, %2 sc1" : "=&v"(r[i])
                     : "v"((unsigned)out_row(I.po, localrows, row >> 4, row & 15) * 2048u + (unsigned)(I.co * 128 + (((lane & 7) ^ (lane >> 3)) * 16))),
                       "s"(Xin) : "memory");
    }
}
DI void res_write(u32x4 (&r)[4], char* smem, int off, int w, int lane) {
    asm volatile("" : "+v"(r[0]), "+v"(r[1]), "+v"(r[2]), "+v"(r[3]));
#pragma unroll
    for (int i = 0; i < 4; ++i) *(__attribute__((address_space(3))) u32x4*)((lds_ptr)smem + off + (w + 4 * i) * 1024 + lane * 16) = r[i];
}

template <bool AFTER_LOCAL>
DI void out_compute(char* smem, char* ws, int set, u64 tag, const __hip_bfloat16* __restrict__ ofrag,
                    __hip_bfloat16* __restrict__ Xout, float* __restrict__ statp SP_PARAM) {
    constexpr int XOFF = AFTER_LOCAL ? 131072 : 98304;
    int tz = threadIdx.x; asm volatile("" : "+v"(tz));
    const int lane = tz & 63;
    const int w = __builtin_amdgcn_readfirstlane(tz >> 6) & 3;
    const Ids I = ids();
    const int panel = I.po, ct = I.co;
    const int l15 = lane & 15, g = lane >> 4;
    auto orow = [&](int T, int a) -> int { return out_row(panel, AFTER_LOCAL, T, a); };
    const int q4 = l15 >> 2, p4 = l15 & 3;
    const int krow = 4 * g + q4;
    const int sw = out_swz(krow);
    const lds_cptr sm = (lds_cptr)smem;
    lds_cptr bw[4], xo[4];
#pragma unroll
    for (int t = 0; t < 4; ++t) {
        bw[t] = sm + krow * 128 + ((((t >> 1) * 4 + (p4 >> 1) * 2 + (t & 1)) ^ sw) * 16) + (p4 & 1) * 8;
        xo[t] = sm + XOFF + (w * 32 + l15) * 128 + ((((t >> 1) * 4 + (g >> 1) * 2 + (t & 1)) ^ (l15 & 7)) * 16) + (g & 1) * 8;
    }
    unsigned so[2], sto, ao[2];
#pragma unroll
    for (int r2 = 0; r2 < 2; ++r2) {
        so[r2] = (unsigned)orow(2 * w + r2, l15 & 7) * 2048u + (unsigned)(ct * 128 + (l15 >> 3) * 64 + g * 16);
        ao[r2] = (unsigned)((panel * 8 + 2 * w + r2) * 32768 + lane * 16);
    }
    sto = (unsigned)(((panel * 16 + ct) * 128 + 2 * w * 16 + l15) * 8);
    asm volatile("" : "+v"(bw[0]), "+v"(bw[1]), "+v"(bw[2]), "+v"(bw[3]), "+v"(xo[0]), "+v"(xo[1]), "+v"(xo[2]), "+v"(xo[3]),
                 "+v"(so[0]), "+v"(so[1]), "+v"(sto), "+v"(ao[0]), "+v"(ao[1]));
    const __amdgpu_buffer_rsrc_t ra = __builtin_amdgcn_make_buffer_rsrc((void*)ofrag, 0, 4 << 20, 0x00020000);
    asm volatile("" :: "s"(ra));
    u64 land;
    STAMP(AFTER_LOCAL ? 3 : 10);
    signal_poll<true, false>(w, ws, flag_off(set, I.pq, l15, I.cq), flag_off(set, panel, ct, l15), 0xffffull, tag, land);
    BAR();
    STAMP(AFTER_LOCAL ? 4 : 11);
#define AWIN 6
    u32x4 a[AWIN][2];
    auto lda = [&](int j) {
        a[j % AWIN][0] = __builtin_amdgcn_raw_buffer_load_b128(ra, ao[0] + (j & 3) * 1024, (j >> 2) * 4096, 16);
        a[j % AWIN][1] = __builtin_amdgcn_raw_buffer_load_b128(ra, ao[1] + (j & 3) * 1024, (j >> 2) * 4096, 16);
    };
#pragma unroll
    for (int j = 0; j < AWIN; ++j) { lda(j); SBAR(); }
    auto boff = [&](int j) { return (j & 15) * 4096 - ((!AFTER_LOCAL && j >= 24) ? 32768 : 0); };
    s16x4 F[2][4][2];
#pragma unroll
    for (int h = 0; h < 2; ++h)
#pragma unroll
        for (int t = 0; t < 4; ++t) { F[h][t][0] = lds_tr(bw[t] + boff(h)); F[h][t][1] = lds_tr(bw[t] + boff(h) + 2048); }
    f32x4 acc[2][4];
#pragma unroll
    for (int r2 = 0; r2 < 2; ++r2)
#pragma unroll
        for (int c = 0; c < 4; ++c) {
            const u32x2 xr = *(const __attribute__((address_space(3))) u32x2*)(xo[c] + r2 * 2048);
            acc[r2][c] = f32x4{bf_lo(xr[0]), bf_hi(xr[0]), bf_lo(xr[1]), bf_hi(xr[1])};
        }
    WAIT_LGKM();
#pragma unroll
    for (int j = 0; j < 32; ++j) {
        s16x4 (&C)[4][2] = F[j & 1];
        const bf16x8 a0 = __builtin_bit_cast(bf16x8, a[j % AWIN][0]), a1 = __builtin_bit_cast(bf16x8, a[j % AWIN][1]);
        const int nb = boff(j + 2);
        if (j + 2 == 16 || (!AFTER_LOCAL && j + 2 == 24)) {
#pragma unroll
            for (int t = 0; t < 4; ++t) bw[t] += 65536;
            asm volatile("" : "+v"(bw[0]), "+v"(bw[1]), "+v"(bw[2]), "+v"(bw[3]));
        }
#pragma unroll
        for (int t = 0; t < 4; ++t) {
            const bf16x8 bb = cat8(C[t][0], C[t][1]);
            acc[0][t] = MFMA16(bb, a0, acc[0][t]);
            if (t > 0 && j < 30) C[t - 1][1] = lds_tr(bw[t - 1] + nb + 2048);
            SBAR();
            acc[1][t] = MFMA16(bb, a1, acc[1][t]);
            if (j < 30) C[t][0] = lds_tr(bw[t] + nb);
            if (t == 3 && j < 30) C[3][1] = lds_tr(bw[3] + nb + 2048);
            if (t == 3 && j + AWIN < 32) lda(j + AWIN);
            SBAR();
        }
    }
    asm volatile("" :: "v"(land));
    STAMP(AFTER_LOCAL ? 5 : 12);
    u32x4 y[2][2];
#pragma unroll
    for (int r2 = 0; r2 < 2; ++r2)
#pragma unroll
        for (int u = 0; u < 2; ++u) {
            const f32x4 c0 = acc[r2][2 * u], c1 = acc[r2][2 * u + 1];
            const auto sa = __builtin_amdgcn_permlane16_swap(pack_bf16(c0[0], c0[1]), pack_bf16(c1[0], c1[1]), false, false);
            const auto sb = __builtin_amdgcn_permlane16_swap(pack_bf16(c0[2], c0[3]), pack_bf16(c1[2], c1[3]), false, false);
            y[r2][u] = u32x4{sa[0], sb[0], sa[1], sb[1]};
            if (u) {
                u32x4 za, zb;
#pragma unroll
                for (int e = 0; e < 4; ++e) {
                    za[e] = __builtin_amdgcn_update_dpp(y[r2][0][e], y[r2][1][e], 0x128, 0xF, 0xC, false);
                    zb[e] = __builtin_amdgcn_update_dpp(y[r2][1][e], y[r2][0][e], 0x128, 0xF, 0x3, false);
                }
                st16(Xout, so[r2], za);
                st16(Xout, so[r2] + (AFTER_LOCAL ? 16 : 8) * 2048, zb);
                SBAR();
            }
        }
    if (!AFTER_LOCAL && statp == nullptr) return;
    const bf16x8 ones = __builtin_bit_cast(bf16x8, (u32x4{0x3f803f80u, 0x3f803f80u, 0x3f803f80u, 0x3f803f80u}));
    const f32x4 zero4 = {0, 0, 0, 0};
    f32x4 d1[2], d2[2];
#pragma unroll
    for (int r2 = 0; r2 < 2; ++r2) {
        const bf16x8 y0 = __builtin_bit_cast(bf16x8, y[r2][0]), y1 = __builtin_bit_cast(bf16x8, y[r2][1]);
        d2[r2] = MFMA16(y0, y0, zero4); d1[r2] = MFMA16(ones, y0, zero4);
        d2[r2] = MFMA16(y1, y1, d2[r2]); d1[r2] = MFMA16(ones, y1, d1[r2]);
    }
    const bool b0 = p4 & 1, b1 = p4 & 2;
    if (g == q4) {
#pragma unroll
        for (int r2 = 0; r2 < 2; ++r2) {
            float e0 = d2[r2][0], e1 = d2[r2][1], e2 = d2[r2][2], e3 = d2[r2][3];
            asm("" : "+v"(e0), "+v"(e1), "+v"(e2), "+v"(e3));
            const float lo = b0 ? e1 : e0, hi = b0 ? e3 : e2;
            const f32x2 sv = {d1[r2][0], b1 ? hi : lo};
            if (r2) st8o<128>(statp, sto, sv); else st8o<0>(statp, sto, sv);
        }
    }
}

#define ROWSUM(p0, p1, c) __builtin_amdgcn_smfmac_f32_16x16x64_bf16(sel, __builtin_bit_cast(bf16v_t, __builtin_shufflevector(p0, p1, 0, 1, 2, 3, 4, 5, 6, 7)), c, sidx, 0, 0)
constexpr int ATT_STAGE = 32768;
typedef __attribute__((ext_vector_type(8))) unsigned u32x8;
typedef __attribute__((ext_vector_type(32))) unsigned u32x32;

DI void attn_loader(char* smem, char* ws, int set, u64 tag, const __hip_bfloat16* __restrict__ kvf, const void* Wo, const void* Xin SP_PARAM) {
    int tid = threadIdx.x; asm volatile("" : "+v"(tid));
    const int lane = tid & 63;
    const int w = __builtin_amdgcn_readfirstlane(tid >> 6) & 3;
    const Ids I = ids();
    const int panel = I.pq, ct = I.cq;
    const unsigned lds0 = (unsigned)(uintptr_t)smem;
    const char* kvh = (const char*)kvf + (size_t)(ct * 2 + (w >> 1)) * 262144 + (w & 1) * 8192;
    const unsigned voff = (unsigned)(lane * 16);
    auto order = [&](int st) { return ((panel & 8) + st - ((st - 1 < (panel & 7)) ? 1 : 0)) & 15; };
    auto load = [&](int st) {
        const char* src = kvh + (size_t)order(st) * 16384;
        const unsigned dst = lds0 + w * 8192 + (st & 3) * ATT_STAGE;
#pragma unroll
        for (int u = 0; u < 2; ++u)
            asm volatile("s_mov_b32 m0, %0\n\ts_nop 0\n\tglobal_load_lds_dwordx4 %1, %2 sc1\n\tglobal_load_lds_dwordx4 %1, %2 offset:1024 sc1\n\t"
                         "global_load_lds_dwordx4 %1, %2 offset:2048 sc1\n\tglobal_load_lds_dwordx4 %1, %2 offset:3072 sc1"
                         :: "s"(dst + u * 4096), "v"(voff), "s"(src + u * 4096) : "memory");
    };
    const unsigned poff = flag_off(set, ct, panel, lane & 15);
    u64 land;
    __builtin_amdgcn_s_setprio(3);
    BAR();
    signal_poll<false, true>(w, ws, flag_off(set, ct, lane & 15, panel), poff, 1ull << order(1), tag, land);
    STAMP(9);
    load(1);
    asm volatile("global_load_dwordx2 %0, %1, %2 sc1\n\ts_waitcnt vmcnt(0)" : "+v"(land) : "v"(poff), "s"(ws + WS_FLAG) : "memory");
    STAMP(10);
    BAR();
    if (!__all(land == tag)) flag_poll(ws, poff, tag);
    load(2); load(3);
    __builtin_amdgcn_s_setprio(0);
    STAMP(11);
#pragma unroll 1
    for (int st = 1; st < 13; ++st) {
        VM_BAR(8);
        if (st == 6) wo_quarter(Wo, 3, lds0 + 131072, w, lane);
        load(st + 3);
    }
    VM_BAR(8); wo_quarter(Wo, 0, lds0, w, lane);
    VM_BAR(8); wo_quarter(Wo, 1, lds0 + 32768, w, lane);
    u32x4 res[4];
    res_load(res, Xin, false, w, lane);
    BAR();     wo_quarter(Wo, 2, lds0 + 65536, w, lane);
    STAMP(12);
    BAR();
    asm volatile("s_waitcnt vmcnt(0)" ::: "memory");
    res_write(res, smem, 98304, w, lane);
    WAIT_LGKM_BAR();
}

DI void attn_compute(char* smem, const u32x4 (&q4)[4], const __hip_bfloat16* __restrict__ kvf, __hip_bfloat16* __restrict__ ofrag SP_PARAM) {
    int tz = threadIdx.x; asm volatile("" : "+v"(tz));
    const int lane = tz & 63;
    const int w = __builtin_amdgcn_readfirstlane(tz >> 6) & 3;
    const int wm = w & 1, wn = w >> 1;
    const Ids I = ids();
    const int panel = I.pq, head = I.cq * 2 + wn;
    const int l15 = lane & 15;
    const unsigned se = ((l15 & 2) == 0 && (l15 & 4) == 0) ? 0x3f803f80u : 0u, so = ((l15 & 2) == 0 && (l15 & 4) != 0) ? 0x3f803f80u : 0u;
    const bf8_t sel = __builtin_bit_cast(bf8_t, u32x4{se, se, so, so});
    const int sidx = (l15 & 1) ? (int)0xEEEEEEEEu : 0x44444444;
    const lds_cptr sm = (lds_cptr)smem + wn * 16384 + lane * 16;
    const f32x16 z16 = {0, 0, 0, 0, 0, 0, 0, 0, 0, 0, 0, 0, 0, 0, 0, 0};
    const bf16x8 qA0 = __builtin_bit_cast(bf16x8, q4[0]), qA1 = __builtin_bit_cast(bf16x8, q4[1]);
    const bf16x8 qB0 = __builtin_bit_cast(bf16x8, q4[2]), qB1 = __builtin_bit_cast(bf16x8, q4[3]);
    f32x16 oA, oB, nmA, nmB, sA, sB;
    f32x4 lsA, lsB;
    u32x8 pB;
    bf16x8 v11;
    asm volatile(".irp r,2,4,6,8,10,12,14,16,18,20,22,24,26,28,30,32,106,108,110,112,134,136,138,140,170,172\n\tv_mov_b64 v[\\r:\\r+1], 0\n\t.endr"
                 : "={v[2:17]}"(oA), "={v[18:33]}"(oB), "={v[106:113]}"(pB), "={v[134:137]}"(lsA), "={v[138:141]}"(lsB), "={v[170:173]}"(v11));
    WAIT_LGKM_BAR();
    STAMP(8);
    bf16x8 k00 = lds_b128(sm), k10 = lds_b128(sm + 1024), k01 = lds_b128(sm + 4096), k11 = lds_b128(sm + 4096 + 1024);
    bf16x8 v00 = lds_b128(sm + 2048), v10 = lds_b128(sm + 3072), v01 = lds_b128(sm + 4096 + 2048);
    {
        sA = MFMA32(k00, qA0, z16); sB = MFMA32(k00, qB0, z16);
        sA = MFMA32(k10, qA1, sA); sB = MFMA32(k10, qB1, sB);
        float mA = sA[0], mB = sB[0];
#pragma unroll
        for (int r = 1; r < 16; ++r) { mA = fmaxf(mA, sA[r]); mB = fmaxf(mB, sB[r]); }
        mA = max_h(mA); mB = max_h(mB);
#pragma unroll
        for (int r = 0; r < 16; ++r) { nmA[r] = -mA; nmB[r] = -mB; sA[r] -= mA; }
    }
    const unsigned a0 = (unsigned)(uintptr_t)sm;
    asm volatile(
        ".macro ATT_RD dst, y\n\t"
        "ds_read_b128 v[\\dst:\\dst+3], v[175+(((\\y)&0x1ffff)>>16)] offset:(\\y)&0xffff\n\t"
        ".endm\n\t"
        ".macro ATT_E4 c\n\t"
        "v_exp_f32 v[\\c], v[\\c]\n\tv_exp_f32 v[\\c+1], v[\\c+1]\n\tv_exp_f32 v[\\c+2], v[\\c+2]\n\tv_exp_f32 v[\\c+3], v[\\c+3]\n\t"
        ".endm\n\t"
        ".macro ATT_C4 p, c\n\t"
        "v_cvt_pk_bf16_f32 v[\\p], v[\\c], v[\\c+1]\n\tv_cvt_pk_bf16_f32 v[\\p+1], v[\\c+2], v[\\c+3]\n\t"
        "v_cvt_pk_bf16_f32 v[\\p+2], v[\\c+4], v[\\c+5]\n\tv_cvt_pk_bf16_f32 v[\\p+3], v[\\c+6], v[\\c+7]\n\t"
        ".endm\n\t"
        ".macro ATT_STEP cur, nxt, ka, kb, q, nm, oc, od, lsd, pc, pd, vd1, vc0, r0, y0, r1, y1, bar, rd\n\t"
        "v_mfma_f32_32x32x16_bf16 v[\\nxt:\\nxt+15], v[\\ka:\\ka+3], v[\\q:\\q+3], v[\\nm:\\nm+15]\n\t"
        "ATT_E4 \\cur\n\t"
        "v_mfma_f32_32x32x16_bf16 v[\\nxt:\\nxt+15], v[\\kb:\\kb+3], v[\\q+4:\\q+7], v[\\nxt:\\nxt+15]\n\t"
        "ATT_E4 \\cur+4\n\t"
        "v_mfma_f32_32x32x16_bf16 v[\\od:\\od+15], v[\\vd1:\\vd1+3], v[\\pd+4:\\pd+7], v[\\od:\\od+15]\n\t"
        "s_waitcnt lgkmcnt(0)\n\t"
        "ATT_C4 \\pc, \\cur\n\t"
        "v_exp_f32 v[\\cur+8], v[\\cur+8]\n\tv_exp_f32 v[\\cur+9], v[\\cur+9]\n\t"
        "v_mfma_f32_32x32x16_bf16 v[\\oc:\\oc+15], v[\\vc0:\\vc0+3], v[\\pc:\\pc+3], v[\\oc:\\oc+15]\n\t"
        ".if \\bar\n\ts_barrier\n\t.endif\n\t"
        "ATT_E4 \\cur+10\n\t"
        "v_smfmac_f32_16x16x64_bf16 v[\\lsd:\\lsd+3], v[130:133], v[\\pd:\\pd+7], v174\n\t"
        ".if \\rd & 4\n\tATT_RD 142, 32768\n\tATT_RD 150, 33792\n\tATT_RD 158, 34816\n\t.endif\n\t"
        ".if \\rd & 1\n\tATT_RD \\r0, \\y0\n\t.endif\n\t"
        ".if \\rd & 2\n\tATT_RD \\r1, \\y1\n\t.endif\n\t"
        "v_exp_f32 v[\\cur+14], v[\\cur+14]\n\tv_exp_f32 v[\\cur+15], v[\\cur+15]\n\t"
        "ATT_C4 \\pc+4, \\cur+8\n\t"
        ".if \\rd & 4\n\ts_waitcnt lgkmcnt(3)\n\t.endif\n\t"
        ".endm\n\t"
        ".macro ATT_TILE p, y1, y2, bar, ra=3, rb=3\n\t"
        "ATT_STEP 66, 82, 142+\\p, 150+\\p, 122, 50, 2, 18, 138, 98, 106, 170-\\p, 158+\\p, 170-\\p, \\y1+3072, 142+\\p, \\y2, \\bar, \\ra\n\t"
        "ATT_STEP 82, 66, 146-\\p, 154-\\p, 114, 34, 18, 2, 134, 106, 98, 166+\\p, 158+\\p, 158+\\p, \\y2+2048, 150+\\p, \\y2+1024, 0, \\rb\n\t"
        ".endm\n\t"
        ".macro ATT_STAGE b\n\t"
        "ATT_TILE 0, \\b+4096, \\b+8192, 0\n\t"
        "ATT_TILE 4, \\b+8192, \\b+12288, 0\n\t"
        "ATT_TILE 0, \\b+12288, \\b+32768, 1\n\t"
        "ATT_TILE 4, \\b+32768, \\b+36864, 0\n\t"
        ".endm\n\t"
        "v_add_u32 v176, 0x10000, v175\n\t"
        "s_mov_b32 s96, 4\n\t"
        "s_waitcnt vmcnt(0) lgkmcnt(0)\n\t"
        "s_barrier\n\t"
        "ATT_TILE 0, 4096, 8192, 0\n\tATT_TILE 4, 8192, 12288, 0\n\tATT_TILE 0, 12288, 0, 0, 1, 0\n\tATT_TILE 4, 32768, 36864, 1, 7, 3\n\t"
        "s_branch att_entry_%=\n\t"
        ".p2align 6\n"
        "att_loop_%=:\n\t"
        "ATT_STAGE 0\n"
        "att_entry_%=:\n\t"
        "ATT_STAGE 32768\n\tATT_STAGE 65536\n\t"
        "s_sub_u32 s96, s96, 1\n\t"
        "ATT_STAGE 98304\n\t"
        "s_cmp_lg_u32 s96, 0\n\t"
        "s_cbranch_scc1 att_loop_%=\n\t"
        "s_nop 1\n\t"
        "v_mfma_f32_32x32x16_bf16 v[18:33], v[170:173], v[110:113], v[18:33]\n\t"
        "v_smfmac_f32_16x16x64_bf16 v[138:141], v[130:133], v[106:113], v174\n\t"
        "s_waitcnt lgkmcnt(0)\n\ts_nop 10\n\t"
        ".purgem ATT_RD\n\t.purgem ATT_E4\n\t.purgem ATT_C4\n\t.purgem ATT_STEP\n\t.purgem ATT_TILE\n\t.purgem ATT_STAGE"
        : "+{v[2:17]}"(oA), "+{v[18:33]}"(oB), "+{v[66:81]}"(sA), "+{v[106:113]}"(pB), "+{v[134:137]}"(lsA), "+{v[138:141]}"(lsB),
          "+{v[142:145]}"(k00), "+{v[146:149]}"(k01), "+{v[150:153]}"(k10), "+{v[154:157]}"(k11),
          "+{v[158:161]}"(v00), "+{v[162:165]}"(v01), "+{v[166:169]}"(v10), "+{v[170:173]}"(v11)
        : "{v[34:49]}"(nmA), "{v[50:65]}"(nmB), "{v[114:117]}"(qA0), "{v[118:121]}"(qA1), "{v[122:125]}"(qB0), "{v[126:129]}"(qB1),
          "{v[130:133]}"(sel), "{v174}"(sidx), "{v175}"(a0)
        : "memory", "scc", "s96", "v176", "v82", "v83", "v84", "v85", "v86", "v87", "v88", "v89", "v90", "v91", "v92", "v93", "v94", "v95",
          "v96", "v97", "v98", "v99", "v100", "v101", "v102", "v103", "v104", "v105");
    STAMP(9);
    float lA = lsA[0] + lsA[1], lB = lsB[0] + lsB[1];
    const unsigned dst = (unsigned)((((panel * 8 + wm * 4 + ((lane & 31) >> 4)) * 32 + head) * 64 + (lane & 15) + (lane >> 5) * 16) * 16);
    auto store = [&] {
#pragma unroll
    for (int u = 0; u < 2; ++u) {
        const f32x16 e = (u ? oB : oA) * __builtin_amdgcn_rcpf(u ? lB : lA);
        st16o<0>((char*)ofrag + u * 65536, dst, u32x4{pack_bf16(e[0], e[1]), pack_bf16(e[2], e[3]), pack_bf16(e[8], e[9]), pack_bf16(e[10], e[11])});
        st16o<512>((char*)ofrag + u * 65536, dst, u32x4{pack_bf16(e[4], e[5]), pack_bf16(e[6], e[7]), pack_bf16(e[12], e[13]), pack_bf16(e[14], e[15])});
    }
    };
    store();
    {
        const f32x16 os = oA + oB;
        const float osum = (((os[0] + os[1]) + (os[2] + os[3])) + ((os[4] + os[5]) + (os[6] + os[7]))) +
                           (((os[8] + os[9]) + (os[10] + os[11])) + ((os[12] + os[13]) + (os[14] + os[15])));
        const bool bad = !(lA < 1.0e24f) || !(lB < 1.0e24f) || !(fabsf(osum) < 3.0e38f);
        if (__any(bad)) {
            const __amdgpu_buffer_rsrc_t rk = __builtin_amdgcn_make_buffer_rsrc((char*)kvf + (size_t)head * 262144, 0, 262144, 0x00020000);
            oA = z16; oB = z16; lA = 0.f; lB = 0.f;
            float mA = -INFINITY, mB = -INFINITY;
#pragma unroll 1
            for (int t = 0; t < 64; ++t) {
                bf16x8 f[4];
#pragma unroll
                for (int i = 0; i < 4; ++i) f[i] = __builtin_bit_cast(bf16x8, __builtin_amdgcn_raw_buffer_load_b128(rk, lane * 16 + i * 1024, t * 4096, 16));
                auto upd = [&](bf16x8 q0, bf16x8 q1, f32x16& o, float& l, float& m) {
                    f32x16 s = MFMA32(f[0], q0, z16); s = MFMA32(f[1], q1, s);
                    float tm = s[0];
#pragma unroll
                    for (int r = 1; r < 16; ++r) tm = fmaxf(tm, s[r]);
                    tm = max_h(tm);
                    const float mn = fmaxf(m, tm);
                    const float alpha = __builtin_amdgcn_exp2f(m - mn);
                    m = mn;
#pragma unroll
                    for (int r = 0; r < 16; ++r) { s[r] = __builtin_amdgcn_exp2f(s[r] - mn); o[r] *= alpha; }
                    const u32x4 p0 = {pack_bf16(s[0], s[1]), pack_bf16(s[2], s[3]), pack_bf16(s[4], s[5]), pack_bf16(s[6], s[7])};
                    const u32x4 p1 = {pack_bf16(s[8], s[9]), pack_bf16(s[10], s[11]), pack_bf16(s[12], s[13]), pack_bf16(s[14], s[15])};
                    f32x4 t4 = {0, 0, 0, 0};
                    o = MFMA32(f[2], __builtin_bit_cast(bf16x8, p0), o);
                    o = MFMA32(f[3], __builtin_bit_cast(bf16x8, p1), o);
                    t4 = ROWSUM(p0, p1, t4);
                    l = l * alpha + (t4[0] + t4[1]);
                };
                upd(qA0, qA1, oA, lA, mA);
                upd(qB0, qB1, oB, lB, mB);
            }
            store();
        }
    }
}

DI void ln_row(const u32x4 (&v)[2], __hip_bfloat16* __restrict__ Y, unsigned off) {
    float s1 = 0.f, s2 = 0.f;
#pragma unroll
    for (int e = 0; e < 8; ++e) s1 = dot2bf(v[e >> 2][e & 3], 0x3f803f80u, s1);
    const float mean = wave_sum(s1) * (1.f / 1024.f);
#pragma unroll
    for (int e = 0; e < 8; ++e) {
        const float d0 = bf_lo(v[e >> 2][e & 3]) - mean, d1 = bf_hi(v[e >> 2][e & 3]) - mean;
        s2 += d0 * d0 + d1 * d1;
    }
    const float mb = round_bf16(mean);
    const float rden = __builtin_amdgcn_rcpf(round_bf16(__builtin_amdgcn_sqrtf(round_bf16(round_bf16(wave_sum(s2) * (1.f / 1024.f)) + 1e-5f))));
#pragma unroll
    for (int hh = 0; hh < 2; ++hh) {
        u32x4 y;
#pragma unroll
        for (int e = 0; e < 4; ++e) {
            const unsigned d = pack_bf16(bf_lo(v[hh][e]) - mb, bf_hi(v[hh][e]) - mb);
            y[e] = pack_bf16(bf_lo(d) * rden, bf_hi(d) * rden);
        }
        if (hh) st16o<1024>(Y, off, y); else st16o<0>(Y, off, y);
    }
}

extern "C" __global__ __launch_bounds__(512, 2) void mega_kernel(const __hip_bfloat16* x, const __hip_bfloat16* wqkvA,
                                                            const __hip_bfloat16* woA, const __hip_bfloat16* wqkvB,
                                                            const __hip_bfloat16* woB, __hip_bfloat16* out,
                                                            char* ws) {
    char* const smem = smem_base;
    typedef __hip_bfloat16 bf;
    const int w8 = __builtin_amdgcn_readfirstlane(threadIdx.x >> 6);
    if (w8 == 7 && (blockIdx.x >> 3) < 10) code_touch(blockIdx.x >> 3, threadIdx.x & 63);
    asm volatile("" :: "s"(x), "s"(wqkvA), "s"(woA), "s"(wqkvB), "s"(woB), "s"(out), "s"(ws));
    STAMP_DECL; STAMPR(14); STAMP(0);
    u64 tag;
    {
        const int lane = threadIdx.x & 63;
        const Ids I = ids();
        const unsigned off = (unsigned)((I.po * 128 + I.co * 8 + w8) * 2048 + lane * 16);
        u32x4 v[2];
        asm volatile("global_load_dwordx4 %0, %2, %3\n\tglobal_load_dwordx4 %1, %2, %3 offset:1024" : "=&v"(v[0]), "=&v"(v[1]) : "v"(off), "s"(x) : "memory");
        asm volatile("s_load_dwordx2 %0, %1, 0x0" : "=s"(tag) : "s"(ws + WS_EPOCH) : "memory");
        if (w8 == 7) {
            const int l = min(lane, 12), k = l % 5;
            const unsigned o = (l >= 10 || k == 3) ? flag_off(l, I.cq, I.pq, 0) : (k == 0 || k == 2) ? flag_off(l, I.pq, I.cq, 0) : flag_off(l, I.po, I.co, 0);
            touch_at(ws + WS_FLAG, o);
        } else if (w8 >= 4 && w8 - 4 < NTOUCH) touch(wqkvA, (w8 - 4) * 256 + blockIdx.x, lane);
        if (w8 == 7 || (w8 >= 4 && w8 - 4 < NTOUCH)) asm volatile("s_waitcnt vmcnt(1)" ::: "memory"); else asm volatile("s_waitcnt vmcnt(0)" ::: "memory");
        asm volatile("" : "+v"(v[0]), "+v"(v[1]));
        ln_row(v, (bf*)(ws + WS_Y0), off);
        asm volatile("s_waitcnt lgkmcnt(0)" : "+s"(tag) :: "memory");
        tag += 1;
    }
    if (w8 >= 4) {
        int w = w8 & 3;
#pragma unroll 1
        for (int it = 0; it < 2; ++it) {
            int lane = threadIdx.x; asm volatile("" : "+v"(lane)); lane &= 63;
            asm volatile("" : "+s"(x), "+s"(wqkvA), "+s"(woA), "+s"(wqkvB), "+s"(woB), "+s"(ws), "+s"(w));
            const unsigned lds0 = (unsigned)(uintptr_t)smem;
            u32x4 ca[8], cb[8];
            qkv_loader<true>(smem, ws, it * 5, tag, (const bf*)(ws + (it ? WS_X2 : WS_Y0)), wqkvA, (const float*)(ws + WS_ST + 262144),
                             ws + WS_CSP, it == 0, woA, [&] {
                if (w < 3) {
                    colsum_load(ca, wqkvA, w, lane); colsum_load(cb, wqkvB, w, lane);
                    asm volatile("s_waitcnt vmcnt(16)" ::: "memory");
                } else {
                    touch(woA, blockIdx.x, lane);
                    asm volatile("s_waitcnt vmcnt(1)" ::: "memory");
                }
            } SP_ARG);
            STAMP(3);
#pragma unroll
            for (int q = 0; q < 4; ++q) wo_quarter(woA, q, lds0 + q * 32768, w, lane);
            u32x4 res[4];
            res_load(res, it ? (const void*)(ws + WS_X2) : x, true, w, lane);
            const bool cs = (it == 0 && w < 3);
            STAMP(4);
            BAR();
            asm volatile("s_waitcnt vmcnt(0)" ::: "memory");
            res_write(res, smem, 131072, w, lane);
            WAIT_LGKM_BAR();
            if (cs) {
                asm volatile("" : "+v"(cb[0]), "+v"(cb[1]), "+v"(cb[2]), "+v"(cb[3]), "+v"(cb[4]), "+v"(cb[5]), "+v"(cb[6]), "+v"(cb[7]));
                asm volatile("" : "+v"(ca[0]), "+v"(ca[1]), "+v"(ca[2]), "+v"(ca[3]), "+v"(ca[4]), "+v"(ca[5]), "+v"(ca[6]), "+v"(ca[7]));
                colsum_store(ca, ws + WS_CSP, w, lane);
                colsum_store(cb, ws + WS_CSP + 196608, w, lane);
                asm volatile("s_waitcnt vmcnt(0)" ::: "memory");
                flag_store(ws, 10 + w, ids().cq, ids().pq, tag, lane);
            }
            STAMP(5);
            qkv_loader<false>(smem, ws, it * 5 + 2, tag, (const bf*)(ws + (it ? WS_X3 : WS_X1)), wqkvB, (const float*)(ws + WS_ST + it * 524288), ws + WS_CSP + 196608,
                              false, nullptr, [] {} SP_ARG);
            STAMP(8);
            attn_loader(smem, ws, it * 5 + 3, tag, (const bf*)(ws + WS_KVF + it * 8 * MBy), woB, ws + (it ? WS_X3 : WS_X1) SP_ARG);
            STAMP(13); STAMPR(15); STAMP_FLUSH(it);
        }
        if (blockIdx.x == 0 && threadIdx.x == 256) __hip_atomic_store((g_u64ptr)(ws + WS_EPOCH), tag, __ATOMIC_RELAXED, __HIP_MEMORY_SCOPE_AGENT);
    } else {
        __builtin_amdgcn_s_setprio(2);
#pragma unroll 1
        for (int it = 0; it < 2; ++it) {
            u32x4 q4[4];
            asm volatile("" : "+s"(ws), "+s"(wqkvA), "+s"(wqkvB));
            qkv_compute<true>(smem, wqkvA, q4, nullptr, (bf*)(ws + WS_OF + it * 8 * MBy), it == 0 SP_ARG);
            STAMP(2);
            out_compute<true>(smem, ws, it * 5 + 1, tag, (const bf*)(ws + WS_OF + it * 8 * MBy),
                              (bf*)(ws + (it ? WS_X3 : WS_X1)), (float*)(ws + WS_ST + it * 524288) SP_ARG);
            STAMP(6);
            qkv_compute<false>(smem, wqkvB, q4, (bf*)(ws + WS_KVF + it * 8 * MBy), nullptr, false SP_ARG);
            attn_compute(smem, q4, (const bf*)(ws + WS_KVF + it * 8 * MBy), (bf*)(ws + WS_OF + (it * 8 + 4) * MBy) SP_ARG);
            out_compute<false>(smem, ws, it * 5 + 4, tag, (const bf*)(ws + WS_OF + (it * 8 + 4) * MBy),
                               it ? out : (bf*)(ws + WS_X2), it ? nullptr : (float*)(ws + WS_ST + 262144) SP_ARG);
            STAMP(13); STAMPR(15); STAMP_FLUSH(it);
        }
    }
}

extern "C" __global__ void zz_code_end_kernel() {}

extern "C" void kernel_launch(void* const* d_in, const int* in_sizes, int n_in, void* d_out, int out_size, void* d_ws,
                              size_t ws_size, hipStream_t stream) {
    typedef __hip_bfloat16 bf;
    (void)hipFuncSetAttribute((const void*)mega_kernel, hipFuncAttributeMaxDynamicSharedMemorySize, CH_LDS);
    mega_kernel<<<256, 512, CH_LDS, stream>>>((const bf*)d_in[0], (const bf*)d_in[1], (const bf*)d_in[2], (const bf*)d_in[4], (const bf*)d_in[5],
                                              (bf*)d_out, (char*)d_ws);
}
```

```cpp
#include <hip/hip_runtime.h>
#include <hip/hip_bf16.h>
#include <stdint.h>

typedef __attribute__((ext_vector_type(8))) short bf16x8;
typedef __attribute__((ext_vector_type(4))) short s16x4;
typedef __attribute__((ext_vector_type(4))) float f32x4;
typedef __attribute__((ext_vector_type(2))) float f32x2;
typedef __attribute__((ext_vector_type(16))) float f32x16;
typedef __attribute__((ext_vector_type(4))) unsigned u32x4;
typedef __attribute__((ext_vector_type(2))) unsigned u32x2;
typedef __attribute__((ext_vector_type(2))) __bf16 bf16x2_t;
typedef __attribute__((ext_vector_type(8))) __bf16 bf8_t;
typedef __attribute__((ext_vector_type(16))) __bf16 bf16v_t;
typedef __attribute__((address_space(3))) const char* lds_cptr;
typedef __attribute__((address_space(3))) char* lds_ptr;

#define DI __device__ __forceinline__
#define MFMA16(a, b, c) __builtin_amdgcn_mfma_f32_16x16x32_bf16((a), (b), (c), 0, 0, 0)
#define MFMA32(a, b, c) __builtin_amdgcn_mfma_f32_32x32x16_bf16((a), (b), (c), 0, 0, 0)
#define SBAR() __builtin_amdgcn_sched_barrier(0)
#define STAMP(i)
#define STAMPR(i)
#define STAMP_DECL
#define STAMP_FLUSH(it)
#define SP_PARAM
#define SP_ARG
typedef unsigned long long u64;
#define SPRIO 3
#define QUNR 3
#define NTOUCH 3

constexpr float QSCALE = 0.17677669529663687f * 1.4426950408889634f;
constexpr float ATT_SCALE = 0.17677669529663687f;
constexpr float LOG2E = 1.4426950408889634f;
constexpr float NEG_MASK = -1e9f / ATT_SCALE;

DI unsigned pack_bf16(float lo, float hi) {
    f32x2 v = {lo, hi};
    bf16x2_t r = __builtin_convertvector(v, bf16x2_t);
    return __builtin_bit_cast(unsigned, r);
}
DI float bf_lo(unsigned u) { return __uint_as_float(u << 16); }
DI float bf_hi(unsigned u) { return __uint_as_float(u & 0xffff0000u); }
DI float round_bf16(float x) { return bf_lo(pack_bf16(x, 0.f)); }
DI void ln_stats(float sum, float sumsq, float& mean_out, float& rstd_out) {
    float mean = sum * (1.f / 1024.f);
    asm("" : "+v"(mean));
    const float var = fmaxf(sumsq * (1.f / 1024.f) - mean * mean, 0.f);
    mean_out = round_bf16(mean);
    rstd_out = __builtin_amdgcn_rcpf(round_bf16(__builtin_amdgcn_sqrtf(round_bf16(round_bf16(var) + 1e-5f))));
}

DI void glds16(const void* sbase, unsigned voff, unsigned ldsaddr) {
    asm volatile("s_mov_b32 m0, %0\n\ts_nop 0\n\tglobal_load_lds_dwordx4 %1, %2"
                 :: "s"(ldsaddr), "v"(voff), "s"(sbase) : "memory");
}
DI void glds16c(const void* sbase, unsigned voff, unsigned ldsaddr) {
    asm volatile("s_mov_b32 m0, %0\n\ts_nop 0\n\tglobal_load_lds_dwordx4 %1, %2 sc1"
                 :: "s"(ldsaddr), "v"(voff), "s"(sbase) : "memory");
}
DI void st16(void* sbase, unsigned voff, u32x4 v) {
    asm volatile("global_store_dwordx4 %0, %1, %2 sc1\n\ts_nop 1" :: "v"(voff), "v"(v), "s"(sbase) : "memory");
}
template <int OFF> DI void st16o(void* sbase, unsigned voff, u32x4 v) {
    asm volatile("global_store_dwordx4 %0, %1, %2 offset:%3 sc1\n\ts_nop 1" :: "v"(voff), "v"(v), "s"(sbase), "n"(OFF) : "memory");
}
DI bf16x8 lds_b128(lds_cptr p) { return *(const __attribute__((address_space(3))) bf16x8*)p; }
DI s16x4 lds_tr(lds_cptr p) {
    return __builtin_amdgcn_ds_read_tr16_b64_v4i16((__attribute__((address_space(3))) s16x4*)p);
}
DI bf16x8 cat8(s16x4 a, s16x4 b) { return __builtin_shufflevector(a, b, 0, 1, 2, 3, 4, 5, 6, 7); }
DI float dot2bf(unsigned a, unsigned b, float c) {
    return __builtin_amdgcn_fdot2_f32_bf16(__builtin_bit_cast(bf16x2_t, a), __builtin_bit_cast(bf16x2_t, b), c, false);
}
DI float sum_g(float v) {
    auto a = __builtin_amdgcn_permlane16_swap(__float_as_uint(v), __float_as_uint(v), false, false);
    v = __uint_as_float(a[0]) + __uint_as_float(a[1]);
    auto b = __builtin_amdgcn_permlane32_swap(__float_as_uint(v), __float_as_uint(v), false, false);
    return __uint_as_float(b[0]) + __uint_as_float(b[1]);
}
DI float wave_sum(float v) {
    v += __builtin_bit_cast(float, __builtin_amdgcn_update_dpp(0, __builtin_bit_cast(int, v), 0xB1, 0xF, 0xF, true));
    v += __builtin_bit_cast(float, __builtin_amdgcn_update_dpp(0, __builtin_bit_cast(int, v), 0x4E, 0xF, 0xF, true));
    v += __builtin_bit_cast(float, __builtin_amdgcn_update_dpp(0, __builtin_bit_cast(int, v), 0x141, 0xF, 0xF, true));
    v += __builtin_bit_cast(float, __builtin_amdgcn_update_dpp(0, __builtin_bit_cast(int, v), 0x140, 0xF, 0xF, true));
    return sum_g(v);
}
DI float amax3(float a, float b, float c) { float r; asm("v_max3_f32 %0, %1, %2, %3" : "=v"(r) : "v"(a), "v"(b), "v"(c)); return r; }
DI float addf(float a, float b) { float r; asm("v_add_f32 %0, %1, %2" : "=v"(r) : "v"(a), "v"(b)); return r; }
DI float max2f(float a, float b) { return __builtin_amdgcn_fmed3f(a, b, INFINITY); }
DI float max_h(float v) {
    auto b = __builtin_amdgcn_permlane32_swap(__float_as_uint(v), __float_as_uint(v), false, false);
    return max2f(__uint_as_float(b[0]), __uint_as_float(b[1]));
}

template <int OFF> DI void st8o(void* sbase, unsigned voff, f32x2 v) {
    asm volatile("global_store_dwordx2 %0, %1, %2 offset:%3 sc1\n\ts_nop 1" :: "v"(voff), "v"(v), "s"(sbase), "n"(OFF) : "memory");
}
constexpr unsigned OFF_DUMP = 159744;
extern __shared__ __attribute__((aligned(16))) char smem_base[];
DI void touch_at(const void* p, unsigned off) {
    asm volatile("s_mov_b32 m0, %0\n\ts_nop 0\n\tglobal_load_lds_dword %1, %2"
                 :: "s"((unsigned)(uintptr_t)smem_base + OFF_DUMP), "v"(off), "s"(p) : "memory");
}
DI void touch(const void* p, int slice, int lane) { touch_at(p, (unsigned)(slice * 64 + lane) * 128u); }
DI void code_touch(int slice, int lane) {
    unsigned long long c0, c1;
    asm volatile("s_getpc_b64 s[96:97]\n\ts_add_u32 s96, s96, mega_kernel@rel32@lo+4\n\ts_addc_u32 s97, s97, mega_kernel@rel32@hi+12\n\ts_mov_b64 %0, s[96:97]\n\t"
                 "s_getpc_b64 s[96:97]\n\ts_add_u32 s96, s96, zz_code_end_kernel@rel32@lo+4\n\ts_addc_u32 s97, s97, zz_code_end_kernel@rel32@hi+12\n\ts_mov_b64 %1, s[96:97]"
                 : "=s"(c0), "=s"(c1) :: "s96", "s97", "scc");
    touch_at((const void*)c0, min((unsigned)(slice * 64 + lane) * 128u, (unsigned)(c1 - c0)));
}

#define BAR() asm volatile("s_barrier" ::: "memory")
#define WAIT_LGKM() do { SBAR(); __builtin_amdgcn_s_waitcnt(0xC07F); SBAR(); } while (0)
#define WAIT_LGKM_BAR() do { SBAR(); __builtin_amdgcn_s_waitcnt(0xC07F); asm volatile("s_barrier" ::: "memory"); SBAR(); } while (0)
#define VM_BAR(n) asm volatile("s_waitcnt vmcnt(" #n ")\n\ts_barrier" ::: "memory")
constexpr int CH_LDS = 163840;
constexpr int OFF_SYNC = CH_LDS - 64;
typedef __attribute__((address_space(3))) unsigned* lds_uptr;
typedef __attribute__((address_space(1))) u64* g_u64ptr;

constexpr size_t MBy = 1 << 20;
constexpr size_t WS_X3 = 0, WS_X2 = 4 * MBy, WS_X1 = 8 * MBy, WS_OF = 12 * MBy  , WS_KVF = 36 * MBy  ,
                 WS_ST = 52 * MBy  , WS_CSP = 54 * MBy  , WS_FLAG = 55 * MBy  ,
                 WS_EPOCH = WS_FLAG + 15 * 65536, WS_Y0 = 56 * MBy;
DI unsigned flag_off(int set, int group, int consumer, int producer) { return (unsigned)(set * 65536 + group * 4096 + consumer * 256 + producer * 8); }
DI void flag_store(char* ws, int set, int group, int producer, u64 tag, int lane) {
    asm volatile("" : "+s"(tag));
    if (lane < 16) __hip_atomic_store((g_u64ptr)(ws + WS_FLAG + flag_off(set, group, lane, producer)), tag, __ATOMIC_RELAXED, __HIP_MEMORY_SCOPE_AGENT);
}
DI void flag_poll(const char* ws, unsigned off, u64 tag) {
    asm volatile("" : "+s"(tag));
#pragma unroll 1
    for (int spin = 0; spin < (1 << 16); ++spin) {
        const u64 v = __hip_atomic_load((g_u64ptr)(ws + WS_FLAG + off), __ATOMIC_RELAXED, __HIP_MEMORY_SCOPE_AGENT);
        if (__all(v == tag)) break;
        __builtin_amdgcn_s_sleep(1);
    }
    asm volatile("" ::: "memory");
}
#define POLL_INS "global_atomic_or_x2 %[land], %[po], %[zero], %[base] sc0 sc1"
template <bool DRAIN, bool ALLPOLL>
DI void signal_poll(int follower, const char* ws, unsigned soff, unsigned poff, u64 pmask, u64 tag, u64& land) {
    u64 tagv, zero;
    asm volatile("s_not_b64 s[96:97], %[ts]\n\tv_mov_b64 %[tag], %[ts]\n\tv_mov_b64 %[zero], 0\n\tv_mov_b64 %[land], s[96:97]\n\t"
                 ".if %[dr]\n\ts_waitcnt vmcnt(0)\n\t.endif\n\ts_barrier\n\t"
                 "s_cmp_lg_u32 %[fl], 0\n\ts_cbranch_scc1 .Lsig_fol_%=\n\t"
                 "s_mov_b64 exec, 0xffff\n\t"
                 "global_store_dwordx2 %[so], %[tag], %[base] sc1\n"
                 ".if %[ap]\n.Lsig_fol_%=:\n.endif\n\t"
                 "s_mov_b64 exec, %[pm]\n\t"
                 "s_movk_i32 s96, 0x7fff\n"
                 ".Lsig_loop_%=:\n\t"
                 POLL_INS "\n\t"
                 "s_sleep 1\n\t"
                 "s_waitcnt vmcnt(3)\n\t"
                 "v_cmp_ne_u64 vcc, %[land], %[tag]\n\t"
                 "s_sub_u32 s96, s96, 1\n\t"
                 "s_cbranch_scc1 .Lsig_out_%=\n\t"
                 "s_cbranch_vccnz .Lsig_loop_%=\n"
                 ".Lsig_out_%=:\n\t"
                 "s_mov_b64 exec, -1\n"
                 ".if !%[ap]\n.Lsig_fol_%=:\n.endif"
                 : [land] "=&v"(land), [tag] "=&v"(tagv), [zero] "=&v"(zero)
                 : [fl] "s"(follower), [so] "v"(soff), [po] "v"(poff), [ts] "s"(tag), [base] "s"(ws + WS_FLAG), [pm] "s"(pmask), [ap] "n"(ALLPOLL ? 1 : 0), [dr] "n"(DRAIN ? 1 : 0)
                 : "memory", "scc", "vcc", "s96", "s97");
}
struct Ids { int pq, cq, po, co; };
DI Ids ids() {
    int bid = blockIdx.x; asm volatile("" : "+s"(bid));
    const int xcd = bid & 7, idx = bid >> 3;
    int pq, cq, po, co;
#define IDS_BODY pq = (xcd & 1) * 8 + (idx & 7); cq = (xcd >> 1) * 4 + (idx >> 3); po = (xcd & 3) * 4 + (idx & 3); co = (xcd >> 2) * 8 + (idx >> 2);
    IDS_BODY
    return Ids{pq, cq, po, co};
}

DI void slow_warm(unsigned dump, const void* W, int w, int lane) {
    int bid = blockIdx.x; asm volatile("" : "+s"(bid));
    const int xcd = bid & 7, idx = bid >> 3;
    const int sc = (3 - (int)((uintptr_t)W >> 7)) & 7;
    if ((sc >> 2) != ((xcd >> 1) & 1)) return;
    const char* src = (const char*)W + (idx + 32 * w) * 49152 + (sc + (xcd >> 2) * 8) * 128;
#pragma unroll
    for (int G = 0; G < 3; ++G) glds16(src + G * 2048, (unsigned)((lane >> 3) * 6144 + (lane & 7) * 16), dump);
}

template <int V> struct IC { static constexpr int value = V; };
template <bool LOCAL> struct QC {
    static constexpr int AROWS = LOCAL ? 144 : 128;
    static constexpr int ABYTES = AROWS * 128;
    static constexpr int STAGE = ABYTES + 24576;
    static constexpr int OFF_MEAN = 147456;
    static constexpr int OFF_RSTD = OFF_MEAN + 576;
    static constexpr int OFF_CS = OFF_RSTD + 576;
};
constexpr int OFF_EX = 131072;

DI void colsum_load(u32x4 (&c)[8], const void* W, int G, int lane) {
    const Ids I = ids();
    const unsigned off = (unsigned)(I.pq * 64 + (lane >> 3)) * 6144u + (unsigned)(G * 2048 + I.cq * 128 + (lane & 7) * 16);
#pragma unroll
    for (int i = 0; i < 8; ++i)
        asm volatile("global_load_dwordx4 %0, %1, %2" : "=&v"(c[i]) : "v"(off), "s"((const char*)W + i * 49152) : "memory");
}
DI void colsum_store(const u32x4 (&c)[8], char* dst, int G, int lane) {
    const Ids I = ids();
    const int m = lane & 15;
    const unsigned one = (m & 1) ? 0xbf800000u : 0xbf80u;
    const u32x4 sl = {(m >> 1) == 0 ? one : 0u, (m >> 1) == 1 ? one : 0u, (m >> 1) == 2 ? one : 0u, (m >> 1) == 3 ? one : 0u};
    f32x4 d = {0, 0, 0, 0};
#pragma unroll
    for (int i = 0; i < 8; ++i) d = MFMA16(__builtin_bit_cast(bf16x8, sl), __builtin_bit_cast(bf16x8, c[i]), d);
#pragma unroll
    for (int r = 0; r < 4; ++r)
        d[r] += __int_as_float(__builtin_amdgcn_update_dpp(0, __float_as_int(d[r]), 0x128, 0xF, 0xF, true));
    if (lane < 32 && (lane & 8) == 0)
        st16(dst, (unsigned)((I.cq * 16 + I.pq) * 768 + G * 256 + (lane & 7) * 32 + (lane >> 4) * 16), __builtin_bit_cast(u32x4, d));
}

template <bool LOCAL>
struct WFirst {
    unsigned v[2];
    DI WFirst(int w, int ct, int lane) {
#pragma unroll
        for (int i = 0; i < 2; ++i) {
            const int k = 8 * i + (lane >> 3), pos = lane & 7;
            v[i] = (unsigned)k * 6144u + (unsigned)(w * 1024 + ct * 64 + ((pos >> 1) ^ (((k >> 1) & 1) | (i << 1))) * 16 + (pos & 1) * 8) * 2u;
        }
        asm volatile("" : "+v"(v[0]), "+v"(v[1]));
    }
    DI void issue(const void* W, unsigned lds0, int w) const {
#pragma unroll
        for (int i = 0; i < 16; ++i)
            glds16((const char*)W + (size_t)(i >> 1) * (16 * 6144), v[i & 1], lds0 + (i >> 3) * QC<LOCAL>::STAGE + QC<LOCAL>::ABYTES + (w * 8 + (i & 7)) * 1024);
    }
};

template <bool LOCAL, class F>
DI void qkv_loader(char* smem, char* ws, int set, u64 tag, const __hip_bfloat16* __restrict__ X, const __hip_bfloat16* __restrict__ W,
                   const float* __restrict__ statp, const char* __restrict__ csp, bool ident, const void* nextW, F tail SP_PARAM) {
    typedef QC<LOCAL> C;
    constexpr int NT = 16;
    int tid = threadIdx.x; asm volatile("" : "+v"(tid));
    const int lane = tid & 63;
    const int w = __builtin_amdgcn_readfirstlane(tid >> 6) & 3;
    const int idx = blockIdx.x >> 3;
    const Ids I = ids();
    const int panel = I.pq, ct = I.cq;
    const int R0 = panel * 128;
    const unsigned lds0 = (unsigned)(uintptr_t)smem;
    auto grow = [&](int i) -> int {
        if (!LOCAL) return R0 + i;
        if (i < 128) { const int t = (i >> 4) & 3, a = i & 15; return R0 + (i & 64) + 2 * (16 * (t & 1) + a) + (t >> 1); }
        const int a = i & 15; const int r = R0 - 16 + 2 * (a & 7) + (a >> 3);
        return r < 0 ? 0 : r;
    };
    unsigned voffA[5], voffB[6];
#pragma unroll
    for (int i = 0; i < 6; ++i) {
        const int j = w + 4 * i, G = j >> 3, k = 8 * (j & 7) + (lane >> 3);
        const int pos = lane & 7, f = ((k >> 1) & 1) | (((k >> 3) & 1) << 1);
        const int seg = (pos >> 1) ^ f;
        voffB[i] = (unsigned)k * 6144u + (unsigned)(G * 1024 + ct * 64 + seg * 16 + (pos & 1) * 8) * 2u;
    }
    auto issueW = [&](int t, int buf) {
        const char* wb = (const char*)W + (size_t)t * (64 * 6144);
        const unsigned sb = lds0 + buf * C::STAGE;
#pragma unroll
        for (int i = 0; i < 6; ++i) glds16(wb, voffB[i], sb + C::ABYTES + (w + 4 * i) * 1024);
    };
    asm volatile("" : "+v"(voffB[0]), "+v"(voffB[1]), "+v"(voffB[2]), "+v"(voffB[3]), "+v"(voffB[4]), "+v"(voffB[5]));
    const WFirst<LOCAL> first(w, ct, lane);
    const int lt = min(tid - 256, C::AROWS - 1);
    const int gr = LOCAL ? grow(lt) - R0 : ((lt & 64) + (lt & 1) * 32 + ((lt & 63) >> 1));
    unsigned sp = (unsigned)(panel * 16384 + gr * 8 - (gr < 0 ? 15360 : 0));
    lds_ptr mp = (lds_ptr)smem + C::OFF_MEAN + lt * 4, cp = (lds_ptr)smem + C::OFF_CS + lane * 12;
    asm volatile("" : "+v"(sp), "+v"(mp), "+v"(cp));
#pragma unroll
    for (int i = 0; i < 5; ++i) {
        const int j = (i < 4) ? (w + 4 * i) : (16 + (w & 1));
        const int row = 8 * j + (lane >> 3);
        const int c = (lane & 7) ^ ((row >> 1) & 7);
        voffA[i] = (unsigned)grow(row < C::AROWS ? row : 0) * 2048u + c * 16;
        asm volatile("" : "+v"(voffA[i]));
    }
    auto issueA = [&](int t, int buf) {
        const char* xa = (const char*)X + t * 128;
        const unsigned sb = lds0 + buf * C::STAGE;
        if (LOCAL && w < 2) glds16c(xa, voffA[4], sb + (16 + w) * 1024);
#pragma unroll
        for (int i = 0; i < 4; ++i) glds16c(xa, voffA[i], sb + (w + 4 * i) * 1024);
    };
    u64 land;
    if (w == 3) __builtin_amdgcn_s_setprio(3);
    signal_poll<LOCAL, false>(w - 3, ws, flag_off(set, I.po, lane & 15, I.co),
                              LOCAL ? flag_off(set, (lane & 16) && panel ? panel - 1 : panel, ct, lane & 15)
                                    : (lane < 16 ? flag_off(set, panel, ct, lane) : flag_off(9 + (lane >> 4), ct, panel, lane & 15)),
                              LOCAL ? 0xffffffffull : ~0ull, tag, land);
    if (w < 3) first.issue(W, lds0, w);
    BAR();
    STAMP(LOCAL ? 1 : 6);
    issueA(0, 0);
    issueA(1, 1);
    VM_BAR(4);
    asm volatile("" :: "v"(land));
    __builtin_amdgcn_s_setprio(0);
    STAMP(LOCAL ? 2 : 7);
    issueW(2, 2);
    issueA(2, 2);
    VM_BAR(10);
    issueA(3, 0); issueW(3, 0);
    int buf = 1;
#pragma unroll 1
    for (int t = 1; t < NT - 3; ++t) {
        VM_BAR(10);
        issueA(t + 3, buf); issueW(t + 3, buf);
        if (t == 10 && idx < 3) code_touch(idx * 4 + w, lane);
        if (LOCAL && t == 11)
            glds16(nextW, (unsigned)((idx + 32 * w) * 8 + (lane >> 3)) * 2048u +
                   (unsigned)(((I.co & 8) + ((3 - (int)((uintptr_t)nextW >> 7)) & 7)) * 128 + (lane & 7) * 16), lds0 + OFF_DUMP);
        buf = (buf == 2) ? 0 : buf + 1;
    }
    VM_BAR(10);
    typedef __attribute__((ext_vector_type(3))) float f32x3;
    f32x3 cs[16];
    f32x2 sv[16];
    const bool side = !ident && (LOCAL || w != 2);
    if (side && w == 3) {
#pragma unroll
        for (int i = 0; i < 16; i += 4)
            asm volatile("global_load_dwordx3 %0, %4, %5 sc1\n\tglobal_load_dwordx3 %1, %4, %5 offset:768 sc1\n\t"
                         "global_load_dwordx3 %2, %4, %5 offset:1536 sc1\n\tglobal_load_dwordx3 %3, %4, %5 offset:2304 sc1"
                         : "=&v"(cs[i]), "=&v"(cs[i + 1]), "=&v"(cs[i + 2]), "=&v"(cs[i + 3])
                         : "v"(lane * 12), "s"(csp + (ct * 16 + i) * 768) : "memory");
    } else if (side) {
#pragma unroll
        for (int i = 0; i < 16; i += 4)
            asm volatile("global_load_dwordx2 %0, %4, %5 sc1\n\tglobal_load_dwordx2 %1, %4, %5 offset:1024 sc1\n\t"
                         "global_load_dwordx2 %2, %4, %5 offset:2048 sc1\n\tglobal_load_dwordx2 %3, %4, %5 offset:3072 sc1"
                         : "=&v"(sv[i]), "=&v"(sv[i + 1]), "=&v"(sv[i + 2]), "=&v"(sv[i + 3])
                         : "v"(sp), "s"((const char*)statp + i * 1024) : "memory");
    }
    if (side) asm volatile("s_waitcnt vmcnt(16)" ::: "memory");
    else if (ident) tail();
    else asm volatile("s_waitcnt vmcnt(0)" ::: "memory");
    BAR();
    if (side) {
        typedef __attribute__((address_space(3))) float* lds_f;
        asm volatile("s_waitcnt vmcnt(0)" ::: "memory");
        __builtin_amdgcn_s_setprio(SPRIO);
        if (w == 3) {
#pragma unroll
            for (int i = 0; i < 16; ++i) asm volatile("" : "+v"(cs[i]));
#pragma unroll
            for (int n = 8; n; n >>= 1)
#pragma unroll
                for (int i = 0; i < n; ++i)
#pragma unroll
                    for (int e = 0; e < 3; ++e) cs[i][e] = addf(cs[i][e], cs[i + n][e]);
#pragma unroll
            for (int e = 0; e < 3; ++e) ((lds_f)cp)[e] = cs[0][e];
        } else {
#pragma unroll
            for (int i = 0; i < 16; ++i) asm volatile("" : "+v"(sv[i]));
#pragma unroll
            for (int n = 8; n; n >>= 1)
#pragma unroll
                for (int i = 0; i < n; ++i) { sv[i][0] = addf(sv[i][0], sv[i + n][0]); sv[i][1] = addf(sv[i][1], sv[i + n][1]); }
            float mean, rstd;
            ln_stats(sv[0][0], sv[0][1], mean, rstd);
            *(lds_f)mp = mean; *(lds_f)(mp + (C::OFF_RSTD - C::OFF_MEAN)) = rstd;
        }
        __builtin_amdgcn_s_setprio(0);
    }
    WAIT_LGKM_BAR();
}

template <bool LOCAL>
DI void qkv_compute(char* smem, const void* W, u32x4 (&qo4)[4], __hip_bfloat16* __restrict__ kvf, __hip_bfloat16* __restrict__ ofrag, bool ident SP_PARAM) {
    typedef QC<LOCAL> C;
    constexpr int NT = 16, ABYTES = C::ABYTES, STAGE = C::STAGE;
    int tz = threadIdx.x; asm volatile("" : "+v"(tz));
    const int lane = tz & 63;
    const int w = __builtin_amdgcn_readfirstlane(tz >> 6) & 3;
    const int wm = w & 1, wn = w >> 1;
    const Ids I = ids();
    const int panel = I.pq, ct = I.cq;
    const int l15 = lane & 15, g = lane >> 4;
    const lds_cptr sm = (lds_cptr)smem;
    lds_ptr smw = (lds_ptr)smem;

    VM_BAR(0);
    f32x4 acc[3][4][2];
    f32x4 acch[2];
    f32x2 z2 = {0, 0}; asm volatile("" : "+v"(z2));
    const f32x4 z4 = {z2[0], z2[1], z2[0], z2[1]};
#pragma unroll
    for (int a = 0; a < 3; ++a)
#pragma unroll
        for (int t = 0; t < 4; ++t) { acc[a][t][0] = z4; acc[a][t][1] = z4; }
    acch[0] = z4; acch[1] = z4;

    struct Frags { bf16x8 a[4]; bf16x8 h; s16x4 b[3][2][2]; };
    auto rd = [&](auto WM, Frags& F, lds_cptr pa, lds_cptr pb0, lds_cptr pb1, int n) {
        switch (n) {
            case 0: F.b[0][0][0] = lds_tr(pb0); break;
            case 1: F.b[0][0][1] = lds_tr(pb0 + 512); break;
            case 2: F.a[0] = lds_b128(pa); break;
            case 3: F.b[0][1][0] = lds_tr(pb1); break;
            case 4: F.b[0][1][1] = lds_tr(pb1 + 512); break;
            case 5: F.a[1] = lds_b128(pa + 2048); break;
            case 6: F.a[2] = lds_b128(pa + 4096); break;
            case 7: F.a[3] = lds_b128(pa + 6144); break;
            case 8: F.b[1][0][0] = lds_tr(pb0 + 8192); break;
            case 9: F.b[1][0][1] = lds_tr(pb0 + 8192 + 512); break;
            case 10: F.b[1][1][0] = lds_tr(pb1 + 8192); break;
            case 11: F.b[1][1][1] = lds_tr(pb1 + 8192 + 512); break;
            case 12: F.b[2][0][0] = lds_tr(pb0 + 16384); break;
            case 13: F.b[2][0][1] = lds_tr(pb0 + 16384 + 512); break;
            case 14: F.b[2][1][0] = lds_tr(pb1 + 16384); break;
            case 15: F.b[2][1][1] = lds_tr(pb1 + 16384 + 512); break;
            case 16: if (LOCAL) F.h = lds_b128(pa + 16384 - WM.value * 8192); break;
            default: break;
        }
    };
    auto half = [&](auto WM, const Frags& C, Frags& N, bool doread, lds_cptr pa, lds_cptr pb0, lds_cptr pb1, auto tail) {
#pragma unroll
        for (int G = 0; G < 3; ++G) {
            const bf16x8 b0 = cat8(C.b[G][0][0], C.b[G][0][1]), b1 = cat8(C.b[G][1][0], C.b[G][1][1]);
#pragma unroll
            for (int t = 0; t < 4; ++t) {
                const int n = G * 8 + t * 2;
                if (G < 2) acc[G][t][0] = MFMA16(b0, C.a[t], acc[G][t][0]); else acc[G][t][0] = MFMA16(C.a[t], b0, acc[G][t][0]);
                if (n == 0) SBAR();
                if (doread) rd(WM, N, pa, pb0, pb1, n);
                tail(n);
                SBAR();
                if (G < 2) acc[G][t][1] = MFMA16(b1, C.a[t], acc[G][t][1]); else acc[G][t][1] = MFMA16(C.a[t], b1, acc[G][t][1]);
                if (doread) rd(WM, N, pa, pb0, pb1, n + 1);
                tail(n + 1);
                SBAR();
            }
        }
        if (LOCAL) {
            if (WM.value == 0) { acch[0] = MFMA16(cat8(C.b[1][0][0], C.b[1][0][1]), C.h, acch[0]); acch[1] = MFMA16(cat8(C.b[1][1][0], C.b[1][1][1]), C.h, acch[1]); }
            else         { acch[0] = MFMA16(C.h, cat8(C.b[2][0][0], C.b[2][0][1]), acch[0]); acch[1] = MFMA16(C.h, cat8(C.b[2][1][0], C.b[2][1][1]), acch[1]); }
            SBAR();
        }
    };

    Frags F0, F1;
    f32x4 cq[2], ck[2], mu4[5], rs4[5]; float cv[2], mu[5], rs[5];
    u32x4 qfr[4], kfr[4];
    u32x4 vfr[4], cc;
    f32x4 mwin, mfirst;
    typedef __attribute__((address_space(3))) u32x4* lds_u4;
    volatile __attribute__((address_space(3))) unsigned* fl = (volatile __attribute__((address_space(3))) unsigned*)(smw + OFF_SYNC + 16);
    constexpr int OFF_CST = C::OFF_CS + 768;
    const bool negpos = (wm == 0) && (panel == 0);
    if (LOCAL) {
        const unsigned keepv = negpos ? __float_as_uint(NEG_MASK) : 0u;
        u32x4 mw, mf;
        bool cur[4];
#pragma unroll
        for (int r = 0; r < 4; ++r) {
            const int d = l15 - (4 * g + r);
            const unsigned out = (unsigned)(((d << 28) >> 31));
            mw[r] = out & 0xff800000u;
            mf[r] = (out & 0xff800000u) | (~out & keepv);
            cur[r] = d >= 0;
        }
        const unsigned e0 = (unsigned)(OFF_EX + wn * 6144);
        lds_u4 cst = (lds_u4)(smw + OFF_CST) + lane;
        fl[w] = 0u;
        cst[0] = mw; cst[64 + wm * 64] = mf;
        cst[192 + w * 64] = u32x4{(cur[0] ? 0xffffu : 0u) | (cur[1] ? 0xffff0000u : 0u), (cur[2] ? 0xffffu : 0u) | (cur[3] ? 0xffff0000u : 0u),
                                  e0 + (unsigned)(wm ? lane : 256 + (lane ^ 8)) * 16u, e0 + (unsigned)(wm ? 128 + lane : 320 + (lane ^ 32)) * 16u};
    }
    typedef const __attribute__((address_space(3))) f32x4* lds_f4; typedef const __attribute__((address_space(3))) float* lds_f1;
    auto mainloop = [&](auto WM) {
#pragma unroll
        for (int a = 0; a < 3; ++a)
            asm volatile("" : "+v"(acc[a][0][0]), "+v"(acc[a][0][1]), "+v"(acc[a][1][0]), "+v"(acc[a][1][1]), "+v"(acc[a][2][0]), "+v"(acc[a][2][1]),
                         "+v"(acc[a][3][0]), "+v"(acc[a][3][1]));
        int ty = threadIdx.x; asm volatile("" : "+v"(ty));
        const int l15 = ty & 15, g = (ty >> 4) & 3;
        const int swA = (l15 >> 1) & 7;
        const int q4 = l15 >> 2, p4 = l15 & 3, fk = ((q4 >> 1) & 1) | ((g & 1) << 1);
        lds_cptr pA0 = sm + wm * 8192 + l15 * 128 + ((0 + g) ^ swA) * 16, pA1 = sm + wm * 8192 + l15 * 128 + ((4 + g) ^ swA) * 16;
        lds_cptr pB0 = sm + ABYTES + (8 * g + q4) * 128 + (((2 * wn + 0) ^ fk) * 32) + p4 * 8;
        lds_cptr pB1 = sm + ABYTES + (8 * g + q4) * 128 + (((2 * wn + 1) ^ fk) * 32) + p4 * 8;
        asm volatile("" : "+v"(acch[0]), "+v"(acch[1]), "+v"(pA0), "+v"(pA1), "+v"(pB0), "+v"(pB1));
        slow_warm((unsigned)(uintptr_t)smem + OFF_DUMP, W, w, ty & 63);
        BAR();
        STAMP(LOCAL ? 1 : 7);
        BAR();
#pragma unroll
        for (int n = 0; n < 17; ++n) rd(WM, F0, pA0, pB0, pB1, n);
        WAIT_LGKM();
        int off = 0;
        lds_cptr a = pA1, b0 = pB0, b1 = pB1;
#pragma unroll QUNR
        for (int t = 0; t < NT - 1; ++t) {
            half(WM, F0, F1, true, a, b0 + 4096, b1 + 4096, [&](int n) {
                if (n == 17) { off = (off == 2 * STAGE) ? 0 : off + STAGE; asm volatile("" : "+s"(off)); }
                if (n == 19) { b0 = pB0 + off; asm volatile("" : "+v"(b0)); }
                if (n == 20) { a = pA0 + off; asm volatile("" : "+v"(a)); }
                if (n == 21) { b1 = pB1 + off; asm volatile("" : "+v"(b1)); }
            });
            WAIT_LGKM_BAR();
            half(WM, F1, F0, true, a, b0, b1, [&](int n) {
                if (n == 19) { a = pA1 + off; asm volatile("" : "+v"(a)); }
                if (n == 22) __builtin_amdgcn_s_waitcnt(0xC07F);
            });
        }
        lds_cptr s1 = sm, s4 = sm, c1 = sm, c4 = sm;
        half(WM, F0, F1, true, a, b0 + 4096, b1 + 4096, [&](int n) {
            if (n == 16) { ty = threadIdx.x; asm volatile("" : "+v"(ty)); }
            if (n == 17) { s1 = sm + C::OFF_MEAN + wm * 256 + (ty & 15) * 4; asm volatile("" : "+v"(s1)); }
            if (n == 18) { s4 = sm + C::OFF_MEAN + wm * 256 + (ty & 48); asm volatile("" : "+v"(s4)); }
            if (n == 19) { c1 = sm + C::OFF_CS + wn * 128 + (ty & 15) * 4; asm volatile("" : "+v"(c1)); }
            if (n == 20) { c4 = sm + C::OFF_CS + wn * 128 + (ty & 48); asm volatile("" : "+v"(c4)); }
        });
        WAIT_LGKM_BAR();
        half(WM, F1, F0, false, sm, sm, sm, [&](int n) {
            constexpr int RS = C::OFF_RSTD - C::OFF_MEAN, H = 512 - WM.value * 256;
            if (n < 2) { if (!LOCAL) cq[n] = *(lds_f4)(c4 + n * 64); ck[n] = *(lds_f4)(c4 + 256 + n * 64); }
            else if (n < 6) { mu[n - 2] = *(lds_f1)(s1 + (n - 2) * 64); rs[n - 2] = *(lds_f1)(s1 + RS + (n - 2) * 64); }
            else if (n < 10) { if (!LOCAL || (n & 1)) { mu4[n - 6] = *(lds_f4)(s4 + (n - 6) * 64); rs4[n - 6] = *(lds_f4)(s4 + RS + (n - 6) * 64); } }
            else if (n == 10) { cv[0] = *(lds_f1)(c1 + 512); cv[1] = *(lds_f1)(c1 + 512 + 64); }
            else if (LOCAL && n == 11) {
                if (WM.value == 0) { mu[4] = *(lds_f1)(s1 + H); rs[4] = *(lds_f1)(s1 + RS + H); }
                else { mu4[4] = *(lds_f4)(s4 + H); rs4[4] = *(lds_f4)(s4 + RS + H); }
            }
        });
        if (LOCAL) {
            auto fix = [&](int a, int t) {
#pragma unroll
                for (int f = 0; f < 2; ++f)
                    acc[a][t][f] = (a == 2) ? rs4[t] * (acc[2][t][f] + mu4[t] * cv[f]) : rs[t] * (acc[a][t][f] + mu[t] * (a ? ck[f] : cq[f]));
            };
            auto pk = [&](const f32x4 (&v)[2]) { return u32x4{pack_bf16(v[0][0], v[0][1]), pack_bf16(v[0][2], v[0][3]), pack_bf16(v[1][0], v[1][1]), pack_bf16(v[1][2], v[1][3])}; };
            cq[0] = *(lds_f4)c4; cq[1] = *(lds_f4)(c4 + 64);
            SBAR();
            if (!ident) {
                fix(1, 1); fix(1, 3); fix(2, 1); fix(2, 3);
#pragma unroll
                for (int f = 0; f < 2; ++f)
                    acch[f] = (WM.value == 0) ? rs[4] * (acch[f] + mu[4] * ck[f]) : rs4[4] * (acch[f] + mu4[4] * cv[f]);
            }
            kfr[1] = pk(acc[1][1]); kfr[3] = pk(acc[1][3]); vfr[1] = pk(acc[2][1]); vfr[3] = pk(acc[2][3]);
            lds_u4 ex = (lds_u4)(smw + OFF_EX + wn * 6144) + (ty & 63);
            if (WM.value == 0) { ex[0] = kfr[1]; ex[64] = kfr[3]; ex[128] = vfr[1]; ex[192] = vfr[3]; }
            ex[256 + WM.value * 64] = pk(acch);
#pragma unroll
            for (int t = 0; t < 4; t += 2) { mu4[t] = *(lds_f4)(s4 + t * 64); rs4[t] = *(lds_f4)(s4 + (C::OFF_RSTD - C::OFF_MEAN) + t * 64); }
            SBAR();
            if (!ident) { fix(0, 0); fix(0, 1); fix(0, 2); fix(0, 3); }
#pragma unroll
            for (int t = 0; t < 4; ++t) qfr[t] = pk(acc[0][t]);
            WAIT_LGKM();
            fl[w] = 1u;
            lds_u4 cst = (lds_u4)(smw + OFF_CST) + (ty & 63);
            mwin = __builtin_bit_cast(f32x4, cst[0]); mfirst = __builtin_bit_cast(f32x4, cst[64 + WM.value * 64]); cc = cst[192 + w * 64];
            SBAR();
            if (!ident) { fix(1, 0); fix(1, 2); fix(2, 0); fix(2, 2); }
            kfr[0] = pk(acc[1][0]); kfr[2] = pk(acc[1][2]); vfr[0] = pk(acc[2][0]); vfr[2] = pk(acc[2][2]);
        }
    };
    const int head = ct * 2 + wn;
    unsigned qo = 0, ko = 0, vo = 0;
    if (LOCAL) {
        qo = (unsigned)((((panel * 8 + wm * 4) * 32 + head) * 64 + lane) * 16);
        asm volatile("" : "+v"(qo));
    } else {
        ko = (unsigned)((wm * 8 + (g & 1)) * 1024 + ((l15 & 7) + ((l15 & 8) << 1) + 32 * (g >> 1)) * 16);
        vo = (unsigned)((wm * 8 + 2 + (g >> 1)) * 1024 + (l15 + 32 * (g & 1)) * 16);
        asm volatile("" : "+v"(ko), "+v"(vo));
    }
    if (LOCAL && wm) mainloop(IC<1>{}); else mainloop(IC<0>{});

    if (!LOCAL) {
        auto pk = [&](f32x4 a, f32x4 b) { return u32x4{pack_bf16(a[0], a[1]), pack_bf16(a[2], a[3]), pack_bf16(b[0], b[1]), pack_bf16(b[2], b[3])}; };
        char* kvp = (char*)kvf + (size_t)(head * 16 + panel) * 16384;
        lds_ptr own = smw + wn * 16384;
        SBAR();
#pragma unroll
        for (int t = 0; t < 4; ++t) {
            const u32x4 kk = pk(rs[t] * (acc[1][t][0] + mu[t] * ck[0]), rs[t] * (acc[1][t][1] + mu[t] * ck[1]));
            if (t & 1) st16o<128>(kvp + (t >> 1) * 4096, ko, kk); else st16o<0>(kvp + (t >> 1) * 4096, ko, kk);
            *(lds_u4)(own + ko + (t >> 1) * 4096 + (t & 1) * 128) = kk;
            SBAR();
            const int u = t >> 1, f = t & 1;
            const u32x4 vv = pk(rs4[2 * u] * (acc[2][2 * u][f] + mu4[2 * u] * cv[f]), rs4[2 * u + 1] * (acc[2][2 * u + 1][f] + mu4[2 * u + 1] * cv[f]));
            if (f) st16o<256>(kvp + u * 4096, vo, vv); else st16o<0>(kvp + u * 4096, vo, vv);
            *(lds_u4)(own + vo + u * 4096 + f * 256) = vv;
            SBAR();
        }
#pragma unroll
        for (int u = 0; u < 2; ++u) {
            const float r0 = rs[2 * u] * QSCALE, r1 = rs[2 * u + 1] * QSCALE;
            const u32x4 qa = pk(r0 * (acc[0][2 * u][0] + mu[2 * u] * cq[0]), r0 * (acc[0][2 * u][1] + mu[2 * u] * cq[1]));
            const u32x4 qb = pk(r1 * (acc[0][2 * u + 1][0] + mu[2 * u + 1] * cq[0]), r1 * (acc[0][2 * u + 1][1] + mu[2 * u + 1] * cq[1]));
#pragma unroll
            for (int e = 0; e < 4; ++e) {
                const auto x = __builtin_amdgcn_permlane16_swap(qa[e], qb[e], false, false);
                qo4[2 * u][e] = x[0]; qo4[2 * u + 1][e] = x[1];
            }
        }
        return;
    }
    {
        const f32x4 zero4 = {0, 0, 0, 0};
        const unsigned cm01 = cc[0], cm23 = cc[1];
#pragma unroll 1
        for (int spin = 0; spin < (1 << 22); ++spin)
            if (__builtin_amdgcn_readfirstlane(fl[w ^ 1]) != 0u) break;
        asm volatile("" ::: "memory");
        u32x4 kp[2], vp[2];
        kp[0] = *(lds_u4)(smw + cc[2]); kp[1] = *(lds_u4)(smw + (cc[2] ^ (wm ? 1024u : 128u)));
        vp[0] = *(lds_u4)(smw + cc[3]); vp[1] = *(lds_u4)(smw + (cc[3] ^ (wm ? 1024u : 512u)));
        if (negpos) { kp[0] = u32x4{0, 0, 0, 0}; kp[1] = kp[0]; }
        f32x4 sp[4], sc[4];
#pragma unroll
        for (int i = 0; i < 4; ++i) {
            const int t = (i == 0) ? 1 : (i == 1) ? 3 : (i == 2) ? 0 : 2;
            const bf16x8 qb = __builtin_bit_cast(bf16x8, qfr[t]);
            sc[t] = MFMA16(__builtin_bit_cast(bf16x8, kfr[t]), qb, mwin);
            sp[t] = MFMA16(__builtin_bit_cast(bf16x8, (t & 1) ? kfr[t - 1] : kp[t >> 1]), qb, (t & 1) ? mwin : mfirst);
        }
        SBAR();
        float x[4][4], m[4], inv[4], c2[4];
        unsigned a[4][2];
#pragma unroll
        for (int t = 0; t < 4; ++t)
#pragma unroll
            for (int h = 0; h < 2; ++h) {
                const unsigned uc = pack_bf16(sc[t][2 * h], sc[t][2 * h + 1]), up = pack_bf16(sp[t][2 * h], sp[t][2 * h + 1]);
                const unsigned cm = h ? cm23 : cm01;
                a[t][h] = (uc & cm) | (up & ~cm);
            }
        SBAR();
#pragma unroll
        for (int t = 0; t < 4; ++t)
#pragma unroll
            for (int h = 0; h < 2; ++h) { x[t][2 * h] = bf_lo(a[t][h]) * ATT_SCALE; x[t][2 * h + 1] = bf_hi(a[t][h]) * ATT_SCALE; }
        SBAR();
#pragma unroll
        for (int t = 0; t < 4; ++t)
#pragma unroll
            for (int h = 0; h < 2; ++h) a[t][h] = pack_bf16(x[t][2 * h], x[t][2 * h + 1]);
        SBAR();
#pragma unroll
        for (int t = 0; t < 4; ++t)
#pragma unroll
            for (int h = 0; h < 2; ++h) { x[t][2 * h] = bf_lo(a[t][h]); x[t][2 * h + 1] = bf_hi(a[t][h]); }
        SBAR();
#pragma unroll
        for (int t = 0; t < 4; ++t) m[t] = amax3(x[t][0], x[t][1], x[t][2]);
#pragma unroll
        for (int t = 0; t < 4; ++t) m[t] = amax3(m[t], x[t][3], x[t][3]);
        SBAR();
        asm volatile("s_nop 1");
        c2[0] = m[0]; c2[1] = m[1]; c2[2] = m[2]; c2[3] = m[3];
        asm volatile("" : "+v"(c2[0]), "+v"(c2[1]), "+v"(c2[2]), "+v"(c2[3]));
#pragma unroll
        for (int t = 0; t < 4; ++t) {
            const auto u = __builtin_amdgcn_permlane16_swap(__float_as_uint(m[t]), __float_as_uint(c2[t]), false, false);
            m[t] = amax3(__uint_as_float(u[0]), __uint_as_float(u[1]), __uint_as_float(u[1]));
        }
        SBAR();
        asm volatile("s_nop 1");
        c2[0] = m[0]; c2[1] = m[1]; c2[2] = m[2]; c2[3] = m[3];
        asm volatile("" : "+v"(c2[0]), "+v"(c2[1]), "+v"(c2[2]), "+v"(c2[3]));
#pragma unroll
        for (int t = 0; t < 4; ++t) {
            const auto u = __builtin_amdgcn_permlane32_swap(__float_as_uint(m[t]), __float_as_uint(c2[t]), false, false);
            m[t] = amax3(__uint_as_float(u[0]), __uint_as_float(u[1]), __uint_as_float(u[1]));
        }
        SBAR();
#pragma unroll
        for (int t = 0; t < 4; ++t)
#pragma unroll
            for (int r = 0; r < 4; ++r) x[t][r] = (x[t][r] - m[t]) * LOG2E;
        SBAR();
#pragma unroll
        for (int t = 0; t < 4; ++t)
#pragma unroll
            for (int r = 0; r < 4; ++r) x[t][r] = __builtin_amdgcn_exp2f(x[t][r]);
        SBAR();
#pragma unroll
        for (int t = 0; t < 4; ++t) inv[t] = (x[t][0] + x[t][1]) + (x[t][2] + x[t][3]);
        SBAR();
        c2[0] = inv[0]; c2[1] = inv[1]; c2[2] = inv[2]; c2[3] = inv[3];
        asm volatile("" : "+v"(c2[0]), "+v"(c2[1]), "+v"(c2[2]), "+v"(c2[3]));
#pragma unroll
        for (int t = 0; t < 4; ++t) {
            const auto u = __builtin_amdgcn_permlane16_swap(__float_as_uint(inv[t]), __float_as_uint(c2[t]), false, false);
            inv[t] = __uint_as_float(u[0]) + __uint_as_float(u[1]);
        }
        SBAR();
        c2[0] = inv[0]; c2[1] = inv[1]; c2[2] = inv[2]; c2[3] = inv[3];
        asm volatile("" : "+v"(c2[0]), "+v"(c2[1]), "+v"(c2[2]), "+v"(c2[3]));
#pragma unroll
        for (int t = 0; t < 4; ++t) {
            const auto u = __builtin_amdgcn_permlane32_swap(__float_as_uint(inv[t]), __float_as_uint(c2[t]), false, false);
            inv[t] = __builtin_amdgcn_rcpf(__uint_as_float(u[0]) + __uint_as_float(u[1]));
        }
        SBAR();
#pragma unroll
        for (int t = 0; t < 4; ++t)
#pragma unroll
            for (int h = 0; h < 2; ++h) a[t][h] = pack_bf16(x[t][2 * h] * inv[t], x[t][2 * h + 1] * inv[t]);
        SBAR();
        f32x4 o0[4], o1[4];
#pragma unroll
        for (int t = 0; t < 4; ++t) {
            const bf16x8 pb = __builtin_bit_cast(bf16x8, (u32x4{a[t][0] & ~cm01, a[t][1] & ~cm23, a[t][0] & cm01, a[t][1] & cm23}));
            const u32x4 vprev = (t & 1) ? vfr[t - 1] : vp[t >> 1];
            o0[t] = MFMA16(__builtin_bit_cast(bf16x8, (u32x4{vprev[0], vprev[1], vfr[t][0], vfr[t][1]})), pb, zero4);
            o1[t] = MFMA16(__builtin_bit_cast(bf16x8, (u32x4{vprev[2], vprev[3], vfr[t][2], vfr[t][3]})), pb, zero4);
        }
        SBAR();
#pragma unroll
        for (int t = 0; t < 4; ++t)
            st16(ofrag + t * 16384, qo, u32x4{pack_bf16(o0[t][0], o0[t][1]), pack_bf16(o0[t][2], o0[t][3]),
                                                 pack_bf16(o1[t][0], o1[t][1]), pack_bf16(o1[t][2], o1[t][3])});
    }
}

DI int out_row(int panel, bool localrows, int T, int a) {
    return localrows ? (panel * 128 + (T >> 2) * 64 + 2 * (16 * (T & 1) + a) + ((T >> 1) & 1)) : (panel * 128 + T * 16 + a);
}
DI int out_swz(int k) { return ((k >> 1) & 1) | (k & 4); }
DI void wo_quarter(const void* Wo, int q, unsigned ldsdst, int w, int lane) {
    const int kl = lane >> 3, co = ids().co;
    const unsigned voff = (unsigned)(w * 8 + kl) * 2048u + (unsigned)(co * 128 + ((lane & 7) ^ out_swz(kl)) * 16);
    const char* src = (const char*)Wo + (size_t)q * 524288;
#pragma unroll
    for (int i = 0; i < 8; ++i) glds16(src + i * 65536, voff, ldsdst + (w + 4 * i) * 1024);
}
DI void res_load(u32x4 (&r)[4], const void* Xin, bool localrows, int w, int lane) {
    const Ids I = ids();
#pragma unroll
    for (int i = 0; i < 4; ++i) {
        const int row = (w + 4 * i) * 8 + (lane >> 3);
        asm volatile("global_load_dwordx4 %0, %1, %2 sc1" : "=&v"(r[i])
                     : "v"((unsigned)out_row(I.po, localrows, row >> 4, row & 15) * 2048u + (unsigned)(I.co * 128 + (((lane & 7) ^ (lane >> 3)) * 16))),
                       "s"(Xin) : "memory");
    }
}
DI void res_write(u32x4 (&r)[4], char* smem, int off, int w, int lane) {
    asm volatile("" : "+v"(r[0]), "+v"(r[1]), "+v"(r[2]), "+v"(r[3]));
#pragma unroll
    for (int i = 0; i < 4; ++i) *(__attribute__((address_space(3))) u32x4*)((lds_ptr)smem + off + (w + 4 * i) * 1024 + lane * 16) = r[i];
}

template <bool AFTER_LOCAL>
DI void out_compute(char* smem, char* ws, int set, u64 tag, const __hip_bfloat16* __restrict__ ofrag,
                    __hip_bfloat16* __restrict__ Xout, float* __restrict__ statp SP_PARAM) {
    constexpr int XOFF = AFTER_LOCAL ? 131072 : 98304;
    int tz = threadIdx.x; asm volatile("" : "+v"(tz));
    const int lane = tz & 63;
    const int w = __builtin_amdgcn_readfirstlane(tz >> 6) & 3;
    const Ids I = ids();
    const int panel = I.po, ct = I.co;
    const int l15 = lane & 15, g = lane >> 4;
    auto orow = [&](int T, int a) -> int { return out_row(panel, AFTER_LOCAL, T, a); };
    const int q4 = l15 >> 2, p4 = l15 & 3;
    const int krow = 4 * g + q4;
    const int sw = out_swz(krow);
    const lds_cptr sm = (lds_cptr)smem;
    lds_cptr bw[4], xo[4];
#pragma unroll
    for (int t = 0; t < 4; ++t) {
        bw[t] = sm + krow * 128 + ((((t >> 1) * 4 + (p4 >> 1) * 2 + (t & 1)) ^ sw) * 16) + (p4 & 1) * 8;
        xo[t] = sm + XOFF + (w * 32 + l15) * 128 + ((((t >> 1) * 4 + (g >> 1) * 2 + (t & 1)) ^ (l15 & 7)) * 16) + (g & 1) * 8;
    }
    unsigned so[2], sto, ao[2];
#pragma unroll
    for (int r2 = 0; r2 < 2; ++r2) {
        so[r2] = (unsigned)orow(2 * w + r2, l15 & 7) * 2048u + (unsigned)(ct * 128 + (l15 >> 3) * 64 + g * 16);
        ao[r2] = (unsigned)((panel * 8 + 2 * w + r2) * 32768 + lane * 16);
    }
    sto = (unsigned)(((panel * 16 + ct) * 128 + 2 * w * 16 + l15) * 8);
    asm volatile("" : "+v"(bw[0]), "+v"(bw[1]), "+v"(bw[2]), "+v"(bw[3]), "+v"(xo[0]), "+v"(xo[1]), "+v"(xo[2]), "+v"(xo[3]),
                 "+v"(so[0]), "+v"(so[1]), "+v"(sto), "+v"(ao[0]), "+v"(ao[1]));
    const __amdgpu_buffer_rsrc_t ra = __builtin_amdgcn_make_buffer_rsrc((void*)ofrag, 0, 4 << 20, 0x00020000);
    asm volatile("" :: "s"(ra));
    u64 land;
    STAMP(AFTER_LOCAL ? 3 : 10);
    signal_poll<true, false>(w, ws, flag_off(set, I.pq, l15, I.cq), flag_off(set, panel, ct, l15), 0xffffull, tag, land);
    BAR();
    STAMP(AFTER_LOCAL ? 4 : 11);
#define AWIN 6
    u32x4 a[AWIN][2];
    auto lda = [&](int j) {
        a[j % AWIN][0] = __builtin_amdgcn_raw_buffer_load_b128(ra, ao[0] + (j & 3) * 1024, (j >> 2) * 4096, 16);
        a[j % AWIN][1] = __builtin_amdgcn_raw_buffer_load_b128(ra, ao[1] + (j & 3) * 1024, (j >> 2) * 4096, 16);
    };
#pragma unroll
    for (int j = 0; j < AWIN; ++j) { lda(j); SBAR(); }
    auto boff = [&](int j) { return (j & 15) * 4096 - ((!AFTER_LOCAL && j >= 24) ? 32768 : 0); };
    s16x4 F[2][4][2];
#pragma unroll
    for (int h = 0; h < 2; ++h)
#pragma unroll
        for (int t = 0; t < 4; ++t) { F[h][t][0] = lds_tr(bw[t] + boff(h)); F[h][t][1] = lds_tr(bw[t] + boff(h) + 2048); }
    f32x4 acc[2][4];
#pragma unroll
    for (int r2 = 0; r2 < 2; ++r2)
#pragma unroll
        for (int c = 0; c < 4; ++c) {
            const u32x2 xr = *(const __attribute__((address_space(3))) u32x2*)(xo[c] + r2 * 2048);
            acc[r2][c] = f32x4{bf_lo(xr[0]), bf_hi(xr[0]), bf_lo(xr[1]), bf_hi(xr[1])};
        }
    WAIT_LGKM();
#pragma unroll
    for (int j = 0; j < 32; ++j) {
        s16x4 (&C)[4][2] = F[j & 1];
        const bf16x8 a0 = __builtin_bit_cast(bf16x8, a[j % AWIN][0]), a1 = __builtin_bit_cast(bf16x8, a[j % AWIN][1]);
        const int nb = boff(j + 2);
        if (j + 2 == 16 || (!AFTER_LOCAL && j + 2 == 24)) {
#pragma unroll
            for (int t = 0; t < 4; ++t) bw[t] += 65536;
            asm volatile("" : "+v"(bw[0]), "+v"(bw[1]), "+v"(bw[2]), "+v"(bw[3]));
        }
#pragma unroll
        for (int t = 0; t < 4; ++t) {
            const bf16x8 bb = cat8(C[t][0], C[t][1]);
            acc[0][t] = MFMA16(bb, a0, acc[0][t]);
            if (t > 0 && j < 30) C[t - 1][1] = lds_tr(bw[t - 1] + nb + 2048);
            SBAR();
            acc[1][t] = MFMA16(bb, a1, acc[1][t]);
            if (j < 30) C[t][0] = lds_tr(bw[t] + nb);
            if (t == 3 && j < 30) C[3][1] = lds_tr(bw[3] + nb + 2048);
            if (t == 3 && j + AWIN < 32) lda(j + AWIN);
            SBAR();
        }
    }
    asm volatile("" :: "v"(land));
    STAMP(AFTER_LOCAL ? 5 : 12);
    u32x4 y[2][2];
#pragma unroll
    for (int r2 = 0; r2 < 2; ++r2)
#pragma unroll
        for (int u = 0; u < 2; ++u) {
            const f32x4 c0 = acc[r2][2 * u], c1 = acc[r2][2 * u + 1];
            const auto sa = __builtin_amdgcn_permlane16_swap(pack_bf16(c0[0], c0[1]), pack_bf16(c1[0], c1[1]), false, false);
            const auto sb = __builtin_amdgcn_permlane16_swap(pack_bf16(c0[2], c0[3]), pack_bf16(c1[2], c1[3]), false, false);
            y[r2][u] = u32x4{sa[0], sb[0], sa[1], sb[1]};
            if (u) {
                u32x4 za, zb;
#pragma unroll
                for (int e = 0; e < 4; ++e) {
                    za[e] = __builtin_amdgcn_update_dpp(y[r2][0][e], y[r2][1][e], 0x128, 0xF, 0xC, false);
                    zb[e] = __builtin_amdgcn_update_dpp(y[r2][1][e], y[r2][0][e], 0x128, 0xF, 0x3, false);
                }
                st16(Xout, so[r2], za);
                st16(Xout, so[r2] + (AFTER_LOCAL ? 16 : 8) * 2048, zb);
                SBAR();
            }
        }
    if (!AFTER_LOCAL && statp == nullptr) return;
    const bf16x8 ones = __builtin_bit_cast(bf16x8, (u32x4{0x3f803f80u, 0x3f803f80u, 0x3f803f80u, 0x3f803f80u}));
    const f32x4 zero4 = {0, 0, 0, 0};
    f32x4 d1[2], d2[2];
#pragma unroll
    for (int r2 = 0; r2 < 2; ++r2) {
        const bf16x8 y0 = __builtin_bit_cast(bf16x8, y[r2][0]), y1 = __builtin_bit_cast(bf16x8, y[r2][1]);
        d2[r2] = MFMA16(y0, y0, zero4); d1[r2] = MFMA16(ones, y0, zero4);
        d2[r2] = MFMA16(y1, y1, d2[r2]); d1[r2] = MFMA16(ones, y1, d1[r2]);
    }
    const bool b0 = p4 & 1, b1 = p4 & 2;
    if (g == q4) {
#pragma unroll
        for (int r2 = 0; r2 < 2; ++r2) {
            float e0 = d2[r2][0], e1 = d2[r2][1], e2 = d2[r2][2], e3 = d2[r2][3];
            asm("" : "+v"(e0), "+v"(e1), "+v"(e2), "+v"(e3));
            const float lo = b0 ? e1 : e0, hi = b0 ? e3 : e2;
            const f32x2 sv = {d1[r2][0], b1 ? hi : lo};
            if (r2) st8o<128>(statp, sto, sv); else st8o<0>(statp, sto, sv);
        }
    }
}

#define ROWSUM(p0, p1, c) __builtin_amdgcn_smfmac_f32_16x16x64_bf16(sel, __builtin_bit_cast(bf16v_t, __builtin_shufflevector(p0, p1, 0, 1, 2, 3, 4, 5, 6, 7)), c, sidx, 0, 0)
constexpr int ATT_STAGE = 32768;
typedef __attribute__((ext_vector_type(8))) unsigned u32x8;
typedef __attribute__((ext_vector_type(32))) unsigned u32x32;

DI void attn_loader(char* smem, char* ws, int set, u64 tag, const __hip_bfloat16* __restrict__ kvf, const void* Wo, const void* Xin SP_PARAM) {
    int tid = threadIdx.x; asm volatile("" : "+v"(tid));
    const int lane = tid & 63;
    const int w = __builtin_amdgcn_readfirstlane(tid >> 6) & 3;
    const Ids I = ids();
    const int panel = I.pq, ct = I.cq;
    const unsigned lds0 = (unsigned)(uintptr_t)smem;
    const char* kvh = (const char*)kvf + (size_t)(ct * 2 + (w >> 1)) * 262144 + (w & 1) * 8192;
    const unsigned voff = (unsigned)(lane * 16);
    auto order = [&](int st) { return ((panel & 8) + st - ((st - 1 < (panel & 7)) ? 1 : 0)) & 15; };
    auto load = [&](int st) {
        const char* src = kvh + (size_t)order(st) * 16384;
        const unsigned dst = lds0 + w * 8192 + (st & 3) * ATT_STAGE;
#pragma unroll
        for (int u = 0; u < 2; ++u)
            asm volatile("s_mov_b32 m0, %0\n\ts_nop 0\n\tglobal_load_lds_dwordx4 %1, %2 sc1\n\tglobal_load_lds_dwordx4 %1, %2 offset:1024 sc1\n\t"
                         "global_load_lds_dwordx4 %1, %2 offset:2048 sc1\n\tglobal_load_lds_dwordx4 %1, %2 offset:3072 sc1"
                         :: "s"(dst + u * 4096), "v"(voff), "s"(src + u * 4096) : "memory");
    };
    const unsigned poff = flag_off(set, ct, panel, lane & 15);
    u64 land;
    __builtin_amdgcn_s_setprio(3);
    BAR();
    signal_poll<false, true>(w, ws, flag_off(set, ct, lane & 15, panel), poff, 1ull << order(1), tag, land);
    STAMP(9);
    load(1);
    asm volatile("global_load_dwordx2 %0, %1, %2 sc1\n\ts_waitcnt vmcnt(0)" : "+v"(land) : "v"(poff), "s"(ws + WS_FLAG) : "memory");
    STAMP(10);
    BAR();
    if (!__all(land == tag)) flag_poll(ws, poff, tag);
    load(2); load(3);
    __builtin_amdgcn_s_setprio(0);
    STAMP(11);
#pragma unroll 1
    for (int st = 1; st < 13; ++st) {
        VM_BAR(8);
        if (st == 6) wo_quarter(Wo, 3, lds0 + 131072, w, lane);
        load(st + 3);
    }
    VM_BAR(8); wo_quarter(Wo, 0, lds0, w, lane);
    VM_BAR(8); wo_quarter(Wo, 1, lds0 + 32768, w, lane);
    u32x4 res[4];
    res_load(res, Xin, false, w, lane);
    BAR();     wo_quarter(Wo, 2, lds0 + 65536, w, lane);
    STAMP(12);
    BAR();
    asm volatile("s_waitcnt vmcnt(0)" ::: "memory");
    res_write(res, smem, 98304, w, lane);
    WAIT_LGKM_BAR();
}

DI void attn_compute(char* smem, const u32x4 (&q4)[4], const __hip_bfloat16* __restrict__ kvf, __hip_bfloat16* __restrict__ ofrag SP_PARAM) {
    int tz = threadIdx.x; asm volatile("" : "+v"(tz));
    const int lane = tz & 63;
    const int w = __builtin_amdgcn_readfirstlane(tz >> 6) & 3;
    const int wm = w & 1, wn = w >> 1;
    const Ids I = ids();
    const int panel = I.pq, head = I.cq * 2 + wn;
    const int l15 = lane & 15;
    const unsigned se = ((l15 & 2) == 0 && (l15 & 4) == 0) ? 0x3f803f80u : 0u, so = ((l15 & 2) == 0 && (l15 & 4) != 0) ? 0x3f803f80u : 0u;
    const bf8_t sel = __builtin_bit_cast(bf8_t, u32x4{se, se, so, so});
    const int sidx = (l15 & 1) ? (int)0xEEEEEEEEu : 0x44444444;
    const lds_cptr sm = (lds_cptr)smem + wn * 16384 + lane * 16;
    const f32x16 z16 = {0, 0, 0, 0, 0, 0, 0, 0, 0, 0, 0, 0, 0, 0, 0, 0};
    const bf16x8 qA0 = __builtin_bit_cast(bf16x8, q4[0]), qA1 = __builtin_bit_cast(bf16x8, q4[1]);
    const bf16x8 qB0 = __builtin_bit_cast(bf16x8, q4[2]), qB1 = __builtin_bit_cast(bf16x8, q4[3]);
    f32x16 oA, oB, nmA, nmB, sA, sB;
    f32x4 lsA, lsB;
    u32x8 pB;
    bf16x8 v11;
    asm volatile(".irp r,2,4,6,8,10,12,14,16,18,20,22,24,26,28,30,32,106,108,110,112,134,136,138,140,170,172\n\tv_mov_b64 v[\\r:\\r+1], 0\n\t.endr"
                 : "={v[2:17]}"(oA), "={v[18:33]}"(oB), "={v[106:113]}"(pB), "={v[134:137]}"(lsA), "={v[138:141]}"(lsB), "={v[170:173]}"(v11));
    WAIT_LGKM_BAR();
    STAMP(8);
    bf16x8 k00 = lds_b128(sm), k10 = lds_b128(sm + 1024), k01 = lds_b128(sm + 4096), k11 = lds_b128(sm + 4096 + 1024);
    bf16x8 v00 = lds_b128(sm + 2048), v10 = lds_b128(sm + 3072), v01 = lds_b128(sm + 4096 + 2048);
    {
        sA = MFMA32(k00, qA0, z16); sB = MFMA32(k00, qB0, z16);
        sA = MFMA32(k10, qA1, sA); sB = MFMA32(k10, qB1, sB);
        float mA = sA[0], mB = sB[0];
#pragma unroll
        for (int r = 1; r < 16; ++r) { mA = fmaxf(mA, sA[r]); mB = fmaxf(mB, sB[r]); }
        mA = max_h(mA); mB = max_h(mB);
#pragma unroll
        for (int r = 0; r < 16; ++r) { nmA[r] = -mA; nmB[r] = -mB; sA[r] -= mA; }
    }
    const unsigned a0 = (unsigned)(uintptr_t)sm;
    asm volatile(
        ".macro ATT_RD dst, y\n\t"
        "ds_read_b128 v[\\dst:\\dst+3], v[175+(((\\y)&0x1ffff)>>16)] offset:(\\y)&0xffff\n\t"
        ".endm\n\t"
        ".macro ATT_E4 c\n\t"
        "v_exp_f32 v[\\c], v[\\c]\n\tv_exp_f32 v[\\c+1], v[\\c+1]\n\tv_exp_f32 v[\\c+2], v[\\c+2]\n\tv_exp_f32 v[\\c+3], v[\\c+3]\n\t"
        ".endm\n\t"
        ".macro ATT_C4 p, c\n\t"
        "v_cvt_pk_bf16_f32 v[\\p], v[\\c], v[\\c+1]\n\tv_cvt_pk_bf16_f32 v[\\p+1], v[\\c+2], v[\\c+3]\n\t"
        "v_cvt_pk_bf16_f32 v[\\p+2], v[\\c+4], v[\\c+5]\n\tv_cvt_pk_bf16_f32 v[\\p+3], v[\\c+6], v[\\c+7]\n\t"
        ".endm\n\t"
        ".macro ATT_STEP cur, nxt, ka, kb, q, nm, oc, od, lsd, pc, pd, vd1, vc0, r0, y0, r1, y1, bar, rd\n\t"
        "v_mfma_f32_32x32x16_bf16 v[\\nxt:\\nxt+15], v[\\ka:\\ka+3], v[\\q:\\q+3], v[\\nm:\\nm+15]\n\t"
        "ATT_E4 \\cur\n\t"
        "v_mfma_f32_32x32x16_bf16 v[\\nxt:\\nxt+15], v[\\kb:\\kb+3], v[\\q+4:\\q+7], v[\\nxt:\\nxt+15]\n\t"
        "ATT_E4 \\cur+4\n\t"
        "v_mfma_f32_32x32x16_bf16 v[\\od:\\od+15], v[\\vd1:\\vd1+3], v[\\pd+4:\\pd+7], v[\\od:\\od+15]\n\t"
        "s_waitcnt lgkmcnt(0)\n\t"
        "ATT_C4 \\pc, \\cur\n\t"
        "v_exp_f32 v[\\cur+8], v[\\cur+8]\n\tv_exp_f32 v[\\cur+9], v[\\cur+9]\n\t"
        "v_mfma_f32_32x32x16_bf16 v[\\oc:\\oc+15], v[\\vc0:\\vc0+3], v[\\pc:\\pc+3], v[\\oc:\\oc+15]\n\t"
        ".if \\bar\n\ts_barrier\n\t.endif\n\t"
        "ATT_E4 \\cur+10\n\t"
        "v_smfmac_f32_16x16x64_bf16 v[\\lsd:\\lsd+3], v[130:133], v[\\pd:\\pd+7], v174\n\t"
        ".if \\rd & 4\n\tATT_RD 142, 32768\n\tATT_RD 150, 33792\n\tATT_RD 158, 34816\n\t.endif\n\t"
        ".if \\rd & 1\n\tATT_RD \\r0, \\y0\n\t.endif\n\t"
        ".if \\rd & 2\n\tATT_RD \\r1, \\y1\n\t.endif\n\t"
        "v_exp_f32 v[\\cur+14], v[\\cur+14]\n\tv_exp_f32 v[\\cur+15], v[\\cur+15]\n\t"
        "ATT_C4 \\pc+4, \\cur+8\n\t"
        ".if \\rd & 4\n\ts_waitcnt lgkmcnt(3)\n\t.endif\n\t"
        ".endm\n\t"
        ".macro ATT_TILE p, y1, y2, bar, ra=3, rb=3\n\t"
        "ATT_STEP 66, 82, 142+\\p, 150+\\p, 122, 50, 2, 18, 138, 98, 106, 170-\\p, 158+\\p, 170-\\p, \\y1+3072, 142+\\p, \\y2, \\bar, \\ra\n\t"
        "ATT_STEP 82, 66, 146-\\p, 154-\\p, 114, 34, 18, 2, 134, 106, 98, 166+\\p, 158+\\p, 158+\\p, \\y2+2048, 150+\\p, \\y2+1024, 0, \\rb\n\t"
        ".endm\n\t"
        ".macro ATT_STAGE b\n\t"
        "ATT_TILE 0, \\b+4096, \\b+8192, 0\n\t"
        "ATT_TILE 4, \\b+8192, \\b+12288, 0\n\t"
        "ATT_TILE 0, \\b+12288, \\b+32768, 1\n\t"
        "ATT_TILE 4, \\b+32768, \\b+36864, 0\n\t"
        ".endm\n\t"
        "v_add_u32 v176, 0x10000, v175\n\t"
        "s_mov_b32 s96, 4\n\t"
        "s_waitcnt vmcnt(0) lgkmcnt(0)\n\t"
        "s_barrier\n\t"
        "ATT_TILE 0, 4096, 8192, 0\n\tATT_TILE 4, 8192, 12288, 0\n\tATT_TILE 0, 12288, 0, 0, 1, 0\n\tATT_TILE 4, 32768, 36864, 1, 7, 3\n\t"
        "s_branch .Latt_entry_%=\n\t"
        ".p2align 6\n"
        ".Latt_loop_%=:\n\t"
        "ATT_STAGE 0\n"
        ".Latt_entry_%=:\n\t"
        "ATT_STAGE 32768\n\tATT_STAGE 65536\n\t"
        "s_sub_u32 s96, s96, 1\n\t"
        "ATT_STAGE 98304\n\t"
        "s_cmp_lg_u32 s96, 0\n\t"
        "s_cbranch_scc1 .Latt_loop_%=\n\t"
        "s_nop 1\n\t"
        "v_mfma_f32_32x32x16_bf16 v[18:33], v[170:173], v[110:113], v[18:33]\n\t"
        "v_smfmac_f32_16x16x64_bf16 v[138:141], v[130:133], v[106:113], v174\n\t"
        "s_waitcnt lgkmcnt(0)\n\ts_nop 10\n\t"
        ".purgem ATT_RD\n\t.purgem ATT_E4\n\t.purgem ATT_C4\n\t.purgem ATT_STEP\n\t.purgem ATT_TILE\n\t.purgem ATT_STAGE"
        : "+{v[2:17]}"(oA), "+{v[18:33]}"(oB), "+{v[66:81]}"(sA), "+{v[106:113]}"(pB), "+{v[134:137]}"(lsA), "+{v[138:141]}"(lsB),
          "+{v[142:145]}"(k00), "+{v[146:149]}"(k01), "+{v[150:153]}"(k10), "+{v[154:157]}"(k11),
          "+{v[158:161]}"(v00), "+{v[162:165]}"(v01), "+{v[166:169]}"(v10), "+{v[170:173]}"(v11)
        : "{v[34:49]}"(nmA), "{v[50:65]}"(nmB), "{v[114:117]}"(qA0), "{v[118:121]}"(qA1), "{v[122:125]}"(qB0), "{v[126:129]}"(qB1),
          "{v[130:133]}"(sel), "{v174}"(sidx), "{v175}"(a0)
        : "memory", "scc", "s96", "v176", "v82", "v83", "v84", "v85", "v86", "v87", "v88", "v89", "v90", "v91", "v92", "v93", "v94", "v95",
          "v96", "v97", "v98", "v99", "v100", "v101", "v102", "v103", "v104", "v105");
    STAMP(9);
    float lA = lsA[0] + lsA[1], lB = lsB[0] + lsB[1];
    const unsigned dst = (unsigned)((((panel * 8 + wm * 4 + ((lane & 31) >> 4)) * 32 + head) * 64 + (lane & 15) + (lane >> 5) * 16) * 16);
    auto store = [&] {
#pragma unroll
    for (int u = 0; u < 2; ++u) {
        const f32x16 e = (u ? oB : oA) * __builtin_amdgcn_rcpf(u ? lB : lA);
        st16o<0>((char*)ofrag + u * 65536, dst, u32x4{pack_bf16(e[0], e[1]), pack_bf16(e[2], e[3]), pack_bf16(e[8], e[9]), pack_bf16(e[10], e[11])});
        st16o<512>((char*)ofrag + u * 65536, dst, u32x4{pack_bf16(e[4], e[5]), pack_bf16(e[6], e[7]), pack_bf16(e[12], e[13]), pack_bf16(e[14], e[15])});
    }
    };
    store();
    {
        const f32x16 os = oA + oB;
        const float osum = (((os[0] + os[1]) + (os[2] + os[3])) + ((os[4] + os[5]) + (os[6] + os[7]))) +
                           (((os[8] + os[9]) + (os[10] + os[11])) + ((os[12] + os[13]) + (os[14] + os[15])));
        const bool bad = !(lA < 1.0e24f) || !(lB < 1.0e24f) || !(fabsf(osum) < 3.0e38f);
        if (__any(bad)) {
            const __amdgpu_buffer_rsrc_t rk = __builtin_amdgcn_make_buffer_rsrc((char*)kvf + (size_t)head * 262144, 0, 262144, 0x00020000);
            oA = z16; oB = z16; lA = 0.f; lB = 0.f;
            float mA = -INFINITY, mB = -INFINITY;
#pragma unroll 1
            for (int t = 0; t < 64; ++t) {
                bf16x8 f[4];
#pragma unroll
                for (int i = 0; i < 4; ++i) f[i] = __builtin_bit_cast(bf16x8, __builtin_amdgcn_raw_buffer_load_b128(rk, lane * 16 + i * 1024, t * 4096, 16));
                auto upd = [&](bf16x8 q0, bf16x8 q1, f32x16& o, float& l, float& m) {
                    f32x16 s = MFMA32(f[0], q0, z16); s = MFMA32(f[1], q1, s);
                    float tm = s[0];
#pragma unroll
                    for (int r = 1; r < 16; ++r) tm = fmaxf(tm, s[r]);
                    tm = max_h(tm);
                    const float mn = fmaxf(m, tm);
                    const float alpha = __builtin_amdgcn_exp2f(m - mn);
                    m = mn;
#pragma unroll
                    for (int r = 0; r < 16; ++r) { s[r] = __builtin_amdgcn_exp2f(s[r] - mn); o[r] *= alpha; }
                    const u32x4 p0 = {pack_bf16(s[0], s[1]), pack_bf16(s[2], s[3]), pack_bf16(s[4], s[5]), pack_bf16(s[6], s[7])};
                    const u32x4 p1 = {pack_bf16(s[8], s[9]), pack_bf16(s[10], s[11]), pack_bf16(s[12], s[13]), pack_bf16(s[14], s[15])};
                    f32x4 t4 = {0, 0, 0, 0};
                    o = MFMA32(f[2], __builtin_bit_cast(bf16x8, p0), o);
                    o = MFMA32(f[3], __builtin_bit_cast(bf16x8, p1), o);
                    t4 = ROWSUM(p0, p1, t4);
                    l = l * alpha + (t4[0] + t4[1]);
                };
                upd(qA0, qA1, oA, lA, mA);
                upd(qB0, qB1, oB, lB, mB);
            }
            store();
        }
    }
}

DI void ln_row(const u32x4 (&v)[2], __hip_bfloat16* __restrict__ Y, unsigned off) {
    float s1 = 0.f, s2 = 0.f;
#pragma unroll
    for (int e = 0; e < 8; ++e) s1 = dot2bf(v[e >> 2][e & 3], 0x3f803f80u, s1);
    const float mean = wave_sum(s1) * (1.f / 1024.f);
#pragma unroll
    for (int e = 0; e < 8; ++e) {
        const float d0 = bf_lo(v[e >> 2][e & 3]) - mean, d1 = bf_hi(v[e >> 2][e & 3]) - mean;
        s2 += d0 * d0 + d1 * d1;
    }
    const float mb = round_bf16(mean);
    const float rden = __builtin_amdgcn_rcpf(round_bf16(__builtin_amdgcn_sqrtf(round_bf16(round_bf16(wave_sum(s2) * (1.f / 1024.f)) + 1e-5f))));
#pragma unroll
    for (int hh = 0; hh < 2; ++hh) {
        u32x4 y;
#pragma unroll
        for (int e = 0; e < 4; ++e) {
            const unsigned d = pack_bf16(bf_lo(v[hh][e]) - mb, bf_hi(v[hh][e]) - mb);
            y[e] = pack_bf16(bf_lo(d) * rden, bf_hi(d) * rden);
        }
        if (hh) st16o<1024>(Y, off, y); else st16o<0>(Y, off, y);
    }
}

extern "C" __global__ __launch_bounds__(512, 2) void mega_kernel(const __hip_bfloat16* x, const __hip_bfloat16* wqkvA,
                                                            const __hip_bfloat16* woA, const __hip_bfloat16* wqkvB,
                                                            const __hip_bfloat16* woB, __hip_bfloat16* out,
                                                            char* ws) {
    char* const smem = smem_base;
    typedef __hip_bfloat16 bf;
    const int w8 = __builtin_amdgcn_readfirstlane(threadIdx.x >> 6);
    if (w8 == 7 && (blockIdx.x >> 3) < 10) code_touch(blockIdx.x >> 3, threadIdx.x & 63);
    asm volatile("" :: "s"(x), "s"(wqkvA), "s"(woA), "s"(wqkvB), "s"(woB), "s"(out), "s"(ws));
    STAMP_DECL; STAMPR(14); STAMP(0);
    u64 tag;
    {
        const int lane = threadIdx.x & 63;
        const Ids I = ids();
        const unsigned off = (unsigned)((I.po * 128 + I.co * 8 + w8) * 2048 + lane * 16);
        u32x4 v[2];
        asm volatile("global_load_dwordx4 %0, %2, %3\n\tglobal_load_dwordx4 %1, %2, %3 offset:1024" : "=&v"(v[0]), "=&v"(v[1]) : "v"(off), "s"(x) : "memory");
        asm volatile("s_load_dwordx2 %0, %1, 0x0" : "=s"(tag) : "s"(ws + WS_EPOCH) : "memory");
        if (w8 == 7) {
            const int l = min(lane, 12), k = l % 5;
            const unsigned o = (l >= 10 || k == 3) ? flag_off(l, I.cq, I.pq, 0) : (k == 0 || k == 2) ? flag_off(l, I.pq, I.cq, 0) : flag_off(l, I.po, I.co, 0);
            touch_at(ws + WS_FLAG, o);
        } else if (w8 >= 4 && w8 - 4 < NTOUCH) touch(wqkvA, (w8 - 4) * 256 + blockIdx.x, lane);
        if (w8 == 7 || (w8 >= 4 && w8 - 4 < NTOUCH)) asm volatile("s_waitcnt vmcnt(1)" ::: "memory"); else asm volatile("s_waitcnt vmcnt(0)" ::: "memory");
        asm volatile("" : "+v"(v[0]), "+v"(v[1]));
        ln_row(v, (bf*)(ws + WS_Y0), off);
        asm volatile("s_waitcnt lgkmcnt(0)" : "+s"(tag) :: "memory");
        tag += 1;
    }
    if (w8 >= 4) {
        int w = w8 & 3;
#pragma unroll 1
        for (int it = 0; it < 2; ++it) {
            int lane = threadIdx.x; asm volatile("" : "+v"(lane)); lane &= 63;
            asm volatile("" : "+s"(x), "+s"(wqkvA), "+s"(woA), "+s"(wqkvB), "+s"(woB), "+s"(ws), "+s"(w));
            const unsigned lds0 = (unsigned)(uintptr_t)smem;
            u32x4 ca[8], cb[8];
            qkv_loader<true>(smem, ws, it * 5, tag, (const bf*)(ws + (it ? WS_X2 : WS_Y0)), wqkvA, (const float*)(ws + WS_ST + 262144),
                             ws + WS_CSP, it == 0, woA, [&] {
                if (w < 3) {
                    colsum_load(ca, wqkvA, w, lane); colsum_load(cb, wqkvB, w, lane);
                    asm volatile("s_waitcnt vmcnt(16)" ::: "memory");
                } else {
                    touch(woA, blockIdx.x, lane);
                    asm volatile("s_waitcnt vmcnt(1)" ::: "memory");
                }
            } SP_ARG);
            STAMP(3);
#pragma unroll
            for (int q = 0; q < 4; ++q) wo_quarter(woA, q, lds0 + q * 32768, w, lane);
            u32x4 res[4];
            res_load(res, it ? (const void*)(ws + WS_X2) : x, true, w, lane);
            const bool cs = (it == 0 && w < 3);
            STAMP(4);
            BAR();
            asm volatile("s_waitcnt vmcnt(0)" ::: "memory");
            res_write(res, smem, 131072, w, lane);
            WAIT_LGKM_BAR();
            if (cs) {
                asm volatile("" : "+v"(cb[0]), "+v"(cb[1]), "+v"(cb[2]), "+v"(cb[3]), "+v"(cb[4]), "+v"(cb[5]), "+v"(cb[6]), "+v"(cb[7]));
                asm volatile("" : "+v"(ca[0]), "+v"(ca[1]), "+v"(ca[2]), "+v"(ca[3]), "+v"(ca[4]), "+v"(ca[5]), "+v"(ca[6]), "+v"(ca[7]));
                colsum_store(ca, ws + WS_CSP, w, lane);
                colsum_store(cb, ws + WS_CSP + 196608, w, lane);
                asm volatile("s_waitcnt vmcnt(0)" ::: "memory");
                flag_store(ws, 10 + w, ids().cq, ids().pq, tag, lane);
            }
            STAMP(5);
            qkv_loader<false>(smem, ws, it * 5 + 2, tag, (const bf*)(ws + (it ? WS_X3 : WS_X1)), wqkvB, (const float*)(ws + WS_ST + it * 524288), ws + WS_CSP + 196608,
                              false, nullptr, [] {} SP_ARG);
            STAMP(8);
            attn_loader(smem, ws, it * 5 + 3, tag, (const bf*)(ws + WS_KVF + it * 8 * MBy), woB, ws + (it ? WS_X3 : WS_X1) SP_ARG);
            STAMP(13); STAMPR(15); STAMP_FLUSH(it);
        }
        if (blockIdx.x == 0 && threadIdx.x == 256) __hip_atomic_store((g_u64ptr)(ws + WS_EPOCH), tag, __ATOMIC_RELAXED, __HIP_MEMORY_SCOPE_AGENT);
    } else {
        __builtin_amdgcn_s_setprio(2);
#pragma unroll 1
        for (int it = 0; it < 2; ++it) {
            u32x4 q4[4];
            asm volatile("" : "+s"(ws), "+s"(wqkvA), "+s"(wqkvB));
            qkv_compute<true>(smem, wqkvA, q4, nullptr, (bf*)(ws + WS_OF + it * 8 * MBy), it == 0 SP_ARG);
            STAMP(2);
            out_compute<true>(smem, ws, it * 5 + 1, tag, (const bf*)(ws + WS_OF + it * 8 * MBy),
                              (bf*)(ws + (it ? WS_X3 : WS_X1)), (float*)(ws + WS_ST + it * 524288) SP_ARG);
            STAMP(6);
            qkv_compute<false>(smem, wqkvB, q4, (bf*)(ws + WS_KVF + it * 8 * MBy), nullptr, false SP_ARG);
            attn_compute(smem, q4, (const bf*)(ws + WS_KVF + it * 8 * MBy), (bf*)(ws + WS_OF + (it * 8 + 4) * MBy) SP_ARG);
            out_compute<false>(smem, ws, it * 5 + 4, tag, (const bf*)(ws + WS_OF + (it * 8 + 4) * MBy),
                               it ? out : (bf*)(ws + WS_X2), it ? nullptr : (float*)(ws + WS_ST + 262144) SP_ARG);
            STAMP(13); STAMPR(15); STAMP_FLUSH(it);
        }
    }
}

extern "C" __global__ void zz_code_end_kernel() {}

extern "C" void kernel_launch(void* const* d_in, const int* in_sizes, int n_in, void* d_out, int out_size, void* d_ws,
                              size_t ws_size, hipStream_t stream) {
    typedef __hip_bfloat16 bf;
    (void)hipFuncSetAttribute((const void*)mega_kernel, hipFuncAttributeMaxDynamicSharedMemorySize, CH_LDS);
    mega_kernel<<<256, 512, CH_LDS, stream>>>((const bf*)d_in[0], (const bf*)d_in[1], (const bf*)d_in[2], (const bf*)d_in[4], (const bf*)d_in[5],
                                              (bf*)d_out, (char*)d_ws);
}
```

```cpp
#include <hip/hip_runtime.h>
#include <hip/hip_bf16.h>
#include <stdint.h>

typedef __attribute__((ext_vector_type(8))) short bf16x8;
typedef __attribute__((ext_vector_type(4))) short s16x4;
typedef __attribute__((ext_vector_type(4))) float f32x4;
typedef __attribute__((ext_vector_type(2))) float f32x2;
typedef __attribute__((ext_vector_type(16))) float f32x16;
typedef __attribute__((ext_vector_type(4))) unsigned u32x4;
typedef __attribute__((ext_vector_type(2))) unsigned u32x2;
typedef __attribute__((ext_vector_type(2))) __bf16 bf16x2_t;
typedef __attribute__((ext_vector_type(8))) __bf16 bf8_t;
typedef __attribute__((ext_vector_type(16))) __bf16 bf16v_t;
typedef __attribute__((address_space(3))) const char* lds_cptr;
typedef __attribute__((address_space(3))) char* lds_ptr;

#define DI __device__ __forceinline__
#define MFMA16(a, b, c) __builtin_amdgcn_mfma_f32_16x16x32_bf16((a), (b), (c), 0, 0, 0)
#define MFMA32(a, b, c) __builtin_amdgcn_mfma_f32_32x32x16_bf16((a), (b), (c), 0, 0, 0)
#define SBAR() __builtin_amdgcn_sched_barrier(0)
#define STAMP(i)
#define STAMPR(i)
#define STAMP_DECL
#define STAMP_FLUSH(it)
#define SP_PARAM
#define SP_ARG
typedef unsigned long long u64;
#define NTOUCH 3

constexpr float QSCALE = 0.17677669529663687f * 1.4426950408889634f;
constexpr float ATT_SCALE = 0.17677669529663687f;
constexpr float LOG2E = 1.4426950408889634f;
constexpr float NEG_MASK = -1e9f / ATT_SCALE;

DI unsigned pack_bf16(float lo, float hi) {
    f32x2 v = {lo, hi};
    bf16x2_t r = __builtin_convertvector(v, bf16x2_t);
    return __builtin_bit_cast(unsigned, r);
}
DI float bf_lo(unsigned u) { return __uint_as_float(u << 16); }
DI float bf_hi(unsigned u) { return __uint_as_float(u & 0xffff0000u); }
DI float round_bf16(float x) { return bf_lo(pack_bf16(x, 0.f)); }
DI void ln_stats(float sum, float sumsq, float& mean_out, float& rstd_out) {
    float mean = sum * (1.f / 1024.f);
    asm("" : "+v"(mean));
    const float var = fmaxf(sumsq * (1.f / 1024.f) - mean * mean, 0.f);
    mean_out = round_bf16(mean);
    rstd_out = __builtin_amdgcn_rcpf(round_bf16(__builtin_amdgcn_sqrtf(round_bf16(round_bf16(var) + 1e-5f))));
}

DI void glds16(const void* sbase, unsigned voff, unsigned ldsaddr) {
    asm volatile("s_mov_b32 m0, %0\n\ts_nop 0\n\tglobal_load_lds_dwordx4 %1, %2"
                 :: "s"(ldsaddr), "v"(voff), "s"(sbase) : "memory");
}
DI void glds16c(const void* sbase, unsigned voff, unsigned ldsaddr) {
    asm volatile("s_mov_b32 m0, %0\n\ts_nop 0\n\tglobal_load_lds_dwordx4 %1, %2 sc1"
                 :: "s"(ldsaddr), "v"(voff), "s"(sbase) : "memory");
}
DI void st16(void* sbase, unsigned voff, u32x4 v) {
    asm volatile("global_store_dwordx4 %0, %1, %2 sc1\n\ts_nop 1" :: "v"(voff), "v"(v), "s"(sbase) : "memory");
}
template <int OFF> DI void st16o(void* sbase, unsigned voff, u32x4 v) {
    asm volatile("global_store_dwordx4 %0, %1, %2 offset:%3 sc1\n\ts_nop 1" :: "v"(voff), "v"(v), "s"(sbase), "n"(OFF) : "memory");
}
DI bf16x8 lds_b128(lds_cptr p) { return *(const __attribute__((address_space(3))) bf16x8*)p; }
DI s16x4 lds_tr(lds_cptr p) {
    return __builtin_amdgcn_ds_read_tr16_b64_v4i16((__attribute__((address_space(3))) s16x4*)p);
}
DI bf16x8 cat8(s16x4 a, s16x4 b) { return __builtin_shufflevector(a, b, 0, 1, 2, 3, 4, 5, 6, 7); }
DI float dot2bf(unsigned a, unsigned b, float c) {
    return __builtin_amdgcn_fdot2_f32_bf16(__builtin_bit_cast(bf16x2_t, a), __builtin_bit_cast(bf16x2_t, b), c, false);
}
DI float sum_g(float v) {
    auto a = __builtin_amdgcn_permlane16_swap(__float_as_uint(v), __float_as_uint(v), false, false);
    v = __uint_as_float(a[0]) + __uint_as_float(a[1]);
    auto b = __builtin_amdgcn_permlane32_swap(__float_as_uint(v), __float_as_uint(v), false, false);
    return __uint_as_float(b[0]) + __uint_as_float(b[1]);
}
DI float wave_sum(float v) {
    v += __builtin_bit_cast(float, __builtin_amdgcn_update_dpp(0, __builtin_bit_cast(int, v), 0xB1, 0xF, 0xF, true));
    v += __builtin_bit_cast(float, __builtin_amdgcn_update_dpp(0, __builtin_bit_cast(int, v), 0x4E, 0xF, 0xF, true));
    v += __builtin_bit_cast(float, __builtin_amdgcn_update_dpp(0, __builtin_bit_cast(int, v), 0x141, 0xF, 0xF, true));
    v += __builtin_bit_cast(float, __builtin_amdgcn_update_dpp(0, __builtin_bit_cast(int, v), 0x140, 0xF, 0xF, true));
    return sum_g(v);
}
DI float amax3(float a, float b, float c) { float r; asm("v_max3_f32 %0, %1, %2, %3" : "=v"(r) : "v"(a), "v"(b), "v"(c)); return r; }
DI float addf(float a, float b) { float r; asm("v_add_f32 %0, %1, %2" : "=v"(r) : "v"(a), "v"(b)); return r; }
DI float max2f(float a, float b) { return __builtin_amdgcn_fmed3f(a, b, INFINITY); }
DI float max_h(float v) {
    auto b = __builtin_amdgcn_permlane32_swap(__float_as_uint(v), __float_as_uint(v), false, false);
    return max2f(__uint_as_float(b[0]), __uint_as_float(b[1]));
}

template <int OFF> DI void st8o(void* sbase, unsigned voff, f32x2 v) {
    asm volatile("global_store_dwordx2 %0, %1, %2 offset:%3 sc1\n\ts_nop 1" :: "v"(voff), "v"(v), "s"(sbase), "n"(OFF) : "memory");
}
constexpr unsigned OFF_DUMP = 159744;
extern __shared__ __attribute__((aligned(16))) char smem_base[];
DI void touch_at(const void* p, unsigned off) {
    asm volatile("s_mov_b32 m0, %0\n\ts_nop 0\n\tglobal_load_lds_dword %1, %2"
                 :: "s"((unsigned)(uintptr_t)smem_base + OFF_DUMP), "v"(off), "s"(p) : "memory");
}
DI void touch(const void* p, int slice, int lane) { touch_at(p, (unsigned)(slice * 64 + lane) * 128u); }
DI void code_touch(int slice, int lane) {
    unsigned long long c0, c1;
    asm volatile("s_getpc_b64 s[96:97]\n\ts_add_u32 s96, s96, mega_kernel@rel32@lo+4\n\ts_addc_u32 s97, s97, mega_kernel@rel32@hi+12\n\ts_mov_b64 %0, s[96:97]\n\t"
                 "s_getpc_b64 s[96:97]\n\ts_add_u32 s96, s96, zz_code_end_kernel@rel32@lo+4\n\ts_addc_u32 s97, s97, zz_code_end_kernel@rel32@hi+12\n\ts_mov_b64 %1, s[96:97]"
                 : "=s"(c0), "=s"(c1) :: "s96", "s97", "scc");
    touch_at((const void*)c0, min((unsigned)(slice * 64 + lane) * 128u, (unsigned)(c1 - c0)));
}

#define BAR() asm volatile("s_barrier" ::: "memory")
#define WAIT_LGKM() do { SBAR(); __builtin_amdgcn_s_waitcnt(0xC07F); SBAR(); } while (0)
#define WAIT_LGKM_BAR() do { SBAR(); __builtin_amdgcn_s_waitcnt(0xC07F); asm volatile("s_barrier" ::: "memory"); SBAR(); } while (0)
#define VM_BAR(n) asm volatile("s_waitcnt vmcnt(" #n ")\n\ts_barrier" ::: "memory")
constexpr int CH_LDS = 163840;
constexpr int OFF_SYNC = CH_LDS - 64;
typedef __attribute__((address_space(3))) unsigned* lds_uptr;
typedef __attribute__((address_space(1))) u64* g_u64ptr;

constexpr size_t MBy = 1 << 20;
constexpr size_t WS_X3 = 0, WS_X2 = 4 * MBy, WS_X1 = 8 * MBy, WS_OF = 12 * MBy  , WS_KVF = 36 * MBy  ,
                 WS_ST = 52 * MBy  , WS_CSP = 54 * MBy  , WS_FLAG = 55 * MBy  ,
                 WS_EPOCH = WS_FLAG + 15 * 65536, WS_Y0 = 56 * MBy;
DI unsigned flag_off(int set, int group, int consumer, int producer) { return (unsigned)(set * 65536 + group * 4096 + consumer * 256 + producer * 8); }
DI void flag_store(char* ws, int set, int group, int producer, u64 tag, int lane) {
    asm volatile("" : "+s"(tag));
    if (lane < 16) __hip_atomic_store((g_u64ptr)(ws + WS_FLAG + flag_off(set, group, lane, producer)), tag, __ATOMIC_RELAXED, __HIP_MEMORY_SCOPE_AGENT);
}
DI void flag_poll(const char* ws, unsigned off, u64 tag) {
    asm volatile("" : "+s"(tag));
#pragma unroll 1
    for (int spin = 0; spin < (1 << 16); ++spin) {
        const u64 v = __hip_atomic_load((g_u64ptr)(ws + WS_FLAG + off), __ATOMIC_RELAXED, __HIP_MEMORY_SCOPE_AGENT);
        if (__all(v == tag)) break;
        __builtin_amdgcn_s_sleep(1);
    }
    asm volatile("" ::: "memory");
}
#define POLL_INS "global_atomic_or_x2 %[land], %[po], %[zero], %[base] sc0 sc1"
template <bool DRAIN, bool ALLPOLL>
DI void signal_poll(int follower, const char* ws, unsigned soff, unsigned poff, u64 pmask, u64 tag, u64& land) {
    u64 tagv, zero;
    asm volatile("s_not_b64 s[96:97], %[ts]\n\tv_mov_b64 %[tag], %[ts]\n\tv_mov_b64 %[zero], 0\n\tv_mov_b64 %[land], s[96:97]\n\t"
                 ".if %[dr]\n\ts_waitcnt vmcnt(0)\n\t.endif\n\ts_barrier\n\t"
                 "s_cmp_lg_u32 %[fl], 0\n\ts_cbranch_scc1 sig_fol_%=\n\t"
                 "s_mov_b64 exec, 0xffff\n\t"
                 "global_store_dwordx2 %[so], %[tag], %[base] sc1\n"
                 ".if %[ap]\nsig_fol_%=:\n.endif\n\t"
                 "s_mov_b64 exec, %[pm]\n\t"
                 "s_movk_i32 s96, 0x7fff\n"
                 "sig_loop_%=:\n\t"
                 POLL_INS "\n\t"
                 "s_sleep 1\n\t"
                 "s_waitcnt vmcnt(3)\n\t"
                 "v_cmp_ne_u64 vcc, %[land], %[tag]\n\t"
                 "s_sub_u32 s96, s96, 1\n\t"
                 "s_cbranch_scc1 sig_out_%=\n\t"
                 "s_cbranch_vccnz sig_loop_%=\n"
                 "sig_out_%=:\n\t"
                 "s_mov_b64 exec, -1\n"
                 ".if !%[ap]\nsig_fol_%=:\n.endif"
                 : [land] "=&v"(land), [tag] "=&v"(tagv), [zero] "=&v"(zero)
                 : [fl] "s"(follower), [so] "v"(soff), [po] "v"(poff), [ts] "s"(tag), [base] "s"(ws + WS_FLAG), [pm] "s"(pmask), [ap] "n"(ALLPOLL ? 1 : 0), [dr] "n"(DRAIN ? 1 : 0)
                 : "memory", "scc", "vcc", "s96", "s97");
}
struct Ids { int pq, cq, po, co; };
DI Ids ids() {
    int bid = blockIdx.x; asm volatile("" : "+s"(bid));
    const int xcd = bid & 7, idx = bid >> 3;
    int pq, cq, po, co;
#define IDS_BODY pq = (xcd & 1) * 8 + (idx & 7); cq = (xcd >> 1) * 4 + (idx >> 3); po = (xcd & 3) * 4 + (idx & 3); co = (xcd >> 2) * 8 + (idx >> 2);
    IDS_BODY
    return Ids{pq, cq, po, co};
}

DI void slow_warm(unsigned dump, const void* W, int w, int lane) {
    int bid = blockIdx.x; asm volatile("" : "+s"(bid));
    const int xcd = bid & 7, idx = bid >> 3;
    const int sc = (3 - (int)((uintptr_t)W >> 7)) & 7;
    if ((sc >> 2) != ((xcd >> 1) & 1)) return;
    const char* src = (const char*)W + (idx + 32 * w) * 49152 + (sc + (xcd >> 2) * 8) * 128;
#pragma unroll
    for (int G = 0; G < 3; ++G) glds16(src + G * 2048, (unsigned)((lane >> 3) * 6144 + (lane & 7) * 16), dump);
}

template <int V> struct IC { static constexpr int value = V; };
template <bool LOCAL> struct QC {
    static constexpr int AROWS = LOCAL ? 144 : 128;
    static constexpr int ABYTES = AROWS * 128;
    static constexpr int STAGE = ABYTES + 24576;
    static constexpr int OFF_STATP = 129024;
    static constexpr int OFF_MEAN = OFF_STATP + 18432;
    static constexpr int OFF_RSTD = OFF_MEAN + 576;
    static constexpr int OFF_CS = OFF_RSTD + 576;
};
constexpr int OFF_EX = 131072;

DI void colsum_load(u32x4 (&c)[8], const void* W, int G, int lane) {
    const Ids I = ids();
    const unsigned off = (unsigned)(I.pq * 64 + (lane >> 3)) * 6144u + (unsigned)(G * 2048 + I.cq * 128 + (lane & 7) * 16);
#pragma unroll
    for (int i = 0; i < 8; ++i)
        asm volatile("global_load_dwordx4 %0, %1, %2" : "=&v"(c[i]) : "v"(off), "s"((const char*)W + i * 49152) : "memory");
}
DI void colsum_store(const u32x4 (&c)[8], char* dst, int G, int lane) {
    const Ids I = ids();
    const int m = lane & 15;
    const unsigned one = (m & 1) ? 0xbf800000u : 0xbf80u;
    const u32x4 sl = {(m >> 1) == 0 ? one : 0u, (m >> 1) == 1 ? one : 0u, (m >> 1) == 2 ? one : 0u, (m >> 1) == 3 ? one : 0u};
    f32x4 d = {0, 0, 0, 0};
#pragma unroll
    for (int i = 0; i < 8; ++i) d = MFMA16(__builtin_bit_cast(bf16x8, sl), __builtin_bit_cast(bf16x8, c[i]), d);
#pragma unroll
    for (int r = 0; r < 4; ++r)
        d[r] += __int_as_float(__builtin_amdgcn_update_dpp(0, __float_as_int(d[r]), 0x128, 0xF, 0xF, true));
    if (lane < 32 && (lane & 8) == 0)
        st16(dst, (unsigned)((I.cq * 16 + I.pq) * 768 + G * 256 + (lane & 7) * 32 + (lane >> 4) * 16), __builtin_bit_cast(u32x4, d));
}

template <bool LOCAL>
struct WFirst {
    unsigned v[2];
    DI WFirst(int w, int ct, int lane) {
#pragma unroll
        for (int i = 0; i < 2; ++i) {
            const int k = 8 * i + (lane >> 3), pos = lane & 7;
            v[i] = (unsigned)k * 6144u + (unsigned)(w * 1024 + ct * 64 + ((pos >> 1) ^ (((k >> 1) & 1) | (i << 1))) * 16 + (pos & 1) * 8) * 2u;
        }
        asm volatile("" : "+v"(v[0]), "+v"(v[1]));
    }
    DI void issue(const void* W, unsigned lds0, int w) const {
#pragma unroll
        for (int i = 0; i < 16; ++i)
            glds16((const char*)W + (size_t)(i >> 1) * (16 * 6144), v[i & 1], lds0 + (i >> 3) * QC<LOCAL>::STAGE + QC<LOCAL>::ABYTES + (w * 8 + (i & 7)) * 1024);
    }
};

template <bool LOCAL, class F>
DI void qkv_loader(char* smem, char* ws, int set, u64 tag, const __hip_bfloat16* __restrict__ X, const __hip_bfloat16* __restrict__ W,
                   const float* __restrict__ statp, const char* __restrict__ csp, bool ident, const void* nextW, F tail SP_PARAM) {
    typedef QC<LOCAL> C;
    constexpr int NT = 16;
    int tid = threadIdx.x; asm volatile("" : "+v"(tid));
    const int lane = tid & 63;
    const int w = __builtin_amdgcn_readfirstlane(tid >> 6) & 3;
    const int idx = blockIdx.x >> 3;
    const Ids I = ids();
    const int panel = I.pq, ct = I.cq;
    const int R0 = panel * 128;
    const unsigned lds0 = (unsigned)(uintptr_t)smem;
    const lds_cptr sm = (lds_cptr)smem;
    auto grow = [&](int i) -> int {
        if (!LOCAL) return R0 + i;
        if (i < 128) { const int t = (i >> 4) & 3, a = i & 15; return R0 + (i & 64) + 2 * (16 * (t & 1) + a) + (t >> 1); }
        const int a = i & 15; const int r = R0 - 16 + 2 * (a & 7) + (a >> 3);
        return r < 0 ? 0 : r;
    };
    unsigned voffA[5], voffB[6];
#pragma unroll
    for (int i = 0; i < 6; ++i) {
        const int j = w + 4 * i, G = j >> 3, k = 8 * (j & 7) + (lane >> 3);
        const int pos = lane & 7, f = ((k >> 1) & 1) | (((k >> 3) & 1) << 1);
        const int seg = (pos >> 1) ^ f;
        voffB[i] = (unsigned)k * 6144u + (unsigned)(G * 1024 + ct * 64 + seg * 16 + (pos & 1) * 8) * 2u;
    }
    auto issueW = [&](int t, int buf) {
        const char* wb = (const char*)W + (size_t)t * (64 * 6144);
        const unsigned sb = lds0 + buf * C::STAGE;
#pragma unroll
        for (int i = 0; i < 6; ++i) glds16(wb, voffB[i], sb + C::ABYTES + (w + 4 * i) * 1024);
    };
    asm volatile("" : "+v"(voffB[0]), "+v"(voffB[1]), "+v"(voffB[2]), "+v"(voffB[3]), "+v"(voffB[4]), "+v"(voffB[5]));
    const WFirst<LOCAL> first(w, ct, lane);
    const int lt = min(tid - 256, C::AROWS - 1);
    const int gr = LOCAL ? grow(lt) - R0 : ((lt & 64) + (lt & 1) * 32 + ((lt & 63) >> 1));
    lds_cptr sp = sm + C::OFF_STATP + ((lt < 128) ? gr * 8 : (16384 + (panel ? (gr + 16) : 0) * 8));
    lds_ptr mp = (lds_ptr)smem + C::OFF_MEAN + lt * 4, cp = (lds_ptr)smem + C::OFF_CS + lane * 12;
    asm volatile("" : "+v"(sp), "+v"(mp), "+v"(cp));
#pragma unroll
    for (int i = 0; i < 5; ++i) {
        const int j = (i < 4) ? (w + 4 * i) : (16 + (w & 1));
        const int row = 8 * j + (lane >> 3);
        const int c = (lane & 7) ^ ((row >> 1) & 7);
        voffA[i] = (unsigned)grow(row < C::AROWS ? row : 0) * 2048u + c * 16;
        asm volatile("" : "+v"(voffA[i]));
    }
    auto issueA = [&](int t, int buf) {
        const char* xa = (const char*)X + t * 128;
        const unsigned sb = lds0 + buf * C::STAGE;
        if (LOCAL && w < 2) glds16c(xa, voffA[4], sb + (16 + w) * 1024);
#pragma unroll
        for (int i = 0; i < 4; ++i) glds16c(xa, voffA[i], sb + (w + 4 * i) * 1024);
    };
    u64 land;
    if (w == 3) __builtin_amdgcn_s_setprio(3);
    signal_poll<LOCAL, false>(w - 3, ws, flag_off(set, I.po, lane & 15, I.co),
                              LOCAL ? flag_off(set, (lane & 16) && panel ? panel - 1 : panel, ct, lane & 15)
                                    : (lane < 16 ? flag_off(set, panel, ct, lane) : flag_off(9 + (lane >> 4), ct, panel, lane & 15)),
                              LOCAL ? 0xffffffffull : ~0ull, tag, land);
    if (w < 3) first.issue(W, lds0, w);
    BAR();
    STAMP(LOCAL ? 1 : 6);
    issueA(0, 0);
    issueA(1, 1);
    VM_BAR(4);
    asm volatile("" :: "v"(land));
    __builtin_amdgcn_s_setprio(0);
    STAMP(LOCAL ? 2 : 7);
    issueW(2, 2);
    auto issueS = [&](int i) {
        if (i == 8 && idx < 3) code_touch(idx * 4 + w, lane);
        if (LOCAL && i == 9)
            glds16(nextW, (unsigned)((idx + 32 * w) * 8 + (lane >> 3)) * 2048u +
                   (unsigned)(((I.co & 8) + ((3 - (int)((uintptr_t)nextW >> 7)) & 7)) * 128 + (lane & 7) * 16), lds0 + OFF_DUMP);
        if (ident) return;
        if (i < 4) glds16c(statp, (unsigned)((panel * 16 + w + 4 * i) * 1024 + lane * 16), lds0 + C::OFF_STATP + (w + 4 * i) * 1024);
        else if (LOCAL && i == 4 && w < 2) {
            const unsigned o = panel ? (unsigned)(((panel - 1) * 16 + w * 8 + (lane >> 3)) * 1024 + 896 + (lane & 7) * 16)
                                     : (unsigned)((w * 8 + (lane >> 3)) * 1024 + (lane & 7) * 16);
            glds16c(statp, o, lds0 + C::OFF_STATP + (16 + w) * 1024);
        }
    };
    issueA(2, 2);
    VM_BAR(10);
    typedef __attribute__((ext_vector_type(3))) float f32x3;
    f32x3 cs[16];
    if (w == 3 && !ident) {
#pragma unroll
        for (int i = 0; i < 16; i += 4)
            asm volatile("global_load_dwordx3 %0, %4, %5 sc1\n\tglobal_load_dwordx3 %1, %4, %5 offset:768 sc1\n\t"
                         "global_load_dwordx3 %2, %4, %5 offset:1536 sc1\n\tglobal_load_dwordx3 %3, %4, %5 offset:2304 sc1"
                         : "=&v"(cs[i]), "=&v"(cs[i + 1]), "=&v"(cs[i + 2]), "=&v"(cs[i + 3])
                         : "v"(lane * 12), "s"(csp + (ct * 16 + i) * 768) : "memory");
    }
    issueA(3, 0); issueW(3, 0);
    int buf = 1;
#pragma unroll 1
    for (int t = 1; t < NT - 3; ++t) {
        VM_BAR(10);
        issueA(t + 3, buf); issueW(t + 3, buf);
        if (t >= 2) issueS(t - 2);
        buf = (buf == 2) ? 0 : buf + 1;
    }
    VM_BAR(10);
    float sa = 0.f, sb = 0.f;
    auto fold = [&](int n) {
#pragma unroll
        for (int i = 0; i < n; ++i)
#pragma unroll
            for (int e = 0; e < 3; ++e) cs[i][e] = addf(cs[i][e], cs[i + n][e]);
    };
    auto rows = [&](auto STR) {
        f32x2 v[16];
#pragma unroll
        for (int i = 0; i < 16; ++i) v[i] = *(const __attribute__((address_space(3))) f32x2*)(sp + i * STR.value);
        sa = v[0][0]; sb = v[0][1];
#pragma unroll
        for (int i = 1; i < 16; ++i) { sa = addf(sa, v[i][0]); sb = addf(sb, v[i][1]); }
    };
    if (!ident) {
        if (w == 3) {
#pragma unroll
            for (int i = 0; i < 16; ++i) asm volatile("" : "+v"(cs[i]));
            fold(8);
#pragma unroll
            for (int i = 0; i < 8; ++i) asm volatile("" : "+v"(cs[i]));
        } else if (w < 2) rows(IC<1024>{});
        else if (LOCAL) rows(IC<128>{});
    }
    asm volatile("" : "+v"(sa), "+v"(sb));
    if (ident) tail(); else asm volatile("s_waitcnt vmcnt(0)" ::: "memory");
    BAR();
    if (!ident) {
        typedef __attribute__((address_space(3))) float* lds_f;
        if (w == 3) {
            fold(4); fold(2); fold(1);
#pragma unroll
            for (int e = 0; e < 3; ++e) ((lds_f)cp)[e] = cs[0][e];
        } else if (LOCAL || w < 2) {
            float mean, rstd;
            ln_stats(sa, sb, mean, rstd);
            *(lds_f)mp = mean; *(lds_f)(mp + (C::OFF_RSTD - C::OFF_MEAN)) = rstd;
        }
    }
    WAIT_LGKM_BAR();
}

template <bool LOCAL>
DI void qkv_compute(char* smem, const void* W, u32x4 (&qo4)[4], __hip_bfloat16* __restrict__ kvf, __hip_bfloat16* __restrict__ ofrag, bool ident SP_PARAM) {
    typedef QC<LOCAL> C;
    constexpr int NT = 16, ABYTES = C::ABYTES, STAGE = C::STAGE;
    int tz = threadIdx.x; asm volatile("" : "+v"(tz));
    const int lane = tz & 63;
    const int w = __builtin_amdgcn_readfirstlane(tz >> 6) & 3;
    const int wm = w & 1, wn = w >> 1;
    const Ids I = ids();
    const int panel = I.pq, ct = I.cq;
    const int l15 = lane & 15, g = lane >> 4;
    const lds_cptr sm = (lds_cptr)smem;
    lds_ptr smw = (lds_ptr)smem;

    VM_BAR(0);
    f32x4 acc[3][4][2];
    f32x4 acch[2];
    f32x2 z2 = {0, 0}; asm volatile("" : "+v"(z2));
    const f32x4 z4 = {z2[0], z2[1], z2[0], z2[1]};
#pragma unroll
    for (int a = 0; a < 3; ++a)
#pragma unroll
        for (int t = 0; t < 4; ++t) { acc[a][t][0] = z4; acc[a][t][1] = z4; }
    acch[0] = z4; acch[1] = z4;

    struct Frags { bf16x8 a[4]; bf16x8 h; s16x4 b[3][2][2]; };
    auto rd = [&](auto WM, Frags& F, lds_cptr pa, lds_cptr pb0, lds_cptr pb1, int n) {
        switch (n) {
            case 0: F.b[0][0][0] = lds_tr(pb0); break;
            case 1: F.b[0][0][1] = lds_tr(pb0 + 512); break;
            case 2: F.a[0] = lds_b128(pa); break;
            case 3: F.b[0][1][0] = lds_tr(pb1); break;
            case 4: F.b[0][1][1] = lds_tr(pb1 + 512); break;
            case 5: F.a[1] = lds_b128(pa + 2048); break;
            case 6: F.a[2] = lds_b128(pa + 4096); break;
            case 7: F.a[3] = lds_b128(pa + 6144); break;
            case 8: F.b[1][0][0] = lds_tr(pb0 + 8192); break;
            case 9: F.b[1][0][1] = lds_tr(pb0 + 8192 + 512); break;
            case 10: F.b[1][1][0] = lds_tr(pb1 + 8192); break;
            case 11: F.b[1][1][1] = lds_tr(pb1 + 8192 + 512); break;
            case 12: F.b[2][0][0] = lds_tr(pb0 + 16384); break;
            case 13: F.b[2][0][1] = lds_tr(pb0 + 16384 + 512); break;
            case 14: F.b[2][1][0] = lds_tr(pb1 + 16384); break;
            case 15: F.b[2][1][1] = lds_tr(pb1 + 16384 + 512); break;
            case 16: if (LOCAL) F.h = lds_b128(pa + 16384 - WM.value * 8192); break;
            default: break;
        }
    };
    auto half = [&](auto WM, const Frags& C, Frags& N, bool doread, lds_cptr pa, lds_cptr pb0, lds_cptr pb1, auto tail) {
#pragma unroll
        for (int G = 0; G < 3; ++G) {
            const bf16x8 b0 = cat8(C.b[G][0][0], C.b[G][0][1]), b1 = cat8(C.b[G][1][0], C.b[G][1][1]);
#pragma unroll
            for (int t = 0; t < 4; ++t) {
                const int n = G * 8 + t * 2;
                if (G < 2) acc[G][t][0] = MFMA16(b0, C.a[t], acc[G][t][0]); else acc[G][t][0] = MFMA16(C.a[t], b0, acc[G][t][0]);
                if (n == 0) SBAR();
                if (doread) rd(WM, N, pa, pb0, pb1, n);
                tail(n);
                SBAR();
                if (G < 2) acc[G][t][1] = MFMA16(b1, C.a[t], acc[G][t][1]); else acc[G][t][1] = MFMA16(C.a[t], b1, acc[G][t][1]);
                if (doread) rd(WM, N, pa, pb0, pb1, n + 1);
                tail(n + 1);
                SBAR();
            }
        }
        if (LOCAL) {
            if (WM.value == 0) { acch[0] = MFMA16(cat8(C.b[1][0][0], C.b[1][0][1]), C.h, acch[0]); acch[1] = MFMA16(cat8(C.b[1][1][0], C.b[1][1][1]), C.h, acch[1]); }
            else         { acch[0] = MFMA16(C.h, cat8(C.b[2][0][0], C.b[2][0][1]), acch[0]); acch[1] = MFMA16(C.h, cat8(C.b[2][1][0], C.b[2][1][1]), acch[1]); }
            SBAR();
        }
    };

    Frags F0, F1;
    f32x4 cq[2], ck[2], mu4[5], rs4[5]; float cv[2], mu[5], rs[5];
    u32x4 qfr[4], kfr[4];
    u32x4 vfr[4], cc;
    f32x4 mwin, mfirst;
    typedef __attribute__((address_space(3))) u32x4* lds_u4;
    volatile __attribute__((address_space(3))) unsigned* fl = (volatile __attribute__((address_space(3))) unsigned*)(smw + OFF_SYNC + 16);
    constexpr int OFF_CST = C::OFF_CS + 768;
    const bool negpos = (wm == 0) && (panel == 0);
    if (LOCAL) {
        const unsigned keepv = negpos ? __float_as_uint(NEG_MASK) : 0u;
        u32x4 mw, mf;
        bool cur[4];
#pragma unroll
        for (int r = 0; r < 4; ++r) {
            const int d = l15 - (4 * g + r);
            const unsigned out = (unsigned)(((d << 28) >> 31));
            mw[r] = out & 0xff800000u;
            mf[r] = (out & 0xff800000u) | (~out & keepv);
            cur[r] = d >= 0;
        }
        const unsigned e0 = (unsigned)(OFF_EX + wn * 6144);
        lds_u4 cst = (lds_u4)(smw + OFF_CST) + lane;
        fl[w] = 0u;
        cst[0] = mw; cst[64 + wm * 64] = mf;
        cst[192 + w * 64] = u32x4{(cur[0] ? 0xffffu : 0u) | (cur[1] ? 0xffff0000u : 0u), (cur[2] ? 0xffffu : 0u) | (cur[3] ? 0xffff0000u : 0u),
                                  e0 + (unsigned)(wm ? lane : 256 + (lane ^ 8)) * 16u, e0 + (unsigned)(wm ? 128 + lane : 320 + (lane ^ 32)) * 16u};
    }
    typedef const __attribute__((address_space(3))) f32x4* lds_f4; typedef const __attribute__((address_space(3))) float* lds_f1;
    auto mainloop = [&](auto WM) {
#pragma unroll
        for (int a = 0; a < 3; ++a)
            asm volatile("" : "+v"(acc[a][0][0]), "+v"(acc[a][0][1]), "+v"(acc[a][1][0]), "+v"(acc[a][1][1]), "+v"(acc[a][2][0]), "+v"(acc[a][2][1]),
                         "+v"(acc[a][3][0]), "+v"(acc[a][3][1]));
        int ty = threadIdx.x; asm volatile("" : "+v"(ty));
        const int l15 = ty & 15, g = (ty >> 4) & 3;
        const int swA = (l15 >> 1) & 7;
        const int q4 = l15 >> 2, p4 = l15 & 3, fk = ((q4 >> 1) & 1) | ((g & 1) << 1);
        lds_cptr pA0 = sm + wm * 8192 + l15 * 128 + ((0 + g) ^ swA) * 16, pA1 = sm + wm * 8192 + l15 * 128 + ((4 + g) ^ swA) * 16;
        lds_cptr pB0 = sm + ABYTES + (8 * g + q4) * 128 + (((2 * wn + 0) ^ fk) * 32) + p4 * 8;
        lds_cptr pB1 = sm + ABYTES + (8 * g + q4) * 128 + (((2 * wn + 1) ^ fk) * 32) + p4 * 8;
        asm volatile("" : "+v"(acch[0]), "+v"(acch[1]), "+v"(pA0), "+v"(pA1), "+v"(pB0), "+v"(pB1));
        slow_warm((unsigned)(uintptr_t)smem + OFF_DUMP, W, w, ty & 63);
        BAR();
        STAMP(LOCAL ? 1 : 7);
        BAR();
#pragma unroll
        for (int n = 0; n < 17; ++n) rd(WM, F0, pA0, pB0, pB1, n);
        WAIT_LGKM();
        int off = 0;
        lds_cptr a = pA1, b0 = pB0, b1 = pB1;
#pragma unroll 1
        for (int t = 0; t < NT - 1; ++t) {
            half(WM, F0, F1, true, a, b0 + 4096, b1 + 4096, [&](int n) {
                if (n == 17) { off = (off == 2 * STAGE) ? 0 : off + STAGE; asm volatile("" : "+s"(off)); }
                if (n == 19) { b0 = pB0 + off; asm volatile("" : "+v"(b0)); }
                if (n == 20) { a = pA0 + off; asm volatile("" : "+v"(a)); }
                if (n == 21) { b1 = pB1 + off; asm volatile("" : "+v"(b1)); }
            });
            WAIT_LGKM_BAR();
            half(WM, F1, F0, true, a, b0, b1, [&](int n) {
                if (n == 19) { a = pA1 + off; asm volatile("" : "+v"(a)); }
                if (n == 22) __builtin_amdgcn_s_waitcnt(0xC07F);
            });
        }
        lds_cptr s1 = sm, s4 = sm, c1 = sm, c4 = sm;
        half(WM, F0, F1, true, a, b0 + 4096, b1 + 4096, [&](int n) {
            if (n == 16) { ty = threadIdx.x; asm volatile("" : "+v"(ty)); }
            if (n == 17) { s1 = sm + C::OFF_MEAN + wm * 256 + (ty & 15) * 4; asm volatile("" : "+v"(s1)); }
            if (n == 18) { s4 = sm + C::OFF_MEAN + wm * 256 + (ty & 48); asm volatile("" : "+v"(s4)); }
            if (n == 19) { c1 = sm + C::OFF_CS + wn * 128 + (ty & 15) * 4; asm volatile("" : "+v"(c1)); }
            if (n == 20) { c4 = sm + C::OFF_CS + wn * 128 + (ty & 48); asm volatile("" : "+v"(c4)); }
        });
        WAIT_LGKM_BAR();
        half(WM, F1, F0, false, sm, sm, sm, [&](int n) {
            constexpr int RS = C::OFF_RSTD - C::OFF_MEAN, H = 512 - WM.value * 256;
            if (n < 2) { if (!LOCAL) cq[n] = *(lds_f4)(c4 + n * 64); ck[n] = *(lds_f4)(c4 + 256 + n * 64); }
            else if (n < 6) { mu[n - 2] = *(lds_f1)(s1 + (n - 2) * 64); rs[n - 2] = *(lds_f1)(s1 + RS + (n - 2) * 64); }
            else if (n < 10) { if (!LOCAL || (n & 1)) { mu4[n - 6] = *(lds_f4)(s4 + (n - 6) * 64); rs4[n - 6] = *(lds_f4)(s4 + RS + (n - 6) * 64); } }
            else if (n == 10) { cv[0] = *(lds_f1)(c1 + 512); cv[1] = *(lds_f1)(c1 + 512 + 64); }
            else if (LOCAL && n == 11) {
                if (WM.value == 0) { mu[4] = *(lds_f1)(s1 + H); rs[4] = *(lds_f1)(s1 + RS + H); }
                else { mu4[4] = *(lds_f4)(s4 + H); rs4[4] = *(lds_f4)(s4 + RS + H); }
            }
        });
        if (LOCAL) {
            auto fix = [&](int a, int t) {
#pragma unroll
                for (int f = 0; f < 2; ++f)
                    acc[a][t][f] = (a == 2) ? rs4[t] * (acc[2][t][f] + mu4[t] * cv[f]) : rs[t] * (acc[a][t][f] + mu[t] * (a ? ck[f] : cq[f]));
            };
            auto pk = [&](const f32x4 (&v)[2]) { return u32x4{pack_bf16(v[0][0], v[0][1]), pack_bf16(v[0][2], v[0][3]), pack_bf16(v[1][0], v[1][1]), pack_bf16(v[1][2], v[1][3])}; };
            cq[0] = *(lds_f4)c4; cq[1] = *(lds_f4)(c4 + 64);
            SBAR();
            if (!ident) {
                fix(1, 1); fix(1, 3); fix(2, 1); fix(2, 3);
#pragma unroll
                for (int f = 0; f < 2; ++f)
                    acch[f] = (WM.value == 0) ? rs[4] * (acch[f] + mu[4] * ck[f]) : rs4[4] * (acch[f] + mu4[4] * cv[f]);
            }
            kfr[1] = pk(acc[1][1]); kfr[3] = pk(acc[1][3]); vfr[1] = pk(acc[2][1]); vfr[3] = pk(acc[2][3]);
            lds_u4 ex = (lds_u4)(smw + OFF_EX + wn * 6144) + (ty & 63);
            if (WM.value == 0) { ex[0] = kfr[1]; ex[64] = kfr[3]; ex[128] = vfr[1]; ex[192] = vfr[3]; }
            ex[256 + WM.value * 64] = pk(acch);
#pragma unroll
            for (int t = 0; t < 4; t += 2) { mu4[t] = *(lds_f4)(s4 + t * 64); rs4[t] = *(lds_f4)(s4 + (C::OFF_RSTD - C::OFF_MEAN) + t * 64); }
            SBAR();
            if (!ident) { fix(0, 0); fix(0, 1); fix(0, 2); fix(0, 3); }
#pragma unroll
            for (int t = 0; t < 4; ++t) qfr[t] = pk(acc[0][t]);
            WAIT_LGKM();
            fl[w] = 1u;
            lds_u4 cst = (lds_u4)(smw + OFF_CST) + (ty & 63);
            mwin = __builtin_bit_cast(f32x4, cst[0]); mfirst = __builtin_bit_cast(f32x4, cst[64 + WM.value * 64]); cc = cst[192 + w * 64];
            SBAR();
            if (!ident) { fix(1, 0); fix(1, 2); fix(2, 0); fix(2, 2); }
            kfr[0] = pk(acc[1][0]); kfr[2] = pk(acc[1][2]); vfr[0] = pk(acc[2][0]); vfr[2] = pk(acc[2][2]);
        }
    };
    const int head = ct * 2 + wn;
    unsigned qo = 0, ko = 0, vo = 0;
    if (LOCAL) {
        qo = (unsigned)((((panel * 8 + wm * 4) * 32 + head) * 64 + lane) * 16);
        asm volatile("" : "+v"(qo));
    } else {
        ko = (unsigned)((wm * 8 + (g & 1)) * 1024 + ((l15 & 7) + ((l15 & 8) << 1) + 32 * (g >> 1)) * 16);
        vo = (unsigned)((wm * 8 + 2 + (g >> 1)) * 1024 + (l15 + 32 * (g & 1)) * 16);
        asm volatile("" : "+v"(ko), "+v"(vo));
    }
    if (LOCAL && wm) mainloop(IC<1>{}); else mainloop(IC<0>{});

    if (!LOCAL) {
        auto pk = [&](f32x4 a, f32x4 b) { return u32x4{pack_bf16(a[0], a[1]), pack_bf16(a[2], a[3]), pack_bf16(b[0], b[1]), pack_bf16(b[2], b[3])}; };
        char* kvp = (char*)kvf + (size_t)(head * 16 + panel) * 16384;
        lds_ptr own = smw + wn * 16384;
        SBAR();
#pragma unroll
        for (int t = 0; t < 4; ++t) {
            const u32x4 kk = pk(rs[t] * (acc[1][t][0] + mu[t] * ck[0]), rs[t] * (acc[1][t][1] + mu[t] * ck[1]));
            if (t & 1) st16o<128>(kvp + (t >> 1) * 4096, ko, kk); else st16o<0>(kvp + (t >> 1) * 4096, ko, kk);
            *(lds_u4)(own + ko + (t >> 1) * 4096 + (t & 1) * 128) = kk;
            SBAR();
            const int u = t >> 1, f = t & 1;
            const u32x4 vv = pk(rs4[2 * u] * (acc[2][2 * u][f] + mu4[2 * u] * cv[f]), rs4[2 * u + 1] * (acc[2][2 * u + 1][f] + mu4[2 * u + 1] * cv[f]));
            if (f) st16o<256>(kvp + u * 4096, vo, vv); else st16o<0>(kvp + u * 4096, vo, vv);
            *(lds_u4)(own + vo + u * 4096 + f * 256) = vv;
            SBAR();
        }
#pragma unroll
        for (int u = 0; u < 2; ++u) {
            const float r0 = rs[2 * u] * QSCALE, r1 = rs[2 * u + 1] * QSCALE;
            const u32x4 qa = pk(r0 * (acc[0][2 * u][0] + mu[2 * u] * cq[0]), r0 * (acc[0][2 * u][1] + mu[2 * u] * cq[1]));
            const u32x4 qb = pk(r1 * (acc[0][2 * u + 1][0] + mu[2 * u + 1] * cq[0]), r1 * (acc[0][2 * u + 1][1] + mu[2 * u + 1] * cq[1]));
#pragma unroll
            for (int e = 0; e < 4; ++e) {
                const auto x = __builtin_amdgcn_permlane16_swap(qa[e], qb[e], false, false);
                qo4[2 * u][e] = x[0]; qo4[2 * u + 1][e] = x[1];
            }
        }
        return;
    }
    {
        const f32x4 zero4 = {0, 0, 0, 0};
        const unsigned cm01 = cc[0], cm23 = cc[1];
#pragma unroll 1
        for (int spin = 0; spin < (1 << 22); ++spin)
            if (__builtin_amdgcn_readfirstlane(fl[w ^ 1]) != 0u) break;
        asm volatile("" ::: "memory");
        u32x4 kp[2], vp[2];
        kp[0] = *(lds_u4)(smw + cc[2]); kp[1] = *(lds_u4)(smw + (cc[2] ^ (wm ? 1024u : 128u)));
        vp[0] = *(lds_u4)(smw + cc[3]); vp[1] = *(lds_u4)(smw + (cc[3] ^ (wm ? 1024u : 512u)));
        if (negpos) { kp[0] = u32x4{0, 0, 0, 0}; kp[1] = kp[0]; }
        f32x4 sp[4], sc[4];
#pragma unroll
        for (int i = 0; i < 4; ++i) {
            const int t = (i == 0) ? 1 : (i == 1) ? 3 : (i == 2) ? 0 : 2;
            const bf16x8 qb = __builtin_bit_cast(bf16x8, qfr[t]);
            sc[t] = MFMA16(__builtin_bit_cast(bf16x8, kfr[t]), qb, mwin);
            sp[t] = MFMA16(__builtin_bit_cast(bf16x8, (t & 1) ? kfr[t - 1] : kp[t >> 1]), qb, (t & 1) ? mwin : mfirst);
        }
        SBAR();
        float x[4][4], m[4], inv[4], c2[4];
        unsigned a[4][2];
#pragma unroll
        for (int t = 0; t < 4; ++t)
#pragma unroll
            for (int h = 0; h < 2; ++h) {
                const unsigned uc = pack_bf16(sc[t][2 * h], sc[t][2 * h + 1]), up = pack_bf16(sp[t][2 * h], sp[t][2 * h + 1]);
                const unsigned cm = h ? cm23 : cm01;
                a[t][h] = (uc & cm) | (up & ~cm);
            }
        SBAR();
#pragma unroll
        for (int t = 0; t < 4; ++t)
#pragma unroll
            for (int h = 0; h < 2; ++h) { x[t][2 * h] = bf_lo(a[t][h]) * ATT_SCALE; x[t][2 * h + 1] = bf_hi(a[t][h]) * ATT_SCALE; }
        SBAR();
#pragma unroll
        for (int t = 0; t < 4; ++t)
#pragma unroll
            for (int h = 0; h < 2; ++h) a[t][h] = pack_bf16(x[t][2 * h], x[t][2 * h + 1]);
        SBAR();
#pragma unroll
        for (int t = 0; t < 4; ++t)
#pragma unroll
            for (int h = 0; h < 2; ++h) { x[t][2 * h] = bf_lo(a[t][h]); x[t][2 * h + 1] = bf_hi(a[t][h]); }
        SBAR();
#pragma unroll
        for (int t = 0; t < 4; ++t) m[t] = amax3(x[t][0], x[t][1], x[t][2]);
#pragma unroll
        for (int t = 0; t < 4; ++t) m[t] = amax3(m[t], x[t][3], x[t][3]);
        SBAR();
        asm volatile("s_nop 1");
        c2[0] = m[0]; c2[1] = m[1]; c2[2] = m[2]; c2[3] = m[3];
        asm volatile("" : "+v"(c2[0]), "+v"(c2[1]), "+v"(c2[2]), "+v"(c2[3]));
#pragma unroll
        for (int t = 0; t < 4; ++t) {
            const auto u = __builtin_amdgcn_permlane16_swap(__float_as_uint(m[t]), __float_as_uint(c2[t]), false, false);
            m[t] = amax3(__uint_as_float(u[0]), __uint_as_float(u[1]), __uint_as_float(u[1]));
        }
        SBAR();
        asm volatile("s_nop 1");
        c2[0] = m[0]; c2[1] = m[1]; c2[2] = m[2]; c2[3] = m[3];
        asm volatile("" : "+v"(c2[0]), "+v"(c2[1]), "+v"(c2[2]), "+v"(c2[3]));
#pragma unroll
        for (int t = 0; t < 4; ++t) {
            const auto u = __builtin_amdgcn_permlane32_swap(__float_as_uint(m[t]), __float_as_uint(c2[t]), false, false);
            m[t] = amax3(__uint_as_float(u[0]), __uint_as_float(u[1]), __uint_as_float(u[1]));
        }
        SBAR();
#pragma unroll
        for (int t = 0; t < 4; ++t)
#pragma unroll
            for (int r = 0; r < 4; ++r) x[t][r] = (x[t][r] - m[t]) * LOG2E;
        SBAR();
#pragma unroll
        for (int t = 0; t < 4; ++t)
#pragma unroll
            for (int r = 0; r < 4; ++r) x[t][r] = __builtin_amdgcn_exp2f(x[t][r]);
        SBAR();
#pragma unroll
        for (int t = 0; t < 4; ++t) inv[t] = (x[t][0] + x[t][1]) + (x[t][2] + x[t][3]);
        SBAR();
        c2[0] = inv[0]; c2[1] = inv[1]; c2[2] = inv[2]; c2[3] = inv[3];
        asm volatile("" : "+v"(c2[0]), "+v"(c2[1]), "+v"(c2[2]), "+v"(c2[3]));
#pragma unroll
        for (int t = 0; t < 4; ++t) {
            const auto u = __builtin_amdgcn_permlane16_swap(__float_as_uint(inv[t]), __float_as_uint(c2[t]), false, false);
            inv[t] = __uint_as_float(u[0]) + __uint_as_float(u[1]);
        }
        SBAR();
        c2[0] = inv[0]; c2[1] = inv[1]; c2[2] = inv[2]; c2[3] = inv[3];
        asm volatile("" : "+v"(c2[0]), "+v"(c2[1]), "+v"(c2[2]), "+v"(c2[3]));
#pragma unroll
        for (int t = 0; t < 4; ++t) {
            const auto u = __builtin_amdgcn_permlane32_swap(__float_as_uint(inv[t]), __float_as_uint(c2[t]), false, false);
            inv[t] = __builtin_amdgcn_rcpf(__uint_as_float(u[0]) + __uint_as_float(u[1]));
        }
        SBAR();
#pragma unroll
        for (int t = 0; t < 4; ++t)
#pragma unroll
            for (int h = 0; h < 2; ++h) a[t][h] = pack_bf16(x[t][2 * h] * inv[t], x[t][2 * h + 1] * inv[t]);
        SBAR();
        f32x4 o0[4], o1[4];
#pragma unroll
        for (int t = 0; t < 4; ++t) {
            const bf16x8 pb = __builtin_bit_cast(bf16x8, (u32x4{a[t][0] & ~cm01, a[t][1] & ~cm23, a[t][0] & cm01, a[t][1] & cm23}));
            const u32x4 vprev = (t & 1) ? vfr[t - 1] : vp[t >> 1];
            o0[t] = MFMA16(__builtin_bit_cast(bf16x8, (u32x4{vprev[0], vprev[1], vfr[t][0], vfr[t][1]})), pb, zero4);
            o1[t] = MFMA16(__builtin_bit_cast(bf16x8, (u32x4{vprev[2], vprev[3], vfr[t][2], vfr[t][3]})), pb, zero4);
        }
        SBAR();
#pragma unroll
        for (int t = 0; t < 4; ++t)
            st16(ofrag + t * 16384, qo, u32x4{pack_bf16(o0[t][0], o0[t][1]), pack_bf16(o0[t][2], o0[t][3]),
                                                 pack_bf16(o1[t][0], o1[t][1]), pack_bf16(o1[t][2], o1[t][3])});
    }
}

DI int out_row(int panel, bool localrows, int T, int a) {
    return localrows ? (panel * 128 + (T >> 2) * 64 + 2 * (16 * (T & 1) + a) + ((T >> 1) & 1)) : (panel * 128 + T * 16 + a);
}
DI int out_swz(int k) { return ((k >> 1) & 1) | (k & 4); }
DI void wo_quarter(const void* Wo, int q, unsigned ldsdst, int w, int lane) {
    const int kl = lane >> 3, co = ids().co;
    const unsigned voff = (unsigned)(w * 8 + kl) * 2048u + (unsigned)(co * 128 + ((lane & 7) ^ out_swz(kl)) * 16);
    const char* src = (const char*)Wo + (size_t)q * 524288;
#pragma unroll
    for (int i = 0; i < 8; ++i) glds16(src + i * 65536, voff, ldsdst + (w + 4 * i) * 1024);
}
DI void res_load(u32x4 (&r)[4], const void* Xin, bool localrows, int w, int lane) {
    const Ids I = ids();
#pragma unroll
    for (int i = 0; i < 4; ++i) {
        const int row = (w + 4 * i) * 8 + (lane >> 3);
        asm volatile("global_load_dwordx4 %0, %1, %2 sc1" : "=&v"(r[i])
                     : "v"((unsigned)out_row(I.po, localrows, row >> 4, row & 15) * 2048u + (unsigned)(I.co * 128 + (((lane & 7) ^ (lane >> 3)) * 16))),
                       "s"(Xin) : "memory");
    }
}
DI void res_write(u32x4 (&r)[4], char* smem, int off, int w, int lane) {
    asm volatile("" : "+v"(r[0]), "+v"(r[1]), "+v"(r[2]), "+v"(r[3]));
#pragma unroll
    for (int i = 0; i < 4; ++i) *(__attribute__((address_space(3))) u32x4*)((lds_ptr)smem + off + (w + 4 * i) * 1024 + lane * 16) = r[i];
}

template <bool AFTER_LOCAL>
DI void out_compute(char* smem, char* ws, int set, u64 tag, const __hip_bfloat16* __restrict__ ofrag,
                    __hip_bfloat16* __restrict__ Xout, float* __restrict__ statp SP_PARAM) {
    constexpr int XOFF = AFTER_LOCAL ? 131072 : 98304;
    int tz = threadIdx.x; asm volatile("" : "+v"(tz));
    const int lane = tz & 63;
    const int w = __builtin_amdgcn_readfirstlane(tz >> 6) & 3;
    const Ids I = ids();
    const int panel = I.po, ct = I.co;
    const int l15 = lane & 15, g = lane >> 4;
    auto orow = [&](int T, int a) -> int { return out_row(panel, AFTER_LOCAL, T, a); };
    const int q4 = l15 >> 2, p4 = l15 & 3;
    const int krow = 4 * g + q4;
    const int sw = out_swz(krow);
    const lds_cptr sm = (lds_cptr)smem;
    lds_cptr bw[4], xo[4];
#pragma unroll
    for (int t = 0; t < 4; ++t) {
        bw[t] = sm + krow * 128 + ((((t >> 1) * 4 + (p4 >> 1) * 2 + (t & 1)) ^ sw) * 16) + (p4 & 1) * 8;
        xo[t] = sm + XOFF + (w * 32 + l15) * 128 + ((((t >> 1) * 4 + (g >> 1) * 2 + (t & 1)) ^ (l15 & 7)) * 16) + (g & 1) * 8;
    }
    unsigned so[2], sto, ao[2];
#pragma unroll
    for (int r2 = 0; r2 < 2; ++r2) {
        so[r2] = (unsigned)orow(2 * w + r2, l15 & 7) * 2048u + (unsigned)(ct * 128 + (l15 >> 3) * 64 + g * 16);
        ao[r2] = (unsigned)((panel * 8 + 2 * w + r2) * 32768 + lane * 16);
    }
    sto = (unsigned)(((panel * 16 + ct) * 128 + 2 * w * 16 + l15) * 8);
    asm volatile("" : "+v"(bw[0]), "+v"(bw[1]), "+v"(bw[2]), "+v"(bw[3]), "+v"(xo[0]), "+v"(xo[1]), "+v"(xo[2]), "+v"(xo[3]),
                 "+v"(so[0]), "+v"(so[1]), "+v"(sto), "+v"(ao[0]), "+v"(ao[1]));
    const __amdgpu_buffer_rsrc_t ra = __builtin_amdgcn_make_buffer_rsrc((void*)ofrag, 0, 4 << 20, 0x00020000);
    asm volatile("" :: "s"(ra));
    u64 land;
    STAMP(AFTER_LOCAL ? 3 : 10);
    signal_poll<true, false>(w, ws, flag_off(set, I.pq, l15, I.cq), flag_off(set, panel, ct, l15), 0xffffull, tag, land);
    BAR();
    STAMP(AFTER_LOCAL ? 4 : 11);
#define AWIN 6
    u32x4 a[AWIN][2];
    auto lda = [&](int j) {
        a[j % AWIN][0] = __builtin_amdgcn_raw_buffer_load_b128(ra, ao[0] + (j & 3) * 1024, (j >> 2) * 4096, 16);
        a[j % AWIN][1] = __builtin_amdgcn_raw_buffer_load_b128(ra, ao[1] + (j & 3) * 1024, (j >> 2) * 4096, 16);
    };
#pragma unroll
    for (int j = 0; j < AWIN; ++j) { lda(j); SBAR(); }
    auto boff = [&](int j) { return (j & 15) * 4096 - ((!AFTER_LOCAL && j >= 24) ? 32768 : 0); };
    s16x4 F[2][4][2];
#pragma unroll
    for (int h = 0; h < 2; ++h)
#pragma unroll
        for (int t = 0; t < 4; ++t) { F[h][t][0] = lds_tr(bw[t] + boff(h)); F[h][t][1] = lds_tr(bw[t] + boff(h) + 2048); }
    f32x4 acc[2][4];
#pragma unroll
    for (int r2 = 0; r2 < 2; ++r2)
#pragma unroll
        for (int c = 0; c < 4; ++c) {
            const u32x2 xr = *(const __attribute__((address_space(3))) u32x2*)(xo[c] + r2 * 2048);
            acc[r2][c] = f32x4{bf_lo(xr[0]), bf_hi(xr[0]), bf_lo(xr[1]), bf_hi(xr[1])};
        }
    WAIT_LGKM();
#pragma unroll
    for (int j = 0; j < 32; ++j) {
        s16x4 (&C)[4][2] = F[j & 1];
        const bf16x8 a0 = __builtin_bit_cast(bf16x8, a[j % AWIN][0]), a1 = __builtin_bit_cast(bf16x8, a[j % AWIN][1]);
        const int nb = boff(j + 2);
        if (j + 2 == 16 || (!AFTER_LOCAL && j + 2 == 24)) {
#pragma unroll
            for (int t = 0; t < 4; ++t) bw[t] += 65536;
            asm volatile("" : "+v"(bw[0]), "+v"(bw[1]), "+v"(bw[2]), "+v"(bw[3]));
        }
#pragma unroll
        for (int t = 0; t < 4; ++t) {
            const bf16x8 bb = cat8(C[t][0], C[t][1]);
            acc[0][t] = MFMA16(bb, a0, acc[0][t]);
            if (t > 0 && j < 30) C[t - 1][1] = lds_tr(bw[t - 1] + nb + 2048);
            SBAR();
            acc[1][t] = MFMA16(bb, a1, acc[1][t]);
            if (j < 30) C[t][0] = lds_tr(bw[t] + nb);
            if (t == 3 && j < 30) C[3][1] = lds_tr(bw[3] + nb + 2048);
            if (t == 3 && j + AWIN < 32) lda(j + AWIN);
            SBAR();
        }
    }
    asm volatile("" :: "v"(land));
    STAMP(AFTER_LOCAL ? 5 : 12);
    u32x4 y[2][2];
#pragma unroll
    for (int r2 = 0; r2 < 2; ++r2)
#pragma unroll
        for (int u = 0; u < 2; ++u) {
            const f32x4 c0 = acc[r2][2 * u], c1 = acc[r2][2 * u + 1];
            const auto sa = __builtin_amdgcn_permlane16_swap(pack_bf16(c0[0], c0[1]), pack_bf16(c1[0], c1[1]), false, false);
            const auto sb = __builtin_amdgcn_permlane16_swap(pack_bf16(c0[2], c0[3]), pack_bf16(c1[2], c1[3]), false, false);
            y[r2][u] = u32x4{sa[0], sb[0], sa[1], sb[1]};
            if (u) {
                u32x4 za, zb;
#pragma unroll
                for (int e = 0; e < 4; ++e) {
                    za[e] = __builtin_amdgcn_update_dpp(y[r2][0][e], y[r2][1][e], 0x128, 0xF, 0xC, false);
                    zb[e] = __builtin_amdgcn_update_dpp(y[r2][1][e], y[r2][0][e], 0x128, 0xF, 0x3, false);
                }
                st16(Xout, so[r2], za);
                st16(Xout, so[r2] + (AFTER_LOCAL ? 16 : 8) * 2048, zb);
                SBAR();
            }
        }
    if (!AFTER_LOCAL && statp == nullptr) return;
    const bf16x8 ones = __builtin_bit_cast(bf16x8, (u32x4{0x3f803f80u, 0x3f803f80u, 0x3f803f80u, 0x3f803f80u}));
    const f32x4 zero4 = {0, 0, 0, 0};
    f32x4 d1[2], d2[2];
#pragma unroll
    for (int r2 = 0; r2 < 2; ++r2) {
        const bf16x8 y0 = __builtin_bit_cast(bf16x8, y[r2][0]), y1 = __builtin_bit_cast(bf16x8, y[r2][1]);
        d2[r2] = MFMA16(y0, y0, zero4); d1[r2] = MFMA16(ones, y0, zero4);
        d2[r2] = MFMA16(y1, y1, d2[r2]); d1[r2] = MFMA16(ones, y1, d1[r2]);
    }
    const bool b0 = p4 & 1, b1 = p4 & 2;
    if (g == q4) {
#pragma unroll
        for (int r2 = 0; r2 < 2; ++r2) {
            float e0 = d2[r2][0], e1 = d2[r2][1], e2 = d2[r2][2], e3 = d2[r2][3];
            asm("" : "+v"(e0), "+v"(e1), "+v"(e2), "+v"(e3));
            const float lo = b0 ? e1 : e0, hi = b0 ? e3 : e2;
            const f32x2 sv = {d1[r2][0], b1 ? hi : lo};
            if (r2) st8o<128>(statp, sto, sv); else st8o<0>(statp, sto, sv);
        }
    }
}

#define ROWSUM(p0, p1, c) __builtin_amdgcn_smfmac_f32_16x16x64_bf16(sel, __builtin_bit_cast(bf16v_t, __builtin_shufflevector(p0, p1, 0, 1, 2, 3, 4, 5, 6, 7)), c, sidx, 0, 0)
constexpr int ATT_STAGE = 32768;
typedef __attribute__((ext_vector_type(8))) unsigned u32x8;
typedef __attribute__((ext_vector_type(32))) unsigned u32x32;

DI void attn_loader(char* smem, char* ws, int set, u64 tag, const __hip_bfloat16* __restrict__ kvf, const void* Wo, const void* Xin SP_PARAM) {
    int tid = threadIdx.x; asm volatile("" : "+v"(tid));
    const int lane = tid & 63;
    const int w = __builtin_amdgcn_readfirstlane(tid >> 6) & 3;
    const Ids I = ids();
    const int panel = I.pq, ct = I.cq;
    const unsigned lds0 = (unsigned)(uintptr_t)smem;
    const char* kvh = (const char*)kvf + (size_t)(ct * 2 + (w >> 1)) * 262144 + (w & 1) * 8192;
    const unsigned voff = (unsigned)(lane * 16);
    auto order = [&](int st) { return ((panel & 8) + st - ((st - 1 < (panel & 7)) ? 1 : 0)) & 15; };
    auto load = [&](int st) {
        const char* src = kvh + (size_t)order(st) * 16384;
        const unsigned dst = lds0 + w * 8192 + (st & 3) * ATT_STAGE;
#pragma unroll
        for (int u = 0; u < 2; ++u)
            asm volatile("s_mov_b32 m0, %0\n\ts_nop 0\n\tglobal_load_lds_dwordx4 %1, %2 sc1\n\tglobal_load_lds_dwordx4 %1, %2 offset:1024 sc1\n\t"
                         "global_load_lds_dwordx4 %1, %2 offset:2048 sc1\n\tglobal_load_lds_dwordx4 %1, %2 offset:3072 sc1"
                         :: "s"(dst + u * 4096), "v"(voff), "s"(src + u * 4096) : "memory");
    };
    const unsigned poff = flag_off(set, ct, panel, lane & 15);
    u64 land;
    __builtin_amdgcn_s_setprio(3);
    BAR();
    signal_poll<false, true>(w, ws, flag_off(set, ct, lane & 15, panel), poff, 1ull << order(1), tag, land);
    STAMP(9);
    load(1);
    asm volatile("global_load_dwordx2 %0, %1, %2 sc1\n\ts_waitcnt vmcnt(0)" : "+v"(land) : "v"(poff), "s"(ws + WS_FLAG) : "memory");
    STAMP(10);
    BAR();
    if (!__all(land == tag)) flag_poll(ws, poff, tag);
    load(2); load(3);
    __builtin_amdgcn_s_setprio(0);
    STAMP(11);
#pragma unroll 1
    for (int st = 1; st < 13; ++st) {
        VM_BAR(8);
        if (st == 6) wo_quarter(Wo, 3, lds0 + 131072, w, lane);
        load(st + 3);
    }
    VM_BAR(8); wo_quarter(Wo, 0, lds0, w, lane);
    VM_BAR(8); wo_quarter(Wo, 1, lds0 + 32768, w, lane);
    u32x4 res[4];
    res_load(res, Xin, false, w, lane);
    BAR();     wo_quarter(Wo, 2, lds0 + 65536, w, lane);
    STAMP(12);
    BAR();
    asm volatile("s_waitcnt vmcnt(0)" ::: "memory");
    res_write(res, smem, 98304, w, lane);
    WAIT_LGKM_BAR();
}

DI void attn_compute(char* smem, const u32x4 (&q4)[4], const __hip_bfloat16* __restrict__ kvf, __hip_bfloat16* __restrict__ ofrag SP_PARAM) {
    int tz = threadIdx.x; asm volatile("" : "+v"(tz));
    const int lane = tz & 63;
    const int w = __builtin_amdgcn_readfirstlane(tz >> 6) & 3;
    const int wm = w & 1, wn = w >> 1;
    const Ids I = ids();
    const int panel = I.pq, head = I.cq * 2 + wn;
    const int l15 = lane & 15;
    const unsigned se = ((l15 & 2) == 0 && (l15 & 4) == 0) ? 0x3f803f80u : 0u, so = ((l15 & 2) == 0 && (l15 & 4) != 0) ? 0x3f803f80u : 0u;
    const bf8_t sel = __builtin_bit_cast(bf8_t, u32x4{se, se, so, so});
    const int sidx = (l15 & 1) ? (int)0xEEEEEEEEu : 0x44444444;
    const lds_cptr sm = (lds_cptr)smem + wn * 16384 + lane * 16;
    const f32x16 z16 = {0, 0, 0, 0, 0, 0, 0, 0, 0, 0, 0, 0, 0, 0, 0, 0};
    const bf16x8 qA0 = __builtin_bit_cast(bf16x8, q4[0]), qA1 = __builtin_bit_cast(bf16x8, q4[1]);
    const bf16x8 qB0 = __builtin_bit_cast(bf16x8, q4[2]), qB1 = __builtin_bit_cast(bf16x8, q4[3]);
    f32x16 oA, oB, nmA, nmB, sA, sB;
    f32x4 lsA, lsB;
    u32x8 pB;
    bf16x8 v11;
    asm volatile(".irp r,2,4,6,8,10,12,14,16,18,20,22,24,26,28,30,32,106,108,110,112,134,136,138,140,170,172\n\tv_mov_b64 v[\\r:\\r+1], 0\n\t.endr"
                 : "={v[2:17]}"(oA), "={v[18:33]}"(oB), "={v[106:113]}"(pB), "={v[134:137]}"(lsA), "={v[138:141]}"(lsB), "={v[170:173]}"(v11));
    WAIT_LGKM_BAR();
    STAMP(8);
    bf16x8 k00 = lds_b128(sm), k10 = lds_b128(sm + 1024), k01 = lds_b128(sm + 4096), k11 = lds_b128(sm + 4096 + 1024);
    bf16x8 v00 = lds_b128(sm + 2048), v10 = lds_b128(sm + 3072), v01 = lds_b128(sm + 4096 + 2048);
    {
        sA = MFMA32(k00, qA0, z16); sB = MFMA32(k00, qB0, z16);
        sA = MFMA32(k10, qA1, sA); sB = MFMA32(k10, qB1, sB);
        float mA = sA[0], mB = sB[0];
#pragma unroll
        for (int r = 1; r < 16; ++r) { mA = fmaxf(mA, sA[r]); mB = fmaxf(mB, sB[r]); }
        mA = max_h(mA); mB = max_h(mB);
#pragma unroll
        for (int r = 0; r < 16; ++r) { nmA[r] = -mA; nmB[r] = -mB; sA[r] -= mA; }
    }
    const unsigned a0 = (unsigned)(uintptr_t)sm;
    asm volatile(
        ".macro ATT_RD dst, y\n\t"
        "ds_read_b128 v[\\dst:\\dst+3], v[175+(((\\y)&0x1ffff)>>16)] offset:(\\y)&0xffff\n\t"
        ".endm\n\t"
        ".macro ATT_E4 c\n\t"
        "v_exp_f32 v[\\c], v[\\c]\n\tv_exp_f32 v[\\c+1], v[\\c+1]\n\tv_exp_f32 v[\\c+2], v[\\c+2]\n\tv_exp_f32 v[\\c+3], v[\\c+3]\n\t"
        ".endm\n\t"
        ".macro ATT_C4 p, c\n\t"
        "v_cvt_pk_bf16_f32 v[\\p], v[\\c], v[\\c+1]\n\tv_cvt_pk_bf16_f32 v[\\p+1], v[\\c+2], v[\\c+3]\n\t"
        "v_cvt_pk_bf16_f32 v[\\p+2], v[\\c+4], v[\\c+5]\n\tv_cvt_pk_bf16_f32 v[\\p+3], v[\\c+6], v[\\c+7]\n\t"
        ".endm\n\t"
        ".macro ATT_STEP cur, nxt, ka, kb, q, nm, oc, od, lsd, pc, pd, vd1, vc0, r0, y0, r1, y1, bar, rd\n\t"
        "v_mfma_f32_32x32x16_bf16 v[\\nxt:\\nxt+15], v[\\ka:\\ka+3], v[\\q:\\q+3], v[\\nm:\\nm+15]\n\t"
        "ATT_E4 \\cur\n\t"
        "v_mfma_f32_32x32x16_bf16 v[\\nxt:\\nxt+15], v[\\kb:\\kb+3], v[\\q+4:\\q+7], v[\\nxt:\\nxt+15]\n\t"
        "ATT_E4 \\cur+4\n\t"
        "v_mfma_f32_32x32x16_bf16 v[\\od:\\od+15], v[\\vd1:\\vd1+3], v[\\pd+4:\\pd+7], v[\\od:\\od+15]\n\t"
        "s_waitcnt lgkmcnt(0)\n\t"
        "ATT_C4 \\pc, \\cur\n\t"
        "v_exp_f32 v[\\cur+8], v[\\cur+8]\n\tv_exp_f32 v[\\cur+9], v[\\cur+9]\n\t"
        "v_mfma_f32_32x32x16_bf16 v[\\oc:\\oc+15], v[\\vc0:\\vc0+3], v[\\pc:\\pc+3], v[\\oc:\\oc+15]\n\t"
        ".if \\bar\n\ts_barrier\n\t.endif\n\t"
        "ATT_E4 \\cur+10\n\t"
        "v_smfmac_f32_16x16x64_bf16 v[\\lsd:\\lsd+3], v[130:133], v[\\pd:\\pd+7], v174\n\t"
        ".if \\rd & 4\n\tATT_RD 142, 32768\n\tATT_RD 150, 33792\n\tATT_RD 158, 34816\n\t.endif\n\t"
        ".if \\rd & 1\n\tATT_RD \\r0, \\y0\n\t.endif\n\t"
        ".if \\rd & 2\n\tATT_RD \\r1, \\y1\n\t.endif\n\t"
        "v_exp_f32 v[\\cur+14], v[\\cur+14]\n\tv_exp_f32 v[\\cur+15], v[\\cur+15]\n\t"
        "ATT_C4 \\pc+4, \\cur+8\n\t"
        ".if \\rd & 4\n\ts_waitcnt lgkmcnt(3)\n\t.endif\n\t"
        ".endm\n\t"
        ".macro ATT_TILE p, y1, y2, bar, ra=3, rb=3\n\t"
        "ATT_STEP 66, 82, 142+\\p, 150+\\p, 122, 50, 2, 18, 138, 98, 106, 170-\\p, 158+\\p, 170-\\p, \\y1+3072, 142+\\p, \\y2, \\bar, \\ra\n\t"
        "ATT_STEP 82, 66, 146-\\p, 154-\\p, 114, 34, 18, 2, 134, 106, 98, 166+\\p, 158+\\p, 158+\\p, \\y2+2048, 150+\\p, \\y2+1024, 0, \\rb\n\t"
        ".endm\n\t"
        ".macro ATT_STAGE b\n\t"
        "ATT_TILE 0, \\b+4096, \\b+8192, 0\n\t"
        "ATT_TILE 4, \\b+8192, \\b+12288, 0\n\t"
        "ATT_TILE 0, \\b+12288, \\b+32768, 1\n\t"
        "ATT_TILE 4, \\b+32768, \\b+36864, 0\n\t"
        ".endm\n\t"
        "v_add_u32 v176, 0x10000, v175\n\t"
        "s_mov_b32 s96, 4\n\t"
        "s_waitcnt vmcnt(0) lgkmcnt(0)\n\t"
        "s_barrier\n\t"
        "ATT_TILE 0, 4096, 8192, 0\n\tATT_TILE 4, 8192, 12288, 0\n\tATT_TILE 0, 12288, 0, 0, 1, 0\n\tATT_TILE 4, 32768, 36864, 1, 7, 3\n\t"
        "s_branch att_entry_%=\n\t"
        ".p2align 6\n"
        "att_loop_%=:\n\t"
        "ATT_STAGE 0\n"
        "att_entry_%=:\n\t"
        "ATT_STAGE 32768\n\tATT_STAGE 65536\n\t"
        "s_sub_u32 s96, s96, 1\n\t"
        "ATT_STAGE 98304\n\t"
        "s_cmp_lg_u32 s96, 0\n\t"
        "s_cbranch_scc1 att_loop_%=\n\t"
        "s_nop 1\n\t"
        "v_mfma_f32_32x32x16_bf16 v[18:33], v[170:173], v[110:113], v[18:33]\n\t"
        "v_smfmac_f32_16x16x64_bf16 v[138:141], v[130:133], v[106:113], v174\n\t"
        "s_waitcnt lgkmcnt(0)\n\ts_nop 10\n\t"
        ".purgem ATT_RD\n\t.purgem ATT_E4\n\t.purgem ATT_C4\n\t.purgem ATT_STEP\n\t.purgem ATT_TILE\n\t.purgem ATT_STAGE"
        : "+{v[2:17]}"(oA), "+{v[18:33]}"(oB), "+{v[66:81]}"(sA), "+{v[106:113]}"(pB), "+{v[134:137]}"(lsA), "+{v[138:141]}"(lsB),
          "+{v[142:145]}"(k00), "+{v[146:149]}"(k01), "+{v[150:153]}"(k10), "+{v[154:157]}"(k11),
          "+{v[158:161]}"(v00), "+{v[162:165]}"(v01), "+{v[166:169]}"(v10), "+{v[170:173]}"(v11)
        : "{v[34:49]}"(nmA), "{v[50:65]}"(nmB), "{v[114:117]}"(qA0), "{v[118:121]}"(qA1), "{v[122:125]}"(qB0), "{v[126:129]}"(qB1),
          "{v[130:133]}"(sel), "{v174}"(sidx), "{v175}"(a0)
        : "memory", "scc", "s96", "v176", "v82", "v83", "v84", "v85", "v86", "v87", "v88", "v89", "v90", "v91", "v92", "v93", "v94", "v95",
          "v96", "v97", "v98", "v99", "v100", "v101", "v102", "v103", "v104", "v105");
    STAMP(9);
    float lA = lsA[0] + lsA[1], lB = lsB[0] + lsB[1];
    const unsigned dst = (unsigned)((((panel * 8 + wm * 4 + ((lane & 31) >> 4)) * 32 + head) * 64 + (lane & 15) + (lane >> 5) * 16) * 16);
    auto store = [&] {
#pragma unroll
    for (int u = 0; u < 2; ++u) {
        const f32x16 e = (u ? oB : oA) * __builtin_amdgcn_rcpf(u ? lB : lA);
        st16o<0>((char*)ofrag + u * 65536, dst, u32x4{pack_bf16(e[0], e[1]), pack_bf16(e[2], e[3]), pack_bf16(e[8], e[9]), pack_bf16(e[10], e[11])});
        st16o<512>((char*)ofrag + u * 65536, dst, u32x4{pack_bf16(e[4], e[5]), pack_bf16(e[6], e[7]), pack_bf16(e[12], e[13]), pack_bf16(e[14], e[15])});
    }
    };
    store();
    {
        const f32x16 os = oA + oB;
        const float osum = (((os[0] + os[1]) + (os[2] + os[3])) + ((os[4] + os[5]) + (os[6] + os[7]))) +
                           (((os[8] + os[9]) + (os[10] + os[11])) + ((os[12] + os[13]) + (os[14] + os[15])));
        const bool bad = !(lA < 1.0e24f) || !(lB < 1.0e24f) || !(fabsf(osum) < 3.0e38f);
        if (__any(bad)) {
            const __amdgpu_buffer_rsrc_t rk = __builtin_amdgcn_make_buffer_rsrc((char*)kvf + (size_t)head * 262144, 0, 262144, 0x00020000);
            oA = z16; oB = z16; lA = 0.f; lB = 0.f;
            float mA = -INFINITY, mB = -INFINITY;
#pragma unroll 1
            for (int t = 0; t < 64; ++t) {
                bf16x8 f[4];
#pragma unroll
                for (int i = 0; i < 4; ++i) f[i] = __builtin_bit_cast(bf16x8, __builtin_amdgcn_raw_buffer_load_b128(rk, lane * 16 + i * 1024, t * 4096, 16));
                auto upd = [&](bf16x8 q0, bf16x8 q1, f32x16& o, float& l, float& m) {
                    f32x16 s = MFMA32(f[0], q0, z16); s = MFMA32(f[1], q1, s);
                    float tm = s[0];
#pragma unroll
                    for (int r = 1; r < 16; ++r) tm = fmaxf(tm, s[r]);
                    tm = max_h(tm);
                    const float mn = fmaxf(m, tm);
                    const float alpha = __builtin_amdgcn_exp2f(m - mn);
                    m = mn;
#pragma unroll
                    for (int r = 0; r < 16; ++r) { s[r] = __builtin_amdgcn_exp2f(s[r] - mn); o[r] *= alpha; }
                    const u32x4 p0 = {pack_bf16(s[0], s[1]), pack_bf16(s[2], s[3]), pack_bf16(s[4], s[5]), pack_bf16(s[6], s[7])};
                    const u32x4 p1 = {pack_bf16(s[8], s[9]), pack_bf16(s[10], s[11]), pack_bf16(s[12], s[13]), pack_bf16(s[14], s[15])};
                    f32x4 t4 = {0, 0, 0, 0};
                    o = MFMA32(f[2], __builtin_bit_cast(bf16x8, p0), o);
                    o = MFMA32(f[3], __builtin_bit_cast(bf16x8, p1), o);
                    t4 = ROWSUM(p0, p1, t4);
                    l = l * alpha + (t4[0] + t4[1]);
                };
                upd(qA0, qA1, oA, lA, mA);
                upd(qB0, qB1, oB, lB, mB);
            }
            store();
        }
    }
}

DI void ln_row(const u32x4 (&v)[2], __hip_bfloat16* __restrict__ Y, unsigned off) {
    float s1 = 0.f, s2 = 0.f;
#pragma unroll
    for (int e = 0; e < 8; ++e) s1 = dot2bf(v[e >> 2][e & 3], 0x3f803f80u, s1);
    const float mean = wave_sum(s1) * (1.f / 1024.f);
#pragma unroll
    for (int e = 0; e < 8; ++e) {
        const float d0 = bf_lo(v[e >> 2][e & 3]) - mean, d1 = bf_hi(v[e >> 2][e & 3]) - mean;
        s2 += d0 * d0 + d1 * d1;
    }
    const float mb = round_bf16(mean);
    const float rden = __builtin_amdgcn_rcpf(round_bf16(__builtin_amdgcn_sqrtf(round_bf16(round_bf16(wave_sum(s2) * (1.f / 1024.f)) + 1e-5f))));
#pragma unroll
    for (int hh = 0; hh < 2; ++hh) {
        u32x4 y;
#pragma unroll
        for (int e = 0; e < 4; ++e) {
            const unsigned d = pack_bf16(bf_lo(v[hh][e]) - mb, bf_hi(v[hh][e]) - mb);
            y[e] = pack_bf16(bf_lo(d) * rden, bf_hi(d) * rden);
        }
        if (hh) st16o<1024>(Y, off, y); else st16o<0>(Y, off, y);
    }
}

extern "C" __global__ __launch_bounds__(512, 2) void mega_kernel(const __hip_bfloat16* x, const __hip_bfloat16* wqkvA,
                                                            const __hip_bfloat16* woA, const __hip_bfloat16* wqkvB,
                                                            const __hip_bfloat16* woB, __hip_bfloat16* out,
                                                            char* ws) {
    char* const smem = smem_base;
    typedef __hip_bfloat16 bf;
    const int w8 = __builtin_amdgcn_readfirstlane(threadIdx.x >> 6);
    if (w8 == 7 && (blockIdx.x >> 3) < 10) code_touch(blockIdx.x >> 3, threadIdx.x & 63);
    asm volatile("" :: "s"(x), "s"(wqkvA), "s"(woA), "s"(wqkvB), "s"(woB), "s"(out), "s"(ws));
    STAMP_DECL; STAMPR(14); STAMP(0);
    u64 tag;
    {
        const int lane = threadIdx.x & 63;
        const Ids I = ids();
        const unsigned off = (unsigned)((I.po * 128 + I.co * 8 + w8) * 2048 + lane * 16);
        u32x4 v[2];
        asm volatile("global_load_dwordx4 %0, %2, %3\n\tglobal_load_dwordx4 %1, %2, %3 offset:1024" : "=&v"(v[0]), "=&v"(v[1]) : "v"(off), "s"(x) : "memory");
        asm volatile("s_load_dwordx2 %0, %1, 0x0" : "=s"(tag) : "s"(ws + WS_EPOCH) : "memory");
        if (w8 == 7) {
            const int l = min(lane, 12), k = l % 5;
            const unsigned o = (l >= 10 || k == 3) ? flag_off(l, I.cq, I.pq, 0) : (k == 0 || k == 2) ? flag_off(l, I.pq, I.cq, 0) : flag_off(l, I.po, I.co, 0);
            touch_at(ws + WS_FLAG, o);
        } else if (w8 >= 4 && w8 - 4 < NTOUCH) touch(wqkvA, (w8 - 4) * 256 + blockIdx.x, lane);
        if (w8 == 7 || (w8 >= 4 && w8 - 4 < NTOUCH)) asm volatile("s_waitcnt vmcnt(1)" ::: "memory"); else asm volatile("s_waitcnt vmcnt(0)" ::: "memory");
        asm volatile("" : "+v"(v[0]), "+v"(v[1]));
        ln_row(v, (bf*)(ws + WS_Y0), off);
        asm volatile("s_waitcnt lgkmcnt(0)" : "+s"(tag) :: "memory");
        tag += 1;
    }
    if (w8 >= 4) {
        int w = w8 & 3;
#pragma unroll 1
        for (int it = 0; it < 2; ++it) {
            int lane = threadIdx.x; asm volatile("" : "+v"(lane)); lane &= 63;
            asm volatile("" : "+s"(x), "+s"(wqkvA), "+s"(woA), "+s"(wqkvB), "+s"(woB), "+s"(ws), "+s"(w));
            const unsigned lds0 = (unsigned)(uintptr_t)smem;
            u32x4 ca[8], cb[8];
            qkv_loader<true>(smem, ws, it * 5, tag, (const bf*)(ws + (it ? WS_X2 : WS_Y0)), wqkvA, (const float*)(ws + WS_ST + 262144),
                             ws + WS_CSP, it == 0, woA, [&] {
                if (w < 3) {
                    colsum_load(ca, wqkvA, w, lane); colsum_load(cb, wqkvB, w, lane);
                    asm volatile("s_waitcnt vmcnt(16)" ::: "memory");
                } else {
                    touch(woA, blockIdx.x, lane);
                    asm volatile("s_waitcnt vmcnt(1)" ::: "memory");
                }
            } SP_ARG);
            STAMP(3);
#pragma unroll
            for (int q = 0; q < 4; ++q) wo_quarter(woA, q, lds0 + q * 32768, w, lane);
            u32x4 res[4];
            res_load(res, it ? (const void*)(ws + WS_X2) : x, true, w, lane);
            const bool cs = (it == 0 && w < 3);
            STAMP(4);
            BAR();
            asm volatile("s_waitcnt vmcnt(0)" ::: "memory");
            res_write(res, smem, 131072, w, lane);
            WAIT_LGKM_BAR();
            if (cs) {
                asm volatile("" : "+v"(cb[0]), "+v"(cb[1]), "+v"(cb[2]), "+v"(cb[3]), "+v"(cb[4]), "+v"(cb[5]), "+v"(cb[6]), "+v"(cb[7]));
                asm volatile("" : "+v"(ca[0]), "+v"(ca[1]), "+v"(ca[2]), "+v"(ca[3]), "+v"(ca[4]), "+v"(ca[5]), "+v"(ca[6]), "+v"(ca[7]));
                colsum_store(ca, ws + WS_CSP, w, lane);
                colsum_store(cb, ws + WS_CSP + 196608, w, lane);
                asm volatile("s_waitcnt vmcnt(0)" ::: "memory");
                flag_store(ws, 10 + w, ids().cq, ids().pq, tag, lane);
            }
            STAMP(5);
            qkv_loader<false>(smem, ws, it * 5 + 2, tag, (const bf*)(ws + (it ? WS_X3 : WS_X1)), wqkvB, (const float*)(ws + WS_ST + it * 524288), ws + WS_CSP + 196608,
                              false, nullptr, [] {} SP_ARG);
            STAMP(8);
            attn_loader(smem, ws, it * 5 + 3, tag, (const bf*)(ws + WS_KVF + it * 8 * MBy), woB, ws + (it ? WS_X3 : WS_X1) SP_ARG);
            STAMP(13); STAMPR(15); STAMP_FLUSH(it);
        }
        if (blockIdx.x == 0 && threadIdx.x == 256) __hip_atomic_store((g_u64ptr)(ws + WS_EPOCH), tag, __ATOMIC_RELAXED, __HIP_MEMORY_SCOPE_AGENT);
    } else {
        __builtin_amdgcn_s_setprio(2);
#pragma unroll 1
        for (int it = 0; it < 2; ++it) {
            u32x4 q4[4];
            asm volatile("" : "+s"(ws), "+s"(wqkvA), "+s"(wqkvB));
            qkv_compute<true>(smem, wqkvA, q4, nullptr, (bf*)(ws + WS_OF + it * 8 * MBy), it == 0 SP_ARG);
            STAMP(2);
            out_compute<true>(smem, ws, it * 5 + 1, tag, (const bf*)(ws + WS_OF + it * 8 * MBy),
                              (bf*)(ws + (it ? WS_X3 : WS_X1)), (float*)(ws + WS_ST + it * 524288) SP_ARG);
            STAMP(6);
            qkv_compute<false>(smem, wqkvB, q4, (bf*)(ws + WS_KVF + it * 8 * MBy), nullptr, false SP_ARG);
            attn_compute(smem, q4, (const bf*)(ws + WS_KVF + it * 8 * MBy), (bf*)(ws + WS_OF + (it * 8 + 4) * MBy) SP_ARG);
            out_compute<false>(smem, ws, it * 5 + 4, tag, (const bf*)(ws + WS_OF + (it * 8 + 4) * MBy),
                               it ? out : (bf*)(ws + WS_X2), it ? nullptr : (float*)(ws + WS_ST + 262144) SP_ARG);
            STAMP(13); STAMPR(15); STAMP_FLUSH(it);
        }
    }
}

extern "C" __global__ void zz_code_end_kernel() {}

extern "C" void kernel_launch(void* const* d_in, const int* in_sizes, int n_in, void* d_out, int out_size, void* d_ws,
                              size_t ws_size, hipStream_t stream) {
    typedef __hip_bfloat16 bf;
    (void)hipFuncSetAttribute((const void*)mega_kernel, hipFuncAttributeMaxDynamicSharedMemorySize, CH_LDS);
    mega_kernel<<<256, 512, CH_LDS, stream>>>((const bf*)d_in[0], (const bf*)d_in[1], (const bf*)d_in[2], (const bf*)d_in[4], (const bf*)d_in[5],
                                              (bf*)d_out, (char*)d_ws);
}
```

```cpp
#include <hip/hip_runtime.h>
#include <hip/hip_bf16.h>
#include <stdint.h>

typedef __attribute__((ext_vector_type(8))) short bf16x8;
typedef __attribute__((ext_vector_type(4))) short s16x4;
typedef __attribute__((ext_vector_type(4))) float f32x4;
typedef __attribute__((ext_vector_type(2))) float f32x2;
typedef __attribute__((ext_vector_type(16))) float f32x16;
typedef __attribute__((ext_vector_type(4))) unsigned u32x4;
typedef __attribute__((ext_vector_type(2))) unsigned u32x2;
typedef __attribute__((ext_vector_type(2))) __bf16 bf16x2_t;
typedef __attribute__((ext_vector_type(8))) __bf16 bf8_t;
typedef __attribute__((ext_vector_type(16))) __bf16 bf16v_t;
typedef __attribute__((address_space(3))) const char* lds_cptr;
typedef __attribute__((address_space(3))) char* lds_ptr;

#define DI __device__ __forceinline__
#define MFMA16(a, b, c) __builtin_amdgcn_mfma_f32_16x16x32_bf16((a), (b), (c), 0, 0, 0)
#define MFMA32(a, b, c) __builtin_amdgcn_mfma_f32_32x32x16_bf16((a), (b), (c), 0, 0, 0)
#define SBAR() __builtin_amdgcn_sched_barrier(0)
#define STAMP(i)
#define STAMPR(i)
#define STAMP_DECL
#define STAMP_FLUSH(it)
#define SP_PARAM
#define SP_ARG
typedef unsigned long long u64;
#define WO2 7
#define WSL0 1
#define WSL1 9
#define WO0 3
#define WO1 7
#define SPRIO 3
#define QUNR 3
#define NTOUCH 3

constexpr float QSCALE = 0.17677669529663687f * 1.4426950408889634f;
constexpr float ATT_SCALE = 0.17677669529663687f;
constexpr float LOG2E = 1.4426950408889634f;
constexpr float NEG_MASK = -1e9f / ATT_SCALE;

DI int lane_id() { int l; asm volatile("v_mbcnt_lo_u32_b32 %0, -1, 0\n\tv_mbcnt_hi_u32_b32 %0, -1, %0" : "=v"(l)); return l; }
DI unsigned pack_bf16(float lo, float hi) {
    f32x2 v = {lo, hi};
    bf16x2_t r = __builtin_convertvector(v, bf16x2_t);
    return __builtin_bit_cast(unsigned, r);
}
DI float bf_lo(unsigned u) { return __uint_as_float(u << 16); }
DI float bf_hi(unsigned u) { return __uint_as_float(u & 0xffff0000u); }
DI float round_bf16(float x) { return bf_lo(pack_bf16(x, 0.f)); }
DI void ln_stats(float sum, float sumsq, float& mean_out, float& rstd_out) {
    float mean;
    asm("v_mul_f32 %0, 0x3a800000, %1" : "=v"(mean) : "v"(sum));
    const float var = fmaxf(__builtin_fmaf(-mean, mean, sumsq * (1.f / 1024.f)), 0.f);
    mean_out = round_bf16(mean);
    rstd_out = __builtin_amdgcn_rcpf(round_bf16(__builtin_amdgcn_sqrtf(round_bf16(round_bf16(var) + 1e-5f))));
}

DI void glds16(const void* sbase, unsigned voff, unsigned ldsaddr) {
    asm volatile("s_mov_b32 m0, %0\n\ts_nop 0\n\tglobal_load_lds_dwordx4 %1, %2"
                 :: "s"(ldsaddr), "v"(voff), "s"(sbase) : "memory");
}
DI void glds16c(const void* sbase, unsigned voff, unsigned ldsaddr) {
    asm volatile("s_mov_b32 m0, %0\n\ts_nop 0\n\tglobal_load_lds_dwordx4 %1, %2 sc1"
                 :: "s"(ldsaddr), "v"(voff), "s"(sbase) : "memory");
}
DI void st16(void* sbase, unsigned voff, u32x4 v) {
    asm volatile("global_store_dwordx4 %0, %1, %2 sc1\n\ts_nop 1" :: "v"(voff), "v"(v), "s"(sbase) : "memory");
}
template <int OFF> DI void st16o(void* sbase, unsigned voff, u32x4 v) {
    asm volatile("global_store_dwordx4 %0, %1, %2 offset:%3 sc1\n\ts_nop 1" :: "v"(voff), "v"(v), "s"(sbase), "n"(OFF) : "memory");
}
DI bf16x8 lds_b128(lds_cptr p) { return *(const __attribute__((address_space(3))) bf16x8*)p; }
DI s16x4 lds_tr(lds_cptr p) {
    return __builtin_amdgcn_ds_read_tr16_b64_v4i16((__attribute__((address_space(3))) s16x4*)p);
}
DI bf16x8 cat8(s16x4 a, s16x4 b) { return __builtin_shufflevector(a, b, 0, 1, 2, 3, 4, 5, 6, 7); }
DI float dot2bf(unsigned a, unsigned b, float c) {
    return __builtin_amdgcn_fdot2_f32_bf16(__builtin_bit_cast(bf16x2_t, a), __builtin_bit_cast(bf16x2_t, b), c, false);
}
DI float sum_g(float v) {
    auto a = __builtin_amdgcn_permlane16_swap(__float_as_uint(v), __float_as_uint(v), false, false);
    v = __uint_as_float(a[0]) + __uint_as_float(a[1]);
    auto b = __builtin_amdgcn_permlane32_swap(__float_as_uint(v), __float_as_uint(v), false, false);
    return __uint_as_float(b[0]) + __uint_as_float(b[1]);
}
DI float wave_sum(float v) {
    v += __builtin_bit_cast(float, __builtin_amdgcn_update_dpp(0, __builtin_bit_cast(int, v), 0xB1, 0xF, 0xF, true));
    v += __builtin_bit_cast(float, __builtin_amdgcn_update_dpp(0, __builtin_bit_cast(int, v), 0x4E, 0xF, 0xF, true));
    v += __builtin_bit_cast(float, __builtin_amdgcn_update_dpp(0, __builtin_bit_cast(int, v), 0x141, 0xF, 0xF, true));
    v += __builtin_bit_cast(float, __builtin_amdgcn_update_dpp(0, __builtin_bit_cast(int, v), 0x140, 0xF, 0xF, true));
    return sum_g(v);
}
DI float amax3(float a, float b, float c) { float r; asm("v_max3_f32 %0, %1, %2, %3" : "=v"(r) : "v"(a), "v"(b), "v"(c)); return r; }
DI float max2f(float a, float b) { return __builtin_amdgcn_fmed3f(a, b, INFINITY); }
DI float max_h(float v) {
    auto b = __builtin_amdgcn_permlane32_swap(__float_as_uint(v), __float_as_uint(v), false, false);
    return max2f(__uint_as_float(b[0]), __uint_as_float(b[1]));
}

template <int OFF> DI void st8o(void* sbase, unsigned voff, f32x2 v) {
    asm volatile("global_store_dwordx2 %0, %1, %2 offset:%3 sc1\n\ts_nop 1" :: "v"(voff), "v"(v), "s"(sbase), "n"(OFF) : "memory");
}
constexpr unsigned OFF_DUMP = 159744;
extern __shared__ __attribute__((aligned(16))) char smem_base[];
DI void touch_at(const void* p, unsigned off) {
    asm volatile("s_mov_b32 m0, %0\n\ts_nop 0\n\tglobal_load_lds_dword %1, %2"
                 :: "s"((unsigned)(uintptr_t)smem_base + OFF_DUMP), "v"(off), "s"(p) : "memory");
}
DI void touch(const void* p, int slice, int lane) { touch_at(p, (unsigned)(slice * 64 + lane) * 128u); }
DI void code_touch(int slice, int lane) {
    unsigned long long c0, c1;
    asm volatile("s_getpc_b64 s[96:97]\n\ts_add_u32 s96, s96, mega_kernel@rel32@lo+4\n\ts_addc_u32 s97, s97, mega_kernel@rel32@hi+12\n\ts_mov_b64 %0, s[96:97]\n\t"
                 "s_getpc_b64 s[96:97]\n\ts_add_u32 s96, s96, zz_code_end_kernel@rel32@lo+4\n\ts_addc_u32 s97, s97, zz_code_end_kernel@rel32@hi+12\n\ts_mov_b64 %1, s[96:97]"
                 : "=s"(c0), "=s"(c1) :: "s96", "s97", "scc");
    touch_at((const void*)c0, min((unsigned)(slice * 64 + lane) * 128u, (unsigned)(c1 - c0)));
}

#define BAR() asm volatile("s_barrier" ::: "memory")
#define WAIT_LGKM() do { SBAR(); __builtin_amdgcn_s_waitcnt(0xC07F); SBAR(); } while (0)
#define WAIT_LGKM_BAR() do { SBAR(); __builtin_amdgcn_s_waitcnt(0xC07F); asm volatile("s_barrier" ::: "memory"); SBAR(); } while (0)
#define VM_BAR(n) asm volatile("s_waitcnt vmcnt(" #n ")\n\ts_barrier" ::: "memory")
constexpr int CH_LDS = 163840;
typedef __attribute__((address_space(3))) unsigned* lds_uptr;
typedef __attribute__((address_space(1))) u64* g_u64ptr;

constexpr size_t MBy = 1 << 20;
constexpr size_t WS_X3 = 0, WS_X2 = 4 * MBy, WS_X1 = 8 * MBy, WS_OF = 12 * MBy  , WS_KVF = 36 * MBy  ,
                 WS_ST = 52 * MBy  , WS_CSP = 54 * MBy  , WS_FLAG = 55 * MBy  ,
                 WS_EPOCH = WS_FLAG + 15 * 65536, WS_Y0 = 56 * MBy;
DI unsigned flag_off(int set, int group, int consumer, int producer) { return (unsigned)(set * 65536 + group * 4096 + consumer * 256 + producer * 8); }
DI void flag_store(char* ws, int set, int group, int producer, u64 tag, int lane) {
    asm volatile("" : "+s"(tag));
    if (lane < 16) __hip_atomic_store((g_u64ptr)(ws + WS_FLAG + flag_off(set, group, lane, producer)), tag, __ATOMIC_RELAXED, __HIP_MEMORY_SCOPE_AGENT);
}
DI void flag_poll(const char* ws, unsigned off, u64 tag) {
    asm volatile("" : "+s"(tag));
#pragma unroll 1
    for (int spin = 0; spin < (1 << 16); ++spin) {
        const u64 v = __hip_atomic_load((g_u64ptr)(ws + WS_FLAG + off), __ATOMIC_RELAXED, __HIP_MEMORY_SCOPE_AGENT);
        if (__all(v == tag)) break;
        __builtin_amdgcn_s_sleep(1);
    }
    asm volatile("" ::: "memory");
}
#define POLL_INS "global_atomic_or_x2 %[land], %[po], %[zero], %[base] sc0 sc1"
template <bool DRAIN, bool ALLPOLL>
DI void signal_poll(int follower, const char* ws, unsigned soff, unsigned poff, u64 pmask, u64 tag, u64& land) {
    u64 tagv, zero;
    asm volatile("s_not_b64 s[96:97], %[ts]\n\tv_mov_b64 %[tag], %[ts]\n\tv_mov_b64 %[zero], 0\n\tv_mov_b64 %[land], s[96:97]\n\t"
                 ".if %[dr]\n\ts_waitcnt vmcnt(0)\n\t.endif\n\ts_barrier\n\t"
                 "s_cmp_lg_u32 %[fl], 0\n\ts_cbranch_scc1 .Lsig_fol_%=\n\t"
                 "s_mov_b64 exec, 0xffff\n\t"
                 "global_store_dwordx2 %[so], %[tag], %[base] sc1\n"
                 ".if %[ap]\n.Lsig_fol_%=:\n.endif\n\t"
                 "s_mov_b64 exec, %[pm]\n\t"
                 "s_movk_i32 s96, 0x7fff\n"
                 ".Lsig_loop_%=:\n\t"
                 POLL_INS "\n\t"
                 "s_sleep 1\n\t"
                 "s_waitcnt vmcnt(3)\n\t"
                 "v_cmp_ne_u64 vcc, %[land], %[tag]\n\t"
                 "s_sub_u32 s96, s96, 1\n\t"
                 "s_cbranch_scc1 .Lsig_out_%=\n\t"
                 "s_cbranch_vccnz .Lsig_loop_%=\n"
                 ".Lsig_out_%=:\n\t"
                 "s_mov_b64 exec, -1\n"
                 ".if !%[ap]\n.Lsig_fol_%=:\n.endif"
                 : [land] "=&v"(land), [tag] "=&v"(tagv), [zero] "=&v"(zero)
                 : [fl] "s"(follower), [so] "v"(soff), [po] "v"(poff), [ts] "s"(tag), [base] "s"(ws + WS_FLAG), [pm] "s"(pmask), [ap] "n"(ALLPOLL ? 1 : 0), [dr] "n"(DRAIN ? 1 : 0)
                 : "memory", "scc", "vcc", "s96", "s97");
}
struct Ids { int pq, cq, po, co; };
DI Ids ids() {
    int bid = blockIdx.x; asm volatile("" : "+s"(bid));
    const int xcd = bid & 7, idx = bid >> 3;
    int pq, cq, po, co;
#define IDS_BODY pq = (xcd & 1) * 8 + (idx & 7); cq = (xcd >> 1) * 4 + (idx >> 3); po = (xcd & 3) * 4 + (idx & 3); co = (xcd >> 2) * 8 + (idx >> 2);
    IDS_BODY
    return Ids{pq, cq, po, co};
}

DI void slow_warm(unsigned dump, const void* W, int w, int lane) {
    int bid = blockIdx.x; asm volatile("" : "+s"(bid));
    const int xcd = bid & 7, idx = bid >> 3;
    const int sc = (3 - (int)((uintptr_t)W >> 7)) & 7;
    if ((sc >> 2) != ((xcd >> 1) & 1)) return;
    const char* src = (const char*)W + (idx + 32 * w) * 49152 + (sc + (xcd >> 2) * 8) * 128;
#pragma unroll
    for (int G = 0; G < 3; ++G) glds16(src + G * 2048, (unsigned)((lane >> 3) * 6144 + (lane & 7) * 16), dump);
}

template <int V> struct IC { static constexpr int value = V; };
template <bool LOCAL> struct QC {
    static constexpr int AROWS = LOCAL ? 144 : 128;
    static constexpr int ABYTES = AROWS * 128;
    static constexpr int STAGE = ABYTES + 24576;
    static constexpr int OFF_MEAN = 147456;
    static constexpr int OFF_RSTD = OFF_MEAN + 576;
    static constexpr int OFF_CS = OFF_RSTD + 576;
};
constexpr int OFF_EX = 131072;

DI void colsum_load(u32x4 (&c)[8], const void* W, int G, int lane) {
    const Ids I = ids();
    const unsigned off = (unsigned)(I.pq * 64 + (lane >> 3)) * 6144u + (unsigned)(G * 2048 + I.cq * 128 + (lane & 7) * 16);
#pragma unroll
    for (int i = 0; i < 8; ++i)
        asm volatile("global_load_dwordx4 %0, %1, %2" : "=&v"(c[i]) : "v"(off), "s"((const char*)W + i * 49152) : "memory");
}
DI void colsum_store(const u32x4 (&c)[8], char* dst, int G, int lane) {
    const Ids I = ids();
    const int m = lane & 15;
    const unsigned one = (m & 1) ? 0xbf800000u : 0xbf80u;
    const u32x4 sl = {(m >> 1) == 0 ? one : 0u, (m >> 1) == 1 ? one : 0u, (m >> 1) == 2 ? one : 0u, (m >> 1) == 3 ? one : 0u};
    f32x4 d = {0, 0, 0, 0};
#pragma unroll
    for (int i = 0; i < 8; ++i) d = MFMA16(__builtin_bit_cast(bf16x8, sl), __builtin_bit_cast(bf16x8, c[i]), d);
#pragma unroll
    for (int r = 0; r < 4; ++r)
        d[r] += __int_as_float(__builtin_amdgcn_update_dpp(0, __float_as_int(d[r]), 0x128, 0xF, 0xF, true));
    if (lane < 32 && (lane & 8) == 0)
        st16(dst, (unsigned)((I.cq * 16 + I.pq) * 768 + G * 256 + (lane & 7) * 32 + (lane >> 4) * 16), __builtin_bit_cast(u32x4, d));
}

template <bool LOCAL>
struct WFirst {
    unsigned v[2];
    DI WFirst(int w, int ct, int lane) {
#pragma unroll
        for (int i = 0; i < 2; ++i) {
            const int k = 8 * i + (lane >> 3), pos = lane & 7;
            v[i] = (unsigned)k * 6144u + (unsigned)(w * 1024 + ct * 64 + ((pos >> 1) ^ (((k >> 1) & 1) | (i << 1))) * 16 + (pos & 1) * 8) * 2u;
        }
        asm volatile("" : "+v"(v[0]), "+v"(v[1]));
    }
    DI void issue(const void* W, unsigned lds0, int w) const {
#pragma unroll
        for (int i = 0; i < 16; ++i)
            glds16((const char*)W + (size_t)(i >> 1) * (16 * 6144), v[i & 1], lds0 + (i >> 3) * QC<LOCAL>::STAGE + QC<LOCAL>::ABYTES + (w * 8 + (i & 7)) * 1024);
    }
};

template <bool LOCAL, class F, class F2>
DI void qkv_loader(char* smem, int w, char* ws, int set, u64 tag, const __hip_bfloat16* __restrict__ X, const __hip_bfloat16* __restrict__ W,
                   const float* __restrict__ statp, const char* __restrict__ csp, bool ident, const void* nextW, F tail, F2 mid SP_PARAM) {
    typedef QC<LOCAL> C;
    constexpr int NT = 16;
    const int lane = lane_id();
    asm volatile("" : "+s"(w));
    const int idx = blockIdx.x >> 3;
    const Ids I = ids();
    const int panel = I.pq, ct = I.cq;
    const int R0 = panel * 128;
    const unsigned lds0 = (unsigned)(uintptr_t)smem;
    auto grow = [&](int i) -> int {
        if (!LOCAL) return R0 + i;
        if (i < 128) { const int t = (i >> 4) & 3, a = i & 15; return R0 + (i & 64) + 2 * (16 * (t & 1) + a) + (t >> 1); }
        const int a = i & 15; const int r = R0 - 16 + 2 * (a & 7) + (a >> 3);
        return r < 0 ? 0 : r;
    };
    unsigned voffA[5], voffB[6];
#pragma unroll
    for (int i = 0; i < 6; ++i) {
        const int j = w + 4 * i, G = j >> 3, k = 8 * (j & 7) + (lane >> 3);
        const int pos = lane & 7, f = ((k >> 1) & 1) | (((k >> 3) & 1) << 1);
        const int seg = (pos >> 1) ^ f;
        voffB[i] = (unsigned)k * 6144u + (unsigned)(G * 1024 + ct * 64 + seg * 16 + (pos & 1) * 8) * 2u;
    }
    auto issueW = [&](int t, int buf) {
        const char* wb = (const char*)W + (size_t)t * (64 * 6144);
        const unsigned sb = lds0 + buf * C::STAGE;
#pragma unroll
        for (int i = 0; i < 6; ++i) glds16(wb, voffB[i], sb + C::ABYTES + (w + 4 * i) * 1024);
    };
    asm volatile("" : "+v"(voffB[0]), "+v"(voffB[1]), "+v"(voffB[2]), "+v"(voffB[3]), "+v"(voffB[4]), "+v"(voffB[5]));
    const WFirst<LOCAL> first(w, ct, lane);
    const int g = lane >> 4, r = lane & 15, pos = 32 * w + 2 * r + (g >> 1);
    const int lr = !LOCAL ? (pos & 64) + 2 * (pos & 31) + ((pos >> 5) & 1)
                          : (w == 0 && (g & 1)) ? 128 + (r & 7) + 8 * (g >> 1) : (pos & 64) + ((pos & 1) * 2 + ((pos >> 5) & 1)) * 16 + ((pos >> 1) & 15);
    unsigned so = (unsigned)(panel * 16384 + g * 1024 + w * 256 + r * 16), ho = (unsigned)(g * 1024 + (panel ? panel * 16384 - 15488 + (r & 7) * 16 : 0));
    unsigned co = (unsigned)((ct * 16 + g) * 768 + w * 192 + r * 12);
    lds_ptr mp = (lds_ptr)smem + C::OFF_MEAN + lr * 4, cp = (lds_ptr)smem + C::OFF_CS + w * 192 + r * 12 + ((g & 1) ? 8 : g * 2);
    asm volatile("" : "+v"(so), "+v"(ho), "+v"(co), "+v"(mp), "+v"(cp));
#pragma unroll
    for (int i = 0; i < 5; ++i) {
        const int j = (i < 4) ? (w + 4 * i) : (16 + (w & 1));
        const int row = 8 * j + (lane >> 3);
        const int c = (lane & 7) ^ ((row >> 1) & 7);
        voffA[i] = (unsigned)grow(row < C::AROWS ? row : 0) * 2048u + c * 16;
        asm volatile("" : "+v"(voffA[i]));
    }
    auto issueA = [&](int t, int buf) {
        const char* xa = (const char*)X + t * 128;
        const unsigned sb = lds0 + buf * C::STAGE;
        if (LOCAL && w < 2) glds16c(xa, voffA[4], sb + (16 + w) * 1024);
#pragma unroll
        for (int i = 0; i < 4; ++i) glds16c(xa, voffA[i], sb + (w + 4 * i) * 1024);
    };
    u64 land;
    if (w == 3) __builtin_amdgcn_s_setprio(3);
    signal_poll<LOCAL, false>(w - 3, ws, flag_off(set, I.po, lane & 15, I.co),
                              LOCAL ? flag_off(set, (lane & 16) && panel ? panel - 1 : panel, ct, lane & 15)
                                    : (lane < 16 ? flag_off(set, panel, ct, lane) : flag_off(9 + (lane >> 4), ct, panel, lane & 15)),
                              LOCAL ? 0xffffffffull : ~0ull, tag, land);
    if (w < 3) first.issue(W, lds0, w);
    BAR();
    STAMP(LOCAL ? 1 : 6);
    issueA(0, 0);
    issueA(1, 1);
    VM_BAR(4);
    asm volatile("" :: "v"(land));
    __builtin_amdgcn_s_setprio(0);
    STAMP(LOCAL ? 2 : 7);
    issueW(2, 2);
    issueA(2, 2);
    VM_BAR(10);
    issueA(3, 0); issueW(3, 0);
    int buf = 1;
#pragma unroll 1
    for (int t = 1; t < NT - 3; ++t) {
        VM_BAR(10);
        issueA(t + 3, buf); issueW(t + 3, buf);
        if (t == 10 && idx < 3) code_touch(idx * 4 + w, lane);
        if (LOCAL && t == 11)
            glds16(nextW, (unsigned)((idx + 32 * w) * 8 + (lane >> 3)) * 2048u +
                   (unsigned)(((I.co & 8) + ((3 - (int)((uintptr_t)nextW >> 7)) & 7)) * 128 + (lane & 7) * 16), lds0 + OFF_DUMP);
        buf = (buf == 2) ? 0 : buf + 1;
    }
    typedef __attribute__((ext_vector_type(3))) float f32x3;
    const bool halo = LOCAL && w == 0;
    if (!ident) {
        f32x3 cs[4];
        f32x4 sv[4], hv[4];
        typedef __attribute__((address_space(3))) float* lds_f;
        auto add = [](float a, float b) { float r; asm("v_add_f32 %0, %1, %2" : "=v"(r) : "v"(a), "v"(b)); return r; };
        auto fold32 = [](float a, float b) { asm("s_nop 1\n\tv_permlane32_swap_b32 %0, %1\n\tv_add_f32 %0, %0, %1" : "+v"(a), "+v"(b)); return a; };
        auto fold16 = [](float a, float b) { asm("s_nop 1\n\tv_permlane16_swap_b32 %0, %1\n\tv_add_f32 %0, %0, %1" : "+v"(a), "+v"(b)); return a; };
        auto sum4 = [&](auto (&v)[4], int e) { return add(add(v[0][e], v[1][e]), add(v[2][e], v[3][e])); };
#pragma unroll
        for (int i = 0; i < 4; ++i) asm volatile("global_load_dwordx3 %0, %1, %2 sc1" : "=&v"(cs[i]) : "v"(co), "s"(csp + i * 3072) : "memory");
        VM_BAR(14);
#pragma unroll
        for (int i = 0; i < 4; ++i) asm volatile("global_load_dwordx4 %0, %1, %2 sc1" : "=&v"(sv[i]) : "v"(so), "s"((const char*)statp + i * 4096) : "memory");
        if (halo) {
#pragma unroll
            for (int i = 0; i < 4; ++i) asm volatile("global_load_dwordx4 %0, %1, %2 sc1" : "=&v"(hv[i]) : "v"(ho), "s"((const char*)statp + i * 4096) : "memory");
            asm volatile("s_waitcnt vmcnt(8)" ::: "memory");
        } else asm volatile("s_waitcnt vmcnt(4)" ::: "memory");
        asm volatile("" : "+v"(cs[0]), "+v"(cs[1]), "+v"(cs[2]), "+v"(cs[3]));
        const float c2 = sum4(cs, 2);
        *(lds_f)cp = fold16(fold32(sum4(cs, 0), sum4(cs, 1)), fold32(c2, c2));
        BAR();
        asm volatile("s_waitcnt vmcnt(0)" : "+v"(sv[0]), "+v"(sv[1]), "+v"(sv[2]), "+v"(sv[3]) :: "memory");
        mid(0);
        const float s1 = fold32(sum4(sv, 0), sum4(sv, 2)), s2 = fold32(sum4(sv, 1), sum4(sv, 3));
        float h1, h2;
        asm("v_mov_b32 %0, %2\n\tv_mov_b32 %1, %3" : "=&v"(h1), "=&v"(h2) : "v"(s1), "v"(s2));
        if (halo) {
            asm volatile("" : "+v"(hv[0]), "+v"(hv[1]), "+v"(hv[2]), "+v"(hv[3]));
            const float u0 = sum4(hv, 0), u1 = sum4(hv, 1), u2 = sum4(hv, 2), u3 = sum4(hv, 3);
            h1 = fold32(u0, panel ? u2 : u0); h2 = fold32(u1, panel ? u3 : u1);
        }
        float mean, rstd;
        ln_stats(fold16(s1, h1), fold16(s2, h2), mean, rstd);
        *(lds_f)mp = mean; *(lds_f)(mp + (C::OFF_RSTD - C::OFF_MEAN)) = rstd;
    } else { VM_BAR(10); tail(); BAR(); mid(0); }
    mid(1);
    WAIT_LGKM_BAR();
}

template <bool LOCAL>
DI void qkv_compute(char* smem, int w, const void* W, u32x4 (&qo4)[4], __hip_bfloat16* __restrict__ kvf, __hip_bfloat16* __restrict__ ofrag, bool ident SP_PARAM) {
    typedef QC<LOCAL> C;
    constexpr int NT = 16, ABYTES = C::ABYTES, STAGE = C::STAGE;
    const int lane = lane_id();
    asm volatile("" : "+s"(w));
    const int wm = w & 1, wn = w >> 1;
    const Ids I = ids();
    const int panel = I.pq, ct = I.cq;
    const int l15 = lane & 15, g = lane >> 4;
    const lds_cptr sm = (lds_cptr)smem;
    lds_ptr smw = (lds_ptr)smem;

    f32x4 acc[3][4][2];
    f32x4 acch[2];
    f32x2 z2; asm volatile("v_mov_b64 %0, 0" : "=v"(z2));
    const f32x4 z4 = {z2[0], z2[1], z2[0], z2[1]};
#pragma unroll
    for (int a = 0; a < 3; ++a)
#pragma unroll
        for (int t = 0; t < 4; ++t) { acc[a][t][0] = z4; acc[a][t][1] = z4; }
    acch[0] = z4; acch[1] = z4;

    struct Frags { bf16x8 a[4]; bf16x8 h; s16x4 b[3][2][2]; };
    auto rd = [&](auto WM, Frags& F, lds_cptr pa, lds_cptr pb0, lds_cptr pb1, int n) {
        switch (n) {
            case 0: F.b[0][0][0] = lds_tr(pb0); break;
            case 1: F.b[0][0][1] = lds_tr(pb0 + 512); break;
            case 2: F.a[0] = lds_b128(pa); break;
            case 3: F.b[0][1][0] = lds_tr(pb1); break;
            case 4: F.b[0][1][1] = lds_tr(pb1 + 512); break;
            case 5: F.a[1] = lds_b128(pa + 2048); break;
            case 6: F.a[2] = lds_b128(pa + 4096); break;
            case 7: F.a[3] = lds_b128(pa + 6144); break;
            case 8: F.b[1][0][0] = lds_tr(pb0 + 8192); break;
            case 9: F.b[1][0][1] = lds_tr(pb0 + 8192 + 512); break;
            case 10: F.b[1][1][0] = lds_tr(pb1 + 8192); break;
            case 11: F.b[1][1][1] = lds_tr(pb1 + 8192 + 512); break;
            case 12: F.b[2][0][0] = lds_tr(pb0 + 16384); break;
            case 13: F.b[2][0][1] = lds_tr(pb0 + 16384 + 512); break;
            case 14: F.b[2][1][0] = lds_tr(pb1 + 16384); break;
            case 15: F.b[2][1][1] = lds_tr(pb1 + 16384 + 512); break;
            case 16: if (LOCAL) F.h = lds_b128(pa + 16384 - WM.value * 8192); break;
            default: break;
        }
    };
    auto mm = [](f32x4& c, bf16x8 a, bf16x8 b) { asm volatile("v_mfma_f32_16x16x32_bf16 %0, %1, %2, %0" : "+v"(c) : "v"(a), "v"(b)); };
    auto half = [&](auto WM, const Frags& C, Frags& N, bool doread, lds_cptr pa, lds_cptr pb0, lds_cptr pb1, auto tail) {
#pragma unroll
        for (int G = 0; G < 3; ++G) {
            const bf16x8 b0 = cat8(C.b[G][0][0], C.b[G][0][1]), b1 = cat8(C.b[G][1][0], C.b[G][1][1]);
#pragma unroll
            for (int t = 0; t < 4; ++t) {
                const int n = G * 8 + t * 2;
                if (G < 2) mm(acc[G][t][0], b0, C.a[t]); else mm(acc[G][t][0], C.a[t], b0);
                SBAR();
                if (doread) rd(WM, N, pa, pb0, pb1, n);
                tail(n);
                SBAR();
                if (G < 2) mm(acc[G][t][1], b1, C.a[t]); else mm(acc[G][t][1], C.a[t], b1);
                SBAR();
                if (doread) rd(WM, N, pa, pb0, pb1, n + 1);
                tail(n + 1);
                SBAR();
            }
        }
        if (LOCAL) {
            if (WM.value == 0) { mm(acch[0], cat8(C.b[1][0][0], C.b[1][0][1]), C.h); mm(acch[1], cat8(C.b[1][1][0], C.b[1][1][1]), C.h); }
            else               { mm(acch[0], C.h, cat8(C.b[2][0][0], C.b[2][0][1])); mm(acch[1], C.h, cat8(C.b[2][1][0], C.b[2][1][1])); }
            SBAR();
        }
    };

    Frags F0, F1;
    f32x4 cq[2], ck[2], mu4[5], rs4[5]; float cv[2], mu[5], rs[5];
    u32x4 qfr[4], kfr[4];
    u32x4 va[2][2], cc;
    f32x4 mwin, mfirst;
    typedef __attribute__((address_space(3))) u32x4* lds_u4;
    constexpr int OFF_CST = C::OFF_CS + 768;
    const bool negpos = (wm == 0) && (panel == 0);
    if (LOCAL) {
        unsigned keepv = negpos ? __float_as_uint(NEG_MASK) : 0u;
        asm volatile("" : "+s"(keepv));
        u32x4 mw, mf;
        unsigned prv[4];
#pragma unroll
        for (int r = 0; r < 4; ++r) {
            const int d = l15 - (4 * g + r);
            unsigned out = (unsigned)(((d << 28) >> 31));
            prv[r] = (unsigned)(d >> 31);
            asm volatile("" : "+v"(out), "+v"(prv[r]));
            mw[r] = out & 0xff800000u;
            mf[r] = (out & 0xff800000u) | (~out & keepv);
        }
        const unsigned e0 = (unsigned)(OFF_EX + wn * 6144);
        lds_u4 cst = (lds_u4)(smw + OFF_CST) + lane;
        u32x4 c3 = {~((prv[0] >> 16) | (prv[1] << 16)), ~((prv[2] >> 16) | (prv[3] << 16)),
                    e0 + (unsigned)(wm ? lane : 256 + (lane ^ 8)) * 16u, e0 + (unsigned)(wm ? 128 + lane : 320 + (lane ^ 32)) * 16u};
        asm volatile("" : "+v"(mw), "+v"(mf), "+v"(c3), "+v"(cst));
        VM_BAR(0);
        cst[0] = mw; cst[64 + wm * 64] = mf; cst[192 + w * 64] = c3;
        SBAR();
    } else VM_BAR(0);
    typedef const __attribute__((address_space(3))) f32x4* lds_f4; typedef const __attribute__((address_space(3))) float* lds_f1;
    auto mainloop = [&](auto WM) {
#pragma unroll
        for (int a = 0; a < 3; ++a)
            asm volatile("" : "+v"(acc[a][0][0]), "+v"(acc[a][0][1]), "+v"(acc[a][1][0]), "+v"(acc[a][1][1]), "+v"(acc[a][2][0]), "+v"(acc[a][2][1]),
                         "+v"(acc[a][3][0]), "+v"(acc[a][3][1]));
        int ty = lane_id();
        const int l15 = ty & 15, g = ty >> 4;
        const int swA = (l15 >> 1) & 7;
        const int q4 = l15 >> 2, p4 = l15 & 3, fk = ((q4 >> 1) & 1) | ((g & 1) << 1);
        lds_cptr pA0 = sm + wm * 8192 + l15 * 128 + ((0 + g) ^ swA) * 16, pA1 = sm + wm * 8192 + l15 * 128 + ((4 + g) ^ swA) * 16;
        lds_cptr pB0 = sm + ABYTES + (8 * g + q4) * 128 + (((2 * wn + 0) ^ fk) * 32) + p4 * 8;
        lds_cptr pB1 = sm + ABYTES + (8 * g + q4) * 128 + (((2 * wn + 1) ^ fk) * 32) + p4 * 8;
        asm volatile("" : "+v"(acch[0]), "+v"(acch[1]), "+v"(pA0), "+v"(pA1), "+v"(pB0), "+v"(pB1));
        slow_warm((unsigned)(uintptr_t)smem + OFF_DUMP, W, w, ty);
        BAR();
        STAMP(LOCAL ? 1 : 7);
        BAR();
#pragma unroll
        for (int n = 0; n < 17; ++n) rd(WM, F0, pA0, pB0, pB1, n);
        WAIT_LGKM();
        int off = 0;
        lds_cptr a = pA1, b0 = pB0, b1 = pB1;
#pragma unroll QUNR
        for (int t = 0; t < NT - 1; ++t) {
            half(WM, F0, F1, true, a, b0 + 4096, b1 + 4096, [&](int n) {
                if (n == 17) { off = (off == 2 * STAGE) ? 0 : off + STAGE; asm volatile("" : "+s"(off)); }
                if (n == 19) { b0 = pB0 + off; asm volatile("" : "+v"(b0)); }
                if (n == 20) { a = pA0 + off; asm volatile("" : "+v"(a)); }
                if (n == 21) { b1 = pB1 + off; asm volatile("" : "+v"(b1)); }
            });
            WAIT_LGKM_BAR();
            half(WM, F1, F0, true, a, b0, b1, [&](int n) {
                if (n == 19) { a = pA1 + off; asm volatile("" : "+v"(a)); }
                if (n == 22) __builtin_amdgcn_s_waitcnt(0xC07F);
            });
        }
        lds_cptr s1 = sm, s4 = sm, c1 = sm, c4 = sm;
        half(WM, F0, F1, true, a, b0 + 4096, b1 + 4096, [&](int n) {
            if (n == 16) ty = lane_id();
            if (n == 17) { s1 = sm + C::OFF_MEAN + wm * 256 + (ty & 15) * 4; asm volatile("" : "+v"(s1)); }
            if (n == 18) { s4 = sm + C::OFF_MEAN + wm * 256 + (ty & 48); asm volatile("" : "+v"(s4)); }
            if (n == 19) { c1 = sm + C::OFF_CS + wn * 128 + (ty & 15) * 4; asm volatile("" : "+v"(c1)); }
            if (n == 20) { c4 = sm + C::OFF_CS + wn * 128 + (ty & 48); asm volatile("" : "+v"(c4)); }
        });
        WAIT_LGKM_BAR();
        half(WM, F1, F0, false, sm, sm, sm, [&](int n) {
            constexpr int RS = C::OFF_RSTD - C::OFF_MEAN, H = 512 - WM.value * 256;
            if (n < 2) { if (!LOCAL) cq[n] = *(lds_f4)(c4 + n * 64); ck[n] = *(lds_f4)(c4 + 256 + n * 64); }
            else if (n < 6) { mu[n - 2] = *(lds_f1)(s1 + (n - 2) * 64); rs[n - 2] = *(lds_f1)(s1 + RS + (n - 2) * 64); }
            else if (n < 10) { if (!LOCAL || (n & 1)) { mu4[n - 6] = *(lds_f4)(s4 + (n - 6) * 64); rs4[n - 6] = *(lds_f4)(s4 + RS + (n - 6) * 64); } }
            else if (n == 10) { cv[0] = *(lds_f1)(c1 + 512); cv[1] = *(lds_f1)(c1 + 512 + 64); }
            else if (LOCAL && n == 11) {
                if (WM.value == 0) { mu[4] = *(lds_f1)(s1 + H); rs[4] = *(lds_f1)(s1 + RS + H); }
                else { mu4[4] = *(lds_f4)(s4 + H); rs4[4] = *(lds_f4)(s4 + RS + H); }
            }
        });
        asm volatile("s_nop 7");
        SBAR();
        if (LOCAL) {
            auto fix = [&](int a, int t) {
#pragma unroll
                for (int f = 0; f < 2; ++f)
                    acc[a][t][f] = (a == 2) ? rs4[t] * (acc[2][t][f] + mu4[t] * cv[f]) : rs[t] * (acc[a][t][f] + mu[t] * (a ? ck[f] : cq[f]));
            };
            auto pk = [&](const f32x4 (&v)[2]) { return u32x4{pack_bf16(v[0][0], v[0][1]), pack_bf16(v[0][2], v[0][3]), pack_bf16(v[1][0], v[1][1]), pack_bf16(v[1][2], v[1][3])}; };
            cq[0] = *(lds_f4)c4; cq[1] = *(lds_f4)(c4 + 64);
            SBAR();
            if (!ident) {
                fix(1, 1); fix(1, 3); fix(2, 1); fix(2, 3);
#pragma unroll
                for (int f = 0; f < 2; ++f)
                    acch[f] = (WM.value == 0) ? rs[4] * (acch[f] + mu[4] * ck[f]) : rs4[4] * (acch[f] + mu4[4] * cv[f]);
            }
            auto pkv = [&](int t) {
#pragma unroll
                for (int f = 0; f < 2; ++f) {
                    va[t >> 1][f][(t & 1) * 2] = pack_bf16(acc[2][t][f][0], acc[2][t][f][1]);
                    va[t >> 1][f][(t & 1) * 2 + 1] = pack_bf16(acc[2][t][f][2], acc[2][t][f][3]);
                }
            };
            kfr[1] = pk(acc[1][1]); kfr[3] = pk(acc[1][3]); pkv(1); pkv(3);
            lds_u4 ex = (lds_u4)(smw + OFF_EX + wn * 6144) + ty;
            u32x4 hf = pk(acch);
            if (WM.value == 0) {
                ex[0] = kfr[1]; ex[64] = kfr[3];
#pragma unroll
                for (int u = 0; u < 2; ++u)
                    asm volatile("ds_write2_b64 %0, %1, %2 offset1:1" :: "v"(ex + 128 + u * 64), "v"(u32x2{va[u][0][2], va[u][0][3]}),
                                 "v"(u32x2{va[u][1][2], va[u][1][3]}) : "memory");
                if (negpos) hf = u32x4{0, 0, 0, 0};
            }
            ex[256 + WM.value * 64] = hf;
#pragma unroll
            for (int t = 0; t < 4; t += 2) { mu4[t] = *(lds_f4)(s4 + t * 64); rs4[t] = *(lds_f4)(s4 + (C::OFF_RSTD - C::OFF_MEAN) + t * 64); }
            lds_u4 cst = (lds_u4)(smw + OFF_CST) + ty;
            cc = cst[192 + w * 64];
            mwin = __builtin_bit_cast(f32x4, cst[0]); mfirst = __builtin_bit_cast(f32x4, cst[64 + WM.value * 64]);
            SBAR();
            if (!ident) { fix(0, 0); fix(0, 1); fix(0, 2); fix(0, 3); fix(1, 0); fix(1, 2); fix(2, 0); fix(2, 2); }
#pragma unroll
            for (int t = 0; t < 4; ++t) qfr[t] = pk(acc[0][t]);
            kfr[0] = pk(acc[1][0]); kfr[2] = pk(acc[1][2]); pkv(0); pkv(2);
            asm volatile("" : "+v"(qfr[0]), "+v"(qfr[1]), "+v"(qfr[2]), "+v"(qfr[3]), "+v"(kfr[0]), "+v"(kfr[2]),
                         "+v"(va[0][0]), "+v"(va[0][1]), "+v"(va[1][0]), "+v"(va[1][1]));
        }
    };
    const int head = ct * 2 + wn;
    unsigned qo = 0, ko = 0, vo = 0;
    if (LOCAL) {
        qo = (unsigned)((((panel * 8 + wm * 4) * 32 + head) * 64 + lane) * 16);
        asm volatile("" : "+v"(qo));
    } else {
        ko = (unsigned)((wm * 8 + (g & 1)) * 1024 + ((l15 & 7) + ((l15 & 8) << 1) + 32 * (g >> 1)) * 16);
        vo = (unsigned)((wm * 8 + 2 + (g >> 1)) * 1024 + (l15 + 32 * (g & 1)) * 16);
        asm volatile("" : "+v"(ko), "+v"(vo));
    }
    if (LOCAL && wm) mainloop(IC<1>{}); else mainloop(IC<0>{});

    if (!LOCAL) {
        auto pk = [&](f32x4 a, f32x4 b) { return u32x4{pack_bf16(a[0], a[1]), pack_bf16(a[2], a[3]), pack_bf16(b[0], b[1]), pack_bf16(b[2], b[3])}; };
        char* kvp = (char*)kvf + (size_t)(head * 16 + panel) * 16384;
        lds_ptr own = smw + wn * 16384;
        SBAR();
#pragma unroll
        for (int t = 0; t < 4; ++t) {
            const u32x4 kk = pk(rs[t] * (acc[1][t][0] + mu[t] * ck[0]), rs[t] * (acc[1][t][1] + mu[t] * ck[1]));
            if (t & 1) st16o<128>(kvp + (t >> 1) * 4096, ko, kk); else st16o<0>(kvp + (t >> 1) * 4096, ko, kk);
            *(lds_u4)(own + ko + (t >> 1) * 4096 + (t & 1) * 128) = kk;
            SBAR();
            const int u = t >> 1, f = t & 1;
            const u32x4 vv = pk(rs4[2 * u] * (acc[2][2 * u][f] + mu4[2 * u] * cv[f]), rs4[2 * u + 1] * (acc[2][2 * u + 1][f] + mu4[2 * u + 1] * cv[f]));
            if (f) st16o<256>(kvp + u * 4096, vo, vv); else st16o<0>(kvp + u * 4096, vo, vv);
            *(lds_u4)(own + vo + u * 4096 + f * 256) = vv;
            SBAR();
        }
#pragma unroll
        for (int u = 0; u < 2; ++u) {
            const float r0 = rs[2 * u] * QSCALE, r1 = rs[2 * u + 1] * QSCALE;
            const u32x4 qa = pk(r0 * (acc[0][2 * u][0] + mu[2 * u] * cq[0]), r0 * (acc[0][2 * u][1] + mu[2 * u] * cq[1]));
            const u32x4 qb = pk(r1 * (acc[0][2 * u + 1][0] + mu[2 * u + 1] * cq[0]), r1 * (acc[0][2 * u + 1][1] + mu[2 * u + 1] * cq[1]));
#pragma unroll
            for (int e = 0; e < 4; ++e) {
                const auto x = __builtin_amdgcn_permlane16_swap(qa[e], qb[e], false, false);
                qo4[2 * u][e] = x[0]; qo4[2 * u + 1][e] = x[1];
            }
        }
        return;
    }
    {
        const f32x4 zero4 = {0, 0, 0, 0};
        const unsigned cm01 = cc[0], cm23 = cc[1];
        auto bf8 = [](u32x4 v) { return __builtin_bit_cast(bf16x8, v); };
        f32x4 sp[4], sc[4];
        sc[1] = MFMA16(bf8(kfr[1]), bf8(qfr[1]), mwin); sp[1] = MFMA16(bf8(kfr[0]), bf8(qfr[1]), mwin);
        sc[3] = MFMA16(bf8(kfr[3]), bf8(qfr[3]), mwin); sp[3] = MFMA16(bf8(kfr[2]), bf8(qfr[3]), mwin);
        lds_cptr ka[2] = {sm + cc[2], sm + (cc[2] ^ (wm ? 1024u : 128u))}, vo2[2] = {sm + cc[3], sm + (cc[3] ^ (wm ? 1024u : 512u))};
        asm volatile("" : "+v"(ka[0]), "+v"(ka[1]), "+v"(vo2[0]), "+v"(vo2[1]));
        WAIT_LGKM_BAR();
        const bf16x8 kp[2] = {lds_b128(ka[0]), lds_b128(ka[1])};
        u32x2 vp[2][2];
#pragma unroll
        for (int u = 0; u < 2; ++u)
            asm volatile("ds_read_b64 %0, %2\n\tds_read_b64 %1, %2 offset:8" : "=&v"(vp[u][0]), "=&v"(vp[u][1]) : "v"(vo2[u]) : "memory");
        sc[0] = MFMA16(bf8(kfr[0]), bf8(qfr[0]), mwin); sc[2] = MFMA16(bf8(kfr[2]), bf8(qfr[2]), mwin);
        SBAR();
        float x[4][4], m[4];
        unsigned a[4][2];
        auto logits = [&](int t0, int st) {
#pragma unroll
            for (int t = t0; t < 4; t += 2)
#pragma unroll
                for (int h = 0; h < 2; ++h) {
                    if (st == 0) {
                        const unsigned uc = pack_bf16(sc[t][2 * h], sc[t][2 * h + 1]), up = pack_bf16(sp[t][2 * h], sp[t][2 * h + 1]);
                        asm("v_bfi_b32 %0, %1, %2, %3" : "=v"(a[t][h]) : "v"(h ? cm23 : cm01), "v"(uc), "v"(up));
                    } else if (st == 1) { x[t][2 * h] = bf_lo(a[t][h]) * ATT_SCALE; x[t][2 * h + 1] = bf_hi(a[t][h]) * ATT_SCALE; }
                    else if (st == 2) a[t][h] = pack_bf16(x[t][2 * h], x[t][2 * h + 1]);
                    else { x[t][2 * h] = bf_lo(a[t][h]); x[t][2 * h + 1] = bf_hi(a[t][h]); }
                }
            SBAR();
        };
        logits(1, 0); logits(1, 1);
        sp[0] = MFMA16(kp[0], bf8(qfr[0]), mfirst); sp[2] = MFMA16(kp[1], bf8(qfr[2]), mfirst);
        SBAR();
        logits(1, 2); logits(1, 3);
        logits(0, 0); logits(0, 1); logits(0, 2); logits(0, 3);
        auto across = [&](float (&v)[4], auto op, auto fin) {
            const auto p = __builtin_amdgcn_permlane16_swap(__float_as_uint(v[0]), __float_as_uint(v[1]), false, false);
            const auto q = __builtin_amdgcn_permlane16_swap(__float_as_uint(v[2]), __float_as_uint(v[3]), false, false);
            const auto r = __builtin_amdgcn_permlane32_swap(__float_as_uint(op(__uint_as_float(p[0]), __uint_as_float(p[1]))),
                                                            __float_as_uint(op(__uint_as_float(q[0]), __uint_as_float(q[1]))), false, false);
            const unsigned z = __float_as_uint(fin(op(__uint_as_float(r[0]), __uint_as_float(r[1]))));
            const auto u = __builtin_amdgcn_permlane32_swap(z, z, false, false);
            const auto e = __builtin_amdgcn_permlane16_swap(u[0], u[0], false, false), f = __builtin_amdgcn_permlane16_swap(u[1], u[1], false, false);
            v[0] = __uint_as_float(e[0]); v[1] = __uint_as_float(e[1]); v[2] = __uint_as_float(f[0]); v[3] = __uint_as_float(f[1]);
        };
#pragma unroll
        for (int t = 0; t < 4; ++t) m[t] = amax3(x[t][0], x[t][1], x[t][2]);
        asm("v_max_f32 %0, %0, %4\n\tv_max_f32 %1, %1, %5\n\tv_max_f32 %2, %2, %6\n\tv_max_f32 %3, %3, %7\n\ts_nop 0"
            : "+v"(m[0]), "+v"(m[1]), "+v"(m[2]), "+v"(m[3]) : "v"(x[0][3]), "v"(x[1][3]), "v"(x[2][3]), "v"(x[3][3]));
        across(m, [](float p, float q) { float r; asm("v_max_f32 %0, %1, %2\n\ts_nop 0" : "=v"(r) : "v"(p), "v"(q)); return r; },
               [](float p) { return p * -LOG2E; });
        SBAR();
#pragma unroll
        for (int t = 0; t < 4; ++t)
#pragma unroll
            for (int r = 0; r < 4; ++r) x[t][r] = __builtin_fmaf(x[t][r], LOG2E, m[t]);
        SBAR();
#pragma unroll
        for (int t = 0; t < 4; ++t)
#pragma unroll
            for (int r = 0; r < 4; ++r) x[t][r] = __builtin_amdgcn_exp2f(x[t][r]);
        SBAR();
#pragma unroll
        for (int t = 0; t < 4; ++t) m[t] = (x[t][0] + x[t][1]) + (x[t][2] + x[t][3]);
        asm volatile("s_waitcnt lgkmcnt(0)" : "+v"(vp[0][0]), "+v"(vp[0][1]), "+v"(vp[1][0]), "+v"(vp[1][1]) :: "memory");
        u32x4 ve[2][2];
#pragma unroll
        for (int u = 0; u < 2; ++u)
#pragma unroll
            for (int f = 0; f < 2; ++f) ve[u][f] = u32x4{vp[u][f][0], vp[u][f][1], va[u][f][0], va[u][f][1]};
        across(m, [](float p, float q) { return p + q; }, [](float p) { return __builtin_amdgcn_rcpf(p); });
        asm volatile("" : "+v"(ve[0][0]), "+v"(ve[0][1]), "+v"(ve[1][0]), "+v"(ve[1][1]));
        SBAR();
#pragma unroll
        for (int t = 0; t < 4; ++t)
#pragma unroll
            for (int r = 0; r < 4; ++r) x[t][r] *= m[t];
        SBAR();
#pragma unroll
        for (int t = 0; t < 4; ++t)
#pragma unroll
            for (int h = 0; h < 2; ++h) a[t][h] = pack_bf16(x[t][2 * h], x[t][2 * h + 1]);
        SBAR();
        f32x4 o0[4], o1[4];
#pragma unroll
        for (int t = 0; t < 4; ++t) {
            const bf16x8 pb = bf8(u32x4{a[t][0] & ~cm01, a[t][1] & ~cm23, a[t][0] & cm01, a[t][1] & cm23});
            o0[t] = MFMA16(bf8((t & 1) ? va[t >> 1][0] : ve[t >> 1][0]), pb, zero4);
            o1[t] = MFMA16(bf8((t & 1) ? va[t >> 1][1] : ve[t >> 1][1]), pb, zero4);
        }
        SBAR();
#pragma unroll
        for (int t = 0; t < 4; ++t)
            st16(ofrag + t * 16384, qo, u32x4{pack_bf16(o0[t][0], o0[t][1]), pack_bf16(o0[t][2], o0[t][3]),
                                                 pack_bf16(o1[t][0], o1[t][1]), pack_bf16(o1[t][2], o1[t][3])});
    }
}

DI int out_row(int panel, bool localrows, int T, int a) {
    return localrows ? (panel * 128 + (T >> 2) * 64 + 2 * (16 * (T & 1) + a) + ((T >> 1) & 1)) : (panel * 128 + T * 16 + a);
}
DI int out_swz(int k) { return ((k >> 1) & 1) | (k & 4); }
DI unsigned wo_off(int w, int lane) {
    const int kl = lane >> 3;
    unsigned v = (unsigned)(w * 8 + kl) * 2048u + (unsigned)(ids().co * 128 + ((lane & 7) ^ out_swz(kl)) * 16);
    asm volatile("" : "+v"(v));
    return v;
}
DI void wo_load(const void* Wo, unsigned voff, int i0, int i1, unsigned lds0, int w) {
#pragma unroll
    for (int i = i0; i < i1; ++i) glds16((const char*)Wo + (size_t)i * 65536, voff, lds0 + (w + 4 * i) * 1024);
}
DI void res_load(u32x4 (&r)[4], const void* Xin, bool localrows, int w, int lane) {
    const Ids I = ids();
#pragma unroll
    for (int i = 0; i < 4; ++i) {
        const int row = (w + 4 * i) * 8 + (lane >> 3);
        asm volatile("global_load_dwordx4 %0, %1, %2 sc1" : "=&v"(r[i])
                     : "v"((unsigned)out_row(I.po, localrows, row >> 4, row & 15) * 2048u + (unsigned)(I.co * 128 + (((lane & 7) ^ (lane >> 3)) * 16))),
                       "s"(Xin) : "memory");
    }
}
DI void res_write(u32x4 (&r)[4], char* smem, int off, int w, int lane) {
    asm volatile("" : "+v"(r[0]), "+v"(r[1]), "+v"(r[2]), "+v"(r[3]));
#pragma unroll
    for (int i = 0; i < 4; ++i) *(__attribute__((address_space(3))) u32x4*)((lds_ptr)smem + off + (w + 4 * i) * 1024 + lane * 16) = r[i];
}

template <bool AFTER_LOCAL>
DI void out_compute(char* smem, int w, char* ws, int set, u64 tag, const __hip_bfloat16* __restrict__ ofrag,
                    __hip_bfloat16* __restrict__ Xout, float* __restrict__ statp SP_PARAM) {
    constexpr int XOFF = AFTER_LOCAL ? 131072 : 98304;
    const int lane = lane_id();
    asm volatile("" : "+s"(w));
    const Ids I = ids();
    const int panel = I.po, ct = I.co;
    const int l15 = lane & 15, g = lane >> 4;
    auto orow = [&](int T, int a) -> int { return out_row(panel, AFTER_LOCAL, T, a); };
    const int q4 = l15 >> 2, p4 = l15 & 3;
    const int krow = 4 * g + q4;
    const int sw = out_swz(krow);
    const lds_cptr sm = (lds_cptr)smem;
    lds_cptr bw[4], xo[4];
#pragma unroll
    for (int t = 0; t < 4; ++t) {
        bw[t] = sm + krow * 128 + ((((t >> 1) * 4 + (p4 >> 1) * 2 + (t & 1)) ^ sw) * 16) + (p4 & 1) * 8;
        xo[t] = sm + XOFF + (w * 32 + l15) * 128 + ((((t >> 1) * 4 + (g >> 1) * 2 + (t & 1)) ^ (l15 & 7)) * 16) + (g & 1) * 8;
    }
    unsigned so[2], sto, ao[2];
#pragma unroll
    for (int r2 = 0; r2 < 2; ++r2) {
        so[r2] = (unsigned)orow(2 * w + r2, l15 & 7) * 2048u + (unsigned)(ct * 128 + (l15 >> 3) * 64 + g * 16);
        ao[r2] = (unsigned)((panel * 8 + 2 * w + r2) * 32768 + lane * 16);
    }
    sto = (unsigned)(((panel * 16 + ct) * 128 + 2 * w * 16 + l15) * 8);
    asm volatile("" : "+v"(bw[0]), "+v"(bw[1]), "+v"(bw[2]), "+v"(bw[3]), "+v"(xo[0]), "+v"(xo[1]), "+v"(xo[2]), "+v"(xo[3]),
                 "+v"(so[0]), "+v"(so[1]), "+v"(sto), "+v"(ao[0]), "+v"(ao[1]));
    const __amdgpu_buffer_rsrc_t ra = __builtin_amdgcn_make_buffer_rsrc((void*)ofrag, 0, 4 << 20, 0x00020000);
    asm volatile("" :: "s"(ra));
    u64 land;
    STAMP(AFTER_LOCAL ? 3 : 10);
    signal_poll<true, false>(w, ws, flag_off(set, I.pq, l15, I.cq), flag_off(set, panel, ct, l15), 0xffffull, tag, land);
    BAR();
    STAMP(AFTER_LOCAL ? 4 : 11);
#define AWIN 6
    u32x4 a[AWIN][2];
    auto lda = [&](int j) {
        a[j % AWIN][0] = __builtin_amdgcn_raw_buffer_load_b128(ra, ao[0] + (j & 3) * 1024, (j >> 2) * 4096, 16);
        a[j % AWIN][1] = __builtin_amdgcn_raw_buffer_load_b128(ra, ao[1] + (j & 3) * 1024, (j >> 2) * 4096, 16);
    };
#pragma unroll
    for (int j = 0; j < AWIN; ++j) { lda(j); SBAR(); }
    auto boff = [&](int j) { return (j & 15) * 4096 - ((!AFTER_LOCAL && j >= 24) ? 32768 : 0); };
    s16x4 F[2][4][2];
#pragma unroll
    for (int h = 0; h < 2; ++h)
#pragma unroll
        for (int t = 0; t < 4; ++t) { F[h][t][0] = lds_tr(bw[t] + boff(h)); F[h][t][1] = lds_tr(bw[t] + boff(h) + 2048); }
    f32x4 acc[2][4];
#pragma unroll
    for (int r2 = 0; r2 < 2; ++r2)
#pragma unroll
        for (int c = 0; c < 4; ++c) {
            const u32x2 xr = *(const __attribute__((address_space(3))) u32x2*)(xo[c] + r2 * 2048);
            acc[r2][c] = f32x4{bf_lo(xr[0]), bf_hi(xr[0]), bf_lo(xr[1]), bf_hi(xr[1])};
        }
    WAIT_LGKM();
#pragma unroll
    for (int j = 0; j < 32; ++j) {
        s16x4 (&C)[4][2] = F[j & 1];
        const bf16x8 a0 = __builtin_bit_cast(bf16x8, a[j % AWIN][0]), a1 = __builtin_bit_cast(bf16x8, a[j % AWIN][1]);
        const int nb = boff(j + 2);
        if (j + 2 == 16 || (!AFTER_LOCAL && j + 2 == 24)) {
#pragma unroll
            for (int t = 0; t < 4; ++t) bw[t] += 65536;
            asm volatile("" : "+v"(bw[0]), "+v"(bw[1]), "+v"(bw[2]), "+v"(bw[3]));
        }
#pragma unroll
        for (int t = 0; t < 4; ++t) {
            const bf16x8 bb = cat8(C[t][0], C[t][1]);
            acc[0][t] = MFMA16(bb, a0, acc[0][t]);
            if (t > 0 && j < 30) C[t - 1][1] = lds_tr(bw[t - 1] + nb + 2048);
            SBAR();
            acc[1][t] = MFMA16(bb, a1, acc[1][t]);
            if (j < 30) C[t][0] = lds_tr(bw[t] + nb);
            if (t == 3 && j < 30) C[3][1] = lds_tr(bw[3] + nb + 2048);
            if (t == 3 && j + AWIN < 32) lda(j + AWIN);
            SBAR();
        }
    }
    asm volatile("" :: "v"(land));
    STAMP(AFTER_LOCAL ? 5 : 12);
    u32x4 y[2][2];
#pragma unroll
    for (int r2 = 0; r2 < 2; ++r2)
#pragma unroll
        for (int u = 0; u < 2; ++u) {
            const f32x4 c0 = acc[r2][2 * u], c1 = acc[r2][2 * u + 1];
            const auto sa = __builtin_amdgcn_permlane16_swap(pack_bf16(c0[0], c0[1]), pack_bf16(c1[0], c1[1]), false, false);
            const auto sb = __builtin_amdgcn_permlane16_swap(pack_bf16(c0[2], c0[3]), pack_bf16(c1[2], c1[3]), false, false);
            y[r2][u] = u32x4{sa[0], sb[0], sa[1], sb[1]};
            if (u) {
                u32x4 za, zb;
#pragma unroll
                for (int e = 0; e < 4; ++e) {
                    za[e] = __builtin_amdgcn_update_dpp(y[r2][0][e], y[r2][1][e], 0x128, 0xF, 0xC, false);
                    zb[e] = __builtin_amdgcn_update_dpp(y[r2][1][e], y[r2][0][e], 0x128, 0xF, 0x3, false);
                }
                st16(Xout, so[r2], za);
                st16(Xout, so[r2] + (AFTER_LOCAL ? 16 : 8) * 2048, zb);
                SBAR();
            }
        }
    if (!AFTER_LOCAL && statp == nullptr) return;
    const bf16x8 ones = __builtin_bit_cast(bf16x8, (u32x4{0x3f803f80u, 0x3f803f80u, 0x3f803f80u, 0x3f803f80u}));
    const f32x4 zero4 = {0, 0, 0, 0};
    f32x4 d1[2], d2[2];
#pragma unroll
    for (int r2 = 0; r2 < 2; ++r2) {
        const bf16x8 y0 = __builtin_bit_cast(bf16x8, y[r2][0]), y1 = __builtin_bit_cast(bf16x8, y[r2][1]);
        d2[r2] = MFMA16(y0, y0, zero4); d1[r2] = MFMA16(ones, y0, zero4);
        d2[r2] = MFMA16(y1, y1, d2[r2]); d1[r2] = MFMA16(ones, y1, d1[r2]);
    }
    const bool b0 = p4 & 1, b1 = p4 & 2;
    if (g == q4) {
#pragma unroll
        for (int r2 = 0; r2 < 2; ++r2) {
            float e0 = d2[r2][0], e1 = d2[r2][1], e2 = d2[r2][2], e3 = d2[r2][3];
            asm("" : "+v"(e0), "+v"(e1), "+v"(e2), "+v"(e3));
            const float lo = b0 ? e1 : e0, hi = b0 ? e3 : e2;
            const f32x2 sv = {d1[r2][0], b1 ? hi : lo};
            if (r2) st8o<128>(statp, sto, sv); else st8o<0>(statp, sto, sv);
        }
    }
}

#define ROWSUM(p0, p1, c) __builtin_amdgcn_smfmac_f32_16x16x64_bf16(sel, __builtin_bit_cast(bf16v_t, __builtin_shufflevector(p0, p1, 0, 1, 2, 3, 4, 5, 6, 7)), c, sidx, 0, 0)
constexpr int ATT_STAGE = 32768;
typedef __attribute__((ext_vector_type(8))) unsigned u32x8;
typedef __attribute__((ext_vector_type(32))) unsigned u32x32;

DI void attn_loader(char* smem, int w, char* ws, int set, u64 tag, const __hip_bfloat16* __restrict__ kvf, const void* Wo, const void* Xin SP_PARAM) {
    const int lane = lane_id();
    asm volatile("" : "+s"(w));
    const Ids I = ids();
    const int panel = I.pq, ct = I.cq;
    const unsigned lds0 = (unsigned)(uintptr_t)smem;
    const char* kvh = (const char*)kvf + (size_t)(ct * 2 + (w >> 1)) * 262144 + (w & 1) * 8192;
    const unsigned voff = (unsigned)(lane * 16);
    auto order = [&](int st) { return ((panel & 8) + st - ((st - 1 < (panel & 7)) ? 1 : 0)) & 15; };
    auto load = [&](int st) {
        const char* src = kvh + (size_t)order(st) * 16384;
        const unsigned dst = lds0 + w * 8192 + (st & 3) * ATT_STAGE;
#pragma unroll
        for (int u = 0; u < 2; ++u)
            asm volatile("s_mov_b32 m0, %0\n\ts_nop 0\n\tglobal_load_lds_dwordx4 %1, %2 sc1\n\tglobal_load_lds_dwordx4 %1, %2 offset:1024 sc1\n\t"
                         "global_load_lds_dwordx4 %1, %2 offset:2048 sc1\n\tglobal_load_lds_dwordx4 %1, %2 offset:3072 sc1"
                         :: "s"(dst + u * 4096), "v"(voff), "s"(src + u * 4096) : "memory");
    };
    const unsigned wv = wo_off(w, lane);
    const unsigned poff = flag_off(set, ct, panel, lane & 15);
    u64 land;
    __builtin_amdgcn_s_setprio(3);
    BAR();
    signal_poll<false, true>(w, ws, flag_off(set, ct, lane & 15, panel), poff, 1ull << order(1), tag, land);
    STAMP(9);
    load(1);
    asm volatile("global_load_dwordx2 %0, %1, %2 sc1\n\ts_waitcnt vmcnt(0)" : "+v"(land) : "v"(poff), "s"(ws + WS_FLAG) : "memory");
    STAMP(10);
    BAR();
    if (!__all(land == tag)) flag_poll(ws, poff, tag);
    load(2); load(3);
    __builtin_amdgcn_s_setprio(0);
    STAMP(11);
#pragma unroll 1
    for (int st = 1; st < 13; ++st) {
        VM_BAR(8);
        if (st == 6) wo_load(Wo, wv, 24, 32, lds0 + 32768, w);
        load(st + 3);
    }
    VM_BAR(8); wo_load(Wo, wv, 0, 8, lds0, w);
    VM_BAR(8); wo_load(Wo, wv, 8, 16, lds0, w);
    u32x4 res[4];
    res_load(res, Xin, false, w, lane);
    BAR();     wo_load(Wo, wv, 16, 24, lds0, w);
    STAMP(12);
    BAR();
    asm volatile("s_waitcnt vmcnt(0)" ::: "memory");
    res_write(res, smem, 98304, w, lane);
    WAIT_LGKM_BAR();
}

DI void attn_compute(char* smem, int w, const u32x4 (&q4)[4], const __hip_bfloat16* __restrict__ kvf, __hip_bfloat16* __restrict__ ofrag SP_PARAM) {
    const int lane = lane_id();
    asm volatile("" : "+s"(w));
    const int wm = w & 1, wn = w >> 1;
    const Ids I = ids();
    const int panel = I.pq, head = I.cq * 2 + wn;
    const int l15 = lane & 15;
    const unsigned se = ((l15 & 2) == 0 && (l15 & 4) == 0) ? 0x3f803f80u : 0u, so = ((l15 & 2) == 0 && (l15 & 4) != 0) ? 0x3f803f80u : 0u;
    const bf8_t sel = __builtin_bit_cast(bf8_t, u32x4{se, se, so, so});
    const int sidx = (l15 & 1) ? (int)0xEEEEEEEEu : 0x44444444;
    const lds_cptr sm = (lds_cptr)smem + wn * 16384 + lane * 16;
    const f32x16 z16 = {0, 0, 0, 0, 0, 0, 0, 0, 0, 0, 0, 0, 0, 0, 0, 0};
    const bf16x8 qA0 = __builtin_bit_cast(bf16x8, q4[0]), qA1 = __builtin_bit_cast(bf16x8, q4[1]);
    const bf16x8 qB0 = __builtin_bit_cast(bf16x8, q4[2]), qB1 = __builtin_bit_cast(bf16x8, q4[3]);
    f32x16 oA, oB, nmA, nmB, sA, sB;
    f32x4 lsA, lsB;
    u32x8 pB;
    bf16x8 v11;
    asm volatile(".irp r,2,4,6,8,10,12,14,16,18,20,22,24,26,28,30,32,106,108,110,112,134,136,138,140,170,172\n\tv_mov_b64 v[\\r:\\r+1], 0\n\t.endr"
                 : "={v[2:17]}"(oA), "={v[18:33]}"(oB), "={v[106:113]}"(pB), "={v[134:137]}"(lsA), "={v[138:141]}"(lsB), "={v[170:173]}"(v11));
    WAIT_LGKM_BAR();
    STAMP(8);
    bf16x8 k00 = lds_b128(sm), k10 = lds_b128(sm + 1024), k01 = lds_b128(sm + 4096), k11 = lds_b128(sm + 4096 + 1024);
    bf16x8 v00 = lds_b128(sm + 2048), v10 = lds_b128(sm + 3072), v01 = lds_b128(sm + 4096 + 2048);
    {
        sA = MFMA32(k00, qA0, z16); sB = MFMA32(k00, qB0, z16);
        sA = MFMA32(k10, qA1, sA); sB = MFMA32(k10, qB1, sB);
        float mA = sA[0], mB = sB[0];
#pragma unroll
        for (int r = 1; r < 16; ++r) { mA = fmaxf(mA, sA[r]); mB = fmaxf(mB, sB[r]); }
        mA = max_h(mA); mB = max_h(mB);
#pragma unroll
        for (int r = 0; r < 16; ++r) { nmA[r] = -mA; nmB[r] = -mB; sA[r] -= mA; }
    }
    const unsigned a0 = (unsigned)(uintptr_t)sm;
    asm volatile(
        ".macro ATT_RD dst, y\n\t"
        "ds_read_b128 v[\\dst:\\dst+3], v[175+(((\\y)&0x1ffff)>>16)] offset:(\\y)&0xffff\n\t"
        ".endm\n\t"
        ".macro ATT_E4 c\n\t"
        "v_exp_f32 v[\\c], v[\\c]\n\tv_exp_f32 v[\\c+1], v[\\c+1]\n\tv_exp_f32 v[\\c+2], v[\\c+2]\n\tv_exp_f32 v[\\c+3], v[\\c+3]\n\t"
        ".endm\n\t"
        ".macro ATT_C4 p, c\n\t"
        "v_cvt_pk_bf16_f32 v[\\p], v[\\c], v[\\c+1]\n\tv_cvt_pk_bf16_f32 v[\\p+1], v[\\c+2], v[\\c+3]\n\t"
        "v_cvt_pk_bf16_f32 v[\\p+2], v[\\c+4], v[\\c+5]\n\tv_cvt_pk_bf16_f32 v[\\p+3], v[\\c+6], v[\\c+7]\n\t"
        ".endm\n\t"
        ".macro ATT_STEP cur, nxt, ka, kb, q, nm, oc, od, lsd, pc, pd, vd1, vc0, r0, y0, r1, y1, bar, rd\n\t"
        "v_mfma_f32_32x32x16_bf16 v[\\nxt:\\nxt+15], v[\\ka:\\ka+3], v[\\q:\\q+3], v[\\nm:\\nm+15]\n\t"
        "ATT_E4 \\cur\n\t"
        "v_mfma_f32_32x32x16_bf16 v[\\nxt:\\nxt+15], v[\\kb:\\kb+3], v[\\q+4:\\q+7], v[\\nxt:\\nxt+15]\n\t"
        "ATT_E4 \\cur+4\n\t"
        "v_mfma_f32_32x32x16_bf16 v[\\od:\\od+15], v[\\vd1:\\vd1+3], v[\\pd+4:\\pd+7], v[\\od:\\od+15]\n\t"
        "s_waitcnt lgkmcnt(0)\n\t"
        "ATT_C4 \\pc, \\cur\n\t"
        "v_exp_f32 v[\\cur+8], v[\\cur+8]\n\tv_exp_f32 v[\\cur+9], v[\\cur+9]\n\t"
        "v_mfma_f32_32x32x16_bf16 v[\\oc:\\oc+15], v[\\vc0:\\vc0+3], v[\\pc:\\pc+3], v[\\oc:\\oc+15]\n\t"
        ".if \\bar\n\ts_barrier\n\t.endif\n\t"
        "ATT_E4 \\cur+10\n\t"
        "v_smfmac_f32_16x16x64_bf16 v[\\lsd:\\lsd+3], v[130:133], v[\\pd:\\pd+7], v174\n\t"
        ".if \\rd & 4\n\tATT_RD 142, 32768\n\tATT_RD 150, 33792\n\tATT_RD 158, 34816\n\t.endif\n\t"
        ".if \\rd & 1\n\tATT_RD \\r0, \\y0\n\t.endif\n\t"
        ".if \\rd & 2\n\tATT_RD \\r1, \\y1\n\t.endif\n\t"
        "v_exp_f32 v[\\cur+14], v[\\cur+14]\n\tv_exp_f32 v[\\cur+15], v[\\cur+15]\n\t"
        "ATT_C4 \\pc+4, \\cur+8\n\t"
        ".if \\rd & 4\n\ts_waitcnt lgkmcnt(3)\n\t.endif\n\t"
        ".endm\n\t"
        ".macro ATT_TILE p, y1, y2, bar, ra=3, rb=3\n\t"
        "ATT_STEP 66, 82, 142+\\p, 150+\\p, 122, 50, 2, 18, 138, 98, 106, 170-\\p, 158+\\p, 170-\\p, \\y1+3072, 142+\\p, \\y2, \\bar, \\ra\n\t"
        "ATT_STEP 82, 66, 146-\\p, 154-\\p, 114, 34, 18, 2, 134, 106, 98, 166+\\p, 158+\\p, 158+\\p, \\y2+2048, 150+\\p, \\y2+1024, 0, \\rb\n\t"
        ".endm\n\t"
        ".macro ATT_STAGE b\n\t"
        "ATT_TILE 0, \\b+4096, \\b+8192, 0\n\t"
        "ATT_TILE 4, \\b+8192, \\b+12288, 0\n\t"
        "ATT_TILE 0, \\b+12288, \\b+32768, 1\n\t"
        "ATT_TILE 4, \\b+32768, \\b+36864, 0\n\t"
        ".endm\n\t"
        "v_add_u32 v176, 0x10000, v175\n\t"
        "s_mov_b32 s96, 4\n\t"
        "s_waitcnt vmcnt(0) lgkmcnt(0)\n\t"
        "s_barrier\n\t"
        "ATT_TILE 0, 4096, 8192, 0\n\tATT_TILE 4, 8192, 12288, 0\n\tATT_TILE 0, 12288, 0, 0, 1, 0\n\tATT_TILE 4, 32768, 36864, 1, 7, 3\n\t"
        "s_branch .Latt_entry_%=\n\t"
        ".p2align 6\n"
        ".Latt_loop_%=:\n\t"
        "ATT_STAGE 0\n"
        ".Latt_entry_%=:\n\t"
        "ATT_STAGE 32768\n\tATT_STAGE 65536\n\t"
        "s_sub_u32 s96, s96, 1\n\t"
        "ATT_STAGE 98304\n\t"
        "s_cmp_lg_u32 s96, 0\n\t"
        "s_cbranch_scc1 .Latt_loop_%=\n\t"
        "s_nop 1\n\t"
        "v_mfma_f32_32x32x16_bf16 v[18:33], v[170:173], v[110:113], v[18:33]\n\t"
        "v_smfmac_f32_16x16x64_bf16 v[138:141], v[130:133], v[106:113], v174\n\t"
        "s_waitcnt lgkmcnt(0)\n\ts_nop 10\n\t"
        ".purgem ATT_RD\n\t.purgem ATT_E4\n\t.purgem ATT_C4\n\t.purgem ATT_STEP\n\t.purgem ATT_TILE\n\t.purgem ATT_STAGE"
        : "+{v[2:17]}"(oA), "+{v[18:33]}"(oB), "+{v[66:81]}"(sA), "+{v[106:113]}"(pB), "+{v[134:137]}"(lsA), "+{v[138:141]}"(lsB),
          "+{v[142:145]}"(k00), "+{v[146:149]}"(k01), "+{v[150:153]}"(k10), "+{v[154:157]}"(k11),
          "+{v[158:161]}"(v00), "+{v[162:165]}"(v01), "+{v[166:169]}"(v10), "+{v[170:173]}"(v11)
        : "{v[34:49]}"(nmA), "{v[50:65]}"(nmB), "{v[114:117]}"(qA0), "{v[118:121]}"(qA1), "{v[122:125]}"(qB0), "{v[126:129]}"(qB1),
          "{v[130:133]}"(sel), "{v174}"(sidx), "{v175}"(a0)
        : "memory", "scc", "s96", "v176", "v82", "v83", "v84", "v85", "v86", "v87", "v88", "v89", "v90", "v91", "v92", "v93", "v94", "v95",
          "v96", "v97", "v98", "v99", "v100", "v101", "v102", "v103", "v104", "v105");
    STAMP(9);
    float lA = lsA[0] + lsA[1], lB = lsB[0] + lsB[1];
    const unsigned dst = (unsigned)((((panel * 8 + wm * 4 + ((lane & 31) >> 4)) * 32 + head) * 64 + (lane & 15) + (lane >> 5) * 16) * 16);
    auto store = [&] {
#pragma unroll
    for (int u = 0; u < 2; ++u) {
        const f32x16 e = (u ? oB : oA) * __builtin_amdgcn_rcpf(u ? lB : lA);
        st16o<0>((char*)ofrag + u * 65536, dst, u32x4{pack_bf16(e[0], e[1]), pack_bf16(e[2], e[3]), pack_bf16(e[8], e[9]), pack_bf16(e[10], e[11])});
        st16o<512>((char*)ofrag + u * 65536, dst, u32x4{pack_bf16(e[4], e[5]), pack_bf16(e[6], e[7]), pack_bf16(e[12], e[13]), pack_bf16(e[14], e[15])});
    }
    };
    store();
    {
        const f32x16 os = oA + oB;
        const float osum = (((os[0] + os[1]) + (os[2] + os[3])) + ((os[4] + os[5]) + (os[6] + os[7]))) +
                           (((os[8] + os[9]) + (os[10] + os[11])) + ((os[12] + os[13]) + (os[14] + os[15])));
        const bool bad = !(lA < 1.0e24f) || !(lB < 1.0e24f) || !(fabsf(osum) < 3.0e38f);
        if (__any(bad)) {
            const __amdgpu_buffer_rsrc_t rk = __builtin_amdgcn_make_buffer_rsrc((char*)kvf + (size_t)head * 262144, 0, 262144, 0x00020000);
            oA = z16; oB = z16; lA = 0.f; lB = 0.f;
            float mA = -INFINITY, mB = -INFINITY;
#pragma unroll 1
            for (int t = 0; t < 64; ++t) {
                bf16x8 f[4];
#pragma unroll
                for (int i = 0; i < 4; ++i) f[i] = __builtin_bit_cast(bf16x8, __builtin_amdgcn_raw_buffer_load_b128(rk, lane * 16 + i * 1024, t * 4096, 16));
                auto upd = [&](bf16x8 q0, bf16x8 q1, f32x16& o, float& l, float& m) {
                    f32x16 s = MFMA32(f[0], q0, z16); s = MFMA32(f[1], q1, s);
                    float tm = s[0];
#pragma unroll
                    for (int r = 1; r < 16; ++r) tm = fmaxf(tm, s[r]);
                    tm = max_h(tm);
                    const float mn = fmaxf(m, tm);
                    const float alpha = __builtin_amdgcn_exp2f(m - mn);
                    m = mn;
#pragma unroll
                    for (int r = 0; r < 16; ++r) { s[r] = __builtin_amdgcn_exp2f(s[r] - mn); o[r] *= alpha; }
                    const u32x4 p0 = {pack_bf16(s[0], s[1]), pack_bf16(s[2], s[3]), pack_bf16(s[4], s[5]), pack_bf16(s[6], s[7])};
                    const u32x4 p1 = {pack_bf16(s[8], s[9]), pack_bf16(s[10], s[11]), pack_bf16(s[12], s[13]), pack_bf16(s[14], s[15])};
                    f32x4 t4 = {0, 0, 0, 0};
                    o = MFMA32(f[2], __builtin_bit_cast(bf16x8, p0), o);
                    o = MFMA32(f[3], __builtin_bit_cast(bf16x8, p1), o);
                    t4 = ROWSUM(p0, p1, t4);
                    l = l * alpha + (t4[0] + t4[1]);
                };
                upd(qA0, qA1, oA, lA, mA);
                upd(qB0, qB1, oB, lB, mB);
            }
            store();
        }
    }
}

DI void ln_row(const u32x4 (&v)[2], __hip_bfloat16* __restrict__ Y, unsigned off) {
    float s1 = 0.f, s2 = 0.f;
#pragma unroll
    for (int e = 0; e < 8; ++e) s1 = dot2bf(v[e >> 2][e & 3], 0x3f803f80u, s1);
    const float mean = wave_sum(s1) * (1.f / 1024.f);
#pragma unroll
    for (int e = 0; e < 8; ++e) {
        const float d0 = bf_lo(v[e >> 2][e & 3]) - mean, d1 = bf_hi(v[e >> 2][e & 3]) - mean;
        s2 += d0 * d0 + d1 * d1;
    }
    const float mb = round_bf16(mean);
    const float rden = __builtin_amdgcn_rcpf(round_bf16(__builtin_amdgcn_sqrtf(round_bf16(round_bf16(wave_sum(s2) * (1.f / 1024.f)) + 1e-5f))));
#pragma unroll
    for (int hh = 0; hh < 2; ++hh) {
        u32x4 y;
#pragma unroll
        for (int e = 0; e < 4; ++e) {
            const unsigned d = pack_bf16(bf_lo(v[hh][e]) - mb, bf_hi(v[hh][e]) - mb);
            y[e] = pack_bf16(bf_lo(d) * rden, bf_hi(d) * rden);
        }
        if (hh) st16o<1024>(Y, off, y); else st16o<0>(Y, off, y);
    }
}

extern "C" __global__ __launch_bounds__(512, 2) void mega_kernel(const __hip_bfloat16* x, const __hip_bfloat16* wqkvA,
                                                            const __hip_bfloat16* woA, const __hip_bfloat16* wqkvB,
                                                            const __hip_bfloat16* woB, __hip_bfloat16* out,
                                                            char* ws) {
    char* const smem = smem_base;
    typedef __hip_bfloat16 bf;
    const int w8 = __builtin_amdgcn_readfirstlane(threadIdx.x >> 6);
    if (w8 == 7 && (blockIdx.x >> 3) < 10) code_touch(blockIdx.x >> 3, threadIdx.x & 63);
    asm volatile("" :: "s"(x), "s"(wqkvA), "s"(woA), "s"(wqkvB), "s"(woB), "s"(out), "s"(ws));
    STAMP_DECL; STAMPR(14); STAMP(0);
    u64 tag;
    {
        const int lane = threadIdx.x & 63;
        const Ids I = ids();
        const unsigned off = (unsigned)((I.po * 128 + I.co * 8 + w8) * 2048 + lane * 16);
        u32x4 v[2];
        asm volatile("global_load_dwordx4 %0, %2, %3\n\tglobal_load_dwordx4 %1, %2, %3 offset:1024" : "=&v"(v[0]), "=&v"(v[1]) : "v"(off), "s"(x) : "memory");
        asm volatile("s_load_dwordx2 %0, %1, 0x0" : "=s"(tag) : "s"(ws + WS_EPOCH) : "memory");
        if (w8 == 7) {
            const int l = min(lane, 12), k = l % 5;
            const unsigned o = (l >= 10 || k == 3) ? flag_off(l, I.cq, I.pq, 0) : (k == 0 || k == 2) ? flag_off(l, I.pq, I.cq, 0) : flag_off(l, I.po, I.co, 0);
            touch_at(ws + WS_FLAG, o);
        } else if (w8 >= 4 && w8 - 4 < NTOUCH) touch(wqkvA, (w8 - 4) * 256 + blockIdx.x, lane);
        if (w8 == 7 || (w8 >= 4 && w8 - 4 < NTOUCH)) asm volatile("s_waitcnt vmcnt(1)" ::: "memory"); else asm volatile("s_waitcnt vmcnt(0)" ::: "memory");
        asm volatile("" : "+v"(v[0]), "+v"(v[1]));
        ln_row(v, (bf*)(ws + WS_Y0), off);
        asm volatile("s_waitcnt lgkmcnt(0)" : "+s"(tag) :: "memory");
        tag += 1;
    }
    if (w8 >= 4) {
        int w = w8 & 3;
#pragma unroll 1
        for (int it = 0; it < 2; ++it) {
            const int lane = lane_id();
            asm volatile("" : "+s"(x), "+s"(wqkvA), "+s"(woA), "+s"(wqkvB), "+s"(woB), "+s"(ws), "+s"(w));
            const unsigned lds0 = (unsigned)(uintptr_t)smem;
            u32x4 ca[8], cb[8];
            const unsigned wv = wo_off(w, lane);
            qkv_loader<true>(smem, w, ws, it * 5, tag, (const bf*)(ws + (it ? WS_X2 : WS_Y0)), wqkvA, (const float*)(ws + WS_ST + 262144),
                             ws + WS_CSP, it == 0, woA, [&] {
                if (w < 3) {
                    colsum_load(ca, wqkvA, w, lane); colsum_load(cb, wqkvB, w, lane);
                    asm volatile("s_waitcnt vmcnt(16)" ::: "memory");
                } else {
                    touch(woA, blockIdx.x, lane);
                    asm volatile("s_waitcnt vmcnt(1)" ::: "memory");
                }
            }, [&](int part) {
                if (part == 0) wo_load(woA, wv, 32 - WO0, 32, lds0, w); else wo_load(woA, wv, 32 - WO1, 32 - WO0, lds0, w);
            } SP_ARG);
            STAMP(3);
            u32x4 res[4];
            res_load(res, it ? (const void*)(ws + WS_X2) : x, true, w, lane);
            if (it) __builtin_amdgcn_s_sleep(WSL1); else __builtin_amdgcn_s_sleep(WSL0);
            wo_load(woA, wv, 0, WO2, lds0, w);
            BAR();
            wo_load(woA, wv, WO2, 32 - WO1, lds0, w);
            const bool cs = (it == 0 && w < 3);
            STAMP(4);
            BAR();
            asm volatile("s_waitcnt vmcnt(0)" ::: "memory");
            res_write(res, smem, 131072, w, lane);
            WAIT_LGKM_BAR();
            if (cs) {
                asm volatile("" : "+v"(cb[0]), "+v"(cb[1]), "+v"(cb[2]), "+v"(cb[3]), "+v"(cb[4]), "+v"(cb[5]), "+v"(cb[6]), "+v"(cb[7]));
                asm volatile("" : "+v"(ca[0]), "+v"(ca[1]), "+v"(ca[2]), "+v"(ca[3]), "+v"(ca[4]), "+v"(ca[5]), "+v"(ca[6]), "+v"(ca[7]));
                colsum_store(ca, ws + WS_CSP, w, lane);
                colsum_store(cb, ws + WS_CSP + 196608, w, lane);
                asm volatile("s_waitcnt vmcnt(0)" ::: "memory");
                flag_store(ws, 10 + w, ids().cq, ids().pq, tag, lane);
            }
            STAMP(5);
            qkv_loader<false>(smem, w, ws, it * 5 + 2, tag, (const bf*)(ws + (it ? WS_X3 : WS_X1)), wqkvB, (const float*)(ws + WS_ST + it * 524288), ws + WS_CSP + 196608,
                              false, nullptr, [] {}, [](int) {} SP_ARG);
            STAMP(8);
            attn_loader(smem, w, ws, it * 5 + 3, tag, (const bf*)(ws + WS_KVF + it * 8 * MBy), woB, ws + (it ? WS_X3 : WS_X1) SP_ARG);
            STAMP(13); STAMPR(15); STAMP_FLUSH(it);
        }
        if (blockIdx.x == 0 && w == 0 && lane_id() == 0) __hip_atomic_store((g_u64ptr)(ws + WS_EPOCH), tag, __ATOMIC_RELAXED, __HIP_MEMORY_SCOPE_AGENT);
    } else {
        __builtin_amdgcn_s_setprio(2);
#pragma unroll 1
        for (int it = 0; it < 2; ++it) {
            u32x4 q4[4];
            asm volatile("" : "+s"(ws), "+s"(wqkvA), "+s"(wqkvB));
            qkv_compute<true>(smem, w8, wqkvA, q4, nullptr, (bf*)(ws + WS_OF + it * 8 * MBy), it == 0 SP_ARG);
            STAMP(2);
            out_compute<true>(smem, w8, ws, it * 5 + 1, tag, (const bf*)(ws + WS_OF + it * 8 * MBy),
                              (bf*)(ws + (it ? WS_X3 : WS_X1)), (float*)(ws + WS_ST + it * 524288) SP_ARG);
            STAMP(6);
            qkv_compute<false>(smem, w8, wqkvB, q4, (bf*)(ws + WS_KVF + it * 8 * MBy), nullptr, false SP_ARG);
            attn_compute(smem, w8, q4, (const bf*)(ws + WS_KVF + it * 8 * MBy), (bf*)(ws + WS_OF + (it * 8 + 4) * MBy) SP_ARG);
            out_compute<false>(smem, w8, ws, it * 5 + 4, tag, (const bf*)(ws + WS_OF + (it * 8 + 4) * MBy),
                               it ? out : (bf*)(ws + WS_X2), it ? nullptr : (float*)(ws + WS_ST + 262144) SP_ARG);
            STAMP(13); STAMPR(15); STAMP_FLUSH(it);
        }
    }
}

extern "C" __global__ void zz_code_end_kernel() {}

extern "C" void kernel_launch(void* const* d_in, const int* in_sizes, int n_in, void* d_out, int out_size, void* d_ws,
                              size_t ws_size, hipStream_t stream) {
    typedef __hip_bfloat16 bf;
    (void)hipFuncSetAttribute((const void*)mega_kernel, hipFuncAttributeMaxDynamicSharedMemorySize, CH_LDS);
    mega_kernel<<<256, 512, CH_LDS, stream>>>((const bf*)d_in[0], (const bf*)d_in[1], (const bf*)d_in[2], (const bf*)d_in[4], (const bf*)d_in[5],
                                              (bf*)d_out, (char*)d_ws);
}
```

```cpp
#include <hip/hip_runtime.h>
#include <hip/hip_bf16.h>
#include <stdint.h>

typedef __attribute__((ext_vector_type(8))) short bf16x8;
typedef __attribute__((ext_vector_type(4))) short s16x4;
typedef __attribute__((ext_vector_type(4))) float f32x4;
typedef __attribute__((ext_vector_type(2))) float f32x2;
typedef __attribute__((ext_vector_type(16))) float f32x16;
typedef __attribute__((ext_vector_type(4))) unsigned u32x4;
typedef __attribute__((ext_vector_type(2))) unsigned u32x2;
typedef __attribute__((ext_vector_type(2))) __bf16 bf16x2_t;
typedef __attribute__((ext_vector_type(8))) __bf16 bf8_t;
typedef __attribute__((ext_vector_type(16))) __bf16 bf16v_t;
typedef __attribute__((address_space(3))) const char* lds_cptr;
typedef __attribute__((address_space(3))) char* lds_ptr;

#define DI __device__ __forceinline__
#define MFMA16(a, b, c) __builtin_amdgcn_mfma_f32_16x16x32_bf16((a), (b), (c), 0, 0, 0)
#define MFMA32(a, b, c) __builtin_amdgcn_mfma_f32_32x32x16_bf16((a), (b), (c), 0, 0, 0)
#define SBAR() __builtin_amdgcn_sched_barrier(0)
#define STAMP(i)
#define STAMPR(i)
#define STAMP_DECL
#define STAMP_FLUSH(it)
#define SP_PARAM
#define SP_ARG
typedef unsigned long long u64;
typedef __attribute__((ext_vector_type(32))) unsigned u32x32;
#define B2POS 0
#define WO2 7
#define WSL0 4
#define WSL1 15
#define WO0 3
#define WO1 7
#define SPRIO 3
#define QUNR 3
#define NTOUCH 3

constexpr float QSCALE = 0.17677669529663687f * 1.4426950408889634f;
constexpr float ATT_SCALE = 0.17677669529663687f;
constexpr float LOG2E = 1.4426950408889634f;
constexpr float NEG_MASK = -1e9f / ATT_SCALE;

DI int lane_id() { int l; asm volatile("v_mbcnt_lo_u32_b32 %0, -1, 0\n\tv_mbcnt_hi_u32_b32 %0, -1, %0" : "=v"(l)); return l; }
DI unsigned pack_bf16(float lo, float hi) {
    f32x2 v = {lo, hi};
    bf16x2_t r = __builtin_convertvector(v, bf16x2_t);
    return __builtin_bit_cast(unsigned, r);
}
DI float bf_lo(unsigned u) { return __uint_as_float(u << 16); }
DI float bf_hi(unsigned u) { return __uint_as_float(u & 0xffff0000u); }
DI float round_bf16(float x) { return bf_lo(pack_bf16(x, 0.f)); }
DI void ln_stats(float sum, float sumsq, float& mean_out, float& rstd_out) {
    float mean;
    asm("v_mul_f32 %0, 0x3a800000, %1" : "=v"(mean) : "v"(sum));
    const float var = fmaxf(__builtin_fmaf(-mean, mean, sumsq * (1.f / 1024.f)), 0.f);
    mean_out = round_bf16(mean);
    rstd_out = __builtin_amdgcn_rcpf(round_bf16(__builtin_amdgcn_sqrtf(round_bf16(round_bf16(var) + 1e-5f))));
}

DI void glds16(const void* sbase, unsigned voff, unsigned ldsaddr) {
    asm volatile("s_mov_b32 m0, %0\n\ts_nop 0\n\tglobal_load_lds_dwordx4 %1, %2"
                 :: "s"(ldsaddr), "v"(voff), "s"(sbase) : "memory");
}
DI void glds16c(const void* sbase, unsigned voff, unsigned ldsaddr) {
    asm volatile("s_mov_b32 m0, %0\n\ts_nop 0\n\tglobal_load_lds_dwordx4 %1, %2 sc1"
                 :: "s"(ldsaddr), "v"(voff), "s"(sbase) : "memory");
}
DI void st16(void* sbase, unsigned voff, u32x4 v) {
    asm volatile("global_store_dwordx4 %0, %1, %2 sc1\n\ts_nop 1" :: "v"(voff), "v"(v), "s"(sbase) : "memory");
}
template <int OFF> DI void st16o(void* sbase, unsigned voff, u32x4 v) {
    asm volatile("global_store_dwordx4 %0, %1, %2 offset:%3 sc1\n\ts_nop 1" :: "v"(voff), "v"(v), "s"(sbase), "n"(OFF) : "memory");
}
DI bf16x8 lds_b128(lds_cptr p) { return *(const __attribute__((address_space(3))) bf16x8*)p; }
DI s16x4 lds_tr(lds_cptr p) {
    return __builtin_amdgcn_ds_read_tr16_b64_v4i16((__attribute__((address_space(3))) s16x4*)p);
}
DI bf16x8 cat8(s16x4 a, s16x4 b) { return __builtin_shufflevector(a, b, 0, 1, 2, 3, 4, 5, 6, 7); }
DI float dot2bf(unsigned a, unsigned b, float c) {
    return __builtin_amdgcn_fdot2_f32_bf16(__builtin_bit_cast(bf16x2_t, a), __builtin_bit_cast(bf16x2_t, b), c, false);
}
DI float sum_g(float v) {
    auto a = __builtin_amdgcn_permlane16_swap(__float_as_uint(v), __float_as_uint(v), false, false);
    v = __uint_as_float(a[0]) + __uint_as_float(a[1]);
    auto b = __builtin_amdgcn_permlane32_swap(__float_as_uint(v), __float_as_uint(v), false, false);
    return __uint_as_float(b[0]) + __uint_as_float(b[1]);
}
DI float wave_sum(float v) {
    v += __builtin_bit_cast(float, __builtin_amdgcn_update_dpp(0, __builtin_bit_cast(int, v), 0xB1, 0xF, 0xF, true));
    v += __builtin_bit_cast(float, __builtin_amdgcn_update_dpp(0, __builtin_bit_cast(int, v), 0x4E, 0xF, 0xF, true));
    v += __builtin_bit_cast(float, __builtin_amdgcn_update_dpp(0, __builtin_bit_cast(int, v), 0x141, 0xF, 0xF, true));
    v += __builtin_bit_cast(float, __builtin_amdgcn_update_dpp(0, __builtin_bit_cast(int, v), 0x140, 0xF, 0xF, true));
    return sum_g(v);
}
DI float amax3(float a, float b, float c) { float r; asm("v_max3_f32 %0, %1, %2, %3" : "=v"(r) : "v"(a), "v"(b), "v"(c)); return r; }
DI float max2f(float a, float b) { return __builtin_amdgcn_fmed3f(a, b, INFINITY); }
DI float max_h(float v) {
    auto b = __builtin_amdgcn_permlane32_swap(__float_as_uint(v), __float_as_uint(v), false, false);
    return max2f(__uint_as_float(b[0]), __uint_as_float(b[1]));
}

template <int OFF> DI void st8o(void* sbase, unsigned voff, f32x2 v) {
    asm volatile("global_store_dwordx2 %0, %1, %2 offset:%3 sc1\n\ts_nop 1" :: "v"(voff), "v"(v), "s"(sbase), "n"(OFF) : "memory");
}
constexpr unsigned OFF_DUMP = 159744;
extern __shared__ __attribute__((aligned(16))) char smem_base[];
DI void touch_at(const void* p, unsigned off) {
    asm volatile("s_mov_b32 m0, %0\n\ts_nop 0\n\tglobal_load_lds_dword %1, %2"
                 :: "s"((unsigned)(uintptr_t)smem_base + OFF_DUMP), "v"(off), "s"(p) : "memory");
}
DI void touch(const void* p, int slice, int lane) { touch_at(p, (unsigned)(slice * 64 + lane) * 128u); }
DI void code_touch(int slice, int lane) {
    unsigned long long c0, c1;
    asm volatile("s_getpc_b64 s[96:97]\n\ts_add_u32 s96, s96, mega_kernel@rel32@lo+4\n\ts_addc_u32 s97, s97, mega_kernel@rel32@hi+12\n\ts_mov_b64 %0, s[96:97]\n\t"
                 "s_getpc_b64 s[96:97]\n\ts_add_u32 s96, s96, zz_code_end_kernel@rel32@lo+4\n\ts_addc_u32 s97, s97, zz_code_end_kernel@rel32@hi+12\n\ts_mov_b64 %1, s[96:97]"
                 : "=s"(c0), "=s"(c1) :: "s96", "s97", "scc");
    touch_at((const void*)c0, min((unsigned)(slice * 64 + lane) * 128u, (unsigned)(c1 - c0)));
}

#define BAR() asm volatile("s_barrier" ::: "memory")
#define WAIT_LGKM() do { SBAR(); __builtin_amdgcn_s_waitcnt(0xC07F); SBAR(); } while (0)
#define WAIT_LGKM_BAR() do { SBAR(); __builtin_amdgcn_s_waitcnt(0xC07F); asm volatile("s_barrier" ::: "memory"); SBAR(); } while (0)
#define VM_BAR(n) asm volatile("s_waitcnt vmcnt(" #n ")\n\ts_barrier" ::: "memory")
constexpr int CH_LDS = 163840;
typedef __attribute__((address_space(3))) unsigned* lds_uptr;
typedef __attribute__((address_space(1))) u64* g_u64ptr;

constexpr size_t MBy = 1 << 20;
constexpr size_t WS_X3 = 0, WS_X2 = 4 * MBy, WS_X1 = 8 * MBy, WS_OF = 12 * MBy  , WS_KVF = 36 * MBy  ,
                 WS_ST = 52 * MBy  , WS_CSP = 54 * MBy  , WS_FLAG = 55 * MBy  ,
                 WS_EPOCH = WS_CSP + 524288, WS_Y0 = 56 * MBy;
DI unsigned flag_off(int set, int group, int consumer, int producer) { return (unsigned)(set * 65536 + group * 4096 + consumer * 256 + producer * 8); }
DI void flag_store(char* ws, int set, int group, int producer, u64 tag, int lane) {
    asm volatile("" : "+s"(tag));
    if (lane < 16) __hip_atomic_store((g_u64ptr)(ws + WS_FLAG + flag_off(set, group, lane, producer)), tag, __ATOMIC_RELAXED, __HIP_MEMORY_SCOPE_AGENT);
}
DI void flag_poll(const char* ws, unsigned off, u64 tag) {
    asm volatile("" : "+s"(tag));
#pragma unroll 1
    for (int spin = 0; spin < (1 << 16); ++spin) {
        const u64 v = __hip_atomic_load((g_u64ptr)(ws + WS_FLAG + off), __ATOMIC_RELAXED, __HIP_MEMORY_SCOPE_AGENT);
        if (__all(v == tag)) break;
        __builtin_amdgcn_s_sleep(1);
    }
    asm volatile("" ::: "memory");
}
#define POLL_INS "global_atomic_or_x2 %[land], %[po], %[zero], %[base] sc0 sc1"
template <bool DRAIN, bool ALLPOLL>
DI void signal_poll(int follower, const char* ws, unsigned soff, unsigned poff, u64 pmask, u64 tag, u64& land) {
    u64 tagv, zero;
    asm volatile("s_not_b64 s[96:97], %[ts]\n\tv_mov_b64 %[tag], %[ts]\n\tv_mov_b64 %[zero], 0\n\tv_mov_b64 %[land], s[96:97]\n\t"
                 ".if %[dr]\n\ts_waitcnt vmcnt(0)\n\t.endif\n\ts_barrier\n\t"
                 "s_cmp_lg_u32 %[fl], 0\n\ts_cbranch_scc1 .Lsig_fol_%=\n\t"
                 "s_mov_b64 exec, 0xffff\n\t"
                 "global_store_dwordx2 %[so], %[tag], %[base] sc1\n"
                 ".if %[ap]\n.Lsig_fol_%=:\n.endif\n\t"
                 "s_mov_b64 exec, %[pm]\n\t"
                 "s_movk_i32 s96, 0x7fff\n"
                 ".Lsig_loop_%=:\n\t"
                 POLL_INS "\n\t"
                 "s_sleep 1\n\t"
                 "s_waitcnt vmcnt(3)\n\t"
                 "v_cmp_ne_u64 vcc, %[land], %[tag]\n\t"
                 "s_sub_u32 s96, s96, 1\n\t"
                 "s_cbranch_scc1 .Lsig_out_%=\n\t"
                 "s_cbranch_vccnz .Lsig_loop_%=\n"
                 ".Lsig_out_%=:\n\t"
                 "s_mov_b64 exec, -1\n"
                 ".if !%[ap]\n.Lsig_fol_%=:\n.endif"
                 : [land] "=&v"(land), [tag] "=&v"(tagv), [zero] "=&v"(zero)
                 : [fl] "s"(follower), [so] "v"(soff), [po] "v"(poff), [ts] "s"(tag), [base] "s"(ws + WS_FLAG), [pm] "s"(pmask), [ap] "n"(ALLPOLL ? 1 : 0), [dr] "n"(DRAIN ? 1 : 0)
                 : "memory", "scc", "vcc", "s96", "s97");
}
struct Ids { int pq, cq, po, co; };
DI Ids ids() {
    int bid = blockIdx.x; asm volatile("" : "+s"(bid));
    const int xcd = bid & 7, idx = bid >> 3;
    int pq, cq, po, co;
#define IDS_BODY pq = (xcd & 1) * 8 + (idx & 7); cq = (xcd >> 1) * 4 + (idx >> 3); po = (xcd & 3) * 4 + (idx & 3); co = (xcd >> 2) * 8 + (idx >> 2);
    IDS_BODY
    return Ids{pq, cq, po, co};
}

DI void slow_warm(unsigned dump, const void* W, int w, int lane) {
    int bid = blockIdx.x; asm volatile("" : "+s"(bid));
    const int xcd = bid & 7, idx = bid >> 3;
    const int sc = (3 - (int)((uintptr_t)W >> 7)) & 7;
    if ((sc >> 2) != ((xcd >> 1) & 1)) return;
    const char* src = (const char*)W + (idx + 32 * w) * 49152 + (sc + (xcd >> 2) * 8) * 128;
#pragma unroll
    for (int G = 0; G < 3; ++G) glds16(src + G * 2048, (unsigned)((lane >> 3) * 6144 + (lane & 7) * 16), dump);
}

template <int V> struct IC { static constexpr int value = V; };
template <bool LOCAL> struct QC {
    static constexpr int AROWS = LOCAL ? 144 : 128;
    static constexpr int ABYTES = AROWS * 128;
    static constexpr int STAGE = ABYTES + 24576;
    static constexpr int OFF_MEAN = 147456;
    static constexpr int OFF_RSTD = OFF_MEAN + 576;
    static constexpr int OFF_CS = OFF_RSTD + 576;
};
constexpr int OFF_EX = 131072;

DI unsigned colsum_off(int G, int lane) {
    const Ids I = ids();
    unsigned off = (unsigned)(I.pq * 64 + (lane >> 3)) * 6144u + (unsigned)(G * 2048 + I.cq * 128 + (lane & 7) * 16);
    asm volatile("" : "+v"(off));
    return off;
}
DI void colsum_load(u32x4 (&c)[8], const void* W, unsigned off) {
#define CS_NEXT "s_add_u32 s96, s96, 0xc000\n\ts_addc_u32 s97, s97, 0\n\t"
    asm volatile("s_cmp_eq_u64 %9, 0\n\ts_cbranch_scc1 .Lcs_skip_%=\n\ts_mov_b64 s[96:97], %9\n\t"
                 "global_load_dwordx4 %0, %8, s[96:97]\n\t" CS_NEXT "global_load_dwordx4 %1, %8, s[96:97]\n\t" CS_NEXT
                 "global_load_dwordx4 %2, %8, s[96:97]\n\t" CS_NEXT "global_load_dwordx4 %3, %8, s[96:97]\n\t" CS_NEXT
                 "global_load_dwordx4 %4, %8, s[96:97]\n\t" CS_NEXT "global_load_dwordx4 %5, %8, s[96:97]\n\t" CS_NEXT
                 "global_load_dwordx4 %6, %8, s[96:97]\n\t" CS_NEXT "global_load_dwordx4 %7, %8, s[96:97]\n"
                 ".Lcs_skip_%=:"
                 : "=&v"(c[0]), "=&v"(c[1]), "=&v"(c[2]), "=&v"(c[3]), "=&v"(c[4]), "=&v"(c[5]), "=&v"(c[6]), "=&v"(c[7])
                 : "v"(off), "s"(W) : "memory", "scc", "s96", "s97");
}
DI void colsum_store(u32x4 (&c)[8], char* ws, int mat, u64 tag, int G, int lane) {
    asm volatile("" : "+v"(c[0]), "+v"(c[1]), "+v"(c[2]), "+v"(c[3]), "+v"(c[4]), "+v"(c[5]), "+v"(c[6]), "+v"(c[7]));
    const Ids I = ids();
    const int m = lane & 15;
    const unsigned one = (m & 1) ? 0xbf800000u : 0xbf80u;
    const u32x4 sl = {(m >> 1) == 0 ? one : 0u, (m >> 1) == 1 ? one : 0u, (m >> 1) == 2 ? one : 0u, (m >> 1) == 3 ? one : 0u};
    f32x4 d = {0, 0, 0, 0};
#pragma unroll
    for (int i = 0; i < 8; ++i) d = MFMA16(__builtin_bit_cast(bf16x8, sl), __builtin_bit_cast(bf16x8, c[i]), d);
#pragma unroll
    for (int r = 0; r < 4; ++r)
        d[r] += __int_as_float(__builtin_amdgcn_update_dpp(0, __float_as_int(d[r]), 0x128, 0xF, 0xF, true));
    if (lane < 32 && (lane & 8) == 0)
        st16(ws + WS_CSP + mat * 196608, (unsigned)((I.cq * 16 + I.pq) * 768 + G * 256 + (lane & 7) * 32 + (lane >> 4) * 16), __builtin_bit_cast(u32x4, d));
    asm volatile("s_waitcnt vmcnt(0)" ::: "memory");
    flag_store(ws, (mat ? 10 : 13) + G, I.cq, I.pq, tag, lane);
}

template <bool LOCAL>
struct WFirst {
    unsigned v[2];
    DI WFirst(int w, int ct, int lane) {
#pragma unroll
        for (int i = 0; i < 2; ++i) {
            const int k = 8 * i + (lane >> 3), pos = lane & 7;
            v[i] = (unsigned)k * 6144u + (unsigned)(w * 1024 + ct * 64 + ((pos >> 1) ^ (((k >> 1) & 1) | (i << 1))) * 16 + (pos & 1) * 8) * 2u;
        }
        asm volatile("" : "+v"(v[0]), "+v"(v[1]));
    }
    DI void issue(const void* W, unsigned lds0, int w) const {
#pragma unroll
        for (int i = 0; i < 16; ++i)
            glds16((const char*)W + (size_t)(i >> 1) * (16 * 6144), v[i & 1], lds0 + (i >> 3) * QC<LOCAL>::STAGE + QC<LOCAL>::ABYTES + (w * 8 + (i & 7)) * 1024);
    }
};

template <bool LOCAL, class F, class F2>
DI void qkv_loader(char* smem, int w, char* ws, int set, u64 tag, const __hip_bfloat16* __restrict__ X, const __hip_bfloat16* __restrict__ W,
                   const float* __restrict__ statp, const char* __restrict__ csp, bool ident, const void* nextW, F tail, F2 mid SP_PARAM) {
    typedef QC<LOCAL> C;
    constexpr int NT = 16;
    const int lane = lane_id();
    asm volatile("" : "+s"(w));
    const int idx = blockIdx.x >> 3;
    const Ids I = ids();
    const int panel = I.pq, ct = I.cq;
    const int R0 = panel * 128;
    const unsigned lds0 = (unsigned)(uintptr_t)smem;
    auto grow = [&](int i) -> int {
        if (!LOCAL) return R0 + i;
        if (i < 128) { const int t = (i >> 4) & 3, a = i & 15; return R0 + (i & 64) + 2 * (16 * (t & 1) + a) + (t >> 1); }
        const int a = i & 15; const int r = R0 - 16 + 2 * (a & 7) + (a >> 3);
        return r < 0 ? 0 : r;
    };
    unsigned voffA[5], voffB[6];
#pragma unroll
    for (int i = 0; i < 6; ++i) {
        const int j = w + 4 * i, G = j >> 3, k = 8 * (j & 7) + (lane >> 3);
        const int pos = lane & 7, f = ((k >> 1) & 1) | (((k >> 3) & 1) << 1);
        const int seg = (pos >> 1) ^ f;
        voffB[i] = (unsigned)k * 6144u + (unsigned)(G * 1024 + ct * 64 + seg * 16 + (pos & 1) * 8) * 2u;
    }
    auto issueW = [&](int t, int buf) {
        const char* wb = (const char*)W + (size_t)t * (64 * 6144);
        const unsigned sb = lds0 + buf * C::STAGE;
#pragma unroll
        for (int i = 0; i < 6; ++i) glds16(wb, voffB[i], sb + C::ABYTES + (w + 4 * i) * 1024);
    };
    asm volatile("" : "+v"(voffB[0]), "+v"(voffB[1]), "+v"(voffB[2]), "+v"(voffB[3]), "+v"(voffB[4]), "+v"(voffB[5]));
    const WFirst<LOCAL> first(w, ct, lane);
    const int g = lane >> 4, r = lane & 15, pos = 32 * w + 2 * r + (g >> 1);
    const int lr = !LOCAL ? (pos & 64) + 2 * (pos & 31) + ((pos >> 5) & 1)
                          : (w == 0 && (g & 1)) ? 128 + (r & 7) + 8 * (g >> 1) : (pos & 64) + ((pos & 1) * 2 + ((pos >> 5) & 1)) * 16 + ((pos >> 1) & 15);
    unsigned so = (unsigned)(panel * 16384 + g * 1024 + w * 256 + r * 16), ho = (unsigned)(g * 1024 + (panel ? panel * 16384 - 15488 + (r & 7) * 16 : 0));
    unsigned co = (unsigned)((ct * 16 + g) * 768 + w * 192 + r * 12);
    lds_ptr mp = (lds_ptr)smem + C::OFF_MEAN + lr * 4, cp = (lds_ptr)smem + C::OFF_CS + w * 192 + r * 12 + ((g & 1) ? 8 : g * 2);
    unsigned fo = flag_off((LOCAL ? 13 : 10) + min(g, 2), ct, panel, r);
    asm volatile("" : "+v"(so), "+v"(ho), "+v"(co), "+v"(mp), "+v"(cp), "+v"(fo));
#pragma unroll
    for (int i = 0; i < 5; ++i) {
        const int j = (i < 4) ? (w + 4 * i) : (16 + (w & 1));
        const int row = 8 * j + (lane >> 3);
        const int c = (lane & 7) ^ ((row >> 1) & 7);
        voffA[i] = (unsigned)grow(row < C::AROWS ? row : 0) * 2048u + c * 16;
        asm volatile("" : "+v"(voffA[i]));
    }
    auto issueA = [&](int t, int buf) {
        const char* xa = (const char*)X + t * 128;
        const unsigned sb = lds0 + buf * C::STAGE;
        if (LOCAL && w < 2) glds16c(xa, voffA[4], sb + (16 + w) * 1024);
#pragma unroll
        for (int i = 0; i < 4; ++i) glds16c(xa, voffA[i], sb + (w + 4 * i) * 1024);
    };
    u64 land;
    if (w == 3) __builtin_amdgcn_s_setprio(3);
    signal_poll<LOCAL, false>(w - 3, ws, flag_off(set, I.po, lane & 15, I.co),
                              flag_off(set, (lane & 16) && panel ? panel - 1 : panel, ct, lane & 15),
                              LOCAL ? 0xffffffffull : 0xffffull, tag, land);
    if (w < 3) first.issue(W, lds0, w);
    BAR();
    STAMP(LOCAL ? 1 : 6);
    issueA(0, 0);
    issueA(1, 1);
    VM_BAR(4);
    asm volatile("" :: "v"(land));
    __builtin_amdgcn_s_setprio(0);
    STAMP(LOCAL ? 2 : 7);
    issueW(2, 2);
    issueA(2, 2);
    VM_BAR(10);
    asm volatile("global_load_dwordx2 %0, %1, %2 sc1" : "=&v"(land) : "v"(fo), "s"(ws + WS_FLAG) : "memory");
    issueA(3, 0); issueW(3, 0);
    int buf = 1;
#pragma unroll 1
    for (int t = 1; t < NT - 3; ++t) {
        VM_BAR(10);
        issueA(t + 3, buf); issueW(t + 3, buf);
        if (t == 10 && idx < 3) code_touch(idx * 4 + w, lane);
        if (LOCAL && t == 11)
            glds16(nextW, (unsigned)((idx + 32 * w) * 8 + (lane >> 3)) * 2048u +
                   (unsigned)(((I.co & 8) + ((3 - (int)((uintptr_t)nextW >> 7)) & 7)) * 128 + (lane & 7) * 16), lds0 + OFF_DUMP);
        buf = (buf == 2) ? 0 : buf + 1;
    }
    typedef __attribute__((ext_vector_type(3))) float f32x3;
    const bool halo = LOCAL && w == 0;
    asm volatile("" : "+v"(land));
    if (!ident) {
        if (!__all(land == tag)) flag_poll(ws, fo, tag);
        f32x3 cs[4];
        f32x4 sv[4], hv[4];
        typedef __attribute__((address_space(3))) float* lds_f;
        auto add = [](float a, float b) { float r; asm("v_add_f32 %0, %1, %2" : "=v"(r) : "v"(a), "v"(b)); return r; };
        auto fold32 = [](float a, float b) { asm("s_nop 1\n\tv_permlane32_swap_b32 %0, %1\n\tv_add_f32 %0, %0, %1" : "+v"(a), "+v"(b)); return a; };
        auto fold16 = [](float a, float b) { asm("s_nop 1\n\tv_permlane16_swap_b32 %0, %1\n\tv_add_f32 %0, %0, %1" : "+v"(a), "+v"(b)); return a; };
        auto sum4 = [&](auto (&v)[4], int e) { return add(add(v[0][e], v[1][e]), add(v[2][e], v[3][e])); };
#pragma unroll
        for (int i = 0; i < 4; ++i) asm volatile("global_load_dwordx3 %0, %1, %2 sc1" : "=&v"(cs[i]) : "v"(co), "s"(csp + i * 3072) : "memory");
        VM_BAR(14);
#pragma unroll
        for (int i = 0; i < 4; ++i) asm volatile("global_load_dwordx4 %0, %1, %2 sc1" : "=&v"(sv[i]) : "v"(so), "s"((const char*)statp + i * 4096) : "memory");
        if (LOCAL)
            asm volatile("s_cmp_lg_u32 %9, 0\n\ts_cbranch_scc1 .Lhv_skip_%=\n\t"
                         "global_load_dwordx4 %0, %4, %5 sc1\n\tglobal_load_dwordx4 %1, %4, %6 sc1\n\t"
                         "global_load_dwordx4 %2, %4, %7 sc1\n\tglobal_load_dwordx4 %3, %4, %8 sc1\n"
                         ".Lhv_skip_%=:" : "=&v"(hv[0]), "=&v"(hv[1]), "=&v"(hv[2]), "=&v"(hv[3])
                         : "v"(ho), "s"(statp), "s"(statp + 1024), "s"(statp + 2048), "s"(statp + 3072), "s"(w) : "memory", "scc");
        if (halo) asm volatile("s_waitcnt vmcnt(8)" ::: "memory"); else asm volatile("s_waitcnt vmcnt(4)" ::: "memory");
        asm volatile("" : "+v"(cs[0]), "+v"(cs[1]), "+v"(cs[2]), "+v"(cs[3]));
        const float c2 = sum4(cs, 2);
        *(lds_f)cp = fold16(fold32(sum4(cs, 0), sum4(cs, 1)), fold32(c2, c2));
        BAR();
        asm volatile("s_waitcnt vmcnt(0)" : "+v"(sv[0]), "+v"(sv[1]), "+v"(sv[2]), "+v"(sv[3]) :: "memory");
        mid(0);
        const float s1 = fold32(sum4(sv, 0), sum4(sv, 2)), s2 = fold32(sum4(sv, 1), sum4(sv, 3));
        float h1, h2;
        asm("v_mov_b32 %0, %2\n\tv_mov_b32 %1, %3" : "=&v"(h1), "=&v"(h2) : "v"(s1), "v"(s2));
        if (halo) {
            asm volatile("" : "+v"(hv[0]), "+v"(hv[1]), "+v"(hv[2]), "+v"(hv[3]));
            const float u0 = sum4(hv, 0), u1 = sum4(hv, 1), u2 = sum4(hv, 2), u3 = sum4(hv, 3);
            h1 = fold32(u0, panel ? u2 : u0); h2 = fold32(u1, panel ? u3 : u1);
        }
        float mean, rstd;
        ln_stats(fold16(s1, h1), fold16(s2, h2), mean, rstd);
        *(lds_f)mp = mean; *(lds_f)(mp + (C::OFF_RSTD - C::OFF_MEAN)) = rstd;
    } else { VM_BAR(10); tail(); BAR(); mid(0); }
    mid(1);
    WAIT_LGKM_BAR();
}

template <bool LOCAL>
DI void qkv_compute(char* smem, int w, const void* W, u32x4 (&qo4)[4], __hip_bfloat16* __restrict__ kvf, __hip_bfloat16* __restrict__ ofrag, bool ident SP_PARAM) {
    typedef QC<LOCAL> C;
    constexpr int NT = 16, ABYTES = C::ABYTES, STAGE = C::STAGE;
    const int lane = lane_id();
    asm volatile("" : "+s"(w));
    const int wm = w & 1, wn = w >> 1;
    const Ids I = ids();
    const int panel = I.pq, ct = I.cq;
    const int l15 = lane & 15, g = lane >> 4;
    const lds_cptr sm = (lds_cptr)smem;
    lds_ptr smw = (lds_ptr)smem;

    f32x4 acc[3][4][2];
    f32x4 acch[2];
    f32x2 z2; asm volatile("v_mov_b64 %0, 0" : "=v"(z2));
    const f32x4 z4 = {z2[0], z2[1], z2[0], z2[1]};
#pragma unroll
    for (int a = 0; a < 3; ++a)
#pragma unroll
        for (int t = 0; t < 4; ++t) { acc[a][t][0] = z4; acc[a][t][1] = z4; }
    acch[0] = z4; acch[1] = z4;

    struct Frags { bf16x8 a[4]; bf16x8 h; s16x4 b[3][2][2]; };
    auto rd = [&](auto WM, Frags& F, lds_cptr pa, lds_cptr pb0, lds_cptr pb1, int n) {
        switch (n) {
            case 0: F.b[0][0][0] = lds_tr(pb0); break;
            case 1: F.b[0][0][1] = lds_tr(pb0 + 512); break;
            case 2: F.a[0] = lds_b128(pa); break;
            case 3: F.b[0][1][0] = lds_tr(pb1); break;
            case 4: F.b[0][1][1] = lds_tr(pb1 + 512); break;
            case 5: F.a[1] = lds_b128(pa + 2048); break;
            case 6: F.a[2] = lds_b128(pa + 4096); break;
            case 7: F.a[3] = lds_b128(pa + 6144); break;
            case 8: F.b[1][0][0] = lds_tr(pb0 + 8192); break;
            case 9: F.b[1][0][1] = lds_tr(pb0 + 8192 + 512); break;
            case 10: F.b[1][1][0] = lds_tr(pb1 + 8192); break;
            case 11: F.b[1][1][1] = lds_tr(pb1 + 8192 + 512); break;
            case 12: F.b[2][0][0] = lds_tr(pb0 + 16384); break;
            case 13: F.b[2][0][1] = lds_tr(pb0 + 16384 + 512); break;
            case 14: F.b[2][1][0] = lds_tr(pb1 + 16384); break;
            case 15: F.b[2][1][1] = lds_tr(pb1 + 16384 + 512); break;
            case 16: if (LOCAL) F.h = lds_b128(pa + 16384 - WM.value * 8192); break;
            default: break;
        }
    };
    auto mm = [](f32x4& c, bf16x8 a, bf16x8 b) { asm volatile("v_mfma_f32_16x16x32_bf16 %0, %1, %2, %0" : "+v"(c) : "v"(a), "v"(b)); };
    auto half = [&](auto WM, const Frags& C, Frags& N, bool doread, lds_cptr pa, lds_cptr pb0, lds_cptr pb1, auto tail, int G0 = 0, int G1 = 3) {
#pragma unroll
        for (int G = G0; G < G1; ++G) {
            const bf16x8 b0 = cat8(C.b[G][0][0], C.b[G][0][1]), b1 = cat8(C.b[G][1][0], C.b[G][1][1]);
#pragma unroll
            for (int t = 0; t < 4; ++t) {
                const int n = (G - G0) * 8 + t * 2;
                if (G < 2) mm(acc[G][t][0], b0, C.a[t]); else mm(acc[G][t][0], C.a[t], b0);
                SBAR();
                if (doread) rd(WM, N, pa, pb0, pb1, n);
                tail(n);
                SBAR();
                if (G < 2) mm(acc[G][t][1], b1, C.a[t]); else mm(acc[G][t][1], C.a[t], b1);
                SBAR();
                if (doread) rd(WM, N, pa, pb0, pb1, n + 1);
                tail(n + 1);
                SBAR();
            }
        }
        if (LOCAL) {
            if (WM.value == 0) { mm(acch[0], cat8(C.b[1][0][0], C.b[1][0][1]), C.h); mm(acch[1], cat8(C.b[1][1][0], C.b[1][1][1]), C.h); }
            else               { mm(acch[0], C.h, cat8(C.b[2][0][0], C.b[2][0][1])); mm(acch[1], C.h, cat8(C.b[2][1][0], C.b[2][1][1])); }
            SBAR();
        }
    };

    Frags F0, F1;
    f32x4 cq[2], ck[2], mu4[5], rs4[5]; float cv[2], mu[5], rs[5];
    u32x4 qfr[4], kfr[4];
    u32x4 va[2][2], cc;
    f32x4 mwin, mfirst;
    typedef __attribute__((address_space(3))) u32x4* lds_u4;
    constexpr int OFF_CST = C::OFF_CS + 768;
    const bool negpos = (wm == 0) && (panel == 0);
    if (LOCAL) {
        unsigned keepv = negpos ? __float_as_uint(NEG_MASK) : 0u;
        asm volatile("" : "+s"(keepv));
        u32x4 mw, mf;
        unsigned prv[4];
#pragma unroll
        for (int r = 0; r < 4; ++r) {
            const int d = l15 - (4 * g + r);
            unsigned out = (unsigned)(((d << 28) >> 31));
            prv[r] = (unsigned)(d >> 31);
            asm volatile("" : "+v"(out), "+v"(prv[r]));
            mw[r] = out & 0xff800000u;
            mf[r] = (out & 0xff800000u) | (~out & keepv);
        }
        const unsigned e0 = (unsigned)(OFF_EX + wn * 6144);
        lds_u4 cst = (lds_u4)(smw + OFF_CST) + lane;
        u32x4 c3 = {~((prv[0] >> 16) | (prv[1] << 16)), ~((prv[2] >> 16) | (prv[3] << 16)),
                    e0 + (unsigned)(wm ? lane : 256 + (lane ^ 8)) * 16u, e0 + (unsigned)(wm ? 128 + lane : 320 + (lane ^ 32)) * 16u};
        asm volatile("" : "+v"(mw), "+v"(mf), "+v"(c3), "+v"(cst));
        VM_BAR(0);
        cst[0] = mw; cst[64 + wm * 64] = mf; cst[192 + w * 64] = c3;
        SBAR();
    } else VM_BAR(0);
    typedef const __attribute__((address_space(3))) f32x4* lds_f4; typedef const __attribute__((address_space(3))) float* lds_f1;
    auto mainloop = [&](auto WM) {
#pragma unroll
        for (int a = 0; a < 3; ++a)
            asm volatile("" : "+v"(acc[a][0][0]), "+v"(acc[a][0][1]), "+v"(acc[a][1][0]), "+v"(acc[a][1][1]), "+v"(acc[a][2][0]), "+v"(acc[a][2][1]),
                         "+v"(acc[a][3][0]), "+v"(acc[a][3][1]));
        int ty = lane_id();
        const int l15 = ty & 15, g = ty >> 4;
        const int swA = (l15 >> 1) & 7;
        const int q4 = l15 >> 2, p4 = l15 & 3, fk = ((q4 >> 1) & 1) | ((g & 1) << 1);
        lds_cptr pA0 = sm + wm * 8192 + l15 * 128 + ((0 + g) ^ swA) * 16, pA1 = sm + wm * 8192 + l15 * 128 + ((4 + g) ^ swA) * 16;
        lds_cptr pB0 = sm + ABYTES + (8 * g + q4) * 128 + (((2 * wn + 0) ^ fk) * 32) + p4 * 8;
        lds_cptr pB1 = sm + ABYTES + (8 * g + q4) * 128 + (((2 * wn + 1) ^ fk) * 32) + p4 * 8;
        asm volatile("" : "+v"(acch[0]), "+v"(acch[1]), "+v"(pA0), "+v"(pA1), "+v"(pB0), "+v"(pB1));
        slow_warm((unsigned)(uintptr_t)smem + OFF_DUMP, W, w, ty);
        BAR();
        STAMP(LOCAL ? 1 : 7);
        BAR();
#pragma unroll
        for (int n = 0; n < 17; ++n) rd(WM, F0, pA0, pB0, pB1, n);
        WAIT_LGKM();
        int off = 0;
        lds_cptr a = pA1, b0 = pB0, b1 = pB1;
#pragma unroll QUNR
        for (int t = 0; t < NT - 1; ++t) {
            half(WM, F0, F1, true, a, b0 + 4096, b1 + 4096, [&](int n) {
                if (n == 17) { off = (off == 2 * STAGE) ? 0 : off + STAGE; asm volatile("" : "+s"(off)); }
                if (n == 19) { b0 = pB0 + off; asm volatile("" : "+v"(b0)); }
                if (n == 20) { a = pA0 + off; asm volatile("" : "+v"(a)); }
                if (n == 21) { b1 = pB1 + off; asm volatile("" : "+v"(b1)); }
            });
            WAIT_LGKM_BAR();
            half(WM, F1, F0, true, a, b0, b1, [&](int n) {
                if (n == 19) { a = pA1 + off; asm volatile("" : "+v"(a)); }
                if (n == 22) __builtin_amdgcn_s_waitcnt(0xC07F);
            });
        }
        lds_cptr s1 = sm, s4 = sm, c1 = sm, c4 = sm;
        half(WM, F0, F1, true, a, b0 + 4096, b1 + 4096, [&](int n) {
            if (n == 16) ty = lane_id();
            if (n == 17) { s1 = sm + C::OFF_MEAN + wm * 256 + (ty & 15) * 4; asm volatile("" : "+v"(s1)); }
            if (n == 18) { s4 = sm + C::OFF_MEAN + wm * 256 + (ty & 48); asm volatile("" : "+v"(s4)); }
            if (n == 19) { c1 = sm + C::OFF_CS + wn * 128 + (ty & 15) * 4; asm volatile("" : "+v"(c1)); }
            if (n == 20) { c4 = sm + C::OFF_CS + wn * 128 + (ty & 48); asm volatile("" : "+v"(c4)); }
        });
        WAIT_LGKM_BAR();
        half(WM, F1, F0, false, sm, sm, sm, [&](int n) {
            constexpr int RS = C::OFF_RSTD - C::OFF_MEAN, H = 512 - WM.value * 256;
            if (n < 2) { if (!LOCAL) cq[n] = *(lds_f4)(c4 + n * 64); ck[n] = *(lds_f4)(c4 + 256 + n * 64); }
            else if (n < 6) { mu[n - 2] = *(lds_f1)(s1 + (n - 2) * 64); rs[n - 2] = *(lds_f1)(s1 + RS + (n - 2) * 64); }
            else if (n < 10) { if (!LOCAL || (n & 1)) { mu4[n - 6] = *(lds_f4)(s4 + (n - 6) * 64); rs4[n - 6] = *(lds_f4)(s4 + RS + (n - 6) * 64); } }
            else if (n == 10) { cv[0] = *(lds_f1)(c1 + 512); cv[1] = *(lds_f1)(c1 + 512 + 64); }
            else if (LOCAL && n == 11) {
                if (WM.value == 0) { mu[4] = *(lds_f1)(s1 + H); rs[4] = *(lds_f1)(s1 + RS + H); }
                else { mu4[4] = *(lds_f4)(s4 + H); rs4[4] = *(lds_f4)(s4 + RS + H); }
            }
        }, LOCAL ? 0 : 1);
        asm volatile("s_nop 7");
        SBAR();
        if (LOCAL) {
            auto fix = [&](int a, int t) {
#pragma unroll
                for (int f = 0; f < 2; ++f)
                    acc[a][t][f] = (a == 2) ? rs4[t] * (acc[2][t][f] + mu4[t] * cv[f]) : rs[t] * (acc[a][t][f] + mu[t] * (a ? ck[f] : cq[f]));
            };
            auto pk = [&](const f32x4 (&v)[2]) { return u32x4{pack_bf16(v[0][0], v[0][1]), pack_bf16(v[0][2], v[0][3]), pack_bf16(v[1][0], v[1][1]), pack_bf16(v[1][2], v[1][3])}; };
            cq[0] = *(lds_f4)c4; cq[1] = *(lds_f4)(c4 + 64);
            SBAR();
            if (!ident) {
                fix(1, 1); fix(1, 3); fix(2, 1); fix(2, 3);
#pragma unroll
                for (int f = 0; f < 2; ++f)
                    acch[f] = (WM.value == 0) ? rs[4] * (acch[f] + mu[4] * ck[f]) : rs4[4] * (acch[f] + mu4[4] * cv[f]);
            }
            auto pkv = [&](int t) {
#pragma unroll
                for (int f = 0; f < 2; ++f) {
                    va[t >> 1][f][(t & 1) * 2] = pack_bf16(acc[2][t][f][0], acc[2][t][f][1]);
                    va[t >> 1][f][(t & 1) * 2 + 1] = pack_bf16(acc[2][t][f][2], acc[2][t][f][3]);
                }
            };
            kfr[1] = pk(acc[1][1]); kfr[3] = pk(acc[1][3]); pkv(1); pkv(3);
            lds_u4 ex = (lds_u4)(smw + OFF_EX + wn * 6144) + ty;
            u32x4 hf = pk(acch);
            if (WM.value == 0) {
                ex[0] = kfr[1]; ex[64] = kfr[3];
#pragma unroll
                for (int u = 0; u < 2; ++u)
                    asm volatile("ds_write2_b64 %0, %1, %2 offset1:1" :: "v"(ex + 128 + u * 64), "v"(u32x2{va[u][0][2], va[u][0][3]}),
                                 "v"(u32x2{va[u][1][2], va[u][1][3]}) : "memory");
                if (negpos) hf = u32x4{0, 0, 0, 0};
            }
            ex[256 + WM.value * 64] = hf;
#pragma unroll
            for (int t = 0; t < 4; t += 2) { mu4[t] = *(lds_f4)(s4 + t * 64); rs4[t] = *(lds_f4)(s4 + (C::OFF_RSTD - C::OFF_MEAN) + t * 64); }
            lds_u4 cst = (lds_u4)(smw + OFF_CST) + ty;
            cc = cst[192 + w * 64];
            mwin = __builtin_bit_cast(f32x4, cst[0]); mfirst = __builtin_bit_cast(f32x4, cst[64 + WM.value * 64]);
            SBAR();
            if (!ident) { fix(0, 0); fix(0, 1); fix(0, 2); fix(0, 3); fix(1, 0); fix(1, 2); fix(2, 0); fix(2, 2); }
#pragma unroll
            for (int t = 0; t < 4; ++t) qfr[t] = pk(acc[0][t]);
            kfr[0] = pk(acc[1][0]); kfr[2] = pk(acc[1][2]); pkv(0); pkv(2);
            asm volatile("" : "+v"(qfr[0]), "+v"(qfr[1]), "+v"(qfr[2]), "+v"(qfr[3]), "+v"(kfr[0]), "+v"(kfr[2]),
                         "+v"(va[0][0]), "+v"(va[0][1]), "+v"(va[1][0]), "+v"(va[1][1]));
        }
    };
    const int head = ct * 2 + wn;
    unsigned qo = 0, ko = 0, vo = 0;
    if (LOCAL) {
        qo = (unsigned)((((panel * 8 + wm * 4) * 32 + head) * 64 + lane) * 16);
        asm volatile("" : "+v"(qo));
    } else {
        ko = (unsigned)((wm * 8 + (g & 1)) * 1024 + ((l15 & 7) + ((l15 & 8) << 1) + 32 * (g >> 1)) * 16);
        vo = (unsigned)((wm * 8 + 2 + (g >> 1)) * 1024 + (l15 + 32 * (g & 1)) * 16);
        asm volatile("" : "+v"(ko), "+v"(vo));
    }
    if (LOCAL && wm) mainloop(IC<1>{}); else mainloop(IC<0>{});

    if (!LOCAL) {
        auto pk = [&](f32x4 a, f32x4 b) { return u32x4{pack_bf16(a[0], a[1]), pack_bf16(a[2], a[3]), pack_bf16(b[0], b[1]), pack_bf16(b[2], b[3])}; };
        char* kvp = (char*)kvf + (size_t)(head * 16 + panel) * 16384;
        lds_ptr own = smw + wn * 16384;
        SBAR();
#pragma unroll
        for (int t = 0; t < 4; ++t) {
            const u32x4 kk = pk(rs[t] * (acc[1][t][0] + mu[t] * ck[0]), rs[t] * (acc[1][t][1] + mu[t] * ck[1]));
            if (t & 1) st16o<128>(kvp + (t >> 1) * 4096, ko, kk); else st16o<0>(kvp + (t >> 1) * 4096, ko, kk);
            *(lds_u4)(own + ko + (t >> 1) * 4096 + (t & 1) * 128) = kk;
            SBAR();
            const int u = t >> 1, f = t & 1;
            const u32x4 vv = pk(rs4[2 * u] * (acc[2][2 * u][f] + mu4[2 * u] * cv[f]), rs4[2 * u + 1] * (acc[2][2 * u + 1][f] + mu4[2 * u + 1] * cv[f]));
            if (f) st16o<256>(kvp + u * 4096, vo, vv); else st16o<0>(kvp + u * 4096, vo, vv);
            *(lds_u4)(own + vo + u * 4096 + f * 256) = vv;
            SBAR();
        }
        half(IC<0>{}, F1, F0, false, sm, sm, sm, [](int) {}, 0, 1);
        asm volatile("s_nop 7");
        SBAR();
#pragma unroll
        for (int u = 0; u < 2; ++u) {
            const float r0 = rs[2 * u] * QSCALE, r1 = rs[2 * u + 1] * QSCALE;
            const u32x4 qa = pk(r0 * (acc[0][2 * u][0] + mu[2 * u] * cq[0]), r0 * (acc[0][2 * u][1] + mu[2 * u] * cq[1]));
            const u32x4 qb = pk(r1 * (acc[0][2 * u + 1][0] + mu[2 * u + 1] * cq[0]), r1 * (acc[0][2 * u + 1][1] + mu[2 * u + 1] * cq[1]));
#pragma unroll
            for (int e = 0; e < 4; ++e) {
                const auto x = __builtin_amdgcn_permlane16_swap(qa[e], qb[e], false, false);
                qo4[2 * u][e] = x[0]; qo4[2 * u + 1][e] = x[1];
            }
        }
        return;
    }
    {
        const f32x4 zero4 = {0, 0, 0, 0};
        const unsigned cm01 = cc[0], cm23 = cc[1];
        auto bf8 = [](u32x4 v) { return __builtin_bit_cast(bf16x8, v); };
        f32x4 sp[4], sc[4];
        sc[1] = MFMA16(bf8(kfr[1]), bf8(qfr[1]), mwin); sp[1] = MFMA16(bf8(kfr[0]), bf8(qfr[1]), mwin);
        sc[3] = MFMA16(bf8(kfr[3]), bf8(qfr[3]), mwin); sp[3] = MFMA16(bf8(kfr[2]), bf8(qfr[3]), mwin);
        lds_cptr ka[2] = {sm + cc[2], sm + (cc[2] ^ (wm ? 1024u : 128u))}, vo2[2] = {sm + cc[3], sm + (cc[3] ^ (wm ? 1024u : 512u))};
        asm volatile("" : "+v"(ka[0]), "+v"(ka[1]), "+v"(vo2[0]), "+v"(vo2[1]));
        WAIT_LGKM_BAR();
        const bf16x8 kp[2] = {lds_b128(ka[0]), lds_b128(ka[1])};
        u32x2 vp[2][2];
#pragma unroll
        for (int u = 0; u < 2; ++u)
            asm volatile("ds_read_b64 %0, %2\n\tds_read_b64 %1, %2 offset:8" : "=&v"(vp[u][0]), "=&v"(vp[u][1]) : "v"(vo2[u]) : "memory");
        sc[0] = MFMA16(bf8(kfr[0]), bf8(qfr[0]), mwin); sc[2] = MFMA16(bf8(kfr[2]), bf8(qfr[2]), mwin);
        SBAR();
        float x[4][4], m[4];
        unsigned a[4][2];
        auto logits = [&](int t0, int st) {
#pragma unroll
            for (int t = t0; t < 4; t += 2)
#pragma unroll
                for (int h = 0; h < 2; ++h) {
                    if (st == 0) {
                        const unsigned uc = pack_bf16(sc[t][2 * h], sc[t][2 * h + 1]), up = pack_bf16(sp[t][2 * h], sp[t][2 * h + 1]);
                        asm("v_bfi_b32 %0, %1, %2, %3" : "=v"(a[t][h]) : "v"(h ? cm23 : cm01), "v"(uc), "v"(up));
                    } else if (st == 1) { x[t][2 * h] = bf_lo(a[t][h]) * ATT_SCALE; x[t][2 * h + 1] = bf_hi(a[t][h]) * ATT_SCALE; }
                    else if (st == 2) a[t][h] = pack_bf16(x[t][2 * h], x[t][2 * h + 1]);
                    else { x[t][2 * h] = bf_lo(a[t][h]); x[t][2 * h + 1] = bf_hi(a[t][h]); }
                }
            SBAR();
        };
        logits(1, 0); logits(1, 1);
        sp[0] = MFMA16(kp[0], bf8(qfr[0]), mfirst); sp[2] = MFMA16(kp[1], bf8(qfr[2]), mfirst);
        SBAR();
        logits(1, 2); logits(1, 3);
        logits(0, 0); logits(0, 1); logits(0, 2); logits(0, 3);
        auto across = [&](float (&v)[4], auto op, auto fin) {
            const auto p = __builtin_amdgcn_permlane16_swap(__float_as_uint(v[0]), __float_as_uint(v[1]), false, false);
            const auto q = __builtin_amdgcn_permlane16_swap(__float_as_uint(v[2]), __float_as_uint(v[3]), false, false);
            const auto r = __builtin_amdgcn_permlane32_swap(__float_as_uint(op(__uint_as_float(p[0]), __uint_as_float(p[1]))),
                                                            __float_as_uint(op(__uint_as_float(q[0]), __uint_as_float(q[1]))), false, false);
            const unsigned z = __float_as_uint(fin(op(__uint_as_float(r[0]), __uint_as_float(r[1]))));
            const auto u = __builtin_amdgcn_permlane32_swap(z, z, false, false);
            const auto e = __builtin_amdgcn_permlane16_swap(u[0], u[0], false, false), f = __builtin_amdgcn_permlane16_swap(u[1], u[1], false, false);
            v[0] = __uint_as_float(e[0]); v[1] = __uint_as_float(e[1]); v[2] = __uint_as_float(f[0]); v[3] = __uint_as_float(f[1]);
        };
#pragma unroll
        for (int t = 0; t < 4; ++t) m[t] = amax3(x[t][0], x[t][1], x[t][2]);
        asm("v_max_f32 %0, %0, %4\n\tv_max_f32 %1, %1, %5\n\tv_max_f32 %2, %2, %6\n\tv_max_f32 %3, %3, %7\n\ts_nop 0"
            : "+v"(m[0]), "+v"(m[1]), "+v"(m[2]), "+v"(m[3]) : "v"(x[0][3]), "v"(x[1][3]), "v"(x[2][3]), "v"(x[3][3]));
        across(m, [](float p, float q) { float r; asm("v_max_f32 %0, %1, %2\n\ts_nop 0" : "=v"(r) : "v"(p), "v"(q)); return r; },
               [](float p) { return p * -LOG2E; });
        SBAR();
#pragma unroll
        for (int t = 0; t < 4; ++t)
#pragma unroll
            for (int r = 0; r < 4; ++r) x[t][r] = __builtin_fmaf(x[t][r], LOG2E, m[t]);
        SBAR();
#pragma unroll
        for (int t = 0; t < 4; ++t)
#pragma unroll
            for (int r = 0; r < 4; ++r) x[t][r] = __builtin_amdgcn_exp2f(x[t][r]);
        SBAR();
#pragma unroll
        for (int t = 0; t < 4; ++t) m[t] = (x[t][0] + x[t][1]) + (x[t][2] + x[t][3]);
        asm volatile("s_waitcnt lgkmcnt(0)" : "+v"(vp[0][0]), "+v"(vp[0][1]), "+v"(vp[1][0]), "+v"(vp[1][1]) :: "memory");
        u32x4 ve[2][2];
#pragma unroll
        for (int u = 0; u < 2; ++u)
#pragma unroll
            for (int f = 0; f < 2; ++f) ve[u][f] = u32x4{vp[u][f][0], vp[u][f][1], va[u][f][0], va[u][f][1]};
        across(m, [](float p, float q) { return p + q; }, [](float p) { return __builtin_amdgcn_rcpf(p); });
        asm volatile("" : "+v"(ve[0][0]), "+v"(ve[0][1]), "+v"(ve[1][0]), "+v"(ve[1][1]));
        SBAR();
#pragma unroll
        for (int t = 0; t < 4; ++t)
#pragma unroll
            for (int r = 0; r < 4; ++r) x[t][r] *= m[t];
        SBAR();
#pragma unroll
        for (int t = 0; t < 4; ++t)
#pragma unroll
            for (int h = 0; h < 2; ++h) a[t][h] = pack_bf16(x[t][2 * h], x[t][2 * h + 1]);
        SBAR();
        f32x4 o0[4], o1[4];
#pragma unroll
        for (int t = 0; t < 4; ++t) {
            const bf16x8 pb = bf8(u32x4{a[t][0] & ~cm01, a[t][1] & ~cm23, a[t][0] & cm01, a[t][1] & cm23});
            o0[t] = MFMA16(bf8((t & 1) ? va[t >> 1][0] : ve[t >> 1][0]), pb, zero4);
            o1[t] = MFMA16(bf8((t & 1) ? va[t >> 1][1] : ve[t >> 1][1]), pb, zero4);
        }
        SBAR();
#pragma unroll
        for (int t = 0; t < 4; ++t)
            st16(ofrag + t * 16384, qo, u32x4{pack_bf16(o0[t][0], o0[t][1]), pack_bf16(o0[t][2], o0[t][3]),
                                                 pack_bf16(o1[t][0], o1[t][1]), pack_bf16(o1[t][2], o1[t][3])});
    }
}

DI int out_row(int panel, bool localrows, int T, int a) {
    return localrows ? (panel * 128 + (T >> 2) * 64 + 2 * (16 * (T & 1) + a) + ((T >> 1) & 1)) : (panel * 128 + T * 16 + a);
}
DI int out_swz(int k) { return ((k >> 1) & 1) | (k & 4); }
DI unsigned wo_off(int w, int lane) {
    const int kl = lane >> 3;
    unsigned v = (unsigned)(w * 8 + kl) * 2048u + (unsigned)(ids().co * 128 + ((lane & 7) ^ out_swz(kl)) * 16);
    asm volatile("" : "+v"(v));
    return v;
}
DI void wo_load(const void* Wo, unsigned voff, int i0, int i1, unsigned lds0, int w) {
#pragma unroll
    for (int i = i0; i < i1; ++i) glds16((const char*)Wo + (size_t)i * 65536, voff, lds0 + (w + 4 * i) * 1024);
}
DI unsigned res_off(bool localrows, int w, int lane) {
    const Ids I = ids();
    const int row = w * 8 + (lane >> 3);
    unsigned v = (unsigned)out_row(I.po, localrows, row >> 4, row & 15) * 2048u + (unsigned)(I.co * 128 + (((lane & 7) ^ (lane >> 3)) * 16));
    asm volatile("" : "+v"(v));
    return v;
}
DI void res_load(u32x4 (&r)[4], const void* Xin, bool localrows, unsigned off) {
#pragma unroll
    for (int i = 0; i < 4; ++i)
        asm volatile("global_load_dwordx4 %0, %1, %2 sc1" : "=&v"(r[i]) : "v"(off), "s"((const char*)Xin + out_row(0, localrows, 2 * i, 0) * 2048) : "memory");
}
DI void res_write(u32x4 (&r)[4], char* smem, int off, int w, int lane) {
    asm volatile("" : "+v"(r[0]), "+v"(r[1]), "+v"(r[2]), "+v"(r[3]));
#pragma unroll
    for (int i = 0; i < 4; ++i) *(__attribute__((address_space(3))) u32x4*)((lds_ptr)smem + off + (w + 4 * i) * 1024 + lane * 16) = r[i];
}

template <bool AFTER_LOCAL>
DI void out_compute(char* smem, int w, char* ws, int set, u64 tag, const __hip_bfloat16* __restrict__ ofrag,
                    __hip_bfloat16* __restrict__ Xout, float* __restrict__ statp SP_PARAM) {
    constexpr int XOFF = AFTER_LOCAL ? 131072 : 98304;
    const int lane = lane_id();
    asm volatile("" : "+s"(w));
    const Ids I = ids();
    const int panel = I.po, ct = I.co;
    const int l15 = lane & 15, g = lane >> 4;
    auto orow = [&](int T, int a) -> int { return out_row(panel, AFTER_LOCAL, T, a); };
    const int q4 = l15 >> 2, p4 = l15 & 3;
    const int krow = 4 * g + q4;
    const int sw = out_swz(krow);
    const lds_cptr sm = (lds_cptr)smem;
    lds_cptr bw[4], xo[4];
#pragma unroll
    for (int t = 0; t < 4; ++t) {
        bw[t] = sm + krow * 128 + ((((t >> 1) * 4 + (p4 >> 1) * 2 + (t & 1)) ^ sw) * 16) + (p4 & 1) * 8;
        xo[t] = sm + XOFF + (w * 32 + l15) * 128 + ((((t >> 1) * 4 + (g >> 1) * 2 + (t & 1)) ^ (l15 & 7)) * 16) + (g & 1) * 8;
    }
    unsigned so[2], sto, ao[2];
#pragma unroll
    for (int r2 = 0; r2 < 2; ++r2) {
        so[r2] = (unsigned)orow(2 * w + r2, l15 & 7) * 2048u + (unsigned)(ct * 128 + (l15 >> 3) * 64 + g * 16);
        ao[r2] = (unsigned)((panel * 8 + 2 * w + r2) * 32768 + lane * 16);
    }
    sto = (unsigned)(((panel * 16 + ct) * 128 + 2 * w * 16 + l15) * 8);
    asm volatile("" : "+v"(bw[0]), "+v"(bw[1]), "+v"(bw[2]), "+v"(bw[3]), "+v"(xo[0]), "+v"(xo[1]), "+v"(xo[2]), "+v"(xo[3]),
                 "+v"(so[0]), "+v"(so[1]), "+v"(sto), "+v"(ao[0]), "+v"(ao[1]));
    bf16x8 ones;
    {
        unsigned o1; asm volatile("v_mov_b32 %0, 0x3f803f80" : "=v"(o1));
        ones = __builtin_bit_cast(bf16x8, (u32x4{o1, o1, o1, o1}));
        asm volatile("" : "+v"(ones));
    }
    const __amdgpu_buffer_rsrc_t ra = __builtin_amdgcn_make_buffer_rsrc((void*)ofrag, 0, 4 << 20, 0x00020000);
    asm volatile("" :: "s"(ra));
    u64 land;
    u32x32 fr;
    STAMP(AFTER_LOCAL ? 3 : 10);
    signal_poll<true, false>(w, ws, flag_off(set, I.pq, l15, I.cq), flag_off(set, panel, ct, l15), 0xffffull, tag, land);
    BAR();
    STAMP(AFTER_LOCAL ? 4 : 11);
    u32x32 aw0, aw1;
    asm volatile(".irp j,0,1,2,3\n\tbuffer_load_dwordx4 v[64+8*\\j:67+8*\\j], %2, %4, 0 offen offset:1024*\\j sc1\n\t"
                 "buffer_load_dwordx4 v[68+8*\\j:71+8*\\j], %3, %4, 0 offen offset:1024*\\j sc1\n\t.endr\n\t"
                 ".irp j,0,1\n\tbuffer_load_dwordx4 v[96+8*\\j:99+8*\\j], %2, %4, %5 offen offset:1024*\\j sc1\n\t"
                 "buffer_load_dwordx4 v[100+8*\\j:103+8*\\j], %3, %4, %5 offen offset:1024*\\j sc1\n\t.endr"
                 : "=&{v[64:95]}"(aw0), "=&{v[96:127]}"(aw1) : "v"(ao[0]), "v"(ao[1]), "s"(ra), "s"(4096) : "memory");
    f32x16 acc[2];
#pragma unroll
    for (int r2 = 0; r2 < 2; ++r2)
#pragma unroll
        for (int c = 0; c < 4; ++c) {
            const u32x2 xr = *(const __attribute__((address_space(3))) u32x2*)(xo[c] + r2 * 2048);
            acc[r2][4 * c] = bf_lo(xr[0]); acc[r2][4 * c + 1] = bf_hi(xr[0]); acc[r2][4 * c + 2] = bf_lo(xr[1]); acc[r2][4 * c + 3] = bf_hi(xr[1]);
        }
    asm volatile(
        ".macro OUT_MM r, t, j\n\t"
        "v_mfma_f32_16x16x32_bf16 v[16*\\r+4*\\t:16*\\r+4*\\t+3], v[32+16*((\\j)&1)+4*\\t:35+16*((\\j)&1)+4*\\t], v[64+8*\\j+4*\\r:67+8*\\j+4*\\r], v[16*\\r+4*\\t:16*\\r+4*\\t+3]\n\t"
        ".endm\n\t"
        ".macro OUT_RD t, hi, j, on\n\t"
        ".if \\on\n\tds_read_b64_tr_b16 v[32+16*((\\j)&1)+4*\\t+2*\\hi:33+16*((\\j)&1)+4*\\t+2*\\hi], v[128+\\t] offset:((\\j)&7)*4096+2048*\\hi\n\t.endif\n\t"
        ".endm\n\t"
        ".macro OUT_UP t, on\n\t.if \\on\n\tv_add_u32 v[128+\\t], s97, v[128+\\t]\n\t.endif\n\t.endm\n\t"
        ".macro OUT_STEP j, ld, rd, up, vm, lg\n\t"
        "OUT_MM 0, 0, \\j\n\tOUT_RD 3, 1, \\j+1, 1\n\tOUT_UP 0, \\up\n\t"
        ".if \\ld && (((\\j+6)&3) == 0)\n\ts_addk_i32 s96, 0x1000\n\t.endif\n\t"
        "OUT_MM 1, 0, \\j\n\tOUT_RD 0, 0, \\j+2, \\rd\n\t"
        ".if \\ld\n\tbuffer_load_dwordx4 v[64+8*((\\j+6)&7):67+8*((\\j+6)&7)], %[a0], %[ra], s96 offen offset:1024*((\\j+6)&3) sc1\n\t.endif\n\t"
        "OUT_MM 0, 1, \\j\n\tOUT_RD 0, 1, \\j+2, \\rd\n\tOUT_UP 1, \\up\n\t"
        "OUT_MM 1, 1, \\j\n\tOUT_RD 1, 0, \\j+2, \\rd\n\t"
        "OUT_MM 0, 2, \\j\n\tOUT_RD 1, 1, \\j+2, \\rd\n\tOUT_UP 2, \\up\n\t"
        "OUT_MM 1, 2, \\j\n\tOUT_RD 2, 0, \\j+2, \\rd\n\t"
        ".if \\ld\n\tbuffer_load_dwordx4 v[68+8*((\\j+6)&7):71+8*((\\j+6)&7)], %[a1], %[ra], s96 offen offset:1024*((\\j+6)&3) sc1\n\t.endif\n\t"
        "OUT_MM 0, 3, \\j\n\tOUT_RD 2, 1, \\j+2, \\rd\n\tOUT_UP 3, \\up\n\t"
        "OUT_MM 1, 3, \\j\n\tOUT_RD 3, 0, \\j+2, \\rd\n\t"
        "s_waitcnt vmcnt(\\vm) lgkmcnt(\\lg)\n\t"
        ".endm\n\t"
        ".macro OUT_PACK y, c\n\t"
        ".irp i,0,1,2,3,4,5,6,7\n\tv_cvt_pk_bf16_f32 v[\\y+\\i], v[\\c+2*\\i], v[\\c+2*\\i+1]\n\t.endr\n\t"
        ".irp i,0,1,4,5\n\tv_permlane16_swap_b32 v[\\y+\\i], v[\\y+\\i+2]\n\t.endr\n\t"
        ".irp i,0,1,2,3\n\tv_mov_b32 v[\\y+8+\\i], v[\\y+\\i]\n\t.endr\n\t"
        "v_mfma_f32_16x16x32_bf16 v[\\y+12:\\y+15], v[\\y:\\y+3], v[\\y:\\y+3], 0\n\t"
        "v_mov_b32_dpp v[\\y+8], v[\\y+4] row_ror:8 row_mask:0xf bank_mask:0xc\n\tv_mov_b32_dpp v[\\y+9], v[\\y+5] row_ror:8 row_mask:0xf bank_mask:0xc\n\t"
        "v_mfma_f32_16x16x32_bf16 v[\\y+16:\\y+19], %[one], v[\\y:\\y+3], 0\n\t"
        "v_mov_b32_dpp v[\\y+10], v[\\y+6] row_ror:8 row_mask:0xf bank_mask:0xc\n\tv_mov_b32_dpp v[\\y+11], v[\\y+7] row_ror:8 row_mask:0xf bank_mask:0xc\n\t"
        "v_mfma_f32_16x16x32_bf16 v[\\y+12:\\y+15], v[\\y+4:\\y+7], v[\\y+4:\\y+7], v[\\y+12:\\y+15]\n\t"
        ".endm\n\t"
        ".macro OUT_REST y\n\t"
        "v_mfma_f32_16x16x32_bf16 v[\\y+16:\\y+19], %[one], v[\\y+4:\\y+7], v[\\y+16:\\y+19]\n\t"
        ".irp i,0,1,2,3\n\tv_mov_b32_dpp v[\\y+4+\\i], v[\\y+\\i] row_ror:8 row_mask:0xf bank_mask:0x3\n\t.endr\n\t"
        ".endm\n\t"
        ".macro OUT_DIAG y\n\t"
        "v_cndmask_b32 v[\\y+12], v[\\y+12], v[\\y+13], %[m0]\n\tv_cndmask_b32 v[\\y+14], v[\\y+14], v[\\y+15], %[m0]\n\t"
        "v_cndmask_b32 v[\\y+17], v[\\y+12], v[\\y+14], %[m1]\n\t"
        ".endm\n\t"
        ".irp t,0,1,2,3\n\tOUT_RD \\t, 0, 0, 1\n\tOUT_RD \\t, 1, 0, 1\n\t.endr\n\t"
        ".irp t,0,1,2\n\tOUT_RD \\t, 0, 1, 1\n\tOUT_RD \\t, 1, 1, 1\n\t.endr\n\tOUT_RD 3, 0, 1, 1\n\t"
        "s_movk_i32 s96, 0x1000\n\ts_mov_b32 s97, 0x8000\n\t"
        "s_waitcnt vmcnt(10) lgkmcnt(0)\n"
        ".Lout_loop_%=:\n\t"
        "OUT_STEP 0, 1, 1, 0, 10, 7\n\tOUT_STEP 1, 1, 1, 0, 10, 7\n\tOUT_STEP 2, 1, 1, 0, 10, 7\n\tOUT_STEP 3, 1, 1, 0, 10, 7\n\t"
        "OUT_STEP 4, 1, 1, 0, 10, 7\n\t"
        "s_cmpk_eq_u32 s96, 0x6000\n\ts_cselect_b32 s97, %[last], s97\n\t"
        "OUT_STEP 5, 1, 1, 0, 10, 7\n\tOUT_STEP 6, 1, 1, 1, 10, 7\n\t"
        "OUT_STEP 7, 1, 1, 0, 10, 7\n\t"
        "s_cmpk_lg_u32 s96, 0x7000\n\ts_cbranch_scc1 .Lout_loop_%=\n\t"
        "OUT_STEP 0, 1, 1, 0, 10, 7\n\tOUT_STEP 1, 1, 1, 0, 10, 7\n\tOUT_STEP 2, 0, 1, 0, 8, 7\n\tOUT_STEP 3, 0, 1, 0, 6, 7\n\t"
        "OUT_STEP 4, 0, 1, 0, 4, 7\n\tOUT_STEP 5, 0, 1, 0, 2, 7\n\tOUT_STEP 6, 0, 0, 0, 0, 0\n\t"
        ".irp r,0,1\n\t.irp t,0,1,2,3\n\tOUT_MM \\r, \\t, 7\n\t.endr\n\t.endr\n\t"
        "s_nop 0\n\t"
        "OUT_PACK 64, 0\n\tglobal_store_dwordx4 %[s0], v[72:75], %[xa] sc1\n\tOUT_REST 64\n\tglobal_store_dwordx4 %[s0], v[68:71], %[xb] sc1\n\t"
        "OUT_PACK 84, 16\n\tglobal_store_dwordx4 %[s1], v[92:95], %[xa] sc1\n\tOUT_REST 84\n\tglobal_store_dwordx4 %[s1], v[88:91], %[xb] sc1\n\t"
        "s_cmp_eq_u64 %[st], 0\n\ts_cbranch_scc1 .Lout_end_%=\n\t"
        "OUT_DIAG 64\n\tOUT_DIAG 84\n\t"
        "s_mov_b64 exec, %[md]\n\t"
        "global_store_dwordx2 %[so], v[80:81], %[st] sc1\n\tglobal_store_dwordx2 %[so], v[100:101], %[st] offset:128 sc1\n\t"
        "s_mov_b64 exec, -1\n"
        ".Lout_end_%=:\n\t"
        "s_nop 1\n\t"
        ".purgem OUT_MM\n\t.purgem OUT_RD\n\t.purgem OUT_UP\n\t.purgem OUT_STEP\n\t.purgem OUT_PACK\n\t.purgem OUT_REST\n\t.purgem OUT_DIAG"
        : "+{v[0:15]}"(acc[0]), "+{v[16:31]}"(acc[1]), "+{v[64:95]}"(aw0), "+{v[96:127]}"(aw1),
          "+{v128}"(bw[0]), "+{v129}"(bw[1]), "+{v130}"(bw[2]), "+{v131}"(bw[3]), "=&{v[32:63]}"(fr)
        : [a0] "v"(ao[0]), [a1] "v"(ao[1]), [ra] "s"(ra), [last] "s"(AFTER_LOCAL ? 32768 : 65536), [one] "v"(ones), [s0] "v"(so[0]), [s1] "v"(so[1]),
          [xa] "s"(Xout), [xb] "s"((char*)Xout + (AFTER_LOCAL ? 16 : 8) * 2048), [st] "s"(statp), [so] "v"(sto),
          [m0] "s"(0xaaaaaaaaaaaaaaaaull), [m1] "s"(0xccccccccccccccccull), [md] "s"(0xf0000f0000f0000full), "v"(land)
        : "memory", "scc", "s96", "s97");
    STAMP(AFTER_LOCAL ? 5 : 12);
}

#define ROWSUM(p0, p1, c) __builtin_amdgcn_smfmac_f32_16x16x64_bf16(sel, __builtin_bit_cast(bf16v_t, __builtin_shufflevector(p0, p1, 0, 1, 2, 3, 4, 5, 6, 7)), c, sidx, 0, 0)
constexpr int ATT_STAGE = 32768;
typedef __attribute__((ext_vector_type(8))) unsigned u32x8;

DI void attn_loader(char* smem, int w, char* ws, int set, u64 tag, const __hip_bfloat16* __restrict__ kvf, const void* Wo, const void* Xin,
                    const void* Wcs SP_PARAM) {
    const int lane = lane_id();
    asm volatile("" : "+s"(w));
    const Ids I = ids();
    const int panel = I.pq, ct = I.cq;
    const unsigned lds0 = (unsigned)(uintptr_t)smem;
    const char* kvh = (const char*)kvf + (size_t)(ct * 2 + (w >> 1)) * 262144 + (w & 1) * 4096;
    const unsigned voff = (unsigned)(lane * 16);
    auto order = [&](int st) { return ((panel & 8) + st - ((st - 1 < (panel & 7)) ? 1 : 0)) & 15; };
    auto load = [&](int st) {
        const char* src = kvh + (size_t)order(st) * 16384;
        const unsigned dst = lds0 + (w >> 1) * 16384 + (w & 1) * 4096 + (st & 3) * ATT_STAGE;
#pragma unroll
        for (int u = 0; u < 2; ++u)
            asm volatile("s_mov_b32 m0, %0\n\ts_nop 0\n\tglobal_load_lds_dwordx4 %1, %2 sc1\n\tglobal_load_lds_dwordx4 %1, %2 offset:1024 sc1\n\t"
                         "global_load_lds_dwordx4 %1, %2 offset:2048 sc1\n\tglobal_load_lds_dwordx4 %1, %2 offset:3072 sc1"
                         :: "s"(dst + u * 8192), "v"(voff), "s"(src + u * 8192) : "memory");
    };
    const unsigned wv = wo_off(w, lane), rv = res_off(false, w, lane), cv = colsum_off(w, lane);
    const unsigned poff = flag_off(set, ct, panel, lane & 15);
    u64 land;
    __builtin_amdgcn_s_setprio(3);
    BAR();
    signal_poll<false, true>(w, ws, flag_off(set, ct, lane & 15, panel), poff, 1ull << order(1), tag, land);
    STAMP(9);
    load(1);
    asm volatile("global_load_dwordx2 %0, %1, %2 sc1\n\ts_waitcnt vmcnt(0)" : "+v"(land) : "v"(poff), "s"(ws + WS_FLAG) : "memory");
    STAMP(10);
    BAR();
    if (!__all(land == tag)) flag_poll(ws, poff, tag);
    load(2); load(3);
    __builtin_amdgcn_s_setprio(0);
    STAMP(11);
#pragma unroll 1
    for (int st = 1; st < 5; ++st) { VM_BAR(8); load(st + 3); }
    u32x4 res[4], ca[8];
    colsum_load(ca, w < 3 ? Wcs : nullptr, cv);
#pragma unroll 1
    for (int st = 5; st < 13; ++st) {
        VM_BAR(8);
        if (st == 9) wo_load(Wo, wv, 24, 32, lds0 + 32768, w);
        load(st + 3);
    }
    VM_BAR(8); wo_load(Wo, wv, 0, 8, lds0, w);
    VM_BAR(8); wo_load(Wo, wv, 8, 16, lds0, w);
    res_load(res, Xin, false, rv);
    BAR();     wo_load(Wo, wv, 16, 24, lds0, w);
    STAMP(12);
    BAR();
    asm volatile("s_waitcnt vmcnt(0)" ::: "memory");
    res_write(res, smem, 98304, w, lane);
    WAIT_LGKM_BAR();
    if (Wcs && w < 3) colsum_store(ca, ws, 0, tag, w, lane);
}

DI void attn_compute(char* smem, int w, const u32x4 (&q4)[4], const __hip_bfloat16* __restrict__ kvf, __hip_bfloat16* __restrict__ ofrag SP_PARAM) {
    const int lane = lane_id();
    asm volatile("" : "+s"(w));
    const int wm = w & 1, wn = w >> 1;
    const Ids I = ids();
    const int panel = I.pq, head = I.cq * 2 + wn;
    const int l15 = lane & 15;
    const unsigned se = ((l15 & 2) == 0 && (l15 & 4) == 0) ? 0x3f803f80u : 0u, so = ((l15 & 2) == 0 && (l15 & 4) != 0) ? 0x3f803f80u : 0u;
    const bf8_t sel = __builtin_bit_cast(bf8_t, u32x4{se, se, so, so});
    const int sidx = (l15 & 1) ? (int)0xEEEEEEEEu : 0x44444444;
    const lds_cptr sm = (lds_cptr)smem + wn * 16384 + lane * 16;
    const f32x16 z16 = {0, 0, 0, 0, 0, 0, 0, 0, 0, 0, 0, 0, 0, 0, 0, 0};
    const bf16x8 qA0 = __builtin_bit_cast(bf16x8, q4[0]), qA1 = __builtin_bit_cast(bf16x8, q4[1]);
    const bf16x8 qB0 = __builtin_bit_cast(bf16x8, q4[2]), qB1 = __builtin_bit_cast(bf16x8, q4[3]);
    f32x16 oA, oB, nmA, nmB, sA, sB;
    f32x4 lsA, lsB;
    u32x8 pB;
    bf16x8 v11;
    asm volatile(".irp r,2,4,6,8,10,12,14,16,18,20,22,24,26,28,30,32,106,108,110,112,134,136,138,140,170,172\n\tv_mov_b64 v[\\r:\\r+1], 0\n\t.endr"
                 : "={v[2:17]}"(oA), "={v[18:33]}"(oB), "={v[106:113]}"(pB), "={v[134:137]}"(lsA), "={v[138:141]}"(lsB), "={v[170:173]}"(v11));
    WAIT_LGKM_BAR();
    STAMP(8);
    bf16x8 k00 = lds_b128(sm), k10 = lds_b128(sm + 1024), k01 = lds_b128(sm + 4096), k11 = lds_b128(sm + 4096 + 1024);
    bf16x8 v00 = lds_b128(sm + 2048), v10 = lds_b128(sm + 3072), v01 = lds_b128(sm + 4096 + 2048);
    {
        sA = MFMA32(k00, qA0, z16); sB = MFMA32(k00, qB0, z16);
        sA = MFMA32(k10, qA1, sA); sB = MFMA32(k10, qB1, sB);
        SBAR(); VM_BAR(0); SBAR();
        float mA = sA[0], mB = sB[0];
#pragma unroll
        for (int r = 1; r < 16; ++r) { mA = fmaxf(mA, sA[r]); mB = fmaxf(mB, sB[r]); }
        mA = max_h(mA); mB = max_h(mB);
#pragma unroll
        for (int r = 0; r < 16; ++r) { nmA[r] = -mA; nmB[r] = -mB; sA[r] -= mA; }
    }
    const unsigned a0 = (unsigned)(uintptr_t)sm;
    asm volatile(
        ".macro ATT_RD dst, y\n\t"
        "ds_read_b128 v[\\dst:\\dst+3], v[175+(((\\y)&0x1ffff)>>16)] offset:(\\y)&0xffff\n\t"
        ".endm\n\t"
        ".macro ATT_E4 c\n\t"
        "v_exp_f32 v[\\c], v[\\c]\n\tv_exp_f32 v[\\c+1], v[\\c+1]\n\tv_exp_f32 v[\\c+2], v[\\c+2]\n\tv_exp_f32 v[\\c+3], v[\\c+3]\n\t"
        ".endm\n\t"
        ".macro ATT_C4 p, c\n\t"
        "v_cvt_pk_bf16_f32 v[\\p], v[\\c], v[\\c+1]\n\tv_cvt_pk_bf16_f32 v[\\p+1], v[\\c+2], v[\\c+3]\n\t"
        "v_cvt_pk_bf16_f32 v[\\p+2], v[\\c+4], v[\\c+5]\n\tv_cvt_pk_bf16_f32 v[\\p+3], v[\\c+6], v[\\c+7]\n\t"
        ".endm\n\t"
        ".macro ATT_STEP cur, nxt, ka, kb, q, nm, oc, od, lsd, pc, pd, vd1, vc0, r0, y0, r1, y1, bar, rd\n\t"
        "v_mfma_f32_32x32x16_bf16 v[\\nxt:\\nxt+15], v[\\ka:\\ka+3], v[\\q:\\q+3], v[\\nm:\\nm+15]\n\t"
        "ATT_E4 \\cur\n\t"
        "v_mfma_f32_32x32x16_bf16 v[\\nxt:\\nxt+15], v[\\kb:\\kb+3], v[\\q+4:\\q+7], v[\\nxt:\\nxt+15]\n\t"
        "ATT_E4 \\cur+4\n\t"
        "v_mfma_f32_32x32x16_bf16 v[\\od:\\od+15], v[\\vd1:\\vd1+3], v[\\pd+4:\\pd+7], v[\\od:\\od+15]\n\t"
        "s_waitcnt lgkmcnt(0)\n\t"
        "ATT_C4 \\pc, \\cur\n\t"
        "v_exp_f32 v[\\cur+8], v[\\cur+8]\n\tv_exp_f32 v[\\cur+9], v[\\cur+9]\n\t"
        "v_mfma_f32_32x32x16_bf16 v[\\oc:\\oc+15], v[\\vc0:\\vc0+3], v[\\pc:\\pc+3], v[\\oc:\\oc+15]\n\t"
        ".if \\bar\n\ts_barrier\n\t.endif\n\t"
        "ATT_E4 \\cur+10\n\t"
        "v_smfmac_f32_16x16x64_bf16 v[\\lsd:\\lsd+3], v[130:133], v[\\pd:\\pd+7], v174\n\t"
        ".if \\rd & 4\n\tATT_RD 142, 32768\n\tATT_RD 150, 33792\n\tATT_RD 158, 34816\n\t.endif\n\t"
        ".if \\rd & 1\n\tATT_RD \\r0, \\y0\n\t.endif\n\t"
        ".if \\rd & 2\n\tATT_RD \\r1, \\y1\n\t.endif\n\t"
        "v_exp_f32 v[\\cur+14], v[\\cur+14]\n\tv_exp_f32 v[\\cur+15], v[\\cur+15]\n\t"
        "ATT_C4 \\pc+4, \\cur+8\n\t"
        ".if \\rd & 4\n\ts_waitcnt lgkmcnt(3)\n\t.endif\n\t"
        ".endm\n\t"
        ".macro ATT_TILE p, y1, y2, bar, ra=3, rb=3\n\t"
        "ATT_STEP 66, 82, 142+\\p, 150+\\p, 122, 50, 2, 18, 138, 98, 106, 170-\\p, 158+\\p, 170-\\p, \\y1+3072, 142+\\p, \\y2, \\bar, \\ra\n\t"
        "ATT_STEP 82, 66, 146-\\p, 154-\\p, 114, 34, 18, 2, 134, 106, 98, 166+\\p, 158+\\p, 158+\\p, \\y2+2048, 150+\\p, \\y2+1024, 0, \\rb\n\t"
        ".endm\n\t"
        ".macro ATT_STAGE b\n\t"
        "ATT_TILE 0, \\b+4096, \\b+8192, 0\n\t"
        "ATT_TILE 4, \\b+8192, \\b+12288, 0\n\t"
        "ATT_TILE 0, \\b+12288, \\b+32768, 1\n\t"
        "ATT_TILE 4, \\b+32768, \\b+36864, 0\n\t"
        ".endm\n\t"
        "v_add_u32 v176, 0x10000, v175\n\t"
        "s_mov_b32 s96, 4\n\t"
        "s_waitcnt lgkmcnt(0)\n\t"
        "ATT_TILE 0, 4096, 8192, 0\n\tATT_TILE 4, 8192, 12288, 0\n\tATT_TILE 0, 12288, 0, 0, 1, 0\n\tATT_TILE 4, 32768, 36864, 1, 7, 3\n\t"
        "s_branch .Latt_entry_%=\n\t"
        ".p2align 6\n"
        ".Latt_loop_%=:\n\t"
        "ATT_STAGE 0\n"
        ".Latt_entry_%=:\n\t"
        "ATT_STAGE 32768\n\tATT_STAGE 65536\n\t"
        "s_sub_u32 s96, s96, 1\n\t"
        "ATT_STAGE 98304\n\t"
        "s_cmp_lg_u32 s96, 0\n\t"
        "s_cbranch_scc1 .Latt_loop_%=\n\t"
        "s_nop 1\n\t"
        "v_mfma_f32_32x32x16_bf16 v[18:33], v[170:173], v[110:113], v[18:33]\n\t"
        "v_smfmac_f32_16x16x64_bf16 v[138:141], v[130:133], v[106:113], v174\n\t"
        "s_waitcnt lgkmcnt(0)\n\ts_nop 10\n\t"
        ".purgem ATT_RD\n\t.purgem ATT_E4\n\t.purgem ATT_C4\n\t.purgem ATT_STEP\n\t.purgem ATT_TILE\n\t.purgem ATT_STAGE"
        : "+{v[2:17]}"(oA), "+{v[18:33]}"(oB), "+{v[66:81]}"(sA), "+{v[106:113]}"(pB), "+{v[134:137]}"(lsA), "+{v[138:141]}"(lsB),
          "+{v[142:145]}"(k00), "+{v[146:149]}"(k01), "+{v[150:153]}"(k10), "+{v[154:157]}"(k11),
          "+{v[158:161]}"(v00), "+{v[162:165]}"(v01), "+{v[166:169]}"(v10), "+{v[170:173]}"(v11)
        : "{v[34:49]}"(nmA), "{v[50:65]}"(nmB), "{v[114:117]}"(qA0), "{v[118:121]}"(qA1), "{v[122:125]}"(qB0), "{v[126:129]}"(qB1),
          "{v[130:133]}"(sel), "{v174}"(sidx), "{v175}"(a0)
        : "memory", "scc", "s96", "v176", "v82", "v83", "v84", "v85", "v86", "v87", "v88", "v89", "v90", "v91", "v92", "v93", "v94", "v95",
          "v96", "v97", "v98", "v99", "v100", "v101", "v102", "v103", "v104", "v105");
    STAMP(9);
    float lA = lsA[0] + lsA[1], lB = lsB[0] + lsB[1];
    const unsigned dst = (unsigned)((((panel * 8 + wm * 4 + ((lane & 31) >> 4)) * 32 + head) * 64 + (lane & 15) + (lane >> 5) * 16) * 16);
    auto store = [&] {
#pragma unroll
    for (int u = 0; u < 2; ++u) {
        const f32x16 e = (u ? oB : oA) * __builtin_amdgcn_rcpf(u ? lB : lA);
        st16o<0>((char*)ofrag + u * 65536, dst, u32x4{pack_bf16(e[0], e[1]), pack_bf16(e[2], e[3]), pack_bf16(e[8], e[9]), pack_bf16(e[10], e[11])});
        st16o<512>((char*)ofrag + u * 65536, dst, u32x4{pack_bf16(e[4], e[5]), pack_bf16(e[6], e[7]), pack_bf16(e[12], e[13]), pack_bf16(e[14], e[15])});
    }
    };
    store();
    {
        const f32x16 os = oA + oB;
        const float osum = (((os[0] + os[1]) + (os[2] + os[3])) + ((os[4] + os[5]) + (os[6] + os[7]))) +
                           (((os[8] + os[9]) + (os[10] + os[11])) + ((os[12] + os[13]) + (os[14] + os[15])));
        const bool bad = !(lA < 1.0e24f) || !(lB < 1.0e24f) || !(fabsf(osum) < 3.0e38f);
        if (__any(bad)) {
            const __amdgpu_buffer_rsrc_t rk = __builtin_amdgcn_make_buffer_rsrc((char*)kvf + (size_t)head * 262144, 0, 262144, 0x00020000);
            oA = z16; oB = z16; lA = 0.f; lB = 0.f;
            float mA = -INFINITY, mB = -INFINITY;
#pragma unroll 1
            for (int t = 0; t < 64; ++t) {
                bf16x8 f[4];
#pragma unroll
                for (int i = 0; i < 4; ++i) f[i] = __builtin_bit_cast(bf16x8, __builtin_amdgcn_raw_buffer_load_b128(rk, lane * 16 + i * 1024, t * 4096, 16));
                auto upd = [&](bf16x8 q0, bf16x8 q1, f32x16& o, float& l, float& m) {
                    f32x16 s = MFMA32(f[0], q0, z16); s = MFMA32(f[1], q1, s);
                    float tm = s[0];
#pragma unroll
                    for (int r = 1; r < 16; ++r) tm = fmaxf(tm, s[r]);
                    tm = max_h(tm);
                    const float mn = fmaxf(m, tm);
                    const float alpha = __builtin_amdgcn_exp2f(m - mn);
                    m = mn;
#pragma unroll
                    for (int r = 0; r < 16; ++r) { s[r] = __builtin_amdgcn_exp2f(s[r] - mn); o[r] *= alpha; }
                    const u32x4 p0 = {pack_bf16(s[0], s[1]), pack_bf16(s[2], s[3]), pack_bf16(s[4], s[5]), pack_bf16(s[6], s[7])};
                    const u32x4 p1 = {pack_bf16(s[8], s[9]), pack_bf16(s[10], s[11]), pack_bf16(s[12], s[13]), pack_bf16(s[14], s[15])};
                    f32x4 t4 = {0, 0, 0, 0};
                    o = MFMA32(f[2], __builtin_bit_cast(bf16x8, p0), o);
                    o = MFMA32(f[3], __builtin_bit_cast(bf16x8, p1), o);
                    t4 = ROWSUM(p0, p1, t4);
                    l = l * alpha + (t4[0] + t4[1]);
                };
                upd(qA0, qA1, oA, lA, mA);
                upd(qB0, qB1, oB, lB, mB);
            }
            store();
        }
    }
}

DI void ln_row(const u32x4 (&v)[2], __hip_bfloat16* __restrict__ Y, unsigned off) {
    float s1 = 0.f, s2 = 0.f;
#pragma unroll
    for (int e = 0; e < 8; ++e) s1 = dot2bf(v[e >> 2][e & 3], 0x3f803f80u, s1);
    const float mean = wave_sum(s1) * (1.f / 1024.f);
#pragma unroll
    for (int e = 0; e < 8; ++e) {
        const float d0 = bf_lo(v[e >> 2][e & 3]) - mean, d1 = bf_hi(v[e >> 2][e & 3]) - mean;
        s2 += d0 * d0 + d1 * d1;
    }
    const float mb = round_bf16(mean);
    const float rden = __builtin_amdgcn_rcpf(round_bf16(__builtin_amdgcn_sqrtf(round_bf16(round_bf16(wave_sum(s2) * (1.f / 1024.f)) + 1e-5f))));
#pragma unroll
    for (int hh = 0; hh < 2; ++hh) {
        u32x4 y;
#pragma unroll
        for (int e = 0; e < 4; ++e) {
            const unsigned d = pack_bf16(bf_lo(v[hh][e]) - mb, bf_hi(v[hh][e]) - mb);
            y[e] = pack_bf16(bf_lo(d) * rden, bf_hi(d) * rden);
        }
        if (hh) st16o<1024>(Y, off, y); else st16o<0>(Y, off, y);
    }
}

extern "C" __global__ __launch_bounds__(512, 2) void mega_kernel(const __hip_bfloat16* x, const __hip_bfloat16* wqkvA,
                                                            const __hip_bfloat16* woA, const __hip_bfloat16* wqkvB,
                                                            const __hip_bfloat16* woB, __hip_bfloat16* out,
                                                            char* ws) {
    char* const smem = smem_base;
    typedef __hip_bfloat16 bf;
    const int w8 = __builtin_amdgcn_readfirstlane(threadIdx.x >> 6);
    if (w8 == 7 && (blockIdx.x >> 3) < 10) code_touch(blockIdx.x >> 3, threadIdx.x & 63);
    asm volatile("" :: "s"(x), "s"(wqkvA), "s"(woA), "s"(wqkvB), "s"(woB), "s"(out), "s"(ws));
    STAMP_DECL; STAMPR(14); STAMP(0);
    u64 tag;
    {
        const int lane = threadIdx.x & 63;
        const Ids I = ids();
        const unsigned off = (unsigned)((I.po * 128 + I.co * 8 + w8) * 2048 + lane * 16);
        u32x4 v[2];
        asm volatile("global_load_dwordx4 %0, %2, %3\n\tglobal_load_dwordx4 %1, %2, %3 offset:1024" : "=&v"(v[0]), "=&v"(v[1]) : "v"(off), "s"(x) : "memory");
        asm volatile("s_load_dwordx2 %0, %1, 0x0" : "=s"(tag) : "s"(ws + WS_EPOCH) : "memory");
        if (w8 == 7) {
            const int l = lane & 15, k = l % 5;
            const unsigned o = (l >= 10 || k == 3) ? flag_off(l, I.cq, I.pq, 0) : (k == 0 || k == 2) ? flag_off(l, I.pq, I.cq, 0) : flag_off(l, I.po, I.co, 0);
            touch_at(ws + WS_FLAG, o);
        } else if (w8 >= 4 && w8 - 4 < NTOUCH) touch(wqkvA, (w8 - 4) * 256 + blockIdx.x, lane);
        if (w8 == 7 || (w8 >= 4 && w8 - 4 < NTOUCH)) asm volatile("s_waitcnt vmcnt(1)" ::: "memory"); else asm volatile("s_waitcnt vmcnt(0)" ::: "memory");
        asm volatile("" : "+v"(v[0]), "+v"(v[1]));
        ln_row(v, (bf*)(ws + WS_Y0), off);
        asm volatile("s_waitcnt lgkmcnt(0)" : "+s"(tag) :: "memory");
        tag += 1;
    }
    if (w8 >= 4) {
        int w = w8 & 3;
#pragma unroll 1
        for (int it = 0; it < 2; ++it) {
            const int lane = lane_id();
            asm volatile("" : "+s"(x), "+s"(wqkvA), "+s"(woA), "+s"(wqkvB), "+s"(woB), "+s"(ws), "+s"(w));
            const unsigned lds0 = (unsigned)(uintptr_t)smem;
            u32x4 cb[8];
            const unsigned wv = wo_off(w, lane), rv = res_off(true, w, lane), cv = colsum_off(w, lane);
            qkv_loader<true>(smem, w, ws, it * 5, tag, (const bf*)(ws + (it ? WS_X2 : WS_Y0)), wqkvA, (const float*)(ws + WS_ST + 262144),
                             ws + WS_CSP, it == 0, woA, [&] {
                if (w < 3) {
                    colsum_load(cb, wqkvB, cv);
                    asm volatile("s_waitcnt vmcnt(8)" ::: "memory");
                } else {
                    touch(woA, blockIdx.x, lane);
                    asm volatile("s_waitcnt vmcnt(1)" ::: "memory");
                }
            }, [&](int part) {
                if (part == 0) wo_load(woA, wv, 32 - WO0, 32, lds0, w); else wo_load(woA, wv, 32 - WO1, 32 - WO0, lds0, w);
            } SP_ARG);
            STAMP(3);
            u32x4 res[4];
            res_load(res, it ? (const void*)(ws + WS_X2) : x, true, rv);
            if (it) __builtin_amdgcn_s_sleep(WSL1); else __builtin_amdgcn_s_sleep(WSL0);
            wo_load(woA, wv, 0, WO2, lds0, w);
            BAR();
            wo_load(woA, wv, WO2, 32 - WO1, lds0, w);
            const bool cs = (it == 0 && w < 3);
            STAMP(4);
            BAR();
            asm volatile("s_waitcnt vmcnt(0)" ::: "memory");
            res_write(res, smem, 131072, w, lane);
            WAIT_LGKM_BAR();
            if (cs) colsum_store(cb, ws, 1, tag, w, lane);
            STAMP(5);
            qkv_loader<false>(smem, w, ws, it * 5 + 2, tag, (const bf*)(ws + (it ? WS_X3 : WS_X1)), wqkvB, (const float*)(ws + WS_ST + it * 524288), ws + WS_CSP + 196608,
                              false, nullptr, [] {}, [](int) {} SP_ARG);
            STAMP(8);
            attn_loader(smem, w, ws, it * 5 + 3, tag, (const bf*)(ws + WS_KVF + it * 8 * MBy), woB, ws + (it ? WS_X3 : WS_X1),
                        it ? nullptr : wqkvA SP_ARG);
            STAMP(13); STAMPR(15); STAMP_FLUSH(it);
        }
        if (blockIdx.x == 0 && w == 0 && lane_id() == 0) __hip_atomic_store((g_u64ptr)(ws + WS_EPOCH), tag, __ATOMIC_RELAXED, __HIP_MEMORY_SCOPE_AGENT);
    } else {
        __builtin_amdgcn_s_setprio(2);
#pragma unroll 1
        for (int it = 0; it < 2; ++it) {
            u32x4 q4[4];
            asm volatile("" : "+s"(ws), "+s"(wqkvA), "+s"(wqkvB));
            qkv_compute<true>(smem, w8, wqkvA, q4, nullptr, (bf*)(ws + WS_OF + it * 8 * MBy), it == 0 SP_ARG);
            STAMP(2);
            out_compute<true>(smem, w8, ws, it * 5 + 1, tag, (const bf*)(ws + WS_OF + it * 8 * MBy),
                              (bf*)(ws + (it ? WS_X3 : WS_X1)), (float*)(ws + WS_ST + it * 524288) SP_ARG);
            STAMP(6);
            qkv_compute<false>(smem, w8, wqkvB, q4, (bf*)(ws + WS_KVF + it * 8 * MBy), nullptr, false SP_ARG);
            attn_compute(smem, w8, q4, (const bf*)(ws + WS_KVF + it * 8 * MBy), (bf*)(ws + WS_OF + (it * 8 + 4) * MBy) SP_ARG);
            out_compute<false>(smem, w8, ws, it * 5 + 4, tag, (const bf*)(ws + WS_OF + (it * 8 + 4) * MBy),
                               it ? out : (bf*)(ws + WS_X2), it ? nullptr : (float*)(ws + WS_ST + 262144) SP_ARG);
            STAMP(13); STAMPR(15); STAMP_FLUSH(it);
        }
    }
}

extern "C" __global__ void zz_code_end_kernel() {}

extern "C" void kernel_launch(void* const* d_in, const int* in_sizes, int n_in, void* d_out, int out_size, void* d_ws,
                              size_t ws_size, hipStream_t stream) {
    typedef __hip_bfloat16 bf;
    (void)hipFuncSetAttribute((const void*)mega_kernel, hipFuncAttributeMaxDynamicSharedMemorySize, CH_LDS);
    mega_kernel<<<256, 512, CH_LDS, stream>>>((const bf*)d_in[0], (const bf*)d_in[1], (const bf*)d_in[2], (const bf*)d_in[4], (const bf*)d_in[5],
                                              (bf*)d_out, (char*)d_ws);
}
```

```cpp
#include <hip/hip_runtime.h>
#include <hip/hip_bf16.h>
#include <stdint.h>

typedef __attribute__((ext_vector_type(8))) short bf16x8;
typedef __attribute__((ext_vector_type(4))) short s16x4;
typedef __attribute__((ext_vector_type(4))) float f32x4;
typedef __attribute__((ext_vector_type(2))) float f32x2;
typedef __attribute__((ext_vector_type(16))) float f32x16;
typedef __attribute__((ext_vector_type(4))) unsigned u32x4;
typedef __attribute__((ext_vector_type(2))) unsigned u32x2;
typedef __attribute__((ext_vector_type(2))) __bf16 bf16x2_t;
typedef __attribute__((ext_vector_type(8))) __bf16 bf8_t;
typedef __attribute__((ext_vector_type(16))) __bf16 bf16v_t;
typedef __attribute__((address_space(3))) const char* lds_cptr;
typedef __attribute__((address_space(3))) char* lds_ptr;

#define DI __device__ __forceinline__
#define MFMA16(a, b, c) __builtin_amdgcn_mfma_f32_16x16x32_bf16((a), (b), (c), 0, 0, 0)
#define MFMA32(a, b, c) __builtin_amdgcn_mfma_f32_32x32x16_bf16((a), (b), (c), 0, 0, 0)
#define SBAR() __builtin_amdgcn_sched_barrier(0)
#define STAMP(i)
#define STAMPR(i)
#define STAMP_DECL
#define STAMP_FLUSH(it)
#define SP_PARAM
#define SP_ARG
typedef unsigned long long u64;
#define NF 12
typedef __attribute__((ext_vector_type(32))) unsigned u32x32;
#define B2POS 0
#define WO2 7
#define WSL0 0
#define WSL1 8
#define WO1 7
#define WO0 3
#define SPRIO 3
#define QUNR 3
#define NTOUCH 3

constexpr float QSCALE = 0.17677669529663687f * 1.4426950408889634f;
constexpr float ATT_SCALE = 0.17677669529663687f;
constexpr float LOG2E = 1.4426950408889634f;
constexpr float NEG_MASK = -1e9f / ATT_SCALE;

DI int lane_id() { int l; asm volatile("v_mbcnt_lo_u32_b32 %0, -1, 0\n\tv_mbcnt_hi_u32_b32 %0, -1, %0" : "=v"(l)); return l; }
DI unsigned pack_bf16(float lo, float hi) {
    f32x2 v = {lo, hi};
    bf16x2_t r = __builtin_convertvector(v, bf16x2_t);
    return __builtin_bit_cast(unsigned, r);
}
DI float bf_lo(unsigned u) { return __uint_as_float(u << 16); }
DI float bf_hi(unsigned u) { return __uint_as_float(u & 0xffff0000u); }
DI float round_bf16(float x) { return bf_lo(pack_bf16(x, 0.f)); }
DI void ln_stats(float sum, float sumsq, float& mean_out, float& rstd_out) {
    float mean;
    asm("v_mul_f32 %0, 0x3a800000, %1" : "=v"(mean) : "v"(sum));
    const float var = fmaxf(__builtin_fmaf(-mean, mean, sumsq * (1.f / 1024.f)), 0.f);
    mean_out = round_bf16(mean);
    rstd_out = __builtin_amdgcn_rcpf(round_bf16(__builtin_amdgcn_sqrtf(round_bf16(round_bf16(var) + 1e-5f))));
}

DI void glds16(const void* sbase, unsigned voff, unsigned ldsaddr) {
    asm volatile("s_mov_b32 m0, %0\n\ts_nop 0\n\tglobal_load_lds_dwordx4 %1, %2"
                 :: "s"(ldsaddr), "v"(voff), "s"(sbase) : "memory");
}
DI void glds16c(const void* sbase, unsigned voff, unsigned ldsaddr) {
    asm volatile("s_mov_b32 m0, %0\n\ts_nop 0\n\tglobal_load_lds_dwordx4 %1, %2 sc1"
                 :: "s"(ldsaddr), "v"(voff), "s"(sbase) : "memory");
}
DI void st16(void* sbase, unsigned voff, u32x4 v) {
    asm volatile("global_store_dwordx4 %0, %1, %2 sc1\n\ts_nop 1" :: "v"(voff), "v"(v), "s"(sbase) : "memory");
}
template <int OFF> DI void st16o(void* sbase, unsigned voff, u32x4 v) {
    asm volatile("global_store_dwordx4 %0, %1, %2 offset:%3 sc1\n\ts_nop 1" :: "v"(voff), "v"(v), "s"(sbase), "n"(OFF) : "memory");
}
DI bf16x8 lds_b128(lds_cptr p) { return *(const __attribute__((address_space(3))) bf16x8*)p; }
DI s16x4 lds_tr(lds_cptr p) {
    return __builtin_amdgcn_ds_read_tr16_b64_v4i16((__attribute__((address_space(3))) s16x4*)p);
}
DI bf16x8 cat8(s16x4 a, s16x4 b) { return __builtin_shufflevector(a, b, 0, 1, 2, 3, 4, 5, 6, 7); }
DI float dot2bf(unsigned a, unsigned b, float c) {
    return __builtin_amdgcn_fdot2_f32_bf16(__builtin_bit_cast(bf16x2_t, a), __builtin_bit_cast(bf16x2_t, b), c, false);
}
DI float sum_g(float v) {
    auto a = __builtin_amdgcn_permlane16_swap(__float_as_uint(v), __float_as_uint(v), false, false);
    v = __uint_as_float(a[0]) + __uint_as_float(a[1]);
    auto b = __builtin_amdgcn_permlane32_swap(__float_as_uint(v), __float_as_uint(v), false, false);
    return __uint_as_float(b[0]) + __uint_as_float(b[1]);
}
DI float wave_sum(float v) {
    v += __builtin_bit_cast(float, __builtin_amdgcn_update_dpp(0, __builtin_bit_cast(int, v), 0xB1, 0xF, 0xF, true));
    v += __builtin_bit_cast(float, __builtin_amdgcn_update_dpp(0, __builtin_bit_cast(int, v), 0x4E, 0xF, 0xF, true));
    v += __builtin_bit_cast(float, __builtin_amdgcn_update_dpp(0, __builtin_bit_cast(int, v), 0x141, 0xF, 0xF, true));
    v += __builtin_bit_cast(float, __builtin_amdgcn_update_dpp(0, __builtin_bit_cast(int, v), 0x140, 0xF, 0xF, true));
    return sum_g(v);
}
DI float amax3(float a, float b, float c) { float r; asm("v_max3_f32 %0, %1, %2, %3" : "=v"(r) : "v"(a), "v"(b), "v"(c)); return r; }
DI float max2f(float a, float b) { return __builtin_amdgcn_fmed3f(a, b, INFINITY); }
DI float max_h(float v) {
    auto b = __builtin_amdgcn_permlane32_swap(__float_as_uint(v), __float_as_uint(v), false, false);
    return max2f(__uint_as_float(b[0]), __uint_as_float(b[1]));
}

template <int OFF> DI void st8o(void* sbase, unsigned voff, f32x2 v) {
    asm volatile("global_store_dwordx2 %0, %1, %2 offset:%3 sc1\n\ts_nop 1" :: "v"(voff), "v"(v), "s"(sbase), "n"(OFF) : "memory");
}
constexpr unsigned OFF_DUMP = 145408;
extern __shared__ __attribute__((aligned(16))) char smem_base[];
DI void touch_at(const void* p, unsigned off) {
    asm volatile("s_mov_b32 m0, %0\n\ts_nop 0\n\tglobal_load_lds_dword %1, %2"
                 :: "s"((unsigned)(uintptr_t)smem_base + OFF_DUMP), "v"(off), "s"(p) : "memory");
}
DI void touch(const void* p, int slice, int lane) { touch_at(p, (unsigned)(slice * 64 + lane) * 128u); }
DI void code_touch(int slice, int lane) {
    unsigned long long c0, c1;
    asm volatile("s_getpc_b64 s[96:97]\n\ts_add_u32 s96, s96, mega_kernel@rel32@lo+4\n\ts_addc_u32 s97, s97, mega_kernel@rel32@hi+12\n\ts_mov_b64 %0, s[96:97]\n\t"
                 "s_getpc_b64 s[96:97]\n\ts_add_u32 s96, s96, zz_code_end_kernel@rel32@lo+4\n\ts_addc_u32 s97, s97, zz_code_end_kernel@rel32@hi+12\n\ts_mov_b64 %1, s[96:97]"
                 : "=s"(c0), "=s"(c1) :: "s96", "s97", "scc");
    touch_at((const void*)c0, min((unsigned)(slice * 64 + lane) * 128u, (unsigned)(c1 - c0)));
}

#define BAR() asm volatile("s_barrier" ::: "memory")
#define WAIT_LGKM() do { SBAR(); __builtin_amdgcn_s_waitcnt(0xC07F); SBAR(); } while (0)
#define WAIT_LGKM_BAR() do { SBAR(); __builtin_amdgcn_s_waitcnt(0xC07F); asm volatile("s_barrier" ::: "memory"); SBAR(); } while (0)
#define VM_BAR(n) asm volatile("s_waitcnt vmcnt(" #n ")\n\ts_barrier" ::: "memory")
constexpr int CH_LDS = 163840;
typedef __attribute__((address_space(3))) unsigned* lds_uptr;
typedef __attribute__((address_space(1))) u64* g_u64ptr;

constexpr size_t MBy = 1 << 20;
constexpr size_t WS_X3 = 0, WS_X2 = 4 * MBy, WS_X1 = 8 * MBy, WS_OF = 12 * MBy  , WS_KVF = 36 * MBy  ,
                 WS_ST = 52 * MBy  , WS_CSP = 54 * MBy  , WS_FLAG = 55 * MBy  ,
                 WS_EPOCH = WS_CSP + 524288, WS_Y0 = 56 * MBy;
DI unsigned flag_off(int set, int group, int consumer, int producer) { return (unsigned)(set * 65536 + group * 4096 + consumer * 256 + producer * 8); }
DI void flag_store(char* ws, int set, int group, int producer, u64 tag, int lane) {
    asm volatile("" : "+s"(tag));
    if (lane < 16) __hip_atomic_store((g_u64ptr)(ws + WS_FLAG + flag_off(set, group, lane, producer)), tag, __ATOMIC_RELAXED, __HIP_MEMORY_SCOPE_AGENT);
}
DI void flag_poll(const char* ws, unsigned off, u64 tag) {
    asm volatile("" : "+s"(tag));
#pragma unroll 1
    for (int spin = 0; spin < (1 << 16); ++spin) {
        const u64 v = __hip_atomic_load((g_u64ptr)(ws + WS_FLAG + off), __ATOMIC_RELAXED, __HIP_MEMORY_SCOPE_AGENT);
        if (__all(v == tag)) break;
        __builtin_amdgcn_s_sleep(1);
    }
    asm volatile("" ::: "memory");
}
#define POLL_INS "global_atomic_or_x2 %[land], %[po], %[zero], %[base] sc0 sc1"
template <bool DRAIN, bool ALLPOLL>
DI void signal_poll(int follower, const char* ws, unsigned soff, unsigned poff, u64 pmask, u64 tag, u64& land) {
    u64 tagv, zero;
    asm volatile("s_not_b64 s[96:97], %[ts]\n\tv_mov_b64 %[tag], %[ts]\n\tv_mov_b64 %[zero], 0\n\tv_mov_b64 %[land], s[96:97]\n\t"
                 ".if %[dr]\n\ts_waitcnt vmcnt(0)\n\t.endif\n\ts_barrier\n\t"
                 "s_cmp_lg_u32 %[fl], 0\n\ts_cbranch_scc1 .Lsig_fol_%=\n\t"
                 "s_mov_b64 exec, 0xffff\n\t"
                 "global_store_dwordx2 %[so], %[tag], %[base] sc1\n"
                 ".if %[ap]\n.Lsig_fol_%=:\n.endif\n\t"
                 "s_mov_b64 exec, %[pm]\n\t"
                 "s_movk_i32 s96, 0x7fff\n"
                 ".Lsig_loop_%=:\n\t"
                 POLL_INS "\n\t"
                 "s_sleep 1\n\t"
                 "s_waitcnt vmcnt(3)\n\t"
                 "v_cmp_ne_u64 vcc, %[land], %[tag]\n\t"
                 "s_sub_u32 s96, s96, 1\n\t"
                 "s_cbranch_scc1 .Lsig_out_%=\n\t"
                 "s_cbranch_vccnz .Lsig_loop_%=\n"
                 ".Lsig_out_%=:\n\t"
                 "s_mov_b64 exec, -1\n"
                 ".if !%[ap]\n.Lsig_fol_%=:\n.endif"
                 : [land] "=&v"(land), [tag] "=&v"(tagv), [zero] "=&v"(zero)
                 : [fl] "s"(follower), [so] "v"(soff), [po] "v"(poff), [ts] "s"(tag), [base] "s"(ws + WS_FLAG), [pm] "s"(pmask), [ap] "n"(ALLPOLL ? 1 : 0), [dr] "n"(DRAIN ? 1 : 0)
                 : "memory", "scc", "vcc", "s96", "s97");
}
struct Ids { int pq, cq, po, co; };
DI Ids ids() {
    int bid = blockIdx.x; asm volatile("" : "+s"(bid));
    const int xcd = bid & 7, idx = bid >> 3;
    int pq, cq, po, co;
#define IDS_BODY pq = (xcd & 1) * 8 + (idx & 7); cq = (xcd >> 1) * 4 + (idx >> 3); po = (xcd & 3) * 4 + (idx & 3); co = (xcd >> 2) * 8 + (idx >> 2);
    IDS_BODY
    return Ids{pq, cq, po, co};
}

DI void slow_warm(unsigned dump, const void* W, int w, int lane) {
    int bid = blockIdx.x; asm volatile("" : "+s"(bid));
    const int xcd = bid & 7, idx = bid >> 3;
    const int sc = (3 - (int)((uintptr_t)W >> 7)) & 7;
    if ((sc >> 2) != ((xcd >> 1) & 1)) return;
    const char* src = (const char*)W + (idx + 32 * w) * 49152 + (sc + (xcd >> 2) * 8) * 128;
#pragma unroll
    for (int G = 0; G < 3; ++G) glds16(src + G * 2048, (unsigned)((lane >> 3) * 6144 + (lane & 7) * 16), dump);
}

template <int V> struct IC { static constexpr int value = V; };
template <bool LOCAL> struct QC {
    static constexpr int AROWS = LOCAL ? 144 : 128;
    static constexpr int ABYTES = AROWS * 128;
    static constexpr int STAGE = ABYTES + 24576;
    static constexpr int OFF_MEAN = 143360;
    static constexpr int OFF_RSTD = OFF_MEAN + 576;
    static constexpr int OFF_CS = OFF_RSTD + 576;
};
constexpr int OFF_EX = 131072;

DI unsigned colsum_off(int G, int lane) {
    const Ids I = ids();
    unsigned off = (unsigned)(I.pq * 64 + (lane >> 3)) * 6144u + (unsigned)(G * 2048 + I.cq * 128 + (lane & 7) * 16);
    asm volatile("" : "+v"(off));
    return off;
}
DI void colsum_load(u32x4 (&c)[8], const void* W, unsigned off) {
#define CS_NEXT "s_add_u32 s96, s96, 0xc000\n\ts_addc_u32 s97, s97, 0\n\t"
    asm volatile("s_cmp_eq_u64 %9, 0\n\ts_cbranch_scc1 .Lcs_skip_%=\n\ts_mov_b64 s[96:97], %9\n\t"
                 "global_load_dwordx4 %0, %8, s[96:97]\n\t" CS_NEXT "global_load_dwordx4 %1, %8, s[96:97]\n\t" CS_NEXT
                 "global_load_dwordx4 %2, %8, s[96:97]\n\t" CS_NEXT "global_load_dwordx4 %3, %8, s[96:97]\n\t" CS_NEXT
                 "global_load_dwordx4 %4, %8, s[96:97]\n\t" CS_NEXT "global_load_dwordx4 %5, %8, s[96:97]\n\t" CS_NEXT
                 "global_load_dwordx4 %6, %8, s[96:97]\n\t" CS_NEXT "global_load_dwordx4 %7, %8, s[96:97]\n"
                 ".Lcs_skip_%=:"
                 : "=&v"(c[0]), "=&v"(c[1]), "=&v"(c[2]), "=&v"(c[3]), "=&v"(c[4]), "=&v"(c[5]), "=&v"(c[6]), "=&v"(c[7])
                 : "v"(off), "s"(W) : "memory", "scc", "s96", "s97");
}
DI void colsum_store(u32x4 (&c)[8], char* ws, int mat, u64 tag, int G, int lane) {
    asm volatile("" : "+v"(c[0]), "+v"(c[1]), "+v"(c[2]), "+v"(c[3]), "+v"(c[4]), "+v"(c[5]), "+v"(c[6]), "+v"(c[7]));
    const Ids I = ids();
    const int m = lane & 15;
    const unsigned one = (m & 1) ? 0xbf800000u : 0xbf80u;
    const u32x4 sl = {(m >> 1) == 0 ? one : 0u, (m >> 1) == 1 ? one : 0u, (m >> 1) == 2 ? one : 0u, (m >> 1) == 3 ? one : 0u};
    f32x4 d = {0, 0, 0, 0};
#pragma unroll
    for (int i = 0; i < 8; ++i) d = MFMA16(__builtin_bit_cast(bf16x8, sl), __builtin_bit_cast(bf16x8, c[i]), d);
#pragma unroll
    for (int r = 0; r < 4; ++r)
        d[r] += __int_as_float(__builtin_amdgcn_update_dpp(0, __float_as_int(d[r]), 0x128, 0xF, 0xF, true));
    if (lane < 32 && (lane & 8) == 0)
        st16(ws + WS_CSP + mat * 196608, (unsigned)((I.cq * 16 + I.pq) * 768 + G * 256 + (lane & 7) * 32 + (lane >> 4) * 16), __builtin_bit_cast(u32x4, d));
    asm volatile("s_waitcnt vmcnt(0)" ::: "memory");
    flag_store(ws, (mat ? 10 : 13) + G, I.cq, I.pq, tag, lane);
}

template <bool LOCAL>
struct WFirst {
    unsigned v[2];
    DI WFirst(int w, int ct, int lane) {
#pragma unroll
        for (int i = 0; i < 2; ++i) {
            const int k = 8 * i + (lane >> 3), pos = lane & 7;
            v[i] = (unsigned)k * 6144u + (unsigned)(w * 1024 + ct * 64 + ((pos >> 1) ^ (((k >> 1) & 1) | (i << 1))) * 16 + (pos & 1) * 8) * 2u;
        }
        asm volatile("" : "+v"(v[0]), "+v"(v[1]));
    }
    DI void issue(const void* W, unsigned lds0, int w, int i0, int i1) const {
#pragma unroll
        for (int i = i0; i < i1; ++i)
            glds16((const char*)W + (size_t)(i >> 1) * (16 * 6144), v[i & 1], lds0 + (i >> 3) * QC<LOCAL>::STAGE + QC<LOCAL>::ABYTES + (w * 8 + (i & 7)) * 1024);
    }
};

template <bool LOCAL, class F, class F2>
DI void qkv_loader(char* smem, int w, char* ws, int set, u64 tag, const __hip_bfloat16* __restrict__ X, const __hip_bfloat16* __restrict__ W,
                   const float* __restrict__ statp, const char* __restrict__ csp, bool ident, const void* nextW, F tail, F2 mid SP_PARAM) {
    typedef QC<LOCAL> C;
    constexpr int NT = 16;
    const int lane = lane_id();
    asm volatile("" : "+s"(w));
    const int idx = blockIdx.x >> 3;
    const Ids I = ids();
    const int panel = I.pq, ct = I.cq;
    const int R0 = panel * 128;
    const unsigned lds0 = (unsigned)(uintptr_t)smem;
    auto grow = [&](int i) -> int {
        if (!LOCAL) return R0 + i;
        if (i < 128) { const int t = (i >> 4) & 3, a = i & 15; return R0 + (i & 64) + 2 * (16 * (t & 1) + a) + (t >> 1); }
        const int a = i & 15; const int r = R0 - 16 + 2 * (a & 7) + (a >> 3);
        return r < 0 ? 0 : r;
    };
    unsigned voffA[5], voffB[6];
#pragma unroll
    for (int i = 0; i < 6; ++i) {
        const int j = w + 4 * i, G = j >> 3, k = 8 * (j & 7) + (lane >> 3);
        const int pos = lane & 7, f = ((k >> 1) & 1) | (((k >> 3) & 1) << 1);
        const int seg = (pos >> 1) ^ f;
        voffB[i] = (unsigned)k * 6144u + (unsigned)(G * 1024 + ct * 64 + seg * 16 + (pos & 1) * 8) * 2u;
    }
    auto issueW = [&](int t, int buf) {
        const char* wb = (const char*)W + (size_t)t * (64 * 6144);
        const unsigned sb = lds0 + buf * C::STAGE;
#pragma unroll
        for (int i = 0; i < 6; ++i) glds16(wb, voffB[i], sb + C::ABYTES + (w + 4 * i) * 1024);
    };
    asm volatile("" : "+v"(voffB[0]), "+v"(voffB[1]), "+v"(voffB[2]), "+v"(voffB[3]), "+v"(voffB[4]), "+v"(voffB[5]));
    const WFirst<LOCAL> first(w, ct, lane);
    const int g = lane >> 4, r = lane & 15, pos = 32 * w + 2 * r + (g >> 1);
    const int lr = !LOCAL ? (pos & 64) + 2 * (pos & 31) + ((pos >> 5) & 1)
                          : (w == 0 && (g & 1)) ? 128 + (r & 7) + 8 * (g >> 1) : (pos & 64) + ((pos & 1) * 2 + ((pos >> 5) & 1)) * 16 + ((pos >> 1) & 15);
    unsigned so = (unsigned)(panel * 16384 + g * 1024 + w * 256 + r * 16), ho = (unsigned)(g * 1024 + (panel ? panel * 16384 - 15488 + (r & 7) * 16 : 0));
    unsigned co = (unsigned)((ct * 16 + g) * 768 + w * 192 + r * 12);
    lds_ptr mp = (lds_ptr)smem + C::OFF_MEAN + lr * 4, cp = (lds_ptr)smem + C::OFF_CS + w * 192 + r * 12 + ((g & 1) ? 8 : g * 2);
    unsigned fo = flag_off((LOCAL ? 13 : 10) + min(g, 2), ct, panel, r);
    asm volatile("" : "+v"(so), "+v"(ho), "+v"(co), "+v"(mp), "+v"(cp), "+v"(fo));
#pragma unroll
    for (int i = 0; i < 5; ++i) {
        const int j = (i < 4) ? (w + 4 * i) : (16 + (w & 1));
        const int row = 8 * j + (lane >> 3);
        const int c = (lane & 7) ^ ((row >> 1) & 7);
        const int gr = grow(row < C::AROWS ? row : 0);
        voffA[i] = (unsigned)((gr >> 7) * 262144 + (gr & 127) * 128 + c * 16);
        asm volatile("" : "+v"(voffA[i]));
    }
    auto issueA = [&](int t, int buf) {
        const char* xa = (const char*)X + t * 16384;
        const unsigned sb = lds0 + buf * C::STAGE;
        if (LOCAL && w < 2) glds16c(xa, voffA[4], sb + (16 + w) * 1024);
#pragma unroll
        for (int i = 0; i < 4; ++i) glds16c(xa, voffA[i], sb + (w + 4 * i) * 1024);
    };
    u64 land;
    if (w == 3) __builtin_amdgcn_s_setprio(3);
    signal_poll<LOCAL, false>(w - 3, ws, flag_off(set, I.po, lane & 15, I.co),
                              flag_off(set, (lane & 16) && panel ? panel - 1 : panel, ct, lane & 15),
                              LOCAL ? 0xffffffffull : 0xffffull, tag, land);
    if (w < 3) first.issue(W, lds0, w, 0, NF);
    BAR();
    STAMP(LOCAL ? 1 : 6);
    issueA(0, 0);
    issueA(1, 1);
    if (w < 3) { first.issue(W, lds0, w, NF, 16); asm volatile("s_waitcnt vmcnt(%0)" :: "n"(20 - NF) : "memory"); }
    else asm volatile("s_waitcnt vmcnt(4)" ::: "memory");
    BAR();
    asm volatile("" :: "v"(land));
    __builtin_amdgcn_s_setprio(0);
    STAMP(LOCAL ? 2 : 7);
    issueW(2, 2);
    issueA(2, 2);
    VM_BAR(10);
    asm volatile("global_load_dwordx2 %0, %1, %2 sc1" : "=&v"(land) : "v"(fo), "s"(ws + WS_FLAG) : "memory");
    issueA(3, 0); issueW(3, 0);
    int buf = 1;
#pragma unroll 1
    for (int t = 1; t < NT - 3; ++t) {
        VM_BAR(10);
        issueA(t + 3, buf); issueW(t + 3, buf);
        if (t == 10 && idx < 3) code_touch(idx * 4 + w, lane);
        if (LOCAL && t == 11)
            glds16(nextW, (unsigned)((idx + 32 * w) * 8 + (lane >> 3)) * 2048u +
                   (unsigned)(((I.co & 8) + ((3 - (int)((uintptr_t)nextW >> 7)) & 7)) * 128 + (lane & 7) * 16), lds0 + OFF_DUMP);
        buf = (buf == 2) ? 0 : buf + 1;
    }
    typedef __attribute__((ext_vector_type(3))) float f32x3;
    const bool halo = LOCAL && w == 0;
    asm volatile("" : "+v"(land));
    if (!ident) {
        if (!__all(land == tag)) flag_poll(ws, fo, tag);
        f32x3 cs[4];
        f32x4 sv[4], hv[4];
        typedef __attribute__((address_space(3))) float* lds_f;
        auto add = [](float a, float b) { float r; asm("v_add_f32 %0, %1, %2" : "=v"(r) : "v"(a), "v"(b)); return r; };
        auto fold32 = [](float a, float b) { asm("s_nop 1\n\tv_permlane32_swap_b32 %0, %1\n\tv_add_f32 %0, %0, %1" : "+v"(a), "+v"(b)); return a; };
        auto fold16 = [](float a, float b) { asm("s_nop 1\n\tv_permlane16_swap_b32 %0, %1\n\tv_add_f32 %0, %0, %1" : "+v"(a), "+v"(b)); return a; };
        auto sum4 = [&](auto (&v)[4], int e) { return add(add(v[0][e], v[1][e]), add(v[2][e], v[3][e])); };
#pragma unroll
        for (int i = 0; i < 4; ++i) asm volatile("global_load_dwordx3 %0, %1, %2 sc1" : "=&v"(cs[i]) : "v"(co), "s"(csp + i * 3072) : "memory");
        VM_BAR(14);
#pragma unroll
        for (int i = 0; i < 4; ++i) asm volatile("global_load_dwordx4 %0, %1, %2 sc1" : "=&v"(sv[i]) : "v"(so), "s"((const char*)statp + i * 4096) : "memory");
        if (LOCAL)
            asm volatile("s_cmp_lg_u32 %9, 0\n\ts_cbranch_scc1 .Lhv_skip_%=\n\t"
                         "global_load_dwordx4 %0, %4, %5 sc1\n\tglobal_load_dwordx4 %1, %4, %6 sc1\n\t"
                         "global_load_dwordx4 %2, %4, %7 sc1\n\tglobal_load_dwordx4 %3, %4, %8 sc1\n"
                         ".Lhv_skip_%=:" : "=&v"(hv[0]), "=&v"(hv[1]), "=&v"(hv[2]), "=&v"(hv[3])
                         : "v"(ho), "s"(statp), "s"(statp + 1024), "s"(statp + 2048), "s"(statp + 3072), "s"(w) : "memory", "scc");
        if (halo) asm volatile("s_waitcnt vmcnt(8)" ::: "memory"); else asm volatile("s_waitcnt vmcnt(4)" ::: "memory");
        asm volatile("" : "+v"(cs[0]), "+v"(cs[1]), "+v"(cs[2]), "+v"(cs[3]));
        const float c2 = sum4(cs, 2);
        *(lds_f)cp = fold16(fold32(sum4(cs, 0), sum4(cs, 1)), fold32(c2, c2));
        BAR();
        asm volatile("s_waitcnt vmcnt(0)" : "+v"(sv[0]), "+v"(sv[1]), "+v"(sv[2]), "+v"(sv[3]) :: "memory");
        mid(0);
        const float s1 = fold32(sum4(sv, 0), sum4(sv, 2)), s2 = fold32(sum4(sv, 1), sum4(sv, 3));
        float h1, h2;
        asm("v_mov_b32 %0, %2\n\tv_mov_b32 %1, %3" : "=&v"(h1), "=&v"(h2) : "v"(s1), "v"(s2));
        if (halo) {
            asm volatile("" : "+v"(hv[0]), "+v"(hv[1]), "+v"(hv[2]), "+v"(hv[3]));
            const float u0 = sum4(hv, 0), u1 = sum4(hv, 1), u2 = sum4(hv, 2), u3 = sum4(hv, 3);
            h1 = fold32(u0, panel ? u2 : u0); h2 = fold32(u1, panel ? u3 : u1);
        }
        float mean, rstd;
        ln_stats(fold16(s1, h1), fold16(s2, h2), mean, rstd);
        *(lds_f)mp = mean; *(lds_f)(mp + (C::OFF_RSTD - C::OFF_MEAN)) = rstd;
    } else { VM_BAR(10); tail(); BAR(); mid(0); }
    mid(1);
    WAIT_LGKM_BAR();
}

template <bool LOCAL>
DI void qkv_compute(char* smem, int w, const void* W, u32x4 (&qo4)[4], __hip_bfloat16* __restrict__ kvf, __hip_bfloat16* __restrict__ ofrag, bool ident SP_PARAM) {
    typedef QC<LOCAL> C;
    constexpr int NT = 16, ABYTES = C::ABYTES, STAGE = C::STAGE;
    const int lane = lane_id();
    asm volatile("" : "+s"(w));
    const int wm = w & 1, wn = w >> 1;
    const Ids I = ids();
    const int panel = I.pq, ct = I.cq;
    const int l15 = lane & 15, g = lane >> 4;
    const lds_cptr sm = (lds_cptr)smem;
    lds_ptr smw = (lds_ptr)smem;

    f32x4 acc[3][4][2];
    f32x4 acch[2];
    f32x2 z2; asm volatile("v_mov_b64 %0, 0" : "=v"(z2));
    const f32x4 z4 = {z2[0], z2[1], z2[0], z2[1]};
#pragma unroll
    for (int a = 0; a < 3; ++a)
#pragma unroll
        for (int t = 0; t < 4; ++t) { acc[a][t][0] = z4; acc[a][t][1] = z4; }
    acch[0] = z4; acch[1] = z4;

    struct Frags { bf16x8 a[4]; bf16x8 h; s16x4 b[3][2][2]; };
    auto rd = [&](auto WM, Frags& F, lds_cptr pa, lds_cptr pb0, lds_cptr pb1, int n) {
        switch (n) {
            case 0: F.b[0][0][0] = lds_tr(pb0); break;
            case 1: F.b[0][0][1] = lds_tr(pb0 + 512); break;
            case 2: F.a[0] = lds_b128(pa); break;
            case 3: F.b[0][1][0] = lds_tr(pb1); break;
            case 4: F.b[0][1][1] = lds_tr(pb1 + 512); break;
            case 5: F.a[1] = lds_b128(pa + 2048); break;
            case 6: F.a[2] = lds_b128(pa + 4096); break;
            case 7: F.a[3] = lds_b128(pa + 6144); break;
            case 8: F.b[1][0][0] = lds_tr(pb0 + 8192); break;
            case 9: F.b[1][0][1] = lds_tr(pb0 + 8192 + 512); break;
            case 10: F.b[1][1][0] = lds_tr(pb1 + 8192); break;
            case 11: F.b[1][1][1] = lds_tr(pb1 + 8192 + 512); break;
            case 12: F.b[2][0][0] = lds_tr(pb0 + 16384); break;
            case 13: F.b[2][0][1] = lds_tr(pb0 + 16384 + 512); break;
            case 14: F.b[2][1][0] = lds_tr(pb1 + 16384); break;
            case 15: F.b[2][1][1] = lds_tr(pb1 + 16384 + 512); break;
            case 16: if (LOCAL) F.h = lds_b128(pa + 16384 - WM.value * 8192); break;
            default: break;
        }
    };
    auto mm = [](f32x4& c, bf16x8 a, bf16x8 b) { asm volatile("v_mfma_f32_16x16x32_bf16 %0, %1, %2, %0" : "+v"(c) : "v"(a), "v"(b)); };
    auto half = [&](auto WM, const Frags& C, Frags& N, bool doread, lds_cptr pa, lds_cptr pb0, lds_cptr pb1, auto tail, int G0 = 0, int G1 = 3) {
#pragma unroll
        for (int G = G0; G < G1; ++G) {
            const bf16x8 b0 = cat8(C.b[G][0][0], C.b[G][0][1]), b1 = cat8(C.b[G][1][0], C.b[G][1][1]);
#pragma unroll
            for (int t = 0; t < 4; ++t) {
                const int n = (G - G0) * 8 + t * 2;
                if (G < 2) mm(acc[G][t][0], b0, C.a[t]); else mm(acc[G][t][0], C.a[t], b0);
                SBAR();
                if (doread) rd(WM, N, pa, pb0, pb1, n);
                tail(n);
                SBAR();
                if (G < 2) mm(acc[G][t][1], b1, C.a[t]); else mm(acc[G][t][1], C.a[t], b1);
                SBAR();
                if (doread) rd(WM, N, pa, pb0, pb1, n + 1);
                tail(n + 1);
                SBAR();
            }
        }
        if (LOCAL) {
            if (WM.value == 0) { mm(acch[0], cat8(C.b[1][0][0], C.b[1][0][1]), C.h); mm(acch[1], cat8(C.b[1][1][0], C.b[1][1][1]), C.h); }
            else               { mm(acch[0], C.h, cat8(C.b[2][0][0], C.b[2][0][1])); mm(acch[1], C.h, cat8(C.b[2][1][0], C.b[2][1][1])); }
            SBAR();
        }
    };

    Frags F0, F1;
    f32x4 cq[2], ck[2], mu4[5], rs4[5]; float cv[2], mu[5], rs[5];
    u32x4 qfr[4], kfr[4];
    u32x4 va[2][2];
    f32x4 mwin, mfirst;
    typedef __attribute__((address_space(3))) u32x4* lds_u4;
    const bool negpos = (wm == 0) && (panel == 0);
    VM_BAR(0);
    unsigned cm01 = 0, cm23 = 0;
    lds_cptr ka[2] = {sm, sm}, vo2[2] = {sm, sm};
    lds_u4 ex = (lds_u4)(smw + OFF_EX + wn * 6144) + lane;
    lds_cptr s1 = sm + C::OFF_MEAN + wm * 256 + l15 * 4, s4 = sm + C::OFF_MEAN + wm * 256 + g * 16;
    lds_cptr c1 = sm + C::OFF_CS + wn * 128 + l15 * 4, c4 = sm + C::OFF_CS + wn * 128 + g * 16;
    asm volatile("" : "+v"(s1), "+v"(s4), "+v"(c1), "+v"(c4));
    if (LOCAL) {
        unsigned keepv = negpos ? __float_as_uint(NEG_MASK) : 0u;
        asm volatile("" : "+s"(keepv));
        u32x4 mw, mf;
        unsigned prv[4];
#pragma unroll
        for (int r = 0; r < 4; ++r) {
            const int d = l15 - (4 * g + r);
            unsigned out = (unsigned)(((d << 28) >> 31));
            prv[r] = (unsigned)(d >> 31);
            asm volatile("" : "+v"(out), "+v"(prv[r]));
            mw[r] = out & 0xff800000u;
            mf[r] = (out & 0xff800000u) | (~out & keepv);
        }
        const lds_cptr e0 = sm + OFF_EX + wn * 6144;
        ka[0] = e0 + (wm ? lane : 256 + (lane ^ 8)) * 16;         ka[1] = e0 + (wm ? 64 + lane : 256 + lane) * 16;
        vo2[0] = e0 + (wm ? 128 + lane : 320 + (lane ^ 32)) * 16; vo2[1] = e0 + (wm ? 192 + lane : 320 + lane) * 16;
        cm01 = ~((prv[0] >> 16) | (prv[1] << 16)); cm23 = ~((prv[2] >> 16) | (prv[3] << 16));
        mwin = __builtin_bit_cast(f32x4, mw); mfirst = __builtin_bit_cast(f32x4, mf);
        asm volatile("" : "+v"(mwin), "+v"(mfirst), "+v"(cm01), "+v"(cm23), "+v"(ka[0]), "+v"(ka[1]), "+v"(vo2[0]), "+v"(vo2[1]), "+v"(ex));
    }
    typedef const __attribute__((address_space(3))) f32x4* lds_f4; typedef const __attribute__((address_space(3))) float* lds_f1;
    auto mainloop = [&](auto WM) {
#pragma unroll
        for (int a = 0; a < 3; ++a)
            asm volatile("" : "+v"(acc[a][0][0]), "+v"(acc[a][0][1]), "+v"(acc[a][1][0]), "+v"(acc[a][1][1]), "+v"(acc[a][2][0]), "+v"(acc[a][2][1]),
                         "+v"(acc[a][3][0]), "+v"(acc[a][3][1]));
        int ty = lane_id();
        const int l15 = ty & 15, g = ty >> 4;
        const int swA = (l15 >> 1) & 7;
        const int q4 = l15 >> 2, p4 = l15 & 3, fk = ((q4 >> 1) & 1) | ((g & 1) << 1);
        lds_cptr pA0 = sm + wm * 8192 + l15 * 128 + ((0 + g) ^ swA) * 16, pA1 = sm + wm * 8192 + l15 * 128 + ((4 + g) ^ swA) * 16;
        lds_cptr pB0 = sm + ABYTES + (8 * g + q4) * 128 + (((2 * wn + 0) ^ fk) * 32) + p4 * 8;
        lds_cptr pB1 = sm + ABYTES + (8 * g + q4) * 128 + (((2 * wn + 1) ^ fk) * 32) + p4 * 8;
        asm volatile("" : "+v"(acch[0]), "+v"(acch[1]), "+v"(pA0), "+v"(pA1), "+v"(pB0), "+v"(pB1));
        slow_warm((unsigned)(uintptr_t)smem + OFF_DUMP, W, w, ty);
        BAR();
        STAMP(LOCAL ? 1 : 7);
        BAR();
#pragma unroll
        for (int n = 0; n < 17; ++n) rd(WM, F0, pA0, pB0, pB1, n);
        WAIT_LGKM();
        int off = 0;
        lds_cptr a = pA1, b0 = pB0, b1 = pB1;
#pragma unroll QUNR
        for (int t = 0; t < NT - 1; ++t) {
            half(WM, F0, F1, true, a, b0 + 4096, b1 + 4096, [&](int n) {
                if (n == 17) { off = (off == 2 * STAGE) ? 0 : off + STAGE; asm volatile("" : "+s"(off)); }
                if (n == 19) { b0 = pB0 + off; asm volatile("" : "+v"(b0)); }
                if (n == 20) { a = pA0 + off; asm volatile("" : "+v"(a)); }
                if (n == 21) { b1 = pB1 + off; asm volatile("" : "+v"(b1)); }
            });
            WAIT_LGKM_BAR();
            half(WM, F1, F0, true, a, b0, b1, [&](int n) {
                if (n == 19) { a = pA1 + off; asm volatile("" : "+v"(a)); }
                if (n == 22) __builtin_amdgcn_s_waitcnt(0xC07F);
            });
        }
        half(WM, F0, F1, true, a, b0 + 4096, b1 + 4096, [](int) {});
        WAIT_LGKM_BAR();
        half(WM, F1, F0, false, sm, sm, sm, [&](int n) {
            constexpr int RS = C::OFF_RSTD - C::OFF_MEAN, H = 512 - WM.value * 256;
            if (n < 2) { cq[n] = *(lds_f4)(c4 + n * 64); ck[n] = *(lds_f4)(c4 + 256 + n * 64); }
            else if (n < 6) { mu[n - 2] = *(lds_f1)(s1 + (n - 2) * 64); rs[n - 2] = *(lds_f1)(s1 + RS + (n - 2) * 64); }
            else if (n < 10) { mu4[n - 6] = *(lds_f4)(s4 + (n - 6) * 64); rs4[n - 6] = *(lds_f4)(s4 + RS + (n - 6) * 64); }
            else if (n == 10) { cv[0] = *(lds_f1)(c1 + 512); cv[1] = *(lds_f1)(c1 + 512 + 64); }
            else if (LOCAL && n == 11) {
                if (WM.value == 0) { mu[4] = *(lds_f1)(s1 + H); rs[4] = *(lds_f1)(s1 + RS + H); }
                else { mu4[4] = *(lds_f4)(s4 + H); rs4[4] = *(lds_f4)(s4 + RS + H); }
            } else if (n == (LOCAL ? 22 : 15)) __builtin_amdgcn_s_waitcnt(0xC07F);
        }, LOCAL ? 0 : 1);
        asm volatile("s_nop 7");
        SBAR();
        if (LOCAL) {
            auto fix = [&](int a, int t) {
#pragma unroll
                for (int f = 0; f < 2; ++f)
                    acc[a][t][f] = (a == 2) ? rs4[t] * (acc[2][t][f] + mu4[t] * cv[f]) : rs[t] * (acc[a][t][f] + mu[t] * (a ? ck[f] : cq[f]));
            };
            auto pk = [&](const f32x4 (&v)[2]) { return u32x4{pack_bf16(v[0][0], v[0][1]), pack_bf16(v[0][2], v[0][3]), pack_bf16(v[1][0], v[1][1]), pack_bf16(v[1][2], v[1][3])}; };
            if (!ident) {
                fix(1, 1); fix(1, 3); fix(2, 1); fix(2, 3);
#pragma unroll
                for (int f = 0; f < 2; ++f)
                    acch[f] = (WM.value == 0) ? rs[4] * (acch[f] + mu[4] * ck[f]) : rs4[4] * (acch[f] + mu4[4] * cv[f]);
            }
            auto pkv = [&](int t) {
#pragma unroll
                for (int f = 0; f < 2; ++f) {
                    va[t >> 1][f][(t & 1) * 2] = pack_bf16(acc[2][t][f][0], acc[2][t][f][1]);
                    va[t >> 1][f][(t & 1) * 2 + 1] = pack_bf16(acc[2][t][f][2], acc[2][t][f][3]);
                }
            };
            kfr[1] = pk(acc[1][1]); kfr[3] = pk(acc[1][3]); pkv(1); pkv(3);
            u32x4 hf = pk(acch);
            if (WM.value == 0) {
                ex[0] = kfr[1]; ex[64] = kfr[3];
#pragma unroll
                for (int u = 0; u < 2; ++u)
                    asm volatile("ds_write_b64 %0, %1 offset:%3\n\tds_write_b64 %0, %2 offset:%3+8" :: "v"(ex), "v"(u32x2{va[u][0][2], va[u][0][3]}),
                                 "v"(u32x2{va[u][1][2], va[u][1][3]}), "n"(2048 + u * 1024) : "memory");
                if (negpos) hf = u32x4{0, 0, 0, 0};
            }
            ex[256 + WM.value * 64] = hf;
            SBAR();
            if (!ident) { fix(0, 0); fix(0, 1); fix(0, 2); fix(0, 3); fix(1, 0); fix(1, 2); fix(2, 0); fix(2, 2); }
#pragma unroll
            for (int t = 0; t < 4; ++t) qfr[t] = pk(acc[0][t]);
            kfr[0] = pk(acc[1][0]); kfr[2] = pk(acc[1][2]); pkv(0); pkv(2);
            asm volatile("" : "+v"(qfr[0]), "+v"(qfr[1]), "+v"(qfr[2]), "+v"(qfr[3]), "+v"(kfr[0]), "+v"(kfr[2]),
                         "+v"(va[0][0]), "+v"(va[0][1]), "+v"(va[1][0]), "+v"(va[1][1]));
        }
    };
    const int head = ct * 2 + wn;
    unsigned qo = 0, ko = 0, vo = 0;
    if (LOCAL) {
        qo = (unsigned)((((panel * 8 + wm * 4) * 32 + head) * 64 + lane) * 16);
        asm volatile("" : "+v"(qo));
    } else {
        ko = (unsigned)((wm * 8 + (g & 1)) * 1024 + ((l15 & 7) + ((l15 & 8) << 1) + 32 * (g >> 1)) * 16);
        vo = (unsigned)((wm * 8 + 2 + (g >> 1)) * 1024 + (l15 + 32 * (g & 1)) * 16);
        asm volatile("" : "+v"(ko), "+v"(vo));
    }
    if (LOCAL && wm) mainloop(IC<1>{}); else mainloop(IC<0>{});

    if (!LOCAL) {
        auto pk = [&](f32x4 a, f32x4 b) { return u32x4{pack_bf16(a[0], a[1]), pack_bf16(a[2], a[3]), pack_bf16(b[0], b[1]), pack_bf16(b[2], b[3])}; };
        char* kvp = (char*)kvf + (size_t)(head * 16 + panel) * 16384;
        lds_ptr own = smw + wn * 16384;
        SBAR();
#pragma unroll
        for (int t = 0; t < 4; ++t) {
            const u32x4 kk = pk(rs[t] * (acc[1][t][0] + mu[t] * ck[0]), rs[t] * (acc[1][t][1] + mu[t] * ck[1]));
            if (t & 1) st16o<128>(kvp + (t >> 1) * 4096, ko, kk); else st16o<0>(kvp + (t >> 1) * 4096, ko, kk);
            kfr[t] = kk;
            SBAR();
            const int u = t >> 1, f = t & 1;
            const u32x4 vv = pk(rs4[2 * u] * (acc[2][2 * u][f] + mu4[2 * u] * cv[f]), rs4[2 * u + 1] * (acc[2][2 * u + 1][f] + mu4[2 * u + 1] * cv[f]));
            if (f) st16o<256>(kvp + u * 4096, vo, vv); else st16o<0>(kvp + u * 4096, vo, vv);
            qfr[t] = vv;
            SBAR();
        }
        half(IC<0>{}, F1, F0, false, sm, sm, sm, [&](int n) {
            const int t = n >> 1;
            if (n & 1) *(lds_u4)(own + vo + (t >> 1) * 4096 + (t & 1) * 256) = qfr[t];
            else       *(lds_u4)(own + ko + (t >> 1) * 4096 + (t & 1) * 128) = kfr[t];
        }, 0, 1);
        asm volatile("s_nop 7");
        SBAR();
#pragma unroll
        for (int u = 0; u < 2; ++u) {
            const float r0 = rs[2 * u] * QSCALE, r1 = rs[2 * u + 1] * QSCALE;
            const u32x4 qa = pk(r0 * (acc[0][2 * u][0] + mu[2 * u] * cq[0]), r0 * (acc[0][2 * u][1] + mu[2 * u] * cq[1]));
            const u32x4 qb = pk(r1 * (acc[0][2 * u + 1][0] + mu[2 * u + 1] * cq[0]), r1 * (acc[0][2 * u + 1][1] + mu[2 * u + 1] * cq[1]));
#pragma unroll
            for (int e = 0; e < 4; ++e) {
                const auto x = __builtin_amdgcn_permlane16_swap(qa[e], qb[e], false, false);
                qo4[2 * u][e] = x[0]; qo4[2 * u + 1][e] = x[1];
            }
        }
        return;
    }
    {
        const f32x4 zero4 = {0, 0, 0, 0};
        auto bf8 = [](u32x4 v) { return __builtin_bit_cast(bf16x8, v); };
        f32x4 sp[4], sc[4];
        sc[1] = MFMA16(bf8(kfr[1]), bf8(qfr[1]), mwin); sp[1] = MFMA16(bf8(kfr[0]), bf8(qfr[1]), mwin);
        sc[3] = MFMA16(bf8(kfr[3]), bf8(qfr[3]), mwin); sp[3] = MFMA16(bf8(kfr[2]), bf8(qfr[3]), mwin);
        asm volatile("" : "+v"(sc[1]), "+v"(sp[1]), "+v"(sc[3]), "+v"(sp[3]));
        WAIT_LGKM_BAR();
        const bf16x8 kp[2] = {lds_b128(ka[0]), lds_b128(ka[1])};
        u32x2 vp[2][2];
#pragma unroll
        for (int u = 0; u < 2; ++u)
            asm volatile("ds_read_b64 %0, %2\n\tds_read_b64 %1, %2 offset:8" : "=&v"(vp[u][0]), "=&v"(vp[u][1]) : "v"(vo2[u]) : "memory");
        sc[0] = MFMA16(bf8(kfr[0]), bf8(qfr[0]), mwin); sc[2] = MFMA16(bf8(kfr[2]), bf8(qfr[2]), mwin);
        SBAR();
        float x[4][4], m[4];
        unsigned a[4][2];
        auto logits = [&](int t0, int st) {
#pragma unroll
            for (int t = t0; t < 4; t += 2)
#pragma unroll
                for (int h = 0; h < 2; ++h) {
                    if (st == 0) {
                        const unsigned uc = pack_bf16(sc[t][2 * h], sc[t][2 * h + 1]), up = pack_bf16(sp[t][2 * h], sp[t][2 * h + 1]);
                        asm("v_bfi_b32 %0, %1, %2, %3" : "=v"(a[t][h]) : "v"(h ? cm23 : cm01), "v"(uc), "v"(up));
                    } else if (st == 1) { x[t][2 * h] = bf_lo(a[t][h]) * ATT_SCALE; x[t][2 * h + 1] = bf_hi(a[t][h]) * ATT_SCALE; }
                    else if (st == 2) a[t][h] = pack_bf16(x[t][2 * h], x[t][2 * h + 1]);
                    else { x[t][2 * h] = bf_lo(a[t][h]); x[t][2 * h + 1] = bf_hi(a[t][h]); }
                }
            SBAR();
        };
        logits(1, 0); logits(1, 1);
        sp[0] = MFMA16(kp[0], bf8(qfr[0]), mfirst); sp[2] = MFMA16(kp[1], bf8(qfr[2]), mfirst);
        SBAR();
        logits(1, 2); logits(1, 3);
        logits(0, 0); logits(0, 1); logits(0, 2); logits(0, 3);
        auto across = [&](float (&v)[4], auto op, auto fin) {
            const auto p = __builtin_amdgcn_permlane16_swap(__float_as_uint(v[0]), __float_as_uint(v[1]), false, false);
            const auto q = __builtin_amdgcn_permlane16_swap(__float_as_uint(v[2]), __float_as_uint(v[3]), false, false);
            const auto r = __builtin_amdgcn_permlane32_swap(__float_as_uint(op(__uint_as_float(p[0]), __uint_as_float(p[1]))),
                                                            __float_as_uint(op(__uint_as_float(q[0]), __uint_as_float(q[1]))), false, false);
            const unsigned z = __float_as_uint(fin(op(__uint_as_float(r[0]), __uint_as_float(r[1]))));
            const auto u = __builtin_amdgcn_permlane32_swap(z, z, false, false);
            const auto e = __builtin_amdgcn_permlane16_swap(u[0], u[0], false, false), f = __builtin_amdgcn_permlane16_swap(u[1], u[1], false, false);
            v[0] = __uint_as_float(e[0]); v[1] = __uint_as_float(e[1]); v[2] = __uint_as_float(f[0]); v[3] = __uint_as_float(f[1]);
        };
#pragma unroll
        for (int t = 0; t < 4; ++t) m[t] = amax3(x[t][0], x[t][1], x[t][2]);
        asm("v_max_f32 %0, %0, %4\n\tv_max_f32 %1, %1, %5\n\tv_max_f32 %2, %2, %6\n\tv_max_f32 %3, %3, %7\n\ts_nop 0"
            : "+v"(m[0]), "+v"(m[1]), "+v"(m[2]), "+v"(m[3]) : "v"(x[0][3]), "v"(x[1][3]), "v"(x[2][3]), "v"(x[3][3]));
        across(m, [](float p, float q) { float r; asm("v_max_f32 %0, %1, %2\n\ts_nop 0" : "=v"(r) : "v"(p), "v"(q)); return r; },
               [](float p) { return p * -LOG2E; });
        SBAR();
#pragma unroll
        for (int t = 0; t < 4; ++t)
#pragma unroll
            for (int r = 0; r < 4; ++r) x[t][r] = __builtin_fmaf(x[t][r], LOG2E, m[t]);
        SBAR();
#pragma unroll
        for (int t = 0; t < 4; ++t)
#pragma unroll
            for (int r = 0; r < 4; ++r) x[t][r] = __builtin_amdgcn_exp2f(x[t][r]);
        SBAR();
#pragma unroll
        for (int t = 0; t < 4; ++t) m[t] = (x[t][0] + x[t][1]) + (x[t][2] + x[t][3]);
        asm volatile("s_waitcnt lgkmcnt(0)" : "+v"(vp[0][0]), "+v"(vp[0][1]), "+v"(vp[1][0]), "+v"(vp[1][1]) :: "memory");
        u32x4 ve[2][2];
#pragma unroll
        for (int u = 0; u < 2; ++u)
#pragma unroll
            for (int f = 0; f < 2; ++f) ve[u][f] = u32x4{vp[u][f][0], vp[u][f][1], va[u][f][0], va[u][f][1]};
        across(m, [](float p, float q) { return p + q; }, [](float p) { return __builtin_amdgcn_rcpf(p); });
        asm volatile("" : "+v"(ve[0][0]), "+v"(ve[0][1]), "+v"(ve[1][0]), "+v"(ve[1][1]));
        SBAR();
#pragma unroll
        for (int t = 0; t < 4; ++t)
#pragma unroll
            for (int r = 0; r < 4; ++r) x[t][r] *= m[t];
        SBAR();
#pragma unroll
        for (int t = 0; t < 4; ++t)
#pragma unroll
            for (int h = 0; h < 2; ++h) a[t][h] = pack_bf16(x[t][2 * h], x[t][2 * h + 1]);
        SBAR();
        f32x4 o0[4], o1[4];
#pragma unroll
        for (int t = 0; t < 4; ++t) {
            const bf16x8 pb = bf8(u32x4{a[t][0] & ~cm01, a[t][1] & ~cm23, a[t][0] & cm01, a[t][1] & cm23});
            o0[t] = MFMA16(bf8((t & 1) ? va[t >> 1][0] : ve[t >> 1][0]), pb, zero4);
            o1[t] = MFMA16(bf8((t & 1) ? va[t >> 1][1] : ve[t >> 1][1]), pb, zero4);
        }
        SBAR();
#pragma unroll
        for (int t = 0; t < 4; ++t)
            st16(ofrag + t * 16384, qo, u32x4{pack_bf16(o0[t][0], o0[t][1]), pack_bf16(o0[t][2], o0[t][3]),
                                                 pack_bf16(o1[t][0], o1[t][1]), pack_bf16(o1[t][2], o1[t][3])});
    }
}

DI int out_row(int panel, bool localrows, int T, int a) {
    return localrows ? (panel * 128 + (T >> 2) * 64 + 2 * (16 * (T & 1) + a) + ((T >> 1) & 1)) : (panel * 128 + T * 16 + a);
}
DI int out_swz(int k) { return ((k >> 1) & 1) | (k & 4); }
DI unsigned wo_off(int w, int lane) {
    const int kl = lane >> 3;
    unsigned v = (unsigned)(w * 8 + kl) * 2048u + (unsigned)(ids().co * 128 + ((lane & 7) ^ out_swz(kl)) * 16);
    asm volatile("" : "+v"(v));
    return v;
}
DI void wo_load(const void* Wo, unsigned voff, int i0, int i1, unsigned lds0, int w) {
#pragma unroll
    for (int i = i0; i < i1; ++i) glds16((const char*)Wo + (size_t)i * 65536, voff, lds0 + (w + 4 * i) * 1024);
}
DI unsigned res_off(bool localrows, bool tiled, int lane) {
    const Ids I = ids();
    unsigned v = (unsigned)(I.po * 262144 + I.co * (tiled ? 16384 : 128) + out_row(0, localrows, 0, lane >> 3) * (tiled ? 128 : 2048) +
                            (((lane & 7) ^ (lane >> 3)) * 16));
    asm volatile("" : "+v"(v));
    return v;
}
DI void res_load(u32x4 (&res)[6], const void* Xin, bool localrows, bool tiled, unsigned off, int w) {
#pragma unroll
    for (int i = 0; i < 6; ++i) {
        const int j = min(w + 3 * i, 15);
        asm volatile("global_load_dwordx4 %0, %1, %2 sc1" : "=&v"(res[i]) : "v"(off), "s"((const char*)Xin + out_row(0, localrows, j >> 1, (j & 1) * 8) * (tiled ? 128 : 2048)) : "memory");
    }
}
DI void out_handoff(char* smem, int xoff, u32x4 (&res)[6], int w, char* ws, int set, u64 tag, int lane) {
    const Ids I = ids();
    u64 land;
    lds_ptr dst = (lds_ptr)smem + xoff + w * 1024 + lane * 16;
    asm volatile("" : "+v"(dst));
    if (w == 3) __builtin_amdgcn_s_setprio(3);
    signal_poll<false, false>(w - 3, ws, flag_off(set, I.pq, lane & 15, I.cq), flag_off(set, I.po, I.co, lane & 15), 0xffffull, tag, land);
    if (w < 3) {
        asm volatile("s_waitcnt vmcnt(0)" : "+v"(res[0]), "+v"(res[1]), "+v"(res[2]), "+v"(res[3]), "+v"(res[4]), "+v"(res[5]) :: "memory");
#pragma unroll
        for (int i = 0; i < 5; ++i) *(__attribute__((address_space(3))) u32x4*)(dst + i * 3072) = res[i];
        if (w == 0) *(__attribute__((address_space(3))) u32x4*)(dst + 15360) = res[5];
    }
    WAIT_LGKM_BAR();
    asm volatile("s_waitcnt vmcnt(0)" : "+v"(land) :: "memory");
    __builtin_amdgcn_s_setprio(0);
}

template <bool AFTER_LOCAL>
DI void out_compute(char* smem, int w, const __hip_bfloat16* __restrict__ ofrag,
                    __hip_bfloat16* __restrict__ Xout, float* __restrict__ statp SP_PARAM) {
    constexpr int XOFF = AFTER_LOCAL ? 147456 : 98304;
    STAMP(AFTER_LOCAL ? 3 : 10);
    VM_BAR(0);
    const int lane = lane_id();
    asm volatile("" : "+s"(w));
    const Ids I = ids();
    const int panel = I.po, ct = I.co;
    const int l15 = lane & 15, g = lane >> 4;
    const bool tiled = AFTER_LOCAL || statp != nullptr;
    const int rstr = tiled ? 128 : 2048;
    const int q4 = l15 >> 2, p4 = l15 & 3;
    const int krow = 4 * g + q4;
    const int sw = out_swz(krow);
    const lds_cptr sm = (lds_cptr)smem;
    lds_cptr bw[4], xo[4];
#pragma unroll
    for (int t = 0; t < 4; ++t) {
        bw[t] = sm + krow * 128 + ((((t >> 1) * 4 + (p4 >> 1) * 2 + (t & 1)) ^ sw) * 16) + (p4 & 1) * 8;
        xo[t] = sm + XOFF + (w * 32 + l15) * 128 + ((((t >> 1) * 4 + (g >> 1) * 2 + (t & 1)) ^ (l15 & 7)) * 16) + (g & 1) * 8;
    }
    unsigned so[2], sto, ao[2];
#pragma unroll
    for (int r2 = 0; r2 < 2; ++r2) {
        so[r2] = (unsigned)(panel * 262144 + ct * (tiled ? 16384 : 128) + out_row(0, AFTER_LOCAL, 2 * w + r2, l15 & 7) * rstr + (l15 >> 3) * 64 + g * 16);
        ao[r2] = (unsigned)((panel * 8 + 2 * w + r2) * 32768 + lane * 16);
    }
    sto = (unsigned)(((panel * 16 + ct) * 128 + 2 * w * 16 + l15) * 8);
    asm volatile("" : "+v"(bw[0]), "+v"(bw[1]), "+v"(bw[2]), "+v"(bw[3]), "+v"(xo[0]), "+v"(xo[1]), "+v"(xo[2]), "+v"(xo[3]),
                 "+v"(so[0]), "+v"(so[1]), "+v"(sto), "+v"(ao[0]), "+v"(ao[1]));
    bf16x8 ones;
    {
        unsigned o1; asm volatile("v_mov_b32 %0, 0x3f803f80" : "=v"(o1));
        ones = __builtin_bit_cast(bf16x8, (u32x4{o1, o1, o1, o1}));
        asm volatile("" : "+v"(ones));
    }
    const __amdgpu_buffer_rsrc_t ra = __builtin_amdgcn_make_buffer_rsrc((void*)ofrag, 0, 4 << 20, 0x00020000);
    asm volatile("" :: "s"(ra));
    u32x32 fr;
    BAR();
    STAMP(AFTER_LOCAL ? 4 : 11);
    u32x32 aw0, aw1;
    asm volatile(".irp j,0,1,2,3\n\tbuffer_load_dwordx4 v[64+8*\\j:67+8*\\j], %2, %4, 0 offen offset:1024*\\j sc1\n\t"
                 "buffer_load_dwordx4 v[68+8*\\j:71+8*\\j], %3, %4, 0 offen offset:1024*\\j sc1\n\t.endr\n\t"
                 ".irp j,0,1\n\tbuffer_load_dwordx4 v[96+8*\\j:99+8*\\j], %2, %4, %5 offen offset:1024*\\j sc1\n\t"
                 "buffer_load_dwordx4 v[100+8*\\j:103+8*\\j], %3, %4, %5 offen offset:1024*\\j sc1\n\t.endr"
                 : "=&{v[64:95]}"(aw0), "=&{v[96:127]}"(aw1) : "v"(ao[0]), "v"(ao[1]), "s"(ra), "s"(4096) : "memory");
    f32x16 acc[2];
#pragma unroll
    for (int r2 = 0; r2 < 2; ++r2)
#pragma unroll
        for (int c = 0; c < 4; ++c) {
            const u32x2 xr = *(const __attribute__((address_space(3))) u32x2*)(xo[c] + r2 * 2048);
            acc[r2][4 * c] = bf_lo(xr[0]); acc[r2][4 * c + 1] = bf_hi(xr[0]); acc[r2][4 * c + 2] = bf_lo(xr[1]); acc[r2][4 * c + 3] = bf_hi(xr[1]);
        }
    asm volatile(
        ".macro OUT_MM r, t, j\n\t"
        "v_mfma_f32_16x16x32_bf16 v[16*\\r+4*\\t:16*\\r+4*\\t+3], v[32+16*((\\j)&1)+4*\\t:35+16*((\\j)&1)+4*\\t], v[64+8*\\j+4*\\r:67+8*\\j+4*\\r], v[16*\\r+4*\\t:16*\\r+4*\\t+3]\n\t"
        ".endm\n\t"
        ".macro OUT_RD t, hi, j, on\n\t"
        ".if \\on\n\tds_read_b64_tr_b16 v[32+16*((\\j)&1)+4*\\t+2*\\hi:33+16*((\\j)&1)+4*\\t+2*\\hi], v[128+\\t] offset:((\\j)&7)*4096+2048*\\hi\n\t.endif\n\t"
        ".endm\n\t"
        ".macro OUT_UP t, on\n\t.if \\on\n\tv_add_u32 v[128+\\t], s97, v[128+\\t]\n\t.endif\n\t.endm\n\t"
        ".macro OUT_STEP j, ld, rd, up, vm, lg\n\t"
        "OUT_MM 0, 0, \\j\n\tOUT_RD 3, 1, \\j+1, 1\n\tOUT_UP 0, \\up\n\t"
        ".if \\ld && (((\\j+6)&3) == 0)\n\ts_addk_i32 s96, 0x1000\n\t.endif\n\t"
        "OUT_MM 1, 0, \\j\n\tOUT_RD 0, 0, \\j+2, \\rd\n\t"
        ".if \\ld\n\tbuffer_load_dwordx4 v[64+8*((\\j+6)&7):67+8*((\\j+6)&7)], %[a0], %[ra], s96 offen offset:1024*((\\j+6)&3) sc1\n\t.endif\n\t"
        "OUT_MM 0, 1, \\j\n\tOUT_RD 0, 1, \\j+2, \\rd\n\tOUT_UP 1, \\up\n\t"
        "OUT_MM 1, 1, \\j\n\tOUT_RD 1, 0, \\j+2, \\rd\n\t"
        "OUT_MM 0, 2, \\j\n\tOUT_RD 1, 1, \\j+2, \\rd\n\tOUT_UP 2, \\up\n\t"
        "OUT_MM 1, 2, \\j\n\tOUT_RD 2, 0, \\j+2, \\rd\n\t"
        ".if \\ld\n\tbuffer_load_dwordx4 v[68+8*((\\j+6)&7):71+8*((\\j+6)&7)], %[a1], %[ra], s96 offen offset:1024*((\\j+6)&3) sc1\n\t.endif\n\t"
        "OUT_MM 0, 3, \\j\n\tOUT_RD 2, 1, \\j+2, \\rd\n\tOUT_UP 3, \\up\n\t"
        "OUT_MM 1, 3, \\j\n\tOUT_RD 3, 0, \\j+2, \\rd\n\t"
        "s_waitcnt vmcnt(\\vm) lgkmcnt(\\lg)\n\t"
        ".endm\n\t"
        ".macro OUT_PACK y, c\n\t"
        ".irp i,0,1,2,3,4,5,6,7\n\tv_cvt_pk_bf16_f32 v[\\y+\\i], v[\\c+2*\\i], v[\\c+2*\\i+1]\n\t.endr\n\t"
        ".irp i,0,1,4,5\n\tv_permlane16_swap_b32 v[\\y+\\i], v[\\y+\\i+2]\n\t.endr\n\t"
        ".irp i,0,1,2,3\n\tv_mov_b32 v[\\y+8+\\i], v[\\y+\\i]\n\t.endr\n\t"
        "v_mfma_f32_16x16x32_bf16 v[\\y+12:\\y+15], v[\\y:\\y+3], v[\\y:\\y+3], 0\n\t"
        "v_mov_b32_dpp v[\\y+8], v[\\y+4] row_ror:8 row_mask:0xf bank_mask:0xc\n\tv_mov_b32_dpp v[\\y+9], v[\\y+5] row_ror:8 row_mask:0xf bank_mask:0xc\n\t"
        "v_mfma_f32_16x16x32_bf16 v[\\y+16:\\y+19], %[one], v[\\y:\\y+3], 0\n\t"
        "v_mov_b32_dpp v[\\y+10], v[\\y+6] row_ror:8 row_mask:0xf bank_mask:0xc\n\tv_mov_b32_dpp v[\\y+11], v[\\y+7] row_ror:8 row_mask:0xf bank_mask:0xc\n\t"
        "v_mfma_f32_16x16x32_bf16 v[\\y+12:\\y+15], v[\\y+4:\\y+7], v[\\y+4:\\y+7], v[\\y+12:\\y+15]\n\t"
        ".endm\n\t"
        ".macro OUT_REST y\n\t"
        "v_mfma_f32_16x16x32_bf16 v[\\y+16:\\y+19], %[one], v[\\y+4:\\y+7], v[\\y+16:\\y+19]\n\t"
        ".irp i,0,1,2,3\n\tv_mov_b32_dpp v[\\y+4+\\i], v[\\y+\\i] row_ror:8 row_mask:0xf bank_mask:0x3\n\t.endr\n\t"
        ".endm\n\t"
        ".macro OUT_DIAG y\n\t"
        "v_cndmask_b32 v[\\y+12], v[\\y+12], v[\\y+13], %[m0]\n\tv_cndmask_b32 v[\\y+14], v[\\y+14], v[\\y+15], %[m0]\n\t"
        "v_cndmask_b32 v[\\y+17], v[\\y+12], v[\\y+14], %[m1]\n\t"
        ".endm\n\t"
        ".irp t,0,1,2,3\n\tOUT_RD \\t, 0, 0, 1\n\tOUT_RD \\t, 1, 0, 1\n\t.endr\n\t"
        ".irp t,0,1,2\n\tOUT_RD \\t, 0, 1, 1\n\tOUT_RD \\t, 1, 1, 1\n\t.endr\n\tOUT_RD 3, 0, 1, 1\n\t"
        "s_movk_i32 s96, 0x1000\n\ts_mov_b32 s97, 0x8000\n\t"
        "s_waitcnt vmcnt(10) lgkmcnt(0)\n"
        ".Lout_loop_%=:\n\t"
        "OUT_STEP 0, 1, 1, 0, 10, 7\n\tOUT_STEP 1, 1, 1, 0, 10, 7\n\tOUT_STEP 2, 1, 1, 0, 10, 7\n\tOUT_STEP 3, 1, 1, 0, 10, 7\n\t"
        "OUT_STEP 4, 1, 1, 0, 10, 7\n\t"
        "s_cmpk_eq_u32 s96, 0x6000\n\ts_cselect_b32 s97, %[last], s97\n\t"
        "OUT_STEP 5, 1, 1, 0, 10, 7\n\tOUT_STEP 6, 1, 1, 1, 10, 7\n\t"
        "OUT_STEP 7, 1, 1, 0, 10, 7\n\t"
        "s_cmpk_lg_u32 s96, 0x7000\n\ts_cbranch_scc1 .Lout_loop_%=\n\t"
        "OUT_STEP 0, 1, 1, 0, 10, 7\n\tOUT_STEP 1, 1, 1, 0, 10, 7\n\tOUT_STEP 2, 0, 1, 0, 8, 7\n\tOUT_STEP 3, 0, 1, 0, 6, 7\n\t"
        "OUT_STEP 4, 0, 1, 0, 4, 7\n\tOUT_STEP 5, 0, 1, 0, 2, 7\n\tOUT_STEP 6, 0, 0, 0, 0, 0\n\t"
        ".irp r,0,1\n\t.irp t,0,1,2,3\n\tOUT_MM \\r, \\t, 7\n\t.endr\n\t.endr\n\t"
        "s_nop 0\n\t"
        "OUT_PACK 64, 0\n\tglobal_store_dwordx4 %[s0], v[72:75], %[xa] sc1\n\tOUT_REST 64\n\tglobal_store_dwordx4 %[s0], v[68:71], %[xb] sc1\n\t"
        "OUT_PACK 84, 16\n\tglobal_store_dwordx4 %[s1], v[92:95], %[xa] sc1\n\tOUT_REST 84\n\tglobal_store_dwordx4 %[s1], v[88:91], %[xb] sc1\n\t"
        "s_cmp_eq_u64 %[st], 0\n\ts_cbranch_scc1 .Lout_end_%=\n\t"
        "OUT_DIAG 64\n\tOUT_DIAG 84\n\t"
        "s_mov_b64 exec, %[md]\n\t"
        "global_store_dwordx2 %[so], v[80:81], %[st] sc1\n\tglobal_store_dwordx2 %[so], v[100:101], %[st] offset:128 sc1\n\t"
        "s_mov_b64 exec, -1\n"
        ".Lout_end_%=:\n\t"
        "s_nop 1\n\t"
        ".purgem OUT_MM\n\t.purgem OUT_RD\n\t.purgem OUT_UP\n\t.purgem OUT_STEP\n\t.purgem OUT_PACK\n\t.purgem OUT_REST\n\t.purgem OUT_DIAG"
        : "+{v[0:15]}"(acc[0]), "+{v[16:31]}"(acc[1]), "+{v[64:95]}"(aw0), "+{v[96:127]}"(aw1),
          "+{v128}"(bw[0]), "+{v129}"(bw[1]), "+{v130}"(bw[2]), "+{v131}"(bw[3]), "=&{v[32:63]}"(fr)
        : [a0] "v"(ao[0]), [a1] "v"(ao[1]), [ra] "s"(ra), [last] "s"(AFTER_LOCAL ? 32768 : 65536), [one] "v"(ones), [s0] "v"(so[0]), [s1] "v"(so[1]),
          [xa] "s"(Xout), [xb] "s"((char*)Xout + (AFTER_LOCAL ? 16 : 8) * rstr), [st] "s"(statp), [so] "v"(sto),
          [m0] "s"(0xaaaaaaaaaaaaaaaaull), [m1] "s"(0xccccccccccccccccull), [md] "s"(0xf0000f0000f0000full)
        : "memory", "scc", "s96", "s97");
    STAMP(AFTER_LOCAL ? 5 : 12);
}

#define ROWSUM(p0, p1, c) __builtin_amdgcn_smfmac_f32_16x16x64_bf16(sel, __builtin_bit_cast(bf16v_t, __builtin_shufflevector(p0, p1, 0, 1, 2, 3, 4, 5, 6, 7)), c, sidx, 0, 0)
constexpr int ATT_STAGE = 32768;
typedef __attribute__((ext_vector_type(8))) unsigned u32x8;

DI void attn_loader(char* smem, int w, char* ws, int set, u64 tag, const __hip_bfloat16* __restrict__ kvf, const void* Wo, const void* Xin,
                    const void* Wcs SP_PARAM) {
    const int lane = lane_id();
    asm volatile("" : "+s"(w));
    const Ids I = ids();
    const int panel = I.pq, ct = I.cq;
    const unsigned lds0 = (unsigned)(uintptr_t)smem;
    const char* kvh = (const char*)kvf + (size_t)(ct * 2 + (w >> 1)) * 262144 + (w & 1) * 4096;
    const unsigned voff = (unsigned)(lane * 16);
    auto order = [&](int st) { return ((panel & 8) + st - ((st - 1 < (panel & 7)) ? 1 : 0)) & 15; };
    auto load = [&](int st) {
        const char* src = kvh + (size_t)order(st) * 16384;
        const unsigned dst = lds0 + (w >> 1) * 16384 + (w & 1) * 4096 + (st & 3) * ATT_STAGE;
#pragma unroll
        for (int u = 0; u < 2; ++u)
            asm volatile("s_mov_b32 m0, %0\n\ts_nop 0\n\tglobal_load_lds_dwordx4 %1, %2 sc1\n\tglobal_load_lds_dwordx4 %1, %2 offset:1024 sc1\n\t"
                         "global_load_lds_dwordx4 %1, %2 offset:2048 sc1\n\tglobal_load_lds_dwordx4 %1, %2 offset:3072 sc1"
                         :: "s"(dst + u * 8192), "v"(voff), "s"(src + u * 8192) : "memory");
    };
    const unsigned wv = wo_off(w, lane), rv = res_off(false, true, lane), cv = colsum_off(w, lane);
    const unsigned poff = flag_off(set, ct, panel, lane & 15);
    u64 land;
    __builtin_amdgcn_s_setprio(3);
    BAR();
    signal_poll<false, true>(w, ws, flag_off(set, ct, lane & 15, panel), poff, 1ull << order(1), tag, land);
    STAMP(9);
    load(1);
    asm volatile("global_load_dwordx2 %0, %1, %2 sc1\n\ts_waitcnt vmcnt(0)" : "+v"(land) : "v"(poff), "s"(ws + WS_FLAG) : "memory");
    STAMP(10);
    BAR();
    if (!__all(land == tag)) flag_poll(ws, poff, tag);
    load(2); load(3);
    __builtin_amdgcn_s_setprio(0);
    STAMP(11);
#pragma unroll 1
    for (int st = 1; st < 5; ++st) { VM_BAR(8); load(st + 3); }
    u32x4 res[6], ca[8];
    colsum_load(ca, w < 3 ? Wcs : nullptr, cv);
#pragma unroll 1
    for (int st = 5; st < 13; ++st) {
        VM_BAR(8);
        if (st == 9) wo_load(Wo, wv, 24, 32, lds0 + 32768, w);
        load(st + 3);
    }
    VM_BAR(8); wo_load(Wo, wv, 0, 8, lds0, w);
    VM_BAR(8); wo_load(Wo, wv, 8, 16, lds0, w);
    res_load(res, Xin, false, true, rv, w);
    BAR();     wo_load(Wo, wv, 16, 24, lds0, w);
    STAMP(12);
    out_handoff(smem, 98304, res, w, ws, set + 1, tag, lane);
    if (Wcs && w < 3) colsum_store(ca, ws, 0, tag, w, lane);
}

DI void attn_compute(char* smem, int w, const u32x4 (&q4)[4], const __hip_bfloat16* __restrict__ kvf, __hip_bfloat16* __restrict__ ofrag SP_PARAM) {
    const int lane = lane_id();
    asm volatile("" : "+s"(w));
    const int wm = w & 1, wn = w >> 1;
    const Ids I = ids();
    const int panel = I.pq, head = I.cq * 2 + wn;
    const int l15 = lane & 15;
    const unsigned se = ((l15 & 2) == 0 && (l15 & 4) == 0) ? 0x3f803f80u : 0u, so = ((l15 & 2) == 0 && (l15 & 4) != 0) ? 0x3f803f80u : 0u;
    const bf8_t sel = __builtin_bit_cast(bf8_t, u32x4{se, se, so, so});
    const int sidx = (l15 & 1) ? (int)0xEEEEEEEEu : 0x44444444;
    const lds_cptr sm = (lds_cptr)smem + wn * 16384 + lane * 16;
    const f32x16 z16 = {0, 0, 0, 0, 0, 0, 0, 0, 0, 0, 0, 0, 0, 0, 0, 0};
    const bf16x8 qA0 = __builtin_bit_cast(bf16x8, q4[0]), qA1 = __builtin_bit_cast(bf16x8, q4[1]);
    const bf16x8 qB0 = __builtin_bit_cast(bf16x8, q4[2]), qB1 = __builtin_bit_cast(bf16x8, q4[3]);
    f32x16 oA, oB, nmA, nmB, sA, sB;
    f32x4 lsA, lsB;
    u32x8 pB;
    bf16x8 v11;
    asm volatile(".irp r,2,4,6,8,10,12,14,16,18,20,22,24,26,28,30,32,106,108,110,112,134,136,138,140,170,172\n\tv_mov_b64 v[\\r:\\r+1], 0\n\t.endr"
                 : "={v[2:17]}"(oA), "={v[18:33]}"(oB), "={v[106:113]}"(pB), "={v[134:137]}"(lsA), "={v[138:141]}"(lsB), "={v[170:173]}"(v11));
    WAIT_LGKM_BAR();
    STAMP(8);
    bf16x8 k00 = lds_b128(sm), k10 = lds_b128(sm + 1024), k01 = lds_b128(sm + 4096), k11 = lds_b128(sm + 4096 + 1024);
    bf16x8 v00 = lds_b128(sm + 2048), v10 = lds_b128(sm + 3072), v01 = lds_b128(sm + 4096 + 2048);
    {
        sA = MFMA32(k00, qA0, z16); sB = MFMA32(k00, qB0, z16);
        sA = MFMA32(k10, qA1, sA); sB = MFMA32(k10, qB1, sB);
        SBAR(); VM_BAR(0); SBAR();
        float mA = sA[0], mB = sB[0];
#pragma unroll
        for (int r = 1; r < 16; ++r) { mA = fmaxf(mA, sA[r]); mB = fmaxf(mB, sB[r]); }
        mA = max_h(mA); mB = max_h(mB);
#pragma unroll
        for (int r = 0; r < 16; ++r) { nmA[r] = -mA; nmB[r] = -mB; sA[r] -= mA; }
    }
    const unsigned a0 = (unsigned)(uintptr_t)sm;
    asm volatile(
        ".macro ATT_RD dst, y\n\t"
        "ds_read_b128 v[\\dst:\\dst+3], v[175+(((\\y)&0x1ffff)>>16)] offset:(\\y)&0xffff\n\t"
        ".endm\n\t"
        ".macro ATT_E4 c\n\t"
        "v_exp_f32 v[\\c], v[\\c]\n\tv_exp_f32 v[\\c+1], v[\\c+1]\n\tv_exp_f32 v[\\c+2], v[\\c+2]\n\tv_exp_f32 v[\\c+3], v[\\c+3]\n\t"
        ".endm\n\t"
        ".macro ATT_C4 p, c\n\t"
        "v_cvt_pk_bf16_f32 v[\\p], v[\\c], v[\\c+1]\n\tv_cvt_pk_bf16_f32 v[\\p+1], v[\\c+2], v[\\c+3]\n\t"
        "v_cvt_pk_bf16_f32 v[\\p+2], v[\\c+4], v[\\c+5]\n\tv_cvt_pk_bf16_f32 v[\\p+3], v[\\c+6], v[\\c+7]\n\t"
        ".endm\n\t"
        ".macro ATT_STEP cur, nxt, ka, kb, q, nm, oc, od, lsd, pc, pd, vd1, vc0, r0, y0, r1, y1, bar, rd\n\t"
        "v_mfma_f32_32x32x16_bf16 v[\\nxt:\\nxt+15], v[\\ka:\\ka+3], v[\\q:\\q+3], v[\\nm:\\nm+15]\n\t"
        "ATT_E4 \\cur\n\t"
        "v_mfma_f32_32x32x16_bf16 v[\\nxt:\\nxt+15], v[\\kb:\\kb+3], v[\\q+4:\\q+7], v[\\nxt:\\nxt+15]\n\t"
        "ATT_E4 \\cur+4\n\t"
        "v_mfma_f32_32x32x16_bf16 v[\\od:\\od+15], v[\\vd1:\\vd1+3], v[\\pd+4:\\pd+7], v[\\od:\\od+15]\n\t"
        "s_waitcnt lgkmcnt(0)\n\t"
        "ATT_C4 \\pc, \\cur\n\t"
        "v_exp_f32 v[\\cur+8], v[\\cur+8]\n\tv_exp_f32 v[\\cur+9], v[\\cur+9]\n\t"
        "v_mfma_f32_32x32x16_bf16 v[\\oc:\\oc+15], v[\\vc0:\\vc0+3], v[\\pc:\\pc+3], v[\\oc:\\oc+15]\n\t"
        ".if \\bar\n\ts_barrier\n\t.endif\n\t"
        "ATT_E4 \\cur+10\n\t"
        "v_smfmac_f32_16x16x64_bf16 v[\\lsd:\\lsd+3], v[130:133], v[\\pd:\\pd+7], v174\n\t"
        ".if \\rd & 4\n\tATT_RD 142, 32768\n\tATT_RD 150, 33792\n\tATT_RD 158, 34816\n\t.endif\n\t"
        ".if \\rd & 1\n\tATT_RD \\r0, \\y0\n\t.endif\n\t"
        ".if \\rd & 2\n\tATT_RD \\r1, \\y1\n\t.endif\n\t"
        "v_exp_f32 v[\\cur+14], v[\\cur+14]\n\tv_exp_f32 v[\\cur+15], v[\\cur+15]\n\t"
        "ATT_C4 \\pc+4, \\cur+8\n\t"
        ".if \\rd & 4\n\ts_waitcnt lgkmcnt(3)\n\t.endif\n\t"
        ".endm\n\t"
        ".macro ATT_TILE p, y1, y2, bar, ra=3, rb=3\n\t"
        "ATT_STEP 66, 82, 142+\\p, 150+\\p, 122, 50, 2, 18, 138, 98, 106, 170-\\p, 158+\\p, 170-\\p, \\y1+3072, 142+\\p, \\y2, \\bar, \\ra\n\t"
        "ATT_STEP 82, 66, 146-\\p, 154-\\p, 114, 34, 18, 2, 134, 106, 98, 166+\\p, 158+\\p, 158+\\p, \\y2+2048, 150+\\p, \\y2+1024, 0, \\rb\n\t"
        ".endm\n\t"
        ".macro ATT_STAGE b\n\t"
        "ATT_TILE 0, \\b+4096, \\b+8192, 0\n\t"
        "ATT_TILE 4, \\b+8192, \\b+12288, 0\n\t"
        "ATT_TILE 0, \\b+12288, \\b+32768, 1\n\t"
        "ATT_TILE 4, \\b+32768, \\b+36864, 0\n\t"
        ".endm\n\t"
        "v_add_u32 v176, 0x10000, v175\n\t"
        "s_mov_b32 s96, 4\n\t"
        "s_waitcnt lgkmcnt(0)\n\t"
        "ATT_TILE 0, 4096, 8192, 0\n\tATT_TILE 4, 8192, 12288, 0\n\tATT_TILE 0, 12288, 0, 0, 1, 0\n\tATT_TILE 4, 32768, 36864, 1, 7, 3\n\t"
        "s_branch .Latt_entry_%=\n\t"
        ".p2align 6\n"
        ".Latt_loop_%=:\n\t"
        "ATT_STAGE 0\n"
        ".Latt_entry_%=:\n\t"
        "ATT_STAGE 32768\n\tATT_STAGE 65536\n\t"
        "s_sub_u32 s96, s96, 1\n\t"
        "ATT_STAGE 98304\n\t"
        "s_cmp_lg_u32 s96, 0\n\t"
        "s_cbranch_scc1 .Latt_loop_%=\n\t"
        "s_nop 1\n\t"
        "v_mfma_f32_32x32x16_bf16 v[18:33], v[170:173], v[110:113], v[18:33]\n\t"
        "v_smfmac_f32_16x16x64_bf16 v[138:141], v[130:133], v[106:113], v174\n\t"
        "s_waitcnt lgkmcnt(0)\n\ts_nop 10\n\t"
        ".purgem ATT_RD\n\t.purgem ATT_E4\n\t.purgem ATT_C4\n\t.purgem ATT_STEP\n\t.purgem ATT_TILE\n\t.purgem ATT_STAGE"
        : "+{v[2:17]}"(oA), "+{v[18:33]}"(oB), "+{v[66:81]}"(sA), "+{v[106:113]}"(pB), "+{v[134:137]}"(lsA), "+{v[138:141]}"(lsB),
          "+{v[142:145]}"(k00), "+{v[146:149]}"(k01), "+{v[150:153]}"(k10), "+{v[154:157]}"(k11),
          "+{v[158:161]}"(v00), "+{v[162:165]}"(v01), "+{v[166:169]}"(v10), "+{v[170:173]}"(v11)
        : "{v[34:49]}"(nmA), "{v[50:65]}"(nmB), "{v[114:117]}"(qA0), "{v[118:121]}"(qA1), "{v[122:125]}"(qB0), "{v[126:129]}"(qB1),
          "{v[130:133]}"(sel), "{v174}"(sidx), "{v175}"(a0)
        : "memory", "scc", "s96", "v176", "v82", "v83", "v84", "v85", "v86", "v87", "v88", "v89", "v90", "v91", "v92", "v93", "v94", "v95",
          "v96", "v97", "v98", "v99", "v100", "v101", "v102", "v103", "v104", "v105");
    STAMP(9);
    float lA = lsA[0] + lsA[1], lB = lsB[0] + lsB[1];
    const unsigned dst = (unsigned)((((panel * 8 + wm * 4 + ((lane & 31) >> 4)) * 32 + head) * 64 + (lane & 15) + (lane >> 5) * 16) * 16);
    auto store = [&] {
#pragma unroll
    for (int u = 0; u < 2; ++u) {
        const f32x16 e = (u ? oB : oA) * __builtin_amdgcn_rcpf(u ? lB : lA);
        st16o<0>((char*)ofrag + u * 65536, dst, u32x4{pack_bf16(e[0], e[1]), pack_bf16(e[2], e[3]), pack_bf16(e[8], e[9]), pack_bf16(e[10], e[11])});
        st16o<512>((char*)ofrag + u * 65536, dst, u32x4{pack_bf16(e[4], e[5]), pack_bf16(e[6], e[7]), pack_bf16(e[12], e[13]), pack_bf16(e[14], e[15])});
    }
    };
    store();
    {
        const f32x16 os = oA + oB;
        const float osum = (((os[0] + os[1]) + (os[2] + os[3])) + ((os[4] + os[5]) + (os[6] + os[7]))) +
                           (((os[8] + os[9]) + (os[10] + os[11])) + ((os[12] + os[13]) + (os[14] + os[15])));
        const bool bad = !(lA < 1.0e24f) || !(lB < 1.0e24f) || !(fabsf(osum) < 3.0e38f);
        if (__any(bad)) {
            const __amdgpu_buffer_rsrc_t rk = __builtin_amdgcn_make_buffer_rsrc((char*)kvf + (size_t)head * 262144, 0, 262144, 0x00020000);
            oA = z16; oB = z16; lA = 0.f; lB = 0.f;
            float mA = -INFINITY, mB = -INFINITY;
#pragma unroll 1
            for (int t = 0; t < 64; ++t) {
                bf16x8 f[4];
#pragma unroll
                for (int i = 0; i < 4; ++i) f[i] = __builtin_bit_cast(bf16x8, __builtin_amdgcn_raw_buffer_load_b128(rk, lane * 16 + i * 1024, t * 4096, 16));
                auto upd = [&](bf16x8 q0, bf16x8 q1, f32x16& o, float& l, float& m) {
                    f32x16 s = MFMA32(f[0], q0, z16); s = MFMA32(f[1], q1, s);
                    float tm = s[0];
#pragma unroll
                    for (int r = 1; r < 16; ++r) tm = fmaxf(tm, s[r]);
                    tm = max_h(tm);
                    const float mn = fmaxf(m, tm);
                    const float alpha = __builtin_amdgcn_exp2f(m - mn);
                    m = mn;
#pragma unroll
                    for (int r = 0; r < 16; ++r) { s[r] = __builtin_amdgcn_exp2f(s[r] - mn); o[r] *= alpha; }
                    const u32x4 p0 = {pack_bf16(s[0], s[1]), pack_bf16(s[2], s[3]), pack_bf16(s[4], s[5]), pack_bf16(s[6], s[7])};
                    const u32x4 p1 = {pack_bf16(s[8], s[9]), pack_bf16(s[10], s[11]), pack_bf16(s[12], s[13]), pack_bf16(s[14], s[15])};
                    f32x4 t4 = {0, 0, 0, 0};
                    o = MFMA32(f[2], __builtin_bit_cast(bf16x8, p0), o);
                    o = MFMA32(f[3], __builtin_bit_cast(bf16x8, p1), o);
                    t4 = ROWSUM(p0, p1, t4);
                    l = l * alpha + (t4[0] + t4[1]);
                };
                upd(qA0, qA1, oA, lA, mA);
                upd(qB0, qB1, oB, lB, mB);
            }
            store();
        }
    }
}

DI void ln_row(const u32x4 (&v)[2], __hip_bfloat16* __restrict__ Y, unsigned off) {
    float s1 = 0.f, s2 = 0.f;
#pragma unroll
    for (int e = 0; e < 8; ++e) s1 = dot2bf(v[e >> 2][e & 3], 0x3f803f80u, s1);
    const float mean = wave_sum(s1) * (1.f / 1024.f);
#pragma unroll
    for (int e = 0; e < 8; ++e) {
        const float d0 = bf_lo(v[e >> 2][e & 3]) - mean, d1 = bf_hi(v[e >> 2][e & 3]) - mean;
        s2 += d0 * d0 + d1 * d1;
    }
    const float mb = round_bf16(mean);
    const float rden = __builtin_amdgcn_rcpf(round_bf16(__builtin_amdgcn_sqrtf(round_bf16(round_bf16(wave_sum(s2) * (1.f / 1024.f)) + 1e-5f))));
#pragma unroll
    for (int hh = 0; hh < 2; ++hh) {
        u32x4 y;
#pragma unroll
        for (int e = 0; e < 4; ++e) {
            const unsigned d = pack_bf16(bf_lo(v[hh][e]) - mb, bf_hi(v[hh][e]) - mb);
            y[e] = pack_bf16(bf_lo(d) * rden, bf_hi(d) * rden);
        }
        st16((char*)Y + hh * 131072, off, y);
    }
}

extern "C" __global__ __launch_bounds__(512, 2) void mega_kernel(const __hip_bfloat16* x, const __hip_bfloat16* wqkvA,
                                                            const __hip_bfloat16* woA, const __hip_bfloat16* wqkvB,
                                                            const __hip_bfloat16* woB, __hip_bfloat16* out,
                                                            char* ws) {
    char* const smem = smem_base;
    typedef __hip_bfloat16 bf;
    const int w8 = __builtin_amdgcn_readfirstlane(threadIdx.x >> 6);
    if (w8 == 7 && (blockIdx.x >> 3) < 10) code_touch(blockIdx.x >> 3, threadIdx.x & 63);
    asm volatile("" :: "s"(x), "s"(wqkvA), "s"(woA), "s"(wqkvB), "s"(woB), "s"(out), "s"(ws));
    STAMP_DECL; STAMPR(14); STAMP(0);
    u64 tag;
    {
        const int lane = threadIdx.x & 63;
        const Ids I = ids();
        const unsigned off = (unsigned)((I.po * 128 + I.co * 8 + w8) * 2048 + lane * 16);
        u32x4 v[2];
        asm volatile("global_load_dwordx4 %0, %2, %3\n\tglobal_load_dwordx4 %1, %2, %3 offset:1024" : "=&v"(v[0]), "=&v"(v[1]) : "v"(off), "s"(x) : "memory");
        asm volatile("s_load_dwordx2 %0, %1, 0x0" : "=s"(tag) : "s"(ws + WS_EPOCH) : "memory");
        if (w8 == 7) {
            const int l = lane & 15, k = l % 5;
            const unsigned o = (l >= 10 || k == 3) ? flag_off(l, I.cq, I.pq, 0) : (k == 0 || k == 2) ? flag_off(l, I.pq, I.cq, 0) : flag_off(l, I.po, I.co, 0);
            touch_at(ws + WS_FLAG, o);
        } else if (w8 >= 4 && w8 - 4 < NTOUCH) touch(wqkvA, (w8 - 4) * 256 + blockIdx.x, lane);
        if (w8 == 7 || (w8 >= 4 && w8 - 4 < NTOUCH)) asm volatile("s_waitcnt vmcnt(1)" ::: "memory"); else asm volatile("s_waitcnt vmcnt(0)" ::: "memory");
        asm volatile("" : "+v"(v[0]), "+v"(v[1]));
        ln_row(v, (bf*)(ws + WS_Y0), (unsigned)(I.po * 262144 + (lane >> 3) * 16384 + (I.co * 8 + w8) * 128 + (lane & 7) * 16));
        asm volatile("s_waitcnt lgkmcnt(0)" : "+s"(tag) :: "memory");
        tag += 1;
    }
    if (w8 >= 4) {
        int w = w8 & 3;
#pragma unroll 1
        for (int it = 0; it < 2; ++it) {
            const int lane = lane_id();
            asm volatile("" : "+s"(x), "+s"(wqkvA), "+s"(woA), "+s"(wqkvB), "+s"(woB), "+s"(ws), "+s"(w));
            const unsigned lds0 = (unsigned)(uintptr_t)smem;
            u32x4 cb[8];
            const unsigned wv = wo_off(w, lane), rv = res_off(true, it, lane), cv = colsum_off(w, lane);
            qkv_loader<true>(smem, w, ws, it * 5, tag, (const bf*)(ws + (it ? WS_X2 : WS_Y0)), wqkvA, (const float*)(ws + WS_ST + 262144),
                             ws + WS_CSP, it == 0, woA, [&] {
                if (w < 3) {
                    colsum_load(cb, wqkvB, cv);
                    asm volatile("s_waitcnt vmcnt(8)" ::: "memory");
                } else {
                    touch(woA, blockIdx.x, lane);
                    asm volatile("s_waitcnt vmcnt(1)" ::: "memory");
                }
            }, [&](int part) {
                if (part == 0) wo_load(woA, wv, 32 - WO0, 32, lds0, w); else wo_load(woA, wv, 32 - WO1, 32 - WO0, lds0, w);
            } SP_ARG);
            STAMP(3);
            u32x4 res[6];
            res_load(res, it ? (const void*)(ws + WS_X2) : x, true, it, rv, w);
            if (it) __builtin_amdgcn_s_sleep(WSL1); else __builtin_amdgcn_s_sleep(WSL0);
            wo_load(woA, wv, 0, WO2, lds0, w);
            BAR();
            wo_load(woA, wv, WO2, 32 - WO1, lds0, w);
            const bool cs = (it == 0 && w < 3);
            STAMP(4);
            out_handoff(smem, 147456, res, w, ws, it * 5 + 1, tag, lane);
            if (cs) colsum_store(cb, ws, 1, tag, w, lane);
            STAMP(5);
            qkv_loader<false>(smem, w, ws, it * 5 + 2, tag, (const bf*)(ws + (it ? WS_X3 : WS_X1)), wqkvB, (const float*)(ws + WS_ST + it * 524288), ws + WS_CSP + 196608,
                              false, nullptr, [] {}, [](int) {} SP_ARG);
            STAMP(8);
            attn_loader(smem, w, ws, it * 5 + 3, tag, (const bf*)(ws + WS_KVF + it * 8 * MBy), woB, ws + (it ? WS_X3 : WS_X1),
                        it ? nullptr : wqkvA SP_ARG);
            STAMP(13); STAMPR(15); STAMP_FLUSH(it);
        }
        if (blockIdx.x == 0 && w == 0 && lane_id() == 0) __hip_atomic_store((g_u64ptr)(ws + WS_EPOCH), tag, __ATOMIC_RELAXED, __HIP_MEMORY_SCOPE_AGENT);
    } else {
        __builtin_amdgcn_s_setprio(2);
#pragma unroll 1
        for (int it = 0; it < 2; ++it) {
            u32x4 q4[4];
            asm volatile("" : "+s"(ws), "+s"(wqkvA), "+s"(wqkvB));
            qkv_compute<true>(smem, w8, wqkvA, q4, nullptr, (bf*)(ws + WS_OF + it * 8 * MBy), it == 0 SP_ARG);
            STAMP(2);
            out_compute<true>(smem, w8, (const bf*)(ws + WS_OF + it * 8 * MBy),
                              (bf*)(ws + (it ? WS_X3 : WS_X1)), (float*)(ws + WS_ST + it * 524288) SP_ARG);
            STAMP(6);
            qkv_compute<false>(smem, w8, wqkvB, q4, (bf*)(ws + WS_KVF + it * 8 * MBy), nullptr, false SP_ARG);
            attn_compute(smem, w8, q4, (const bf*)(ws + WS_KVF + it * 8 * MBy), (bf*)(ws + WS_OF + (it * 8 + 4) * MBy) SP_ARG);
            out_compute<false>(smem, w8, (const bf*)(ws + WS_OF + (it * 8 + 4) * MBy),
                               it ? out : (bf*)(ws + WS_X2), it ? nullptr : (float*)(ws + WS_ST + 262144) SP_ARG);
            STAMP(13); STAMPR(15); STAMP_FLUSH(it);
        }
    }
}

extern "C" __global__ void zz_code_end_kernel() {}

extern "C" void kernel_launch(void* const* d_in, const int* in_sizes, int n_in, void* d_out, int out_size, void* d_ws,
                              size_t ws_size, hipStream_t stream) {
    typedef __hip_bfloat16 bf;
    (void)hipFuncSetAttribute((const void*)mega_kernel, hipFuncAttributeMaxDynamicSharedMemorySize, CH_LDS);
    mega_kernel<<<256, 512, CH_LDS, stream>>>((const bf*)d_in[0], (const bf*)d_in[1], (const bf*)d_in[2], (const bf*)d_in[4], (const bf*)d_in[5],
                                              (bf*)d_out, (char*)d_ws);
}
```

```cpp
#include <hip/hip_runtime.h>
#include <hip/hip_bf16.h>
#include <stdint.h>

typedef __attribute__((ext_vector_type(8))) short bf16x8;
typedef __attribute__((ext_vector_type(4))) short s16x4;
typedef __attribute__((ext_vector_type(4))) float f32x4;
typedef __attribute__((ext_vector_type(2))) float f32x2;
typedef __attribute__((ext_vector_type(16))) float f32x16;
typedef __attribute__((ext_vector_type(4))) unsigned u32x4;
typedef __attribute__((ext_vector_type(2))) unsigned u32x2;
typedef __attribute__((ext_vector_type(2))) __bf16 bf16x2_t;
typedef __attribute__((ext_vector_type(8))) __bf16 bf8_t;
typedef __attribute__((ext_vector_type(16))) __bf16 bf16v_t;
typedef __attribute__((address_space(3))) const char* lds_cptr;
typedef __attribute__((address_space(3))) char* lds_ptr;

#define DI __device__ __forceinline__
#define MFMA16(a, b, c) __builtin_amdgcn_mfma_f32_16x16x32_bf16((a), (b), (c), 0, 0, 0)
#define MFMA32(a, b, c) __builtin_amdgcn_mfma_f32_32x32x16_bf16((a), (b), (c), 0, 0, 0)
#define SBAR() __builtin_amdgcn_sched_barrier(0)
#define STAMP(i)
#define STAMPR(i)
#define STAMP_DECL
#define STAMP_FLUSH(it)
#define SP_PARAM
#define SP_ARG
typedef unsigned long long u64;
#define NF 12
typedef __attribute__((ext_vector_type(32))) unsigned u32x32;
#define B2POS 0
#define WO2 7
#define WSL0 0
#define WSL1 8
#define WO1 7
#define WO0 3
#define SPRIO 3
#define QUNR 3
#define NTOUCH 3

constexpr float QSCALE = 0.17677669529663687f * 1.4426950408889634f;
constexpr float ATT_SCALE = 0.17677669529663687f;
constexpr float LOG2E = 1.4426950408889634f;
constexpr float NEG_MASK = -1e9f / ATT_SCALE;

DI int lane_id() { int l; asm volatile("v_mbcnt_lo_u32_b32 %0, -1, 0\n\tv_mbcnt_hi_u32_b32 %0, -1, %0" : "=v"(l)); return l; }
DI unsigned pack_bf16(float lo, float hi) {
    f32x2 v = {lo, hi};
    bf16x2_t r = __builtin_convertvector(v, bf16x2_t);
    return __builtin_bit_cast(unsigned, r);
}
DI float bf_lo(unsigned u) { return __uint_as_float(u << 16); }
DI float bf_hi(unsigned u) { return __uint_as_float(u & 0xffff0000u); }
DI float round_bf16(float x) { return bf_lo(pack_bf16(x, 0.f)); }
DI void ln_stats(float sum, float sumsq, float& mean_out, float& rstd_out) {
    float mean;
    asm("v_mul_f32 %0, 0x3a800000, %1" : "=v"(mean) : "v"(sum));
    const float var = fmaxf(__builtin_fmaf(-mean, mean, sumsq * (1.f / 1024.f)), 0.f);
    mean_out = round_bf16(mean);
    rstd_out = __builtin_amdgcn_rcpf(round_bf16(__builtin_amdgcn_sqrtf(round_bf16(round_bf16(var) + 1e-5f))));
}

DI void glds16(const void* sbase, unsigned voff, unsigned ldsaddr) {
    asm volatile("s_mov_b32 m0, %0\n\ts_nop 0\n\tglobal_load_lds_dwordx4 %1, %2"
                 :: "s"(ldsaddr), "v"(voff), "s"(sbase) : "memory");
}
DI void glds16c(const void* sbase, unsigned voff, unsigned ldsaddr) {
    asm volatile("s_mov_b32 m0, %0\n\ts_nop 0\n\tglobal_load_lds_dwordx4 %1, %2 sc1"
                 :: "s"(ldsaddr), "v"(voff), "s"(sbase) : "memory");
}
DI void st16(void* sbase, unsigned voff, u32x4 v) {
    asm volatile("global_store_dwordx4 %0, %1, %2 sc1\n\ts_nop 1" :: "v"(voff), "v"(v), "s"(sbase) : "memory");
}
template <int OFF> DI void st16o(void* sbase, unsigned voff, u32x4 v) {
    asm volatile("global_store_dwordx4 %0, %1, %2 offset:%3 sc1\n\ts_nop 1" :: "v"(voff), "v"(v), "s"(sbase), "n"(OFF) : "memory");
}
DI bf16x8 lds_b128(lds_cptr p) { return *(const __attribute__((address_space(3))) bf16x8*)p; }
DI s16x4 lds_tr(lds_cptr p) {
    return __builtin_amdgcn_ds_read_tr16_b64_v4i16((__attribute__((address_space(3))) s16x4*)p);
}
DI bf16x8 cat8(s16x4 a, s16x4 b) { return __builtin_shufflevector(a, b, 0, 1, 2, 3, 4, 5, 6, 7); }
DI float dot2bf(unsigned a, unsigned b, float c) {
    return __builtin_amdgcn_fdot2_f32_bf16(__builtin_bit_cast(bf16x2_t, a), __builtin_bit_cast(bf16x2_t, b), c, false);
}
DI float sum_g(float v) {
    auto a = __builtin_amdgcn_permlane16_swap(__float_as_uint(v), __float_as_uint(v), false, false);
    v = __uint_as_float(a[0]) + __uint_as_float(a[1]);
    auto b = __builtin_amdgcn_permlane32_swap(__float_as_uint(v), __float_as_uint(v), false, false);
    return __uint_as_float(b[0]) + __uint_as_float(b[1]);
}
DI float wave_sum(float v) {
    v += __builtin_bit_cast(float, __builtin_amdgcn_update_dpp(0, __builtin_bit_cast(int, v), 0xB1, 0xF, 0xF, true));
    v += __builtin_bit_cast(float, __builtin_amdgcn_update_dpp(0, __builtin_bit_cast(int, v), 0x4E, 0xF, 0xF, true));
    v += __builtin_bit_cast(float, __builtin_amdgcn_update_dpp(0, __builtin_bit_cast(int, v), 0x141, 0xF, 0xF, true));
    v += __builtin_bit_cast(float, __builtin_amdgcn_update_dpp(0, __builtin_bit_cast(int, v), 0x140, 0xF, 0xF, true));
    return sum_g(v);
}
DI float amax3(float a, float b, float c) { float r; asm("v_max3_f32 %0, %1, %2, %3" : "=v"(r) : "v"(a), "v"(b), "v"(c)); return r; }
DI float max2f(float a, float b) { return __builtin_amdgcn_fmed3f(a, b, INFINITY); }
DI float max_h(float v) {
    auto b = __builtin_amdgcn_permlane32_swap(__float_as_uint(v), __float_as_uint(v), false, false);
    return max2f(__uint_as_float(b[0]), __uint_as_float(b[1]));
}

template <int OFF> DI void st8o(void* sbase, unsigned voff, f32x2 v) {
    asm volatile("global_store_dwordx2 %0, %1, %2 offset:%3 sc1\n\ts_nop 1" :: "v"(voff), "v"(v), "s"(sbase), "n"(OFF) : "memory");
}
constexpr unsigned OFF_DUMP = 145408;
extern __shared__ __attribute__((aligned(16))) char smem_base[];
DI void touch_at(const void* p, unsigned off) {
    asm volatile("s_mov_b32 m0, %0\n\ts_nop 0\n\tglobal_load_lds_dword %1, %2"
                 :: "s"((unsigned)(uintptr_t)smem_base + OFF_DUMP), "v"(off), "s"(p) : "memory");
}
DI void touch(const void* p, int slice, int lane) { touch_at(p, (unsigned)(slice * 64 + lane) * 128u); }
DI void code_touch(int slice, int lane) {
    unsigned long long c0, c1;
    asm volatile("s_getpc_b64 s[96:97]\n\ts_add_u32 s96, s96, mega_kernel@rel32@lo+4\n\ts_addc_u32 s97, s97, mega_kernel@rel32@hi+12\n\ts_mov_b64 %0, s[96:97]\n\t"
                 "s_getpc_b64 s[96:97]\n\ts_add_u32 s96, s96, zz_code_end_kernel@rel32@lo+4\n\ts_addc_u32 s97, s97, zz_code_end_kernel@rel32@hi+12\n\ts_mov_b64 %1, s[96:97]"
                 : "=s"(c0), "=s"(c1) :: "s96", "s97", "scc");
    touch_at((const void*)c0, min((unsigned)(slice * 64 + lane) * 128u, (unsigned)(c1 - c0)));
}

#define BAR() asm volatile("s_barrier" ::: "memory")
#define WAIT_LGKM() do { SBAR(); __builtin_amdgcn_s_waitcnt(0xC07F); SBAR(); } while (0)
#define WAIT_LGKM_BAR() do { SBAR(); __builtin_amdgcn_s_waitcnt(0xC07F); asm volatile("s_barrier" ::: "memory"); SBAR(); } while (0)
#define VM_BAR(n) asm volatile("s_waitcnt vmcnt(" #n ")\n\ts_barrier" ::: "memory")
constexpr int CH_LDS = 163840;
typedef __attribute__((address_space(3))) unsigned* lds_uptr;
typedef __attribute__((address_space(1))) u64* g_u64ptr;

constexpr size_t MBy = 1 << 20;
constexpr size_t WS_X3 = 0, WS_X2 = 4 * MBy, WS_X1 = 8 * MBy, WS_OF = 12 * MBy  , WS_KVF = 36 * MBy  ,
                 WS_ST = 52 * MBy  , WS_CSP = 54 * MBy  , WS_FLAG = 55 * MBy  ,
                 WS_EPOCH = WS_CSP + 524288, WS_Y0 = 56 * MBy;
DI unsigned flag_off(int set, int group, int consumer, int producer) { return (unsigned)(set * 65536 + group * 4096 + consumer * 256 + producer * 8); }
DI void flag_store(char* ws, int set, int group, int producer, u64 tag, int lane) {
    asm volatile("" : "+s"(tag));
    if (lane < 16) __hip_atomic_store((g_u64ptr)(ws + WS_FLAG + flag_off(set, group, lane, producer)), tag, __ATOMIC_RELAXED, __HIP_MEMORY_SCOPE_AGENT);
}
DI void flag_poll(const char* ws, unsigned off, u64 tag) {
    asm volatile("" : "+s"(tag));
#pragma unroll 1
    for (int spin = 0; spin < (1 << 16); ++spin) {
        const u64 v = __hip_atomic_load((g_u64ptr)(ws + WS_FLAG + off), __ATOMIC_RELAXED, __HIP_MEMORY_SCOPE_AGENT);
        if (__all(v == tag)) break;
        __builtin_amdgcn_s_sleep(1);
    }
    asm volatile("" ::: "memory");
}
#define POLL_INS "global_atomic_or_x2 %[land], %[po], %[zero], %[base] sc0 sc1"
template <bool DRAIN, bool ALLPOLL>
DI void signal_poll(int follower, const char* ws, unsigned soff, unsigned poff, u64 pmask, u64 tag, u64& land) {
    u64 tagv, zero;
    asm volatile("s_not_b64 s[96:97], %[ts]\n\tv_mov_b64 %[tag], %[ts]\n\tv_mov_b64 %[zero], 0\n\tv_mov_b64 %[land], s[96:97]\n\t"
                 ".if %[dr]\n\ts_waitcnt vmcnt(0)\n\t.endif\n\ts_barrier\n\t"
                 "s_cmp_lg_u32 %[fl], 0\n\ts_cbranch_scc1 .Lsig_fol_%=\n\t"
                 "s_mov_b64 exec, 0xffff\n\t"
                 "global_store_dwordx2 %[so], %[tag], %[base] sc1\n"
                 ".if %[ap]\n.Lsig_fol_%=:\n.endif\n\t"
                 "s_mov_b64 exec, %[pm]\n\t"
                 "s_movk_i32 s96, 0x7fff\n"
                 ".Lsig_loop_%=:\n\t"
                 POLL_INS "\n\t"
                 "s_sleep 1\n\t"
                 "s_waitcnt vmcnt(3)\n\t"
                 "v_cmp_ne_u64 vcc, %[land], %[tag]\n\t"
                 "s_sub_u32 s96, s96, 1\n\t"
                 "s_cbranch_scc1 .Lsig_out_%=\n\t"
                 "s_cbranch_vccnz .Lsig_loop_%=\n"
                 ".Lsig_out_%=:\n\t"
                 "s_mov_b64 exec, -1\n"
                 ".if !%[ap]\n.Lsig_fol_%=:\n.endif"
                 : [land] "=&v"(land), [tag] "=&v"(tagv), [zero] "=&v"(zero)
                 : [fl] "s"(follower), [so] "v"(soff), [po] "v"(poff), [ts] "s"(tag), [base] "s"(ws + WS_FLAG), [pm] "s"(pmask), [ap] "n"(ALLPOLL ? 1 : 0), [dr] "n"(DRAIN ? 1 : 0)
                 : "memory", "scc", "vcc", "s96", "s97");
}
struct Ids { int pq, cq, po, co; };
DI Ids ids() {
    int bid = blockIdx.x; asm volatile("" : "+s"(bid));
    const int xcd = bid & 7, idx = bid >> 3;
    int pq, cq, po, co;
#define IDS_BODY pq = (xcd & 1) * 8 + (idx & 7); cq = (xcd >> 1) * 4 + (idx >> 3); po = (xcd & 3) * 4 + (idx & 3); co = (xcd >> 2) * 8 + (idx >> 2);
    IDS_BODY
    return Ids{pq, cq, po, co};
}

DI void slow_warm(unsigned dump, const void* W, int w, int lane) {
    int bid = blockIdx.x; asm volatile("" : "+s"(bid));
    const int xcd = bid & 7, idx = bid >> 3;
    const int sc = (3 - (int)((uintptr_t)W >> 7)) & 7;
    if ((sc >> 2) != ((xcd >> 1) & 1)) return;
    const char* src = (const char*)W + (idx + 32 * w) * 49152 + (sc + (xcd >> 2) * 8) * 128;
#pragma unroll
    for (int G = 0; G < 3; ++G) glds16(src + G * 2048, (unsigned)((lane >> 3) * 6144 + (lane & 7) * 16), dump);
}

template <int V> struct IC { static constexpr int value = V; };
template <bool LOCAL> struct QC {
    static constexpr int AROWS = LOCAL ? 144 : 128;
    static constexpr int ABYTES = AROWS * 128;
    static constexpr int STAGE = ABYTES + 24576;
    static constexpr int OFF_MEAN = 143360;
    static constexpr int OFF_RSTD = OFF_MEAN + 576;
    static constexpr int OFF_CS = OFF_RSTD + 576;
};
constexpr int OFF_EX = 131072;

DI unsigned colsum_off(int G, int lane) {
    const Ids I = ids();
    unsigned off = (unsigned)(I.pq * 64 + (lane >> 3)) * 6144u + (unsigned)(G * 2048 + I.cq * 128 + (lane & 7) * 16);
    asm volatile("" : "+v"(off));
    return off;
}
DI void colsum_load(u32x4 (&c)[8], const void* W, unsigned off) {
#define CS_NEXT "s_add_u32 s96, s96, 0xc000\n\ts_addc_u32 s97, s97, 0\n\t"
    asm volatile("s_cmp_eq_u64 %9, 0\n\ts_cbranch_scc1 .Lcs_skip_%=\n\ts_mov_b64 s[96:97], %9\n\t"
                 "global_load_dwordx4 %0, %8, s[96:97]\n\t" CS_NEXT "global_load_dwordx4 %1, %8, s[96:97]\n\t" CS_NEXT
                 "global_load_dwordx4 %2, %8, s[96:97]\n\t" CS_NEXT "global_load_dwordx4 %3, %8, s[96:97]\n\t" CS_NEXT
                 "global_load_dwordx4 %4, %8, s[96:97]\n\t" CS_NEXT "global_load_dwordx4 %5, %8, s[96:97]\n\t" CS_NEXT
                 "global_load_dwordx4 %6, %8, s[96:97]\n\t" CS_NEXT "global_load_dwordx4 %7, %8, s[96:97]\n"
                 ".Lcs_skip_%=:"
                 : "=&v"(c[0]), "=&v"(c[1]), "=&v"(c[2]), "=&v"(c[3]), "=&v"(c[4]), "=&v"(c[5]), "=&v"(c[6]), "=&v"(c[7])
                 : "v"(off), "s"(W) : "memory", "scc", "s96", "s97");
}
DI void colsum_store(u32x4 (&c)[8], char* ws, int mat, u64 tag, int G, int lane) {
    asm volatile("" : "+v"(c[0]), "+v"(c[1]), "+v"(c[2]), "+v"(c[3]), "+v"(c[4]), "+v"(c[5]), "+v"(c[6]), "+v"(c[7]));
    const Ids I = ids();
    const int m = lane & 15;
    const unsigned one = (m & 1) ? 0xbf800000u : 0xbf80u;
    const u32x4 sl = {(m >> 1) == 0 ? one : 0u, (m >> 1) == 1 ? one : 0u, (m >> 1) == 2 ? one : 0u, (m >> 1) == 3 ? one : 0u};
    f32x4 d = {0, 0, 0, 0};
#pragma unroll
    for (int i = 0; i < 8; ++i) d = MFMA16(__builtin_bit_cast(bf16x8, sl), __builtin_bit_cast(bf16x8, c[i]), d);
#pragma unroll
    for (int r = 0; r < 4; ++r)
        d[r] += __int_as_float(__builtin_amdgcn_update_dpp(0, __float_as_int(d[r]), 0x128, 0xF, 0xF, true));
    if (lane < 32 && (lane & 8) == 0)
        st16(ws + WS_CSP + mat * 196608, (unsigned)((I.cq * 16 + I.pq) * 768 + G * 256 + (lane & 7) * 32 + (lane >> 4) * 16), __builtin_bit_cast(u32x4, d));
    asm volatile("s_waitcnt vmcnt(0)" ::: "memory");
    flag_store(ws, (mat ? 10 : 13) + G, I.cq, I.pq, tag, lane);
}

template <bool LOCAL>
struct WFirst {
    unsigned v[2];
    DI WFirst(int w, int ct, int lane) {
#pragma unroll
        for (int i = 0; i < 2; ++i) {
            const int k = 8 * i + (lane >> 3), pos = lane & 7;
            v[i] = (unsigned)k * 6144u + (unsigned)(w * 1024 + ct * 64 + ((pos >> 1) ^ (((k >> 1) & 1) | (i << 1))) * 16 + (pos & 1) * 8) * 2u;
        }
        asm volatile("" : "+v"(v[0]), "+v"(v[1]));
    }
    DI void issue(const void* W, unsigned lds0, int w, int i0, int i1) const {
#pragma unroll
        for (int i = i0; i < i1; ++i)
            glds16((const char*)W + (size_t)(i >> 1) * (16 * 6144), v[i & 1], lds0 + (i >> 3) * QC<LOCAL>::STAGE + QC<LOCAL>::ABYTES + (w * 8 + (i & 7)) * 1024);
    }
};

template <bool LOCAL, class F, class F2>
DI void qkv_loader(char* smem, int w, char* ws, int set, u64 tag, const __hip_bfloat16* __restrict__ X, const __hip_bfloat16* __restrict__ W,
                   const float* __restrict__ statp, const char* __restrict__ csp, bool ident, const void* nextW, F tail, F2 mid SP_PARAM) {
    typedef QC<LOCAL> C;
    constexpr int NT = 16;
    const int lane = lane_id();
    asm volatile("" : "+s"(w));
    const int idx = blockIdx.x >> 3;
    const Ids I = ids();
    const int panel = I.pq, ct = I.cq;
    const int R0 = panel * 128;
    const unsigned lds0 = (unsigned)(uintptr_t)smem;
    auto grow = [&](int i) -> int {
        if (!LOCAL) return R0 + i;
        if (i < 128) { const int t = (i >> 4) & 3, a = i & 15; return R0 + (i & 64) + 2 * (16 * (t & 1) + a) + (t >> 1); }
        const int a = i & 15; const int r = R0 - 16 + 2 * (a & 7) + (a >> 3);
        return r < 0 ? 0 : r;
    };
    unsigned voffA[5], voffB[6];
#pragma unroll
    for (int i = 0; i < 6; ++i) {
        const int j = w + 4 * i, G = j >> 3, k = 8 * (j & 7) + (lane >> 3);
        const int pos = lane & 7, f = ((k >> 1) & 1) | (((k >> 3) & 1) << 1);
        const int seg = (pos >> 1) ^ f;
        voffB[i] = (unsigned)k * 6144u + (unsigned)(G * 1024 + ct * 64 + seg * 16 + (pos & 1) * 8) * 2u;
    }
    auto issueW = [&](int t, int buf) {
        const char* wb = (const char*)W + (size_t)t * (64 * 6144);
        const unsigned sb = lds0 + buf * C::STAGE;
#pragma unroll
        for (int i = 0; i < 6; ++i) glds16(wb, voffB[i], sb + C::ABYTES + (w + 4 * i) * 1024);
    };
    asm volatile("" : "+v"(voffB[0]), "+v"(voffB[1]), "+v"(voffB[2]), "+v"(voffB[3]), "+v"(voffB[4]), "+v"(voffB[5]));
    const WFirst<LOCAL> first(w, ct, lane);
    const int g = lane >> 4, r = lane & 15, pos = 32 * w + 2 * r + (g >> 1);
    const int lr = !LOCAL ? (pos & 64) + 2 * (pos & 31) + ((pos >> 5) & 1)
                          : (w == 0 && (g & 1)) ? 128 + (r & 7) + 8 * (g >> 1) : (pos & 64) + ((pos & 1) * 2 + ((pos >> 5) & 1)) * 16 + ((pos >> 1) & 15);
    unsigned so = (unsigned)(panel * 16384 + g * 1024 + w * 256 + r * 16), ho = (unsigned)(g * 1024 + (panel ? panel * 16384 - 15488 + (r & 7) * 16 : 0));
    unsigned co = (unsigned)((ct * 16 + g) * 768 + w * 192 + r * 12);
    lds_ptr mp = (lds_ptr)smem + C::OFF_MEAN + lr * 4, cp = (lds_ptr)smem + C::OFF_CS + w * 192 + r * 12 + ((g & 1) ? 8 : g * 2);
    unsigned fo = flag_off((LOCAL ? 13 : 10) + min(g, 2), ct, panel, r);
    asm volatile("" : "+v"(so), "+v"(ho), "+v"(co), "+v"(mp), "+v"(cp), "+v"(fo));
#pragma unroll
    for (int i = 0; i < 5; ++i) {
        const int j = (i < 4) ? (w + 4 * i) : (16 + (w & 1));
        const int row = 8 * j + (lane >> 3);
        const int c = (lane & 7) ^ ((row >> 1) & 7);
        const int gr = grow(row < C::AROWS ? row : 0);
        voffA[i] = (unsigned)((gr >> 7) * 262144 + (gr & 127) * 128 + c * 16);
        asm volatile("" : "+v"(voffA[i]));
    }
    auto issueA = [&](int t, int buf) {
        const char* xa = (const char*)X + t * 16384;
        const unsigned sb = lds0 + buf * C::STAGE;
        if (LOCAL && w < 2) glds16c(xa, voffA[4], sb + (16 + w) * 1024);
#pragma unroll
        for (int i = 0; i < 4; ++i) glds16c(xa, voffA[i], sb + (w + 4 * i) * 1024);
    };
    u64 land;
    if (w == 3) __builtin_amdgcn_s_setprio(3);
    signal_poll<LOCAL, false>(w - 3, ws, flag_off(set, I.po, lane & 15, I.co),
                              flag_off(set, (lane & 16) && panel ? panel - 1 : panel, ct, lane & 15),
                              LOCAL ? 0xffffffffull : 0xffffull, tag, land);
    if (w < 3) first.issue(W, lds0, w, 0, NF);
    BAR();
    STAMP(LOCAL ? 1 : 6);
    issueA(0, 0);
    issueA(1, 1);
    if (w < 3) { first.issue(W, lds0, w, NF, 16); asm volatile("s_waitcnt vmcnt(%0)" :: "n"(20 - NF) : "memory"); }
    else asm volatile("s_waitcnt vmcnt(4)" ::: "memory");
    BAR();
    asm volatile("" :: "v"(land));
    __builtin_amdgcn_s_setprio(0);
    STAMP(LOCAL ? 2 : 7);
    issueW(2, 2);
    issueA(2, 2);
    VM_BAR(10);
    asm volatile("global_load_dwordx2 %0, %1, %2 sc1" : "=&v"(land) : "v"(fo), "s"(ws + WS_FLAG) : "memory");
    issueA(3, 0); issueW(3, 0);
    int buf = 1;
#pragma unroll 1
    for (int t = 1; t < NT - 3; ++t) {
        VM_BAR(10);
        issueA(t + 3, buf); issueW(t + 3, buf);
        if (t == 10 && idx < 3) code_touch(idx * 4 + w, lane);
        if (LOCAL && t == 11)
            glds16(nextW, (unsigned)((idx + 32 * w) * 8 + (lane >> 3)) * 2048u +
                   (unsigned)(((I.co & 8) + ((3 - (int)((uintptr_t)nextW >> 7)) & 7)) * 128 + (lane & 7) * 16), lds0 + OFF_DUMP);
        buf = (buf == 2) ? 0 : buf + 1;
    }
    typedef __attribute__((ext_vector_type(3))) float f32x3;
    const bool halo = LOCAL && w == 0;
    asm volatile("" : "+v"(land));
    if (!ident) {
        if (!__all(land == tag)) flag_poll(ws, fo, tag);
        f32x3 cs[4];
        f32x4 sv[4], hv[4];
        typedef __attribute__((address_space(3))) float* lds_f;
        auto add = [](float a, float b) { float r; asm("v_add_f32 %0, %1, %2" : "=v"(r) : "v"(a), "v"(b)); return r; };
        auto fold32 = [](float a, float b) { asm("s_nop 1\n\tv_permlane32_swap_b32 %0, %1\n\tv_add_f32 %0, %0, %1" : "+v"(a), "+v"(b)); return a; };
        auto fold16 = [](float a, float b) { asm("s_nop 1\n\tv_permlane16_swap_b32 %0, %1\n\tv_add_f32 %0, %0, %1" : "+v"(a), "+v"(b)); return a; };
        auto sum4 = [&](auto (&v)[4], int e) { return add(add(v[0][e], v[1][e]), add(v[2][e], v[3][e])); };
#pragma unroll
        for (int i = 0; i < 4; ++i) asm volatile("global_load_dwordx3 %0, %1, %2 sc1" : "=&v"(cs[i]) : "v"(co), "s"(csp + i * 3072) : "memory");
        VM_BAR(14);
#pragma unroll
        for (int i = 0; i < 4; ++i) asm volatile("global_load_dwordx4 %0, %1, %2 sc1" : "=&v"(sv[i]) : "v"(so), "s"((const char*)statp + i * 4096) : "memory");
        if (LOCAL)
            asm volatile("s_cmp_lg_u32 %9, 0\n\ts_cbranch_scc1 .Lhv_skip_%=\n\t"
                         "global_load_dwordx4 %0, %4, %5 sc1\n\tglobal_load_dwordx4 %1, %4, %6 sc1\n\t"
                         "global_load_dwordx4 %2, %4, %7 sc1\n\tglobal_load_dwordx4 %3, %4, %8 sc1\n"
                         ".Lhv_skip_%=:" : "=&v"(hv[0]), "=&v"(hv[1]), "=&v"(hv[2]), "=&v"(hv[3])
                         : "v"(ho), "s"(statp), "s"(statp + 1024), "s"(statp + 2048), "s"(statp + 3072), "s"(w) : "memory", "scc");
        if (halo) asm volatile("s_waitcnt vmcnt(8)" ::: "memory"); else asm volatile("s_waitcnt vmcnt(4)" ::: "memory");
        asm volatile("" : "+v"(cs[0]), "+v"(cs[1]), "+v"(cs[2]), "+v"(cs[3]));
        const float c2 = sum4(cs, 2);
        *(lds_f)cp = fold16(fold32(sum4(cs, 0), sum4(cs, 1)), fold32(c2, c2));
        BAR();
        asm volatile("s_waitcnt vmcnt(0)" : "+v"(sv[0]), "+v"(sv[1]), "+v"(sv[2]), "+v"(sv[3]) :: "memory");
        mid(0);
        const float s1 = fold32(sum4(sv, 0), sum4(sv, 2)), s2 = fold32(sum4(sv, 1), sum4(sv, 3));
        float h1, h2;
        asm("v_mov_b32 %0, %2\n\tv_mov_b32 %1, %3" : "=&v"(h1), "=&v"(h2) : "v"(s1), "v"(s2));
        if (halo) {
            asm volatile("" : "+v"(hv[0]), "+v"(hv[1]), "+v"(hv[2]), "+v"(hv[3]));
            const float u0 = sum4(hv, 0), u1 = sum4(hv, 1), u2 = sum4(hv, 2), u3 = sum4(hv, 3);
            h1 = fold32(u0, panel ? u2 : u0); h2 = fold32(u1, panel ? u3 : u1);
        }
        float mean, rstd;
        ln_stats(fold16(s1, h1), fold16(s2, h2), mean, rstd);
        *(lds_f)mp = mean; *(lds_f)(mp + (C::OFF_RSTD - C::OFF_MEAN)) = rstd;
    } else { VM_BAR(10); tail(); BAR(); mid(0); }
    mid(1);
    WAIT_LGKM_BAR();
}

template <bool LOCAL>
DI void qkv_compute(char* smem, int w, const void* W, u32x4 (&qo4)[4], __hip_bfloat16* __restrict__ kvf, __hip_bfloat16* __restrict__ ofrag, bool ident SP_PARAM) {
    typedef QC<LOCAL> C;
    constexpr int NT = 16, ABYTES = C::ABYTES, STAGE = C::STAGE;
    const int lane = lane_id();
    asm volatile("" : "+s"(w));
    const int wm = w & 1, wn = w >> 1;
    const Ids I = ids();
    const int panel = I.pq, ct = I.cq;
    const int l15 = lane & 15, g = lane >> 4;
    const lds_cptr sm = (lds_cptr)smem;
    lds_ptr smw = (lds_ptr)smem;

    f32x4 acc[3][4][2];
    f32x4 acch[2];
    f32x2 z2; asm volatile("v_mov_b64 %0, 0" : "=v"(z2));
    const f32x4 z4 = {z2[0], z2[1], z2[0], z2[1]};
#pragma unroll
    for (int a = 0; a < 3; ++a)
#pragma unroll
        for (int t = 0; t < 4; ++t) { acc[a][t][0] = z4; acc[a][t][1] = z4; }
    acch[0] = z4; acch[1] = z4;

    struct Frags { bf16x8 a[4]; bf16x8 h; s16x4 b[3][2][2]; };
    auto rd = [&](auto WM, Frags& F, lds_cptr pa, lds_cptr pb0, lds_cptr pb1, int n) {
        switch (n) {
            case 0: F.b[0][0][0] = lds_tr(pb0); break;
            case 1: F.b[0][0][1] = lds_tr(pb0 + 512); break;
            case 2: F.a[0] = lds_b128(pa); break;
            case 3: F.b[0][1][0] = lds_tr(pb1); break;
            case 4: F.b[0][1][1] = lds_tr(pb1 + 512); break;
            case 5: F.a[1] = lds_b128(pa + 2048); break;
            case 6: F.a[2] = lds_b128(pa + 4096); break;
            case 7: F.a[3] = lds_b128(pa + 6144); break;
            case 8: F.b[1][0][0] = lds_tr(pb0 + 8192); break;
            case 9: F.b[1][0][1] = lds_tr(pb0 + 8192 + 512); break;
            case 10: F.b[1][1][0] = lds_tr(pb1 + 8192); break;
            case 11: F.b[1][1][1] = lds_tr(pb1 + 8192 + 512); break;
            case 12: F.b[2][0][0] = lds_tr(pb0 + 16384); break;
            case 13: F.b[2][0][1] = lds_tr(pb0 + 16384 + 512); break;
            case 14: F.b[2][1][0] = lds_tr(pb1 + 16384); break;
            case 15: F.b[2][1][1] = lds_tr(pb1 + 16384 + 512); break;
            case 16: if (LOCAL) F.h = lds_b128(pa + 16384 - WM.value * 8192); break;
            default: break;
        }
    };
    auto mm = [](f32x4& c, bf16x8 a, bf16x8 b) { asm volatile("v_mfma_f32_16x16x32_bf16 %0, %1, %2, %0" : "+v"(c) : "v"(a), "v"(b)); };
    auto half = [&](auto WM, const Frags& C, Frags& N, bool doread, lds_cptr pa, lds_cptr pb0, lds_cptr pb1, auto tail, int G0 = 0, int G1 = 3) {
#pragma unroll
        for (int G = G0; G < G1; ++G) {
            const bf16x8 b0 = cat8(C.b[G][0][0], C.b[G][0][1]), b1 = cat8(C.b[G][1][0], C.b[G][1][1]);
#pragma unroll
            for (int t = 0; t < 4; ++t) {
                const int n = (G - G0) * 8 + t * 2;
                if (G < 2) mm(acc[G][t][0], b0, C.a[t]); else mm(acc[G][t][0], C.a[t], b0);
                SBAR();
                if (doread) rd(WM, N, pa, pb0, pb1, n);
                tail(n);
                SBAR();
                if (G < 2) mm(acc[G][t][1], b1, C.a[t]); else mm(acc[G][t][1], C.a[t], b1);
                SBAR();
                if (doread) rd(WM, N, pa, pb0, pb1, n + 1);
                tail(n + 1);
                SBAR();
            }
        }
        if (LOCAL) {
            if (WM.value == 0) { mm(acch[0], cat8(C.b[1][0][0], C.b[1][0][1]), C.h); mm(acch[1], cat8(C.b[1][1][0], C.b[1][1][1]), C.h); }
            else               { mm(acch[0], C.h, cat8(C.b[2][0][0], C.b[2][0][1])); mm(acch[1], C.h, cat8(C.b[2][1][0], C.b[2][1][1])); }
            SBAR();
        }
    };

    Frags F0, F1;
    f32x4 cq[2], ck[2], mu4[5], rs4[5]; float cv[2], mu[5], rs[5];
    u32x4 qfr[4], kfr[4];
    u32x4 va[2][2];
    f32x4 mwin, mfirst;
    typedef __attribute__((address_space(3))) u32x4* lds_u4;
    const bool negpos = (wm == 0) && (panel == 0);
    VM_BAR(0);
    unsigned cm01 = 0, cm23 = 0;
    lds_cptr ka[2] = {sm, sm}, vo2[2] = {sm, sm};
    lds_u4 ex = (lds_u4)(smw + OFF_EX + wn * 6144) + lane;
    lds_cptr s1 = sm + C::OFF_MEAN + wm * 256 + l15 * 4, s4 = sm + C::OFF_MEAN + wm * 256 + g * 16;
    lds_cptr c1 = sm + C::OFF_CS + wn * 128 + l15 * 4, c4 = sm + C::OFF_CS + wn * 128 + g * 16;
    asm volatile("" : "+v"(s1), "+v"(s4), "+v"(c1), "+v"(c4));
    if (LOCAL) {
        unsigned keepv = negpos ? __float_as_uint(NEG_MASK) : 0u;
        asm volatile("" : "+s"(keepv));
        u32x4 mw, mf;
        unsigned prv[4];
#pragma unroll
        for (int r = 0; r < 4; ++r) {
            const int d = l15 - (4 * g + r);
            unsigned out = (unsigned)(((d << 28) >> 31));
            prv[r] = (unsigned)(d >> 31);
            asm volatile("" : "+v"(out), "+v"(prv[r]));
            mw[r] = out & 0xff800000u;
            mf[r] = (out & 0xff800000u) | (~out & keepv);
        }
        const lds_cptr e0 = sm + OFF_EX + wn * 6144;
        ka[0] = e0 + (wm ? lane : 256 + (lane ^ 8)) * 16;         ka[1] = e0 + (wm ? 64 + lane : 256 + lane) * 16;
        vo2[0] = e0 + (wm ? 128 + lane : 320 + (lane ^ 32)) * 16; vo2[1] = e0 + (wm ? 192 + lane : 320 + lane) * 16;
        cm01 = ~((prv[0] >> 16) | (prv[1] << 16)); cm23 = ~((prv[2] >> 16) | (prv[3] << 16));
        mwin = __builtin_bit_cast(f32x4, mw); mfirst = __builtin_bit_cast(f32x4, mf);
        asm volatile("" : "+v"(mwin), "+v"(mfirst), "+v"(cm01), "+v"(cm23), "+v"(ka[0]), "+v"(ka[1]), "+v"(vo2[0]), "+v"(vo2[1]), "+v"(ex));
    }
    typedef const __attribute__((address_space(3))) f32x4* lds_f4; typedef const __attribute__((address_space(3))) float* lds_f1;
    auto mainloop = [&](auto WM) {
#pragma unroll
        for (int a = 0; a < 3; ++a)
            asm volatile("" : "+v"(acc[a][0][0]), "+v"(acc[a][0][1]), "+v"(acc[a][1][0]), "+v"(acc[a][1][1]), "+v"(acc[a][2][0]), "+v"(acc[a][2][1]),
                         "+v"(acc[a][3][0]), "+v"(acc[a][3][1]));
        int ty = lane_id();
        const int l15 = ty & 15, g = ty >> 4;
        const int swA = (l15 >> 1) & 7;
        const int q4 = l15 >> 2, p4 = l15 & 3, fk = ((q4 >> 1) & 1) | ((g & 1) << 1);
        lds_cptr pA0 = sm + wm * 8192 + l15 * 128 + ((0 + g) ^ swA) * 16, pA1 = sm + wm * 8192 + l15 * 128 + ((4 + g) ^ swA) * 16;
        lds_cptr pB0 = sm + ABYTES + (8 * g + q4) * 128 + (((2 * wn + 0) ^ fk) * 32) + p4 * 8;
        lds_cptr pB1 = sm + ABYTES + (8 * g + q4) * 128 + (((2 * wn + 1) ^ fk) * 32) + p4 * 8;
        asm volatile("" : "+v"(acch[0]), "+v"(acch[1]), "+v"(pA0), "+v"(pA1), "+v"(pB0), "+v"(pB1));
        slow_warm((unsigned)(uintptr_t)smem + OFF_DUMP, W, w, ty);
        BAR();
        STAMP(LOCAL ? 1 : 7);
        BAR();
#pragma unroll
        for (int n = 0; n < 17; ++n) rd(WM, F0, pA0, pB0, pB1, n);
        WAIT_LGKM();
        int off = 0;
        lds_cptr a = pA1, b0 = pB0, b1 = pB1;
#pragma unroll QUNR
        for (int t = 0; t < NT - 1; ++t) {
            half(WM, F0, F1, true, a, b0 + 4096, b1 + 4096, [&](int n) {
                if (n == 17) { off = (off == 2 * STAGE) ? 0 : off + STAGE; asm volatile("" : "+s"(off)); }
                if (n == 19) { b0 = pB0 + off; asm volatile("" : "+v"(b0)); }
                if (n == 20) { a = pA0 + off; asm volatile("" : "+v"(a)); }
                if (n == 21) { b1 = pB1 + off; asm volatile("" : "+v"(b1)); }
            });
            WAIT_LGKM_BAR();
            half(WM, F1, F0, true, a, b0, b1, [&](int n) {
                if (n == 19) { a = pA1 + off; asm volatile("" : "+v"(a)); }
                if (n == 22) __builtin_amdgcn_s_waitcnt(0xC07F);
            });
        }
        half(WM, F0, F1, true, a, b0 + 4096, b1 + 4096, [](int) {});
        WAIT_LGKM_BAR();
        half(WM, F1, F0, false, sm, sm, sm, [&](int n) {
            constexpr int RS = C::OFF_RSTD - C::OFF_MEAN, H = 512 - WM.value * 256;
            if (n < 2) { cq[n] = *(lds_f4)(c4 + n * 64); ck[n] = *(lds_f4)(c4 + 256 + n * 64); }
            else if (n < 6) { mu[n - 2] = *(lds_f1)(s1 + (n - 2) * 64); rs[n - 2] = *(lds_f1)(s1 + RS + (n - 2) * 64); }
            else if (n < 10) { mu4[n - 6] = *(lds_f4)(s4 + (n - 6) * 64); rs4[n - 6] = *(lds_f4)(s4 + RS + (n - 6) * 64); }
            else if (n == 10) { cv[0] = *(lds_f1)(c1 + 512); cv[1] = *(lds_f1)(c1 + 512 + 64); }
            else if (LOCAL && n == 11) {
                if (WM.value == 0) { mu[4] = *(lds_f1)(s1 + H); rs[4] = *(lds_f1)(s1 + RS + H); }
                else { mu4[4] = *(lds_f4)(s4 + H); rs4[4] = *(lds_f4)(s4 + RS + H); }
            } else if (n == (LOCAL ? 22 : 15)) __builtin_amdgcn_s_waitcnt(0xC07F);
        }, LOCAL ? 0 : 1);
        asm volatile("s_nop 7");
        SBAR();
        if (LOCAL) {
            auto fix = [&](int a, int t) {
#pragma unroll
                for (int f = 0; f < 2; ++f)
                    acc[a][t][f] = (a == 2) ? rs4[t] * (acc[2][t][f] + mu4[t] * cv[f]) : rs[t] * (acc[a][t][f] + mu[t] * (a ? ck[f] : cq[f]));
            };
            auto pk = [&](const f32x4 (&v)[2]) { return u32x4{pack_bf16(v[0][0], v[0][1]), pack_bf16(v[0][2], v[0][3]), pack_bf16(v[1][0], v[1][1]), pack_bf16(v[1][2], v[1][3])}; };
            if (!ident) {
                fix(1, 1); fix(1, 3); fix(2, 1); fix(2, 3);
#pragma unroll
                for (int f = 0; f < 2; ++f)
                    acch[f] = (WM.value == 0) ? rs[4] * (acch[f] + mu[4] * ck[f]) : rs4[4] * (acch[f] + mu4[4] * cv[f]);
            }
            auto pkv = [&](int t) {
#pragma unroll
                for (int f = 0; f < 2; ++f) {
                    va[t >> 1][f][(t & 1) * 2] = pack_bf16(acc[2][t][f][0], acc[2][t][f][1]);
                    va[t >> 1][f][(t & 1) * 2 + 1] = pack_bf16(acc[2][t][f][2], acc[2][t][f][3]);
                }
            };
            kfr[1] = pk(acc[1][1]); kfr[3] = pk(acc[1][3]); pkv(1); pkv(3);
            u32x4 hf = pk(acch);
            if (WM.value == 0) {
                ex[0] = kfr[1]; ex[64] = kfr[3];
#pragma unroll
                for (int u = 0; u < 2; ++u)
                    asm volatile("ds_write_b64 %0, %1 offset:%3\n\tds_write_b64 %0, %2 offset:%3+8" :: "v"(ex), "v"(u32x2{va[u][0][2], va[u][0][3]}),
                                 "v"(u32x2{va[u][1][2], va[u][1][3]}), "n"(2048 + u * 1024) : "memory");
                if (negpos) hf = u32x4{0, 0, 0, 0};
            }
            ex[256 + WM.value * 64] = hf;
            SBAR();
            if (!ident) { fix(0, 0); fix(0, 1); fix(0, 2); fix(0, 3); fix(1, 0); fix(1, 2); fix(2, 0); fix(2, 2); }
#pragma unroll
            for (int t = 0; t < 4; ++t) qfr[t] = pk(acc[0][t]);
            kfr[0] = pk(acc[1][0]); kfr[2] = pk(acc[1][2]); pkv(0); pkv(2);
            asm volatile("" : "+v"(qfr[0]), "+v"(qfr[1]), "+v"(qfr[2]), "+v"(qfr[3]), "+v"(kfr[0]), "+v"(kfr[2]),
                         "+v"(va[0][0]), "+v"(va[0][1]), "+v"(va[1][0]), "+v"(va[1][1]));
        }
    };
    const int head = ct * 2 + wn;
    unsigned qo = 0, ko = 0, vo = 0;
    if (LOCAL) {
        qo = (unsigned)((((panel * 8 + wm * 4) * 32 + head) * 64 + lane) * 16);
        asm volatile("" : "+v"(qo));
    } else {
        ko = (unsigned)((wm * 8 + (g & 1)) * 1024 + ((l15 & 7) + ((l15 & 8) << 1) + 32 * (g >> 1)) * 16);
        vo = (unsigned)((wm * 8 + 2 + (g >> 1)) * 1024 + (l15 + 32 * (g & 1)) * 16);
        asm volatile("" : "+v"(ko), "+v"(vo));
    }
    if (LOCAL && wm) mainloop(IC<1>{}); else mainloop(IC<0>{});

    if (!LOCAL) {
        auto pk = [&](f32x4 a, f32x4 b) { return u32x4{pack_bf16(a[0], a[1]), pack_bf16(a[2], a[3]), pack_bf16(b[0], b[1]), pack_bf16(b[2], b[3])}; };
        char* kvp = (char*)kvf + (size_t)(head * 16 + panel) * 16384;
        lds_ptr own = smw + wn * 16384;
        SBAR();
#pragma unroll
        for (int t = 0; t < 4; ++t) {
            const u32x4 kk = pk(rs[t] * (acc[1][t][0] + mu[t] * ck[0]), rs[t] * (acc[1][t][1] + mu[t] * ck[1]));
            if (t & 1) st16o<128>(kvp + (t >> 1) * 4096, ko, kk); else st16o<0>(kvp + (t >> 1) * 4096, ko, kk);
            kfr[t] = kk;
            SBAR();
            const int u = t >> 1, f = t & 1;
            const u32x4 vv = pk(rs4[2 * u] * (acc[2][2 * u][f] + mu4[2 * u] * cv[f]), rs4[2 * u + 1] * (acc[2][2 * u + 1][f] + mu4[2 * u + 1] * cv[f]));
            if (f) st16o<256>(kvp + u * 4096, vo, vv); else st16o<0>(kvp + u * 4096, vo, vv);
            qfr[t] = vv;
            SBAR();
        }
        half(IC<0>{}, F1, F0, false, sm, sm, sm, [&](int n) {
            const int t = n >> 1;
            if (n & 1) *(lds_u4)(own + vo + (t >> 1) * 4096 + (t & 1) * 256) = qfr[t];
            else       *(lds_u4)(own + ko + (t >> 1) * 4096 + (t & 1) * 128) = kfr[t];
        }, 0, 1);
        asm volatile("s_nop 7");
        SBAR();
#pragma unroll
        for (int u = 0; u < 2; ++u) {
            const float r0 = rs[2 * u] * QSCALE, r1 = rs[2 * u + 1] * QSCALE;
            const u32x4 qa = pk(r0 * (acc[0][2 * u][0] + mu[2 * u] * cq[0]), r0 * (acc[0][2 * u][1] + mu[2 * u] * cq[1]));
            const u32x4 qb = pk(r1 * (acc[0][2 * u + 1][0] + mu[2 * u + 1] * cq[0]), r1 * (acc[0][2 * u + 1][1] + mu[2 * u + 1] * cq[1]));
#pragma unroll
            for (int e = 0; e < 4; ++e) {
                const auto x = __builtin_amdgcn_permlane16_swap(qa[e], qb[e], false, false);
                qo4[2 * u][e] = x[0]; qo4[2 * u + 1][e] = x[1];
            }
        }
        return;
    }
    {
        const f32x4 zero4 = {0, 0, 0, 0};
        auto bf8 = [](u32x4 v) { return __builtin_bit_cast(bf16x8, v); };
        f32x4 sp[4], sc[4];
        sc[1] = MFMA16(bf8(kfr[1]), bf8(qfr[1]), mwin); sp[1] = MFMA16(bf8(kfr[0]), bf8(qfr[1]), mwin);
        sc[3] = MFMA16(bf8(kfr[3]), bf8(qfr[3]), mwin); sp[3] = MFMA16(bf8(kfr[2]), bf8(qfr[3]), mwin);
        asm volatile("" : "+v"(sc[1]), "+v"(sp[1]), "+v"(sc[3]), "+v"(sp[3]));
        WAIT_LGKM_BAR();
        const bf16x8 kp[2] = {lds_b128(ka[0]), lds_b128(ka[1])};
        u32x2 vp[2][2];
#pragma unroll
        for (int u = 0; u < 2; ++u)
            asm volatile("ds_read_b64 %0, %2\n\tds_read_b64 %1, %2 offset:8" : "=&v"(vp[u][0]), "=&v"(vp[u][1]) : "v"(vo2[u]) : "memory");
        sc[0] = MFMA16(bf8(kfr[0]), bf8(qfr[0]), mwin); sc[2] = MFMA16(bf8(kfr[2]), bf8(qfr[2]), mwin);
        SBAR();
        float x[4][4], m[4];
        unsigned a[4][2];
        auto logits = [&](int t0, int st) {
#pragma unroll
            for (int t = t0; t < 4; t += 2)
#pragma unroll
                for (int h = 0; h < 2; ++h) {
                    if (st == 0) {
                        const unsigned uc = pack_bf16(sc[t][2 * h], sc[t][2 * h + 1]), up = pack_bf16(sp[t][2 * h], sp[t][2 * h + 1]);
                        asm("v_bfi_b32 %0, %1, %2, %3" : "=v"(a[t][h]) : "v"(h ? cm23 : cm01), "v"(uc), "v"(up));
                    } else if (st == 1) { x[t][2 * h] = bf_lo(a[t][h]) * ATT_SCALE; x[t][2 * h + 1] = bf_hi(a[t][h]) * ATT_SCALE; }
                    else if (st == 2) a[t][h] = pack_bf16(x[t][2 * h], x[t][2 * h + 1]);
                    else { x[t][2 * h] = bf_lo(a[t][h]); x[t][2 * h + 1] = bf_hi(a[t][h]); }
                }
            SBAR();
        };
        logits(1, 0); logits(1, 1);
        sp[0] = MFMA16(kp[0], bf8(qfr[0]), mfirst); sp[2] = MFMA16(kp[1], bf8(qfr[2]), mfirst);
        SBAR();
        logits(1, 2); logits(1, 3);
        logits(0, 0); logits(0, 1); logits(0, 2); logits(0, 3);
        auto across = [&](float (&v)[4], auto op, auto fin) {
            const auto p = __builtin_amdgcn_permlane16_swap(__float_as_uint(v[0]), __float_as_uint(v[1]), false, false);
            const auto q = __builtin_amdgcn_permlane16_swap(__float_as_uint(v[2]), __float_as_uint(v[3]), false, false);
            const auto r = __builtin_amdgcn_permlane32_swap(__float_as_uint(op(__uint_as_float(p[0]), __uint_as_float(p[1]))),
                                                            __float_as_uint(op(__uint_as_float(q[0]), __uint_as_float(q[1]))), false, false);
            const unsigned z = __float_as_uint(fin(op(__uint_as_float(r[0]), __uint_as_float(r[1]))));
            const auto u = __builtin_amdgcn_permlane32_swap(z, z, false, false);
            const auto e = __builtin_amdgcn_permlane16_swap(u[0], u[0], false, false), f = __builtin_amdgcn_permlane16_swap(u[1], u[1], false, false);
            v[0] = __uint_as_float(e[0]); v[1] = __uint_as_float(e[1]); v[2] = __uint_as_float(f[0]); v[3] = __uint_as_float(f[1]);
        };
#pragma unroll
        for (int t = 0; t < 4; ++t) m[t] = amax3(x[t][0], x[t][1], x[t][2]);
        asm("v_max_f32 %0, %0, %4\n\tv_max_f32 %1, %1, %5\n\tv_max_f32 %2, %2, %6\n\tv_max_f32 %3, %3, %7\n\ts_nop 0"
            : "+v"(m[0]), "+v"(m[1]), "+v"(m[2]), "+v"(m[3]) : "v"(x[0][3]), "v"(x[1][3]), "v"(x[2][3]), "v"(x[3][3]));
        across(m, [](float p, float q) { float r; asm("v_max_f32 %0, %1, %2\n\ts_nop 0" : "=v"(r) : "v"(p), "v"(q)); return r; },
               [](float p) { return p * -LOG2E; });
        SBAR();
#pragma unroll
        for (int t = 0; t < 4; ++t)
#pragma unroll
            for (int r = 0; r < 4; ++r) x[t][r] = __builtin_fmaf(x[t][r], LOG2E, m[t]);
        SBAR();
#pragma unroll
        for (int t = 0; t < 4; ++t)
#pragma unroll
            for (int r = 0; r < 4; ++r) x[t][r] = __builtin_amdgcn_exp2f(x[t][r]);
        SBAR();
#pragma unroll
        for (int t = 0; t < 4; ++t) m[t] = (x[t][0] + x[t][1]) + (x[t][2] + x[t][3]);
        asm volatile("s_waitcnt lgkmcnt(0)" : "+v"(vp[0][0]), "+v"(vp[0][1]), "+v"(vp[1][0]), "+v"(vp[1][1]) :: "memory");
        u32x4 ve[2][2];
#pragma unroll
        for (int u = 0; u < 2; ++u)
#pragma unroll
            for (int f = 0; f < 2; ++f) ve[u][f] = u32x4{vp[u][f][0], vp[u][f][1], va[u][f][0], va[u][f][1]};
        across(m, [](float p, float q) { return p + q; }, [](float p) { return __builtin_amdgcn_rcpf(p); });
        asm volatile("" : "+v"(ve[0][0]), "+v"(ve[0][1]), "+v"(ve[1][0]), "+v"(ve[1][1]));
        SBAR();
#pragma unroll
        for (int t = 0; t < 4; ++t)
#pragma unroll
            for (int r = 0; r < 4; ++r) x[t][r] *= m[t];
        SBAR();
#pragma unroll
        for (int t = 0; t < 4; ++t)
#pragma unroll
            for (int h = 0; h < 2; ++h) a[t][h] = pack_bf16(x[t][2 * h], x[t][2 * h + 1]);
        SBAR();
        f32x4 o0[4], o1[4];
#pragma unroll
        for (int t = 0; t < 4; ++t) {
            const bf16x8 pb = bf8(u32x4{a[t][0] & ~cm01, a[t][1] & ~cm23, a[t][0] & cm01, a[t][1] & cm23});
            o0[t] = MFMA16(bf8((t & 1) ? va[t >> 1][0] : ve[t >> 1][0]), pb, zero4);
            o1[t] = MFMA16(bf8((t & 1) ? va[t >> 1][1] : ve[t >> 1][1]), pb, zero4);
        }
        SBAR();
#pragma unroll
        for (int t = 0; t < 4; ++t)
            st16(ofrag + t * 16384, qo, u32x4{pack_bf16(o0[t][0], o0[t][1]), pack_bf16(o0[t][2], o0[t][3]),
                                                 pack_bf16(o1[t][0], o1[t][1]), pack_bf16(o1[t][2], o1[t][3])});
    }
}

DI int out_row(int panel, bool localrows, int T, int a) {
    return localrows ? (panel * 128 + (T >> 2) * 64 + 2 * (16 * (T & 1) + a) + ((T >> 1) & 1)) : (panel * 128 + T * 16 + a);
}
DI int out_swz(int k) { return ((k >> 1) & 1) | (k & 4); }
DI unsigned wo_off(int w, int lane) {
    const int kl = lane >> 3;
    unsigned v = (unsigned)(w * 8 + kl) * 2048u + (unsigned)(ids().co * 128 + ((lane & 7) ^ out_swz(kl)) * 16);
    asm volatile("" : "+v"(v));
    return v;
}
DI void wo_load(const void* Wo, unsigned voff, int i0, int i1, unsigned lds0, int w) {
#pragma unroll
    for (int i = i0; i < i1; ++i) glds16((const char*)Wo + (size_t)i * 65536, voff, lds0 + (w + 4 * i) * 1024);
}
DI unsigned res_off(bool localrows, bool tiled, int lane) {
    const Ids I = ids();
    unsigned v = (unsigned)(I.po * 262144 + I.co * (tiled ? 16384 : 128) + out_row(0, localrows, 0, lane >> 3) * (tiled ? 128 : 2048) +
                            (((lane & 7) ^ (lane >> 3)) * 16));
    asm volatile("" : "+v"(v));
    return v;
}
DI void res_load(u32x4 (&res)[6], const void* Xin, bool localrows, bool tiled, unsigned off, int w) {
#pragma unroll
    for (int i = 0; i < 6; ++i) {
        const int j = min(w + 3 * i, 15);
        asm volatile("global_load_dwordx4 %0, %1, %2 sc1" : "=&v"(res[i]) : "v"(off), "s"((const char*)Xin + out_row(0, localrows, j >> 1, (j & 1) * 8) * (tiled ? 128 : 2048)) : "memory");
    }
}
DI void out_handoff(char* smem, int xoff, u32x4 (&res)[6], int w, char* ws, int set, u64 tag, int lane) {
    const Ids I = ids();
    u64 land;
    lds_ptr dst = (lds_ptr)smem + xoff + w * 1024 + lane * 16;
    asm volatile("" : "+v"(dst));
    if (w == 3) __builtin_amdgcn_s_setprio(3);
    signal_poll<false, false>(w - 3, ws, flag_off(set, I.pq, lane & 15, I.cq), flag_off(set, I.po, I.co, lane & 15), 0xffffull, tag, land);
    if (w < 3) {
        asm volatile("s_waitcnt vmcnt(0)" : "+v"(res[0]), "+v"(res[1]), "+v"(res[2]), "+v"(res[3]), "+v"(res[4]), "+v"(res[5]) :: "memory");
#pragma unroll
        for (int i = 0; i < 5; ++i) *(__attribute__((address_space(3))) u32x4*)(dst + i * 3072) = res[i];
        if (w == 0) *(__attribute__((address_space(3))) u32x4*)(dst + 15360) = res[5];
    }
    WAIT_LGKM_BAR();
    asm volatile("s_waitcnt vmcnt(0)" : "+v"(land) :: "memory");
    __builtin_amdgcn_s_setprio(0);
}

template <bool AFTER_LOCAL>
DI void out_compute(char* smem, int w, const __hip_bfloat16* __restrict__ ofrag,
                    __hip_bfloat16* __restrict__ Xout, float* __restrict__ statp SP_PARAM) {
    constexpr int XOFF = AFTER_LOCAL ? 147456 : 98304;
    STAMP(AFTER_LOCAL ? 3 : 10);
    VM_BAR(0);
    const int lane = lane_id();
    asm volatile("" : "+s"(w));
    const Ids I = ids();
    const int panel = I.po, ct = I.co;
    const int l15 = lane & 15, g = lane >> 4;
    const bool tiled = AFTER_LOCAL || statp != nullptr;
    const int rstr = tiled ? 128 : 2048;
    const int q4 = l15 >> 2, p4 = l15 & 3;
    const int krow = 4 * g + q4;
    const int sw = out_swz(krow);
    const lds_cptr sm = (lds_cptr)smem;
    lds_cptr bw[4], xo[4];
#pragma unroll
    for (int t = 0; t < 4; ++t) {
        bw[t] = sm + krow * 128 + ((((t >> 1) * 4 + (p4 >> 1) * 2 + (t & 1)) ^ sw) * 16) + (p4 & 1) * 8;
        xo[t] = sm + XOFF + (w * 32 + l15) * 128 + ((((t >> 1) * 4 + (g >> 1) * 2 + (t & 1)) ^ (l15 & 7)) * 16) + (g & 1) * 8;
    }
    unsigned so[2], sto, ao[2];
#pragma unroll
    for (int r2 = 0; r2 < 2; ++r2) {
        so[r2] = (unsigned)(panel * 262144 + ct * (tiled ? 16384 : 128) + out_row(0, AFTER_LOCAL, 2 * w + r2, l15 & 7) * rstr + (l15 >> 3) * 64 + g * 16);
        ao[r2] = (unsigned)((panel * 8 + 2 * w + r2) * 32768 + lane * 16);
    }
    sto = (unsigned)(((panel * 16 + ct) * 128 + 2 * w * 16 + l15) * 8);
    asm volatile("" : "+v"(bw[0]), "+v"(bw[1]), "+v"(bw[2]), "+v"(bw[3]), "+v"(xo[0]), "+v"(xo[1]), "+v"(xo[2]), "+v"(xo[3]),
                 "+v"(so[0]), "+v"(so[1]), "+v"(sto), "+v"(ao[0]), "+v"(ao[1]));
    bf16x8 ones;
    {
        unsigned o1; asm volatile("v_mov_b32 %0, 0x3f803f80" : "=v"(o1));
        ones = __builtin_bit_cast(bf16x8, (u32x4{o1, o1, o1, o1}));
        asm volatile("" : "+v"(ones));
    }
    const __amdgpu_buffer_rsrc_t ra = __builtin_amdgcn_make_buffer_rsrc((void*)ofrag, 0, 4 << 20, 0x00020000);
    asm volatile("" :: "s"(ra));
    u32x32 fr;
    BAR();
    STAMP(AFTER_LOCAL ? 4 : 11);
    u32x32 aw0, aw1;
    asm volatile(".irp j,0,1,2,3\n\tbuffer_load_dwordx4 v[64+8*\\j:67+8*\\j], %2, %4, 0 offen offset:1024*\\j sc1\n\t"
                 "buffer_load_dwordx4 v[68+8*\\j:71+8*\\j], %3, %4, 0 offen offset:1024*\\j sc1\n\t.endr\n\t"
                 ".irp j,0,1\n\tbuffer_load_dwordx4 v[96+8*\\j:99+8*\\j], %2, %4, %5 offen offset:1024*\\j sc1\n\t"
                 "buffer_load_dwordx4 v[100+8*\\j:103+8*\\j], %3, %4, %5 offen offset:1024*\\j sc1\n\t.endr"
                 : "=&{v[64:95]}"(aw0), "=&{v[96:127]}"(aw1) : "v"(ao[0]), "v"(ao[1]), "s"(ra), "s"(4096) : "memory");
    f32x16 acc[2];
#pragma unroll
    for (int r2 = 0; r2 < 2; ++r2)
#pragma unroll
        for (int c = 0; c < 4; ++c) {
            const u32x2 xr = *(const __attribute__((address_space(3))) u32x2*)(xo[c] + r2 * 2048);
            acc[r2][4 * c] = bf_lo(xr[0]); acc[r2][4 * c + 1] = bf_hi(xr[0]); acc[r2][4 * c + 2] = bf_lo(xr[1]); acc[r2][4 * c + 3] = bf_hi(xr[1]);
        }
    asm volatile(
        ".macro OUT_MM r, t, j\n\t"
        "v_mfma_f32_16x16x32_bf16 v[16*\\r+4*\\t:16*\\r+4*\\t+3], v[32+16*((\\j)&1)+4*\\t:35+16*((\\j)&1)+4*\\t], v[64+8*\\j+4*\\r:67+8*\\j+4*\\r], v[16*\\r+4*\\t:16*\\r+4*\\t+3]\n\t"
        ".endm\n\t"
        ".macro OUT_RD t, hi, j, on\n\t"
        ".if \\on\n\tds_read_b64_tr_b16 v[32+16*((\\j)&1)+4*\\t+2*\\hi:33+16*((\\j)&1)+4*\\t+2*\\hi], v[128+\\t] offset:((\\j)&7)*4096+2048*\\hi\n\t.endif\n\t"
        ".endm\n\t"
        ".macro OUT_UP t, on\n\t.if \\on\n\tv_add_u32 v[128+\\t], s97, v[128+\\t]\n\t.endif\n\t.endm\n\t"
        ".macro OUT_STEP j, ld, rd, up, vm, lg\n\t"
        "OUT_MM 0, 0, \\j\n\tOUT_RD 3, 1, \\j+1, 1\n\tOUT_UP 0, \\up\n\t"
        ".if \\ld && (((\\j+6)&3) == 0)\n\ts_addk_i32 s96, 0x1000\n\t.endif\n\t"
        "OUT_MM 1, 0, \\j\n\tOUT_RD 0, 0, \\j+2, \\rd\n\t"
        ".if \\ld\n\tbuffer_load_dwordx4 v[64+8*((\\j+6)&7):67+8*((\\j+6)&7)], %[a0], %[ra], s96 offen offset:1024*((\\j+6)&3) sc1\n\t.endif\n\t"
        "OUT_MM 0, 1, \\j\n\tOUT_RD 0, 1, \\j+2, \\rd\n\tOUT_UP 1, \\up\n\t"
        "OUT_MM 1, 1, \\j\n\tOUT_RD 1, 0, \\j+2, \\rd\n\t"
        "OUT_MM 0, 2, \\j\n\tOUT_RD 1, 1, \\j+2, \\rd\n\tOUT_UP 2, \\up\n\t"
        "OUT_MM 1, 2, \\j\n\tOUT_RD 2, 0, \\j+2, \\rd\n\t"
        ".if \\ld\n\tbuffer_load_dwordx4 v[68+8*((\\j+6)&7):71+8*((\\j+6)&7)], %[a1], %[ra], s96 offen offset:1024*((\\j+6)&3) sc1\n\t.endif\n\t"
        "OUT_MM 0, 3, \\j\n\tOUT_RD 2, 1, \\j+2, \\rd\n\tOUT_UP 3, \\up\n\t"
        "OUT_MM 1, 3, \\j\n\tOUT_RD 3, 0, \\j+2, \\rd\n\t"
        "s_waitcnt vmcnt(\\vm) lgkmcnt(\\lg)\n\t"
        ".endm\n\t"
        ".macro OUT_PACK y, c\n\t"
        ".irp i,0,1,2,3,4,5,6,7\n\tv_cvt_pk_bf16_f32 v[\\y+\\i], v[\\c+2*\\i], v[\\c+2*\\i+1]\n\t.endr\n\t"
        ".irp i,0,1,4,5\n\tv_permlane16_swap_b32 v[\\y+\\i], v[\\y+\\i+2]\n\t.endr\n\t"
        ".irp i,0,1,2,3\n\tv_mov_b32 v[\\y+8+\\i], v[\\y+\\i]\n\t.endr\n\t"
        "v_mfma_f32_16x16x32_bf16 v[\\y+12:\\y+15], v[\\y:\\y+3], v[\\y:\\y+3], 0\n\t"
        "v_mov_b32_dpp v[\\y+8], v[\\y+4] row_ror:8 row_mask:0xf bank_mask:0xc\n\tv_mov_b32_dpp v[\\y+9], v[\\y+5] row_ror:8 row_mask:0xf bank_mask:0xc\n\t"
        "v_mfma_f32_16x16x32_bf16 v[\\y+16:\\y+19], %[one], v[\\y:\\y+3], 0\n\t"
        "v_mov_b32_dpp v[\\y+10], v[\\y+6] row_ror:8 row_mask:0xf bank_mask:0xc\n\tv_mov_b32_dpp v[\\y+11], v[\\y+7] row_ror:8 row_mask:0xf bank_mask:0xc\n\t"
        "v_mfma_f32_16x16x32_bf16 v[\\y+12:\\y+15], v[\\y+4:\\y+7], v[\\y+4:\\y+7], v[\\y+12:\\y+15]\n\t"
        ".endm\n\t"
        ".macro OUT_REST y\n\t"
        "v_mfma_f32_16x16x32_bf16 v[\\y+16:\\y+19], %[one], v[\\y+4:\\y+7], v[\\y+16:\\y+19]\n\t"
        ".irp i,0,1,2,3\n\tv_mov_b32_dpp v[\\y+4+\\i], v[\\y+\\i] row_ror:8 row_mask:0xf bank_mask:0x3\n\t.endr\n\t"
        ".endm\n\t"
        ".macro OUT_DIAG y\n\t"
        "v_cndmask_b32 v[\\y+12], v[\\y+12], v[\\y+13], %[m0]\n\tv_cndmask_b32 v[\\y+14], v[\\y+14], v[\\y+15], %[m0]\n\t"
        "v_cndmask_b32 v[\\y+17], v[\\y+12], v[\\y+14], %[m1]\n\t"
        ".endm\n\t"
        ".irp t,0,1,2,3\n\tOUT_RD \\t, 0, 0, 1\n\tOUT_RD \\t, 1, 0, 1\n\t.endr\n\t"
        ".irp t,0,1,2\n\tOUT_RD \\t, 0, 1, 1\n\tOUT_RD \\t, 1, 1, 1\n\t.endr\n\tOUT_RD 3, 0, 1, 1\n\t"
        "s_movk_i32 s96, 0x1000\n\ts_mov_b32 s97, 0x8000\n\t"
        "s_waitcnt vmcnt(10) lgkmcnt(0)\n"
        ".Lout_loop_%=:\n\t"
        "OUT_STEP 0, 1, 1, 0, 10, 7\n\tOUT_STEP 1, 1, 1, 0, 10, 7\n\tOUT_STEP 2, 1, 1, 0, 10, 7\n\tOUT_STEP 3, 1, 1, 0, 10, 7\n\t"
        "OUT_STEP 4, 1, 1, 0, 10, 7\n\t"
        "s_cmpk_eq_u32 s96, 0x6000\n\ts_cselect_b32 s97, %[last], s97\n\t"
        "OUT_STEP 5, 1, 1, 0, 10, 7\n\tOUT_STEP 6, 1, 1, 1, 10, 7\n\t"
        "OUT_STEP 7, 1, 1, 0, 10, 7\n\t"
        "s_cmpk_lg_u32 s96, 0x7000\n\ts_cbranch_scc1 .Lout_loop_%=\n\t"
        "OUT_STEP 0, 1, 1, 0, 10, 7\n\tOUT_STEP 1, 1, 1, 0, 10, 7\n\tOUT_STEP 2, 0, 1, 0, 8, 7\n\tOUT_STEP 3, 0, 1, 0, 6, 7\n\t"
        "OUT_STEP 4, 0, 1, 0, 4, 7\n\tOUT_STEP 5, 0, 1, 0, 2, 7\n\tOUT_STEP 6, 0, 0, 0, 0, 0\n\t"
        ".irp r,0,1\n\t.irp t,0,1,2,3\n\tOUT_MM \\r, \\t, 7\n\t.endr\n\t.endr\n\t"
        "s_nop 0\n\t"
        "OUT_PACK 64, 0\n\tglobal_store_dwordx4 %[s0], v[72:75], %[xa] sc1\n\tOUT_REST 64\n\tglobal_store_dwordx4 %[s0], v[68:71], %[xb] sc1\n\t"
        "OUT_PACK 84, 16\n\tglobal_store_dwordx4 %[s1], v[92:95], %[xa] sc1\n\tOUT_REST 84\n\tglobal_store_dwordx4 %[s1], v[88:91], %[xb] sc1\n\t"
        "s_cmp_eq_u64 %[st], 0\n\ts_cbranch_scc1 .Lout_end_%=\n\t"
        "OUT_DIAG 64\n\tOUT_DIAG 84\n\t"
        "s_mov_b64 exec, %[md]\n\t"
        "global_store_dwordx2 %[so], v[80:81], %[st] sc1\n\tglobal_store_dwordx2 %[so], v[100:101], %[st] offset:128 sc1\n\t"
        "s_mov_b64 exec, -1\n"
        ".Lout_end_%=:\n\t"
        "s_nop 1\n\t"
        ".purgem OUT_MM\n\t.purgem OUT_RD\n\t.purgem OUT_UP\n\t.purgem OUT_STEP\n\t.purgem OUT_PACK\n\t.purgem OUT_REST\n\t.purgem OUT_DIAG"
        : "+{v[0:15]}"(acc[0]), "+{v[16:31]}"(acc[1]), "+{v[64:95]}"(aw0), "+{v[96:127]}"(aw1),
          "+{v128}"(bw[0]), "+{v129}"(bw[1]), "+{v130}"(bw[2]), "+{v131}"(bw[3]), "=&{v[32:63]}"(fr)
        : [a0] "v"(ao[0]), [a1] "v"(ao[1]), [ra] "s"(ra), [last] "s"(AFTER_LOCAL ? 32768 : 65536), [one] "v"(ones), [s0] "v"(so[0]), [s1] "v"(so[1]),
          [xa] "s"(Xout), [xb] "s"((char*)Xout + (AFTER_LOCAL ? 16 : 8) * rstr), [st] "s"(statp), [so] "v"(sto),
          [m0] "s"(0xaaaaaaaaaaaaaaaaull), [m1] "s"(0xccccccccccccccccull), [md] "s"(0xf0000f0000f0000full)
        : "memory", "scc", "s96", "s97");
    STAMP(AFTER_LOCAL ? 5 : 12);
}

#define ROWSUM(p0, p1, c) __builtin_amdgcn_smfmac_f32_16x16x64_bf16(sel, __builtin_bit_cast(bf16v_t, __builtin_shufflevector(p0, p1, 0, 1, 2, 3, 4, 5, 6, 7)), c, sidx, 0, 0)
constexpr int ATT_STAGE = 32768;
typedef __attribute__((ext_vector_type(8))) unsigned u32x8;

DI void attn_loader(char* smem, int w, char* ws, int set, u64 tag, const __hip_bfloat16* __restrict__ kvf, const void* Wo, const void* Xin,
                    const void* Wcs SP_PARAM) {
    const int lane = lane_id();
    asm volatile("" : "+s"(w));
    const Ids I = ids();
    const int panel = I.pq, ct = I.cq;
    const unsigned lds0 = (unsigned)(uintptr_t)smem;
    const char* kvh = (const char*)kvf + (size_t)(ct * 2 + (w >> 1)) * 262144 + (w & 1) * 4096;
    const unsigned voff = (unsigned)(lane * 16);
    auto order = [&](int st) { return ((panel & 8) + st - ((st - 1 < (panel & 7)) ? 1 : 0)) & 15; };
    auto load = [&](int st) {
        const char* src = kvh + (size_t)order(st) * 16384;
        const unsigned dst = lds0 + (w >> 1) * 16384 + (w & 1) * 4096 + (st & 3) * ATT_STAGE;
#pragma unroll
        for (int u = 0; u < 2; ++u)
            asm volatile("s_mov_b32 m0, %0\n\ts_nop 0\n\tglobal_load_lds_dwordx4 %1, %2 sc1\n\tglobal_load_lds_dwordx4 %1, %2 offset:1024 sc1\n\t"
                         "global_load_lds_dwordx4 %1, %2 offset:2048 sc1\n\tglobal_load_lds_dwordx4 %1, %2 offset:3072 sc1"
                         :: "s"(dst + u * 8192), "v"(voff), "s"(src + u * 8192) : "memory");
    };
    const unsigned wv = wo_off(w, lane), rv = res_off(false, true, lane), cv = colsum_off(w, lane);
    const unsigned poff = flag_off(set, ct, panel, lane & 15);
    u64 land;
    __builtin_amdgcn_s_setprio(3);
    BAR();
    signal_poll<false, true>(w, ws, flag_off(set, ct, lane & 15, panel), poff, 1ull << order(1), tag, land);
    STAMP(9);
    load(1);
    asm volatile("global_load_dwordx2 %0, %1, %2 sc1\n\ts_waitcnt vmcnt(0)" : "+v"(land) : "v"(poff), "s"(ws + WS_FLAG) : "memory");
    STAMP(10);
    BAR();
    if (!__all(land == tag)) flag_poll(ws, poff, tag);
    load(2); load(3);
    __builtin_amdgcn_s_setprio(0);
    STAMP(11);
    VM_BAR(8); load(4);
    VM_BAR(0); load(5); load(6);
    VM_BAR(0); load(7); load(8);
    u32x4 res[6], ca[8];
    colsum_load(ca, w < 3 ? Wcs : nullptr, cv);
#pragma unroll 1
    for (int st = 6; st < 12; st += 2) {
        VM_BAR(0);
        load(st + 3); load(st + 4);
        if (st == 8) wo_load(Wo, wv, 24, 32, lds0 + 32768, w);
    }
    VM_BAR(0); load(15); wo_load(Wo, wv, 0, 8, lds0, w);
    res_load(res, Xin, false, true, rv, w);
    VM_BAR(0); wo_load(Wo, wv, 8, 24, lds0, w);
    STAMP(12);
    out_handoff(smem, 98304, res, w, ws, set + 1, tag, lane);
    if (Wcs && w < 3) colsum_store(ca, ws, 0, tag, w, lane);
}

DI void attn_compute(char* smem, int w, const u32x4 (&q4)[4], const __hip_bfloat16* __restrict__ kvf, __hip_bfloat16* __restrict__ ofrag SP_PARAM) {
    const int lane = lane_id();
    asm volatile("" : "+s"(w));
    const int wm = w & 1, wn = w >> 1;
    const Ids I = ids();
    const int panel = I.pq, head = I.cq * 2 + wn;
    const int l15 = lane & 15;
    const unsigned se = ((l15 & 2) == 0 && (l15 & 4) == 0) ? 0x3f803f80u : 0u, so = ((l15 & 2) == 0 && (l15 & 4) != 0) ? 0x3f803f80u : 0u;
    const bf8_t sel = __builtin_bit_cast(bf8_t, u32x4{se, se, so, so});
    const int sidx = (l15 & 1) ? (int)0xEEEEEEEEu : 0x44444444;
    const lds_cptr sm = (lds_cptr)smem + wn * 16384 + lane * 16;
    const f32x16 z16 = {0, 0, 0, 0, 0, 0, 0, 0, 0, 0, 0, 0, 0, 0, 0, 0};
    const bf16x8 qA0 = __builtin_bit_cast(bf16x8, q4[0]), qA1 = __builtin_bit_cast(bf16x8, q4[1]);
    const bf16x8 qB0 = __builtin_bit_cast(bf16x8, q4[2]), qB1 = __builtin_bit_cast(bf16x8, q4[3]);
    f32x16 oA, oB, nmA, nmB, sA, sB;
    f32x4 lsA, lsB;
    u32x8 pB;
    bf16x8 v11;
    asm volatile(".irp r,106,108,110,112,134,136,138,140,170,172\n\tv_mov_b64 v[\\r:\\r+1], 0\n\t.endr"
                 : "={v[106:113]}"(pB), "={v[134:137]}"(lsA), "={v[138:141]}"(lsB), "={v[170:173]}"(v11));
    WAIT_LGKM_BAR();
    STAMP(8);
    bf16x8 k00 = lds_b128(sm), k10 = lds_b128(sm + 1024), k01 = lds_b128(sm + 4096), k11 = lds_b128(sm + 4096 + 1024);
    bf16x8 v00 = lds_b128(sm + 2048), v10 = lds_b128(sm + 3072), v01 = lds_b128(sm + 4096 + 2048);
    float ob;
    {
        const u32x4 vu = __builtin_bit_cast(u32x4, v00);
        unsigned vm = max(max(vu[0] & 0x7fff0000u, vu[1] & 0x7fff0000u), max(vu[2] & 0x7fff0000u, vu[3] & 0x7fff0000u));
        vm = min(vm, (unsigned)__builtin_amdgcn_update_dpp(0, (int)vm, 0xB1, 0xF, 0xF, true));
        vm = min(vm, (unsigned)__builtin_amdgcn_update_dpp(0, (int)vm, 0x4E, 0xF, 0xF, true));
        vm = min(vm, (unsigned)__builtin_amdgcn_update_dpp(0, (int)vm, 0x141, 0xF, 0xF, true));
        vm = min(vm, (unsigned)__builtin_amdgcn_update_dpp(0, (int)vm, 0x140, 0xF, 0xF, true));
        const auto vs = __builtin_amdgcn_permlane16_swap(vm, vm, false, false);
        ob = __uint_as_float(min(vs[0], vs[1])) * 0.015625f;
        sA = MFMA32(k00, qA0, z16); sB = MFMA32(k00, qB0, z16);
        sA = MFMA32(k10, qA1, sA); sB = MFMA32(k10, qB1, sB);
        SBAR(); VM_BAR(0); SBAR();
        float mA = sA[0], mB = sB[0];
#pragma unroll
        for (int r = 1; r < 16; ++r) { mA = fmaxf(mA, sA[r]); mB = fmaxf(mB, sB[r]); }
        mA = max_h(mA) + 16.f; mB = max_h(mB) + 16.f;
#pragma unroll
        for (int r = 0; r < 16; ++r) { nmA[r] = -mA; nmB[r] = -mB; sA[r] -= mA; oA[r] = ob; oB[r] = ob; }
    }
    const unsigned a0 = (unsigned)(uintptr_t)sm;
    unsigned dst = (unsigned)((((panel * 8 + wm * 4 + ((lane & 31) >> 4)) * 32 + head) * 64 + (lane & 15) + (lane >> 5) * 16) * 16);
    asm volatile("" : "+v"(dst));
    asm volatile(
        ".macro ATT_RD dst, y\n\t"
        "ds_read_b128 v[\\dst:\\dst+3], v[175+(((\\y)&0x1ffff)>>16)] offset:(\\y)&0xffff\n\t"
        ".endm\n\t"
        ".macro ATT_E4 c\n\t"
        "v_exp_f32 v[\\c], v[\\c]\n\tv_exp_f32 v[\\c+1], v[\\c+1]\n\tv_exp_f32 v[\\c+2], v[\\c+2]\n\tv_exp_f32 v[\\c+3], v[\\c+3]\n\t"
        ".endm\n\t"
        ".macro ATT_C4 p, c\n\t"
        "v_cvt_pk_bf16_f32 v[\\p], v[\\c], v[\\c+1]\n\tv_cvt_pk_bf16_f32 v[\\p+1], v[\\c+2], v[\\c+3]\n\t"
        "v_cvt_pk_bf16_f32 v[\\p+2], v[\\c+4], v[\\c+5]\n\tv_cvt_pk_bf16_f32 v[\\p+3], v[\\c+6], v[\\c+7]\n\t"
        ".endm\n\t"
        ".macro ATT_STEP cur, nxt, ka, kb, q, nm, oc, od, lsd, pc, pd, vd1, vc0, r0, y0, r1, y1, bar, rd\n\t"
        "v_mfma_f32_32x32x16_bf16 v[\\nxt:\\nxt+15], v[\\ka:\\ka+3], v[\\q:\\q+3], v[\\nm:\\nm+15]\n\t"
        "ATT_E4 \\cur\n\t"
        "v_mfma_f32_32x32x16_bf16 v[\\nxt:\\nxt+15], v[\\kb:\\kb+3], v[\\q+4:\\q+7], v[\\nxt:\\nxt+15]\n\t"
        "ATT_E4 \\cur+4\n\t"
        "v_mfma_f32_32x32x16_bf16 v[\\od:\\od+15], v[\\vd1:\\vd1+3], v[\\pd+4:\\pd+7], v[\\od:\\od+15]\n\t"
        "s_waitcnt lgkmcnt(0)\n\t"
        "ATT_C4 \\pc, \\cur\n\t"
        "v_exp_f32 v[\\cur+8], v[\\cur+8]\n\tv_exp_f32 v[\\cur+9], v[\\cur+9]\n\t"
        "v_mfma_f32_32x32x16_bf16 v[\\oc:\\oc+15], v[\\vc0:\\vc0+3], v[\\pc:\\pc+3], v[\\oc:\\oc+15]\n\t"
        ".if \\bar\n\ts_barrier\n\t.endif\n\t"
        "ATT_E4 \\cur+10\n\t"
        "v_smfmac_f32_16x16x64_bf16 v[\\lsd:\\lsd+3], v[130:133], v[\\pd:\\pd+7], v174\n\t"
        ".if \\rd & 4\n\tATT_RD 142, 32768\n\tATT_RD 150, 33792\n\tATT_RD 158, 34816\n\t.endif\n\t"
        ".if \\rd & 8\n\tATT_RD \\r1-8, \\y1-1024\n\t.endif\n\t"
        ".if \\rd & 1\n\tATT_RD \\r0, \\y0\n\t.endif\n\t"
        ".if \\rd & 2\n\tATT_RD \\r1, \\y1\n\t.endif\n\t"
        "v_exp_f32 v[\\cur+14], v[\\cur+14]\n\tv_exp_f32 v[\\cur+15], v[\\cur+15]\n\t"
        "ATT_C4 \\pc+4, \\cur+8\n\t"
        ".if \\rd & 4\n\ts_waitcnt lgkmcnt(3)\n\t.endif\n\t"
        ".endm\n\t"
        ".macro ATT_TILE p, y1, y2, bar, ra=3, rb=3\n\t"
        "ATT_STEP 66, 82, 142+\\p, 150+\\p, 122, 50, 2, 18, 138, 98, 106, 170-\\p, 158+\\p, 170-\\p, \\y1+3072, 142+\\p, \\y2, \\bar&1, \\ra\n\t"
        "ATT_STEP 82, 66, 146-\\p, 154-\\p, 114, 34, 18, 2, 134, 106, 98, 166+\\p, 158+\\p, 158+\\p, \\y2+2048, 150+\\p, \\y2+1024, \\bar&2, \\rb\n\t"
        ".endm\n\t"
        ".macro ATT_STAGE b, bar\n\t"
        "ATT_TILE 0, \\b+4096, \\b+8192, 0\n\t"
        "ATT_TILE 4, \\b+8192, \\b+12288, 0\n\t"
        ".if \\bar\n\tATT_TILE 0, \\b+12288, \\b+32768, 2, 1, 11\n\t.else\n\tATT_TILE 0, \\b+12288, \\b+32768, 0\n\t.endif\n\t"
        "ATT_TILE 4, \\b+32768, \\b+36864, 0\n\t"
        ".endm\n\t"
        "v_add_u32 v176, 0x10000, v175\n\t"
        "s_mov_b32 s96, 4\n\t"
        "s_waitcnt lgkmcnt(0)\n\t"
        "ATT_TILE 0, 4096, 8192, 0\n\tATT_TILE 4, 8192, 12288, 0\n\tATT_TILE 0, 12288, 0, 0, 1, 0\n\tATT_TILE 4, 32768, 36864, 1, 7, 3\n\t"
        "ATT_STAGE 32768, 1\n\t"
        "s_branch .Latt_entry_%=\n\t"
        ".p2align 6\n"
        ".Latt_loop_%=:\n\t"
        "ATT_STAGE 0, 1\n\tATT_STAGE 32768, 0\n"
        ".Latt_entry_%=:\n\t"
        "ATT_STAGE 65536, 1\n\t"
        "s_sub_u32 s96, s96, 1\n\t"
        "ATT_STAGE 98304, 0\n\t"
        "s_cmp_lg_u32 s96, 0\n\t"
        "s_cbranch_scc1 .Latt_loop_%=\n\t"
        "s_nop 1\n\t"
        "v_mfma_f32_32x32x16_bf16 v[18:33], v[170:173], v[110:113], v[18:33]\n\t"
        "v_smfmac_f32_16x16x64_bf16 v[138:141], v[130:133], v[106:113], v174\n\t"
        "s_waitcnt lgkmcnt(0)\n\ts_nop 10\n\t"
        ".purgem ATT_RD\n\t.purgem ATT_E4\n\t.purgem ATT_C4\n\t.purgem ATT_STEP\n\t.purgem ATT_TILE\n\t.purgem ATT_STAGE"
        : "+{v[2:17]}"(oA), "+{v[18:33]}"(oB), "+{v[66:81]}"(sA), "+{v[106:113]}"(pB), "+{v[134:137]}"(lsA), "+{v[138:141]}"(lsB),
          "+{v[142:145]}"(k00), "+{v[146:149]}"(k01), "+{v[150:153]}"(k10), "+{v[154:157]}"(k11),
          "+{v[158:161]}"(v00), "+{v[162:165]}"(v01), "+{v[166:169]}"(v10), "+{v[170:173]}"(v11)
        : "{v[34:49]}"(nmA), "{v[50:65]}"(nmB), "{v[114:117]}"(qA0), "{v[118:121]}"(qA1), "{v[122:125]}"(qB0), "{v[126:129]}"(qB1),
          "{v[130:133]}"(sel), "{v174}"(sidx), "{v175}"(a0)
        : "memory", "scc", "s96", "v176", "v82", "v83", "v84", "v85", "v86", "v87", "v88", "v89", "v90", "v91", "v92", "v93", "v94", "v95",
          "v96", "v97", "v98", "v99", "v100", "v101", "v102", "v103", "v104", "v105");
    STAMP(9);
    float lA = lsA[0] + lsA[1], lB = lsB[0] + lsB[1];
    auto store = [&] {
#pragma unroll
    for (int u = 0; u < 2; ++u) {
        const float inv = __builtin_amdgcn_rcpf(u ? lB : lA), sub = -ob * inv;
        const f32x16 e = (u ? oB : oA) * inv + sub;
        st16o<0>((char*)ofrag + u * 65536, dst, u32x4{pack_bf16(e[0], e[1]), pack_bf16(e[2], e[3]), pack_bf16(e[8], e[9]), pack_bf16(e[10], e[11])});
        st16o<512>((char*)ofrag + u * 65536, dst, u32x4{pack_bf16(e[4], e[5]), pack_bf16(e[6], e[7]), pack_bf16(e[12], e[13]), pack_bf16(e[14], e[15])});
    }
    };
    store();
    {
        const f32x16 os = oA + oB;
        const float osum = (((os[0] + os[1]) + (os[2] + os[3])) + ((os[4] + os[5]) + (os[6] + os[7]))) +
                           (((os[8] + os[9]) + (os[10] + os[11])) + ((os[12] + os[13]) + (os[14] + os[15])));
        const bool bad = !(fmaxf(lA, lB) < 1.0e24f) || !(lA > 1.0e-24f) || !(lB > 1.0e-24f) || !(fabsf(osum) < 3.0e38f);
        if (__any(bad)) {
            const __amdgpu_buffer_rsrc_t rk = __builtin_amdgcn_make_buffer_rsrc((char*)kvf + (size_t)head * 262144, 0, 262144, 0x00020000);
            oA = z16; oB = z16; lA = 0.f; lB = 0.f; ob = 0.f;
            float mA = -INFINITY, mB = -INFINITY;
#pragma unroll 1
            for (int t = 0; t < 64; ++t) {
                bf16x8 f[4];
#pragma unroll
                for (int i = 0; i < 4; ++i) f[i] = __builtin_bit_cast(bf16x8, __builtin_amdgcn_raw_buffer_load_b128(rk, lane * 16 + i * 1024, t * 4096, 16));
                auto upd = [&](bf16x8 q0, bf16x8 q1, f32x16& o, float& l, float& m) {
                    f32x16 s = MFMA32(f[0], q0, z16); s = MFMA32(f[1], q1, s);
                    float tm = s[0];
#pragma unroll
                    for (int r = 1; r < 16; ++r) tm = fmaxf(tm, s[r]);
                    tm = max_h(tm);
                    const float mn = fmaxf(m, tm);
                    const float alpha = __builtin_amdgcn_exp2f(m - mn);
                    m = mn;
#pragma unroll
                    for (int r = 0; r < 16; ++r) { s[r] = __builtin_amdgcn_exp2f(s[r] - mn); o[r] *= alpha; }
                    const u32x4 p0 = {pack_bf16(s[0], s[1]), pack_bf16(s[2], s[3]), pack_bf16(s[4], s[5]), pack_bf16(s[6], s[7])};
                    const u32x4 p1 = {pack_bf16(s[8], s[9]), pack_bf16(s[10], s[11]), pack_bf16(s[12], s[13]), pack_bf16(s[14], s[15])};
                    f32x4 t4 = {0, 0, 0, 0};
                    o = MFMA32(f[2], __builtin_bit_cast(bf16x8, p0), o);
                    o = MFMA32(f[3], __builtin_bit_cast(bf16x8, p1), o);
                    t4 = ROWSUM(p0, p1, t4);
                    l = l * alpha + (t4[0] + t4[1]);
                };
                upd(qA0, qA1, oA, lA, mA);
                upd(qB0, qB1, oB, lB, mB);
            }
            store();
        }
    }
}

DI void ln_row(const u32x4 (&v)[2], __hip_bfloat16* __restrict__ Y, unsigned off) {
    float s1 = 0.f, s2 = 0.f;
#pragma unroll
    for (int e = 0; e < 8; ++e) s1 = dot2bf(v[e >> 2][e & 3], 0x3f803f80u, s1);
    const float mean = wave_sum(s1) * (1.f / 1024.f);
#pragma unroll
    for (int e = 0; e < 8; ++e) {
        const float d0 = bf_lo(v[e >> 2][e & 3]) - mean, d1 = bf_hi(v[e >> 2][e & 3]) - mean;
        s2 += d0 * d0 + d1 * d1;
    }
    const float mb = round_bf16(mean);
    const float rden = __builtin_amdgcn_rcpf(round_bf16(__builtin_amdgcn_sqrtf(round_bf16(round_bf16(wave_sum(s2) * (1.f / 1024.f)) + 1e-5f))));
#pragma unroll
    for (int hh = 0; hh < 2; ++hh) {
        u32x4 y;
#pragma unroll
        for (int e = 0; e < 4; ++e) {
            const unsigned d = pack_bf16(bf_lo(v[hh][e]) - mb, bf_hi(v[hh][e]) - mb);
            y[e] = pack_bf16(bf_lo(d) * rden, bf_hi(d) * rden);
        }
        st16((char*)Y + hh * 131072, off, y);
    }
}

extern "C" __global__ __launch_bounds__(512, 2) void mega_kernel(const __hip_bfloat16* x, const __hip_bfloat16* wqkvA,
                                                            const __hip_bfloat16* woA, const __hip_bfloat16* wqkvB,
                                                            const __hip_bfloat16* woB, __hip_bfloat16* out,
                                                            char* ws) {
    char* const smem = smem_base;
    typedef __hip_bfloat16 bf;
    const int w8 = __builtin_amdgcn_readfirstlane(threadIdx.x >> 6);
    if (w8 == 7 && (blockIdx.x >> 3) < 10) code_touch(blockIdx.x >> 3, threadIdx.x & 63);
    asm volatile("" :: "s"(x), "s"(wqkvA), "s"(woA), "s"(wqkvB), "s"(woB), "s"(out), "s"(ws));
    STAMP_DECL; STAMPR(14); STAMP(0);
    u64 tag;
    {
        const int lane = threadIdx.x & 63;
        const Ids I = ids();
        const unsigned off = (unsigned)((I.po * 128 + I.co * 8 + w8) * 2048 + lane * 16);
        u32x4 v[2];
        asm volatile("global_load_dwordx4 %0, %2, %3\n\tglobal_load_dwordx4 %1, %2, %3 offset:1024" : "=&v"(v[0]), "=&v"(v[1]) : "v"(off), "s"(x) : "memory");
        asm volatile("s_load_dwordx2 %0, %1, 0x0" : "=s"(tag) : "s"(ws + WS_EPOCH) : "memory");
        if (w8 == 7) {
            const int l = lane & 15, k = l % 5;
            const unsigned o = (l >= 10 || k == 3) ? flag_off(l, I.cq, I.pq, 0) : (k == 0 || k == 2) ? flag_off(l, I.pq, I.cq, 0) : flag_off(l, I.po, I.co, 0);
            touch_at(ws + WS_FLAG, o);
        } else if (w8 >= 4 && w8 - 4 < NTOUCH) touch(wqkvA, (w8 - 4) * 256 + blockIdx.x, lane);
        if (w8 == 7 || (w8 >= 4 && w8 - 4 < NTOUCH)) asm volatile("s_waitcnt vmcnt(1)" ::: "memory"); else asm volatile("s_waitcnt vmcnt(0)" ::: "memory");
        asm volatile("" : "+v"(v[0]), "+v"(v[1]));
        ln_row(v, (bf*)(ws + WS_Y0), (unsigned)(I.po * 262144 + (lane >> 3) * 16384 + (I.co * 8 + w8) * 128 + (lane & 7) * 16));
        asm volatile("s_waitcnt lgkmcnt(0)" : "+s"(tag) :: "memory");
        tag += 1;
    }
    if (w8 >= 4) {
        int w = w8 & 3;
#pragma unroll 1
        for (int it = 0; it < 2; ++it) {
            const int lane = lane_id();
            asm volatile("" : "+s"(x), "+s"(wqkvA), "+s"(woA), "+s"(wqkvB), "+s"(woB), "+s"(ws), "+s"(w));
            const unsigned lds0 = (unsigned)(uintptr_t)smem;
            u32x4 cb[8];
            const unsigned wv = wo_off(w, lane), rv = res_off(true, it, lane), cv = colsum_off(w, lane);
            qkv_loader<true>(smem, w, ws, it * 5, tag, (const bf*)(ws + (it ? WS_X2 : WS_Y0)), wqkvA, (const float*)(ws + WS_ST + 262144),
                             ws + WS_CSP, it == 0, woA, [&] {
                if (w < 3) {
                    colsum_load(cb, wqkvB, cv);
                    asm volatile("s_waitcnt vmcnt(8)" ::: "memory");
                } else {
                    touch(woA, blockIdx.x, lane);
                    asm volatile("s_waitcnt vmcnt(1)" ::: "memory");
                }
            }, [&](int part) {
                if (part == 0) wo_load(woA, wv, 32 - WO0, 32, lds0, w); else wo_load(woA, wv, 32 - WO1, 32 - WO0, lds0, w);
            } SP_ARG);
            STAMP(3);
            u32x4 res[6];
            res_load(res, it ? (const void*)(ws + WS_X2) : x, true, it, rv, w);
            if (it) __builtin_amdgcn_s_sleep(WSL1); else __builtin_amdgcn_s_sleep(WSL0);
            wo_load(woA, wv, 0, WO2, lds0, w);
            BAR();
            wo_load(woA, wv, WO2, 32 - WO1, lds0, w);
            const bool cs = (it == 0 && w < 3);
            STAMP(4);
            out_handoff(smem, 147456, res, w, ws, it * 5 + 1, tag, lane);
            if (cs) colsum_store(cb, ws, 1, tag, w, lane);
            STAMP(5);
            qkv_loader<false>(smem, w, ws, it * 5 + 2, tag, (const bf*)(ws + (it ? WS_X3 : WS_X1)), wqkvB, (const float*)(ws + WS_ST + it * 524288), ws + WS_CSP + 196608,
                              false, nullptr, [] {}, [](int) {} SP_ARG);
            STAMP(8);
            attn_loader(smem, w, ws, it * 5 + 3, tag, (const bf*)(ws + WS_KVF + it * 8 * MBy), woB, ws + (it ? WS_X3 : WS_X1),
                        it ? nullptr : wqkvA SP_ARG);
            STAMP(13); STAMPR(15); STAMP_FLUSH(it);
        }
        if (blockIdx.x == 0 && w == 0 && lane_id() == 0) __hip_atomic_store((g_u64ptr)(ws + WS_EPOCH), tag, __ATOMIC_RELAXED, __HIP_MEMORY_SCOPE_AGENT);
    } else {
        __builtin_amdgcn_s_setprio(2);
#pragma unroll 1
        for (int it = 0; it < 2; ++it) {
            u32x4 q4[4];
            asm volatile("" : "+s"(ws), "+s"(wqkvA), "+s"(wqkvB));
            qkv_compute<true>(smem, w8, wqkvA, q4, nullptr, (bf*)(ws + WS_OF + it * 8 * MBy), it == 0 SP_ARG);
            STAMP(2);
            out_compute<true>(smem, w8, (const bf*)(ws + WS_OF + it * 8 * MBy),
                              (bf*)(ws + (it ? WS_X3 : WS_X1)), (float*)(ws + WS_ST + it * 524288) SP_ARG);
            STAMP(6);
            qkv_compute<false>(smem, w8, wqkvB, q4, (bf*)(ws + WS_KVF + it * 8 * MBy), nullptr, false SP_ARG);
            attn_compute(smem, w8, q4, (const bf*)(ws + WS_KVF + it * 8 * MBy), (bf*)(ws + WS_OF + (it * 8 + 4) * MBy) SP_ARG);
            out_compute<false>(smem, w8, (const bf*)(ws + WS_OF + (it * 8 + 4) * MBy),
                               it ? out : (bf*)(ws + WS_X2), it ? nullptr : (float*)(ws + WS_ST + 262144) SP_ARG);
            STAMP(13); STAMPR(15); STAMP_FLUSH(it);
        }
    }
}

extern "C" __global__ void zz_code_end_kernel() {}

extern "C" void kernel_launch(void* const* d_in, const int* in_sizes, int n_in, void* d_out, int out_size, void* d_ws,
                              size_t ws_size, hipStream_t stream) {
    typedef __hip_bfloat16 bf;
    (void)hipFuncSetAttribute((const void*)mega_kernel, hipFuncAttributeMaxDynamicSharedMemorySize, CH_LDS);
    mega_kernel<<<256, 512, CH_LDS, stream>>>((const bf*)d_in[0], (const bf*)d_in[1], (const bf*)d_in[2], (const bf*)d_in[4], (const bf*)d_in[5],
                                              (bf*)d_out, (char*)d_ws);
}
```

```cpp
#include <hip/hip_runtime.h>
#include <hip/hip_bf16.h>
#include <stdint.h>

typedef __attribute__((ext_vector_type(8))) short bf16x8;
typedef __attribute__((ext_vector_type(4))) short s16x4;
typedef __attribute__((ext_vector_type(4))) float f32x4;
typedef __attribute__((ext_vector_type(2))) float f32x2;
typedef __attribute__((ext_vector_type(16))) float f32x16;
typedef __attribute__((ext_vector_type(4))) unsigned u32x4;
typedef __attribute__((ext_vector_type(2))) unsigned u32x2;
typedef __attribute__((ext_vector_type(2))) __bf16 bf16x2_t;
typedef __attribute__((ext_vector_type(8))) __bf16 bf8_t;
typedef __attribute__((ext_vector_type(16))) __bf16 bf16v_t;
typedef __attribute__((address_space(3))) const char* lds_cptr;
typedef __attribute__((address_space(3))) char* lds_ptr;

#define DI __device__ __forceinline__
#define MFMA16(a, b, c) __builtin_amdgcn_mfma_f32_16x16x32_bf16((a), (b), (c), 0, 0, 0)
#define MFMA32(a, b, c) __builtin_amdgcn_mfma_f32_32x32x16_bf16((a), (b), (c), 0, 0, 0)
#define SBAR() __builtin_amdgcn_sched_barrier(0)
#define STAMP(i)
#define STAMPR(i)
#define STAMP_DECL
#define STAMP_FLUSH(it)
#define SP_PARAM
#define SP_ARG
typedef unsigned long long u64;
typedef __attribute__((ext_vector_type(32))) unsigned u32x32;

constexpr float QSCALE = 0.17677669529663687f * 1.4426950408889634f;
constexpr float ATT_SCALE = 0.17677669529663687f;
constexpr float LOG2E = 1.4426950408889634f;
constexpr float NEG_MASK = -1e9f / ATT_SCALE;

DI int lane_id() { int l; asm volatile("v_mbcnt_lo_u32_b32 %0, -1, 0\n\tv_mbcnt_hi_u32_b32 %0, -1, %0" : "=v"(l)); return l; }
DI unsigned pack_bf16(float lo, float hi) {
    f32x2 v = {lo, hi};
    bf16x2_t r = __builtin_convertvector(v, bf16x2_t);
    return __builtin_bit_cast(unsigned, r);
}
DI float bf_lo(unsigned u) { return __uint_as_float(u << 16); }
DI float bf_hi(unsigned u) { return __uint_as_float(u & 0xffff0000u); }
DI float round_bf16(float x) { return bf_lo(pack_bf16(x, 0.f)); }
DI void ln_stats(float sum, float sumsq, float& mean_out, float& rstd_out) {
    float mean;
    asm("v_mul_f32 %0, 0x3a800000, %1" : "=v"(mean) : "v"(sum));
    const float var = fmaxf(__builtin_fmaf(-mean, mean, sumsq * (1.f / 1024.f)), 0.f);
    mean_out = round_bf16(mean);
    rstd_out = __builtin_amdgcn_rcpf(round_bf16(__builtin_amdgcn_sqrtf(round_bf16(round_bf16(var) + 1e-5f))));
}

DI void glds16(const void* sbase, unsigned voff, unsigned ldsaddr) {
    asm volatile("s_mov_b32 m0, %0\n\ts_nop 0\n\tglobal_load_lds_dwordx4 %1, %2"
                 :: "s"(ldsaddr), "v"(voff), "s"(sbase) : "memory");
}
DI void glds16c(const void* sbase, unsigned voff, unsigned ldsaddr) {
    asm volatile("s_mov_b32 m0, %0\n\ts_nop 0\n\tglobal_load_lds_dwordx4 %1, %2 sc1"
                 :: "s"(ldsaddr), "v"(voff), "s"(sbase) : "memory");
}
DI void st16(void* sbase, unsigned voff, u32x4 v) {
    asm volatile("global_store_dwordx4 %0, %1, %2 sc1\n\ts_nop 1" :: "v"(voff), "v"(v), "s"(sbase) : "memory");
}
template <int OFF> DI void st16o(void* sbase, unsigned voff, u32x4 v) {
    asm volatile("global_store_dwordx4 %0, %1, %2 offset:%3 sc1\n\ts_nop 1" :: "v"(voff), "v"(v), "s"(sbase), "n"(OFF) : "memory");
}
DI bf16x8 lds_b128(lds_cptr p) { return *(const __attribute__((address_space(3))) bf16x8*)p; }
DI s16x4 lds_tr(lds_cptr p) {
    return __builtin_amdgcn_ds_read_tr16_b64_v4i16((__attribute__((address_space(3))) s16x4*)p);
}
DI bf16x8 cat8(s16x4 a, s16x4 b) { return __builtin_shufflevector(a, b, 0, 1, 2, 3, 4, 5, 6, 7); }
DI float dot2bf(unsigned a, unsigned b, float c) {
    return __builtin_amdgcn_fdot2_f32_bf16(__builtin_bit_cast(bf16x2_t, a), __builtin_bit_cast(bf16x2_t, b), c, false);
}
DI float sum_g(float v) {
    auto a = __builtin_amdgcn_permlane16_swap(__float_as_uint(v), __float_as_uint(v), false, false);
    v = __uint_as_float(a[0]) + __uint_as_float(a[1]);
    auto b = __builtin_amdgcn_permlane32_swap(__float_as_uint(v), __float_as_uint(v), false, false);
    return __uint_as_float(b[0]) + __uint_as_float(b[1]);
}
DI float wave_sum(float v) {
    v += __builtin_bit_cast(float, __builtin_amdgcn_update_dpp(0, __builtin_bit_cast(int, v), 0xB1, 0xF, 0xF, true));
    v += __builtin_bit_cast(float, __builtin_amdgcn_update_dpp(0, __builtin_bit_cast(int, v), 0x4E, 0xF, 0xF, true));
    v += __builtin_bit_cast(float, __builtin_amdgcn_update_dpp(0, __builtin_bit_cast(int, v), 0x141, 0xF, 0xF, true));
    v += __builtin_bit_cast(float, __builtin_amdgcn_update_dpp(0, __builtin_bit_cast(int, v), 0x140, 0xF, 0xF, true));
    return sum_g(v);
}
DI float amax3(float a, float b, float c) { float r; asm("v_max3_f32 %0, %1, %2, %3" : "=v"(r) : "v"(a), "v"(b), "v"(c)); return r; }
DI float max2f(float a, float b) { return __builtin_amdgcn_fmed3f(a, b, INFINITY); }
DI float max_h(float v) {
    auto b = __builtin_amdgcn_permlane32_swap(__float_as_uint(v), __float_as_uint(v), false, false);
    return max2f(__uint_as_float(b[0]), __uint_as_float(b[1]));
}

constexpr unsigned OFF_DUMP = 145408;
extern __shared__ __attribute__((aligned(16))) char smem_base[];
DI void touch_at(const void* p, unsigned off) {
    asm volatile("s_mov_b32 m0, %0\n\ts_nop 0\n\tglobal_load_lds_dword %1, %2"
                 :: "s"((unsigned)(uintptr_t)smem_base + OFF_DUMP), "v"(off), "s"(p) : "memory");
}
DI void touch(const void* p, int slice, int lane) { touch_at(p, (unsigned)(slice * 64 + lane) * 128u); }
DI void code_touch(int slice, int lane) {
    unsigned long long c0, c1;
    asm volatile("s_getpc_b64 s[96:97]\n\ts_add_u32 s96, s96, mega_kernel@rel32@lo+4\n\ts_addc_u32 s97, s97, mega_kernel@rel32@hi+12\n\ts_mov_b64 %0, s[96:97]\n\t"
                 "s_getpc_b64 s[96:97]\n\ts_add_u32 s96, s96, zz_code_end_kernel@rel32@lo+4\n\ts_addc_u32 s97, s97, zz_code_end_kernel@rel32@hi+12\n\ts_mov_b64 %1, s[96:97]"
                 : "=s"(c0), "=s"(c1) :: "s96", "s97", "scc");
    touch_at((const void*)c0, min((unsigned)(slice * 64 + lane) * 128u, (unsigned)(c1 - c0)));
}

#define BAR() asm volatile("s_barrier" ::: "memory")
#define WAIT_LGKM() do { SBAR(); __builtin_amdgcn_s_waitcnt(0xC07F); SBAR(); } while (0)
#define WAIT_LGKM_BAR() do { SBAR(); __builtin_amdgcn_s_waitcnt(0xC07F); asm volatile("s_barrier" ::: "memory"); SBAR(); } while (0)
#define VM_BAR(n) asm volatile("s_waitcnt vmcnt(" #n ")\n\ts_barrier" ::: "memory")
constexpr int CH_LDS = 163840;
typedef __attribute__((address_space(3))) unsigned* lds_uptr;
typedef __attribute__((address_space(1))) u64* g_u64ptr;

constexpr size_t MBy = 1 << 20;
constexpr size_t WS_X3 = 0, WS_X2 = 4 * MBy, WS_X1 = 8 * MBy, WS_OF = 12 * MBy  , WS_KVF = 36 * MBy  ,
                 WS_ST = 52 * MBy  , WS_CSP = 54 * MBy  , WS_FLAG = 55 * MBy  ,
                 WS_EPOCH = WS_CSP + 524288, WS_Y0 = 56 * MBy;
DI unsigned flag_off(int set, int group, int consumer, int producer) { return (unsigned)(set * 65536 + group * 4096 + consumer * 256 + producer * 8); }
DI void flag_store(char* ws, int set, int group, int producer, u64 tag, int lane) {
    asm volatile("" : "+s"(tag));
    if (lane < 16) __hip_atomic_store((g_u64ptr)(ws + WS_FLAG + flag_off(set, group, lane, producer)), tag, __ATOMIC_RELAXED, __HIP_MEMORY_SCOPE_AGENT);
}
DI void flag_poll(const char* ws, unsigned off, u64 tag) {
    asm volatile("" : "+s"(tag));
#pragma unroll 1
    for (int spin = 0; spin < (1 << 16); ++spin) {
        const u64 v = __hip_atomic_load((g_u64ptr)(ws + WS_FLAG + off), __ATOMIC_RELAXED, __HIP_MEMORY_SCOPE_AGENT);
        if (__all(v == tag)) break;
        __builtin_amdgcn_s_sleep(1);
    }
    asm volatile("" ::: "memory");
}
template <bool DRAIN, bool ALLPOLL>
DI void signal_poll(int follower, const char* ws, unsigned soff, unsigned poff, u64 pmask, u64 tag, u64& land) {
    u64 tagv, zero;
    asm volatile("s_not_b64 s[96:97], %[ts]\n\tv_mov_b64 %[tag], %[ts]\n\tv_mov_b64 %[zero], 0\n\tv_mov_b64 %[land], s[96:97]\n\t"
                 ".if %[dr]\n\ts_waitcnt vmcnt(0)\n\t.endif\n\ts_barrier\n\t"
                 "s_cmp_lg_u32 %[fl], 0\n\ts_cbranch_scc1 .Lsig_fol_%=\n\t"
                 "s_mov_b64 exec, 0xffff\n\t"
                 "global_store_dwordx2 %[so], %[tag], %[base] sc1\n"
                 ".if %[ap]\n.Lsig_fol_%=:\n.endif\n\t"
                 "s_mov_b64 exec, %[pm]\n\t"
                 "s_movk_i32 s96, 0x7fff\n"
                 ".Lsig_loop_%=:\n\t"
                 "global_atomic_or_x2 %[land], %[po], %[zero], %[base] sc0 sc1\n\t"
                 "s_sleep 1\n\t"
                 "s_waitcnt vmcnt(3)\n\t"
                 "v_cmp_ne_u64 vcc, %[land], %[tag]\n\t"
                 "s_sub_u32 s96, s96, 1\n\t"
                 "s_cbranch_scc1 .Lsig_out_%=\n\t"
                 "s_cbranch_vccnz .Lsig_loop_%=\n"
                 ".Lsig_out_%=:\n\t"
                 "s_mov_b64 exec, -1\n"
                 ".if !%[ap]\n.Lsig_fol_%=:\n.endif"
                 : [land] "=&v"(land), [tag] "=&v"(tagv), [zero] "=&v"(zero)
                 : [fl] "s"(follower), [so] "v"(soff), [po] "v"(poff), [ts] "s"(tag), [base] "s"(ws + WS_FLAG), [pm] "s"(pmask), [ap] "n"(ALLPOLL ? 1 : 0), [dr] "n"(DRAIN ? 1 : 0)
                 : "memory", "scc", "vcc", "s96", "s97");
}
struct Ids { int pq, cq, po, co; };
DI Ids ids() {
    int bid = blockIdx.x; asm volatile("" : "+s"(bid));
    const int xcd = bid & 7, idx = bid >> 3;
    int pq, cq, po, co;
#define IDS_BODY pq = (xcd & 1) * 8 + (idx & 7); cq = (xcd >> 1) * 4 + (idx >> 3); po = (xcd & 3) * 4 + (idx & 3); co = (xcd >> 2) * 8 + (idx >> 2);
    IDS_BODY
    return Ids{pq, cq, po, co};
}

DI void slow_warm(unsigned dump, const void* W, int w, int lane) {
    int bid = blockIdx.x; asm volatile("" : "+s"(bid));
    const int xcd = bid & 7, idx = bid >> 3;
    const int sc = (3 - (int)((uintptr_t)W >> 7)) & 7;
    if ((sc >> 2) != ((xcd >> 1) & 1)) return;
    const char* src = (const char*)W + (idx + 32 * w) * 49152 + (sc + (xcd >> 2) * 8) * 128;
#pragma unroll
    for (int G = 0; G < 3; ++G) glds16(src + G * 2048, (unsigned)((lane >> 3) * 6144 + (lane & 7) * 16), dump);
}

template <int V> struct IC { static constexpr int value = V; };
template <bool LOCAL> struct QC {
    static constexpr int AROWS = LOCAL ? 144 : 128;
    static constexpr int ABYTES = AROWS * 128;
    static constexpr int STAGE = ABYTES + 24576;
    static constexpr int OFF_MEAN = 143360;
    static constexpr int OFF_RSTD = OFF_MEAN + 576;
    static constexpr int OFF_CS = OFF_RSTD + 576;
};
constexpr int OFF_EX = 131072;

DI unsigned colsum_off(int G, int lane) {
    const Ids I = ids();
    unsigned off = (unsigned)(I.pq * 64 + (lane >> 3)) * 6144u + (unsigned)(G * 2048 + I.cq * 128 + (lane & 7) * 16);
    asm volatile("" : "+v"(off));
    return off;
}
DI void colsum_load(u32x4 (&c)[8], const void* W, unsigned off) {
#define CS_NEXT "s_add_u32 s96, s96, 0xc000\n\ts_addc_u32 s97, s97, 0\n\t"
    asm volatile("s_cmp_eq_u64 %9, 0\n\ts_cbranch_scc1 .Lcs_skip_%=\n\ts_mov_b64 s[96:97], %9\n\t"
                 "global_load_dwordx4 %0, %8, s[96:97]\n\t" CS_NEXT "global_load_dwordx4 %1, %8, s[96:97]\n\t" CS_NEXT
                 "global_load_dwordx4 %2, %8, s[96:97]\n\t" CS_NEXT "global_load_dwordx4 %3, %8, s[96:97]\n\t" CS_NEXT
                 "global_load_dwordx4 %4, %8, s[96:97]\n\t" CS_NEXT "global_load_dwordx4 %5, %8, s[96:97]\n\t" CS_NEXT
                 "global_load_dwordx4 %6, %8, s[96:97]\n\t" CS_NEXT "global_load_dwordx4 %7, %8, s[96:97]\n"
                 ".Lcs_skip_%=:"
                 : "=&v"(c[0]), "=&v"(c[1]), "=&v"(c[2]), "=&v"(c[3]), "=&v"(c[4]), "=&v"(c[5]), "=&v"(c[6]), "=&v"(c[7])
                 : "v"(off), "s"(W) : "memory", "scc", "s96", "s97");
}
DI void colsum_store(u32x4 (&c)[8], char* ws, int mat, int G, int lane) {
    asm volatile("" : "+v"(c[0]), "+v"(c[1]), "+v"(c[2]), "+v"(c[3]), "+v"(c[4]), "+v"(c[5]), "+v"(c[6]), "+v"(c[7]));
    const Ids I = ids();
    const int m = lane & 15;
    const unsigned one = (m & 1) ? 0xbf800000u : 0xbf80u;
    const u32x4 sl = {(m >> 1) == 0 ? one : 0u, (m >> 1) == 1 ? one : 0u, (m >> 1) == 2 ? one : 0u, (m >> 1) == 3 ? one : 0u};
    f32x4 d = {0, 0, 0, 0};
#pragma unroll
    for (int i = 0; i < 8; ++i) d = MFMA16(__builtin_bit_cast(bf16x8, sl), __builtin_bit_cast(bf16x8, c[i]), d);
#pragma unroll
    for (int r = 0; r < 4; ++r)
        d[r] += __int_as_float(__builtin_amdgcn_update_dpp(0, __float_as_int(d[r]), 0x128, 0xF, 0xF, true));
    if (lane < 32 && (lane & 8) == 0)
        st16(ws + WS_CSP + mat * 196608, (unsigned)((I.cq * 16 + I.pq) * 768 + G * 256 + (lane & 7) * 32 + (lane >> 4) * 16), __builtin_bit_cast(u32x4, d));

}

template <bool LOCAL>
struct WFirst {
    unsigned v[2];
    DI WFirst(int w, int ct, int lane) {
#pragma unroll
        for (int i = 0; i < 2; ++i) {
            const int k = 8 * i + (lane >> 3), pos = lane & 7;
            v[i] = (unsigned)k * 6144u + (unsigned)(w * 1024 + ct * 64 + ((pos >> 1) ^ (((k >> 1) & 1) | (i << 1))) * 16 + (pos & 1) * 8) * 2u;
        }
        asm volatile("" : "+v"(v[0]), "+v"(v[1]));
    }
    DI void issue(const void* W, unsigned lds0, int w, int i0, int i1) const {
#pragma unroll
        for (int i = i0; i < i1; ++i)
            glds16((const char*)W + (size_t)(i >> 1) * (16 * 6144), v[i & 1], lds0 + (i >> 3) * QC<LOCAL>::STAGE + QC<LOCAL>::ABYTES + (w * 8 + (i & 7)) * 1024);
    }
};

template <bool LOCAL, class F, class F2>
DI void qkv_loader(char* smem, int w, char* ws, int set, u64 tag, const __hip_bfloat16* __restrict__ X, const __hip_bfloat16* __restrict__ W,
                   const float* __restrict__ statp, const char* __restrict__ csp, bool ident, const void* nextW, F tail, F2 mid SP_PARAM) {
    typedef QC<LOCAL> C;
    constexpr int NT = 16;
    const int lane = lane_id();
    asm volatile("" : "+s"(w));
    const int idx = blockIdx.x >> 3;
    const Ids I = ids();
    const int panel = I.pq, ct = I.cq;
    const int R0 = panel * 128;
    const unsigned lds0 = (unsigned)(uintptr_t)smem;
    auto grow = [&](int i) -> int {
        if (!LOCAL) return R0 + i;
        if (i < 128) { const int t = (i >> 4) & 3, a = i & 15; return R0 + (i & 64) + 2 * (16 * (t & 1) + a) + (t >> 1); }
        const int a = i & 15; const int r = R0 - 16 + 2 * (a & 7) + (a >> 3);
        return r < 0 ? 0 : r;
    };
    unsigned voffA[5], voffB[6];
#pragma unroll
    for (int i = 0; i < 6; ++i) {
        const int j = w + 4 * i, G = j >> 3, k = 8 * (j & 7) + (lane >> 3);
        const int pos = lane & 7, f = ((k >> 1) & 1) | (((k >> 3) & 1) << 1);
        const int seg = (pos >> 1) ^ f;
        voffB[i] = (unsigned)k * 6144u + (unsigned)(G * 1024 + ct * 64 + seg * 16 + (pos & 1) * 8) * 2u;
    }
    auto issueW = [&](int t, int buf) {
        const char* wb = (const char*)W + (size_t)t * (64 * 6144);
        const unsigned sb = lds0 + buf * C::STAGE;
#pragma unroll
        for (int i = 0; i < 6; ++i) glds16(wb, voffB[i], sb + C::ABYTES + (w + 4 * i) * 1024);
    };
    asm volatile("" : "+v"(voffB[0]), "+v"(voffB[1]), "+v"(voffB[2]), "+v"(voffB[3]), "+v"(voffB[4]), "+v"(voffB[5]));
    const WFirst<LOCAL> first(w, ct, lane);
    const int g = lane >> 4, r = lane & 15, pos = 32 * w + 2 * r + (g >> 1);
    const int lr = !LOCAL ? (pos & 64) + 2 * (pos & 31) + ((pos >> 5) & 1)
                          : (w == 0 && (g & 1)) ? 128 + (r & 7) + 8 * (g >> 1) : (pos & 64) + ((pos & 1) * 2 + ((pos >> 5) & 1)) * 16 + ((pos >> 1) & 15);
    unsigned so = (unsigned)(panel * 16384 + g * 1024 + w * 256 + r * 16), ho = (unsigned)(g * 1024 + (panel ? panel * 16384 - 15488 + (r & 7) * 16 : 0));
    unsigned co = (unsigned)((ct * 16 + g) * 768 + w * 192 + r * 12);
    lds_ptr mp = (lds_ptr)smem + C::OFF_MEAN + lr * 4, cp = (lds_ptr)smem + C::OFF_CS + w * 192 + r * 12 + ((g & 1) ? 8 : g * 2);
    unsigned fo = flag_off((LOCAL ? 13 : 10) + min(g, 2), ct, panel, r);
    asm volatile("" : "+v"(so), "+v"(ho), "+v"(co), "+v"(mp), "+v"(cp), "+v"(fo));
#pragma unroll
    for (int i = 0; i < 5; ++i) {
        const int j = (i < 4) ? (w + 4 * i) : (16 + (w & 1));
        const int row = 8 * j + (lane >> 3);
        const int c = (lane & 7) ^ ((row >> 1) & 7);
        const int gr = grow(row < C::AROWS ? row : 0);
        voffA[i] = (unsigned)((gr >> 7) * 262144 + (gr & 127) * 128 + c * 16);
        asm volatile("" : "+v"(voffA[i]));
    }
    auto issueA = [&](int t, int buf) {
        const char* xa = (const char*)X + t * 16384;
        const unsigned sb = lds0 + buf * C::STAGE;
        if (LOCAL && w < 2) glds16c(xa, voffA[4], sb + (16 + w) * 1024);
#pragma unroll
        for (int i = 0; i < 4; ++i) glds16c(xa, voffA[i], sb + (w + 4 * i) * 1024);
    };
    u64 land;
    if (w == 3) __builtin_amdgcn_s_setprio(3);
    signal_poll<LOCAL, false>(w - 3, ws, flag_off(set, I.po, lane & 15, I.co),
                              flag_off(set, (lane & 16) && panel ? panel - 1 : panel, ct, lane & 15),
                              !LOCAL ? 0xffffull : ident ? 0xc000ffffull : 0xffffffffull, tag, land);
    if (w < 3) first.issue(W, lds0, w, 0, 12);
    BAR();
    STAMP(LOCAL ? 1 : 6);
    issueA(0, 0);
    issueA(1, 1);
    if (w < 3) { first.issue(W, lds0, w, 12, 16); asm volatile("s_waitcnt vmcnt(8)" ::: "memory"); }
    else asm volatile("s_waitcnt vmcnt(4)" ::: "memory");
    BAR();
    asm volatile("" :: "v"(land));
    __builtin_amdgcn_s_setprio(0);
    STAMP(LOCAL ? 2 : 7);
    issueW(2, 2);
    issueA(2, 2);
    VM_BAR(10);
    asm volatile("global_load_dwordx2 %0, %1, %2 sc1" : "=&v"(land) : "v"(fo), "s"(ws + WS_FLAG) : "memory");
    issueA(3, 0); issueW(3, 0);
    int buf = 1;
#pragma unroll 1
    for (int t = 1; t < NT - 3; ++t) {
        VM_BAR(10);
        issueA(t + 3, buf); issueW(t + 3, buf);
        if (t == 10 && idx < 3) code_touch(idx * 4 + w, lane);
        if (LOCAL && t == 11)
            glds16(nextW, (unsigned)((idx + 32 * w) * 8 + (lane >> 3)) * 2048u +
                   (unsigned)(((I.co & 8) + ((3 - (int)((uintptr_t)nextW >> 7)) & 7)) * 128 + (lane & 7) * 16), lds0 + OFF_DUMP);
        buf = (buf == 2) ? 0 : buf + 1;
    }
    typedef __attribute__((ext_vector_type(3))) float f32x3;
    const bool halo = LOCAL && w == 0;
    asm volatile("" : "+v"(land));
    if (!ident) {
        if (!__all(land == tag)) flag_poll(ws, fo, tag);
        f32x3 cs[4];
        f32x4 sv[4], hv[4];
        typedef __attribute__((address_space(3))) float* lds_f;
        auto add = [](float a, float b) { float r; asm("v_add_f32 %0, %1, %2" : "=v"(r) : "v"(a), "v"(b)); return r; };
        auto fold32 = [](float a, float b) { asm("s_nop 1\n\tv_permlane32_swap_b32 %0, %1\n\tv_add_f32 %0, %0, %1" : "+v"(a), "+v"(b)); return a; };
        auto fold16 = [](float a, float b) { asm("s_nop 1\n\tv_permlane16_swap_b32 %0, %1\n\tv_add_f32 %0, %0, %1" : "+v"(a), "+v"(b)); return a; };
        auto sum4 = [&](auto (&v)[4], int e) { return add(add(v[0][e], v[1][e]), add(v[2][e], v[3][e])); };
#pragma unroll
        for (int i = 0; i < 4; ++i) asm volatile("global_load_dwordx3 %0, %1, %2 sc1" : "=&v"(cs[i]) : "v"(co), "s"(csp + i * 3072) : "memory");
        VM_BAR(14);
#pragma unroll
        for (int i = 0; i < 4; ++i) asm volatile("global_load_dwordx4 %0, %1, %2 sc1" : "=&v"(sv[i]) : "v"(so), "s"((const char*)statp + i * 4096) : "memory");
        if (LOCAL)
            asm volatile("s_cmp_lg_u32 %9, 0\n\ts_cbranch_scc1 .Lhv_skip_%=\n\t"
                         "global_load_dwordx4 %0, %4, %5 sc1\n\tglobal_load_dwordx4 %1, %4, %6 sc1\n\t"
                         "global_load_dwordx4 %2, %4, %7 sc1\n\tglobal_load_dwordx4 %3, %4, %8 sc1\n"
                         ".Lhv_skip_%=:" : "=&v"(hv[0]), "=&v"(hv[1]), "=&v"(hv[2]), "=&v"(hv[3])
                         : "v"(ho), "s"(statp), "s"(statp + 1024), "s"(statp + 2048), "s"(statp + 3072), "s"(w) : "memory", "scc");
        if (halo) asm volatile("s_waitcnt vmcnt(8)" ::: "memory"); else asm volatile("s_waitcnt vmcnt(4)" ::: "memory");
        asm volatile("" : "+v"(cs[0]), "+v"(cs[1]), "+v"(cs[2]), "+v"(cs[3]));
        const float c2 = sum4(cs, 2);
        *(lds_f)cp = fold16(fold32(sum4(cs, 0), sum4(cs, 1)), fold32(c2, c2));
        BAR();
        asm volatile("s_waitcnt vmcnt(0)" : "+v"(sv[0]), "+v"(sv[1]), "+v"(sv[2]), "+v"(sv[3]) :: "memory");
        mid(0);
        const float s1 = fold32(sum4(sv, 0), sum4(sv, 2)), s2 = fold32(sum4(sv, 1), sum4(sv, 3));
        float h1, h2;
        asm("v_mov_b32 %0, %2\n\tv_mov_b32 %1, %3" : "=&v"(h1), "=&v"(h2) : "v"(s1), "v"(s2));
        if (halo) {
            asm volatile("" : "+v"(hv[0]), "+v"(hv[1]), "+v"(hv[2]), "+v"(hv[3]));
            const float u0 = sum4(hv, 0), u1 = sum4(hv, 1), u2 = sum4(hv, 2), u3 = sum4(hv, 3);
            h1 = fold32(u0, panel ? u2 : u0); h2 = fold32(u1, panel ? u3 : u1);
        }
        float mean, rstd;
        ln_stats(fold16(s1, h1), fold16(s2, h2), mean, rstd);
        *(lds_f)mp = mean; *(lds_f)(mp + (C::OFF_RSTD - C::OFF_MEAN)) = rstd;
    } else { VM_BAR(10); tail(); BAR(); mid(0); }
    mid(1);
    WAIT_LGKM_BAR();
}

template <bool LOCAL>
DI void qkv_compute(char* smem, int w, const void* W, u32x4 (&qo4)[4], __hip_bfloat16* __restrict__ kvf, __hip_bfloat16* __restrict__ ofrag, bool ident SP_PARAM) {
    typedef QC<LOCAL> C;
    constexpr int NT = 16, ABYTES = C::ABYTES, STAGE = C::STAGE;
    const int lane = lane_id();
    asm volatile("" : "+s"(w));
    const int wm = w & 1, wn = w >> 1;
    const Ids I = ids();
    const int panel = I.pq, ct = I.cq;
    const int l15 = lane & 15, g = lane >> 4;
    const lds_cptr sm = (lds_cptr)smem;
    lds_ptr smw = (lds_ptr)smem;

    f32x4 acc[3][4][2];
    f32x4 acch[2];
    f32x2 z2; asm volatile("v_mov_b64 %0, 0" : "=v"(z2));
    const f32x4 z4 = {z2[0], z2[1], z2[0], z2[1]};
#pragma unroll
    for (int a = 0; a < 3; ++a)
#pragma unroll
        for (int t = 0; t < 4; ++t) { acc[a][t][0] = z4; acc[a][t][1] = z4; }
    acch[0] = z4; acch[1] = z4;

    struct Frags { bf16x8 a[4]; bf16x8 h; s16x4 b[3][2][2]; };
    auto rd = [&](auto WM, Frags& F, lds_cptr pa, lds_cptr pb0, lds_cptr pb1, int n) {
        switch (n) {
            case 0: F.b[0][0][0] = lds_tr(pb0); break;
            case 1: F.b[0][0][1] = lds_tr(pb0 + 512); break;
            case 2: F.a[0] = lds_b128(pa); break;
            case 3: F.b[0][1][0] = lds_tr(pb1); break;
            case 4: F.b[0][1][1] = lds_tr(pb1 + 512); break;
            case 5: F.a[1] = lds_b128(pa + 2048); break;
            case 6: F.a[2] = lds_b128(pa + 4096); break;
            case 7: F.a[3] = lds_b128(pa + 6144); break;
            case 8: F.b[1][0][0] = lds_tr(pb0 + 8192); break;
            case 9: F.b[1][0][1] = lds_tr(pb0 + 8192 + 512); break;
            case 10: F.b[1][1][0] = lds_tr(pb1 + 8192); break;
            case 11: F.b[1][1][1] = lds_tr(pb1 + 8192 + 512); break;
            case 12: F.b[2][0][0] = lds_tr(pb0 + 16384); break;
            case 13: F.b[2][0][1] = lds_tr(pb0 + 16384 + 512); break;
            case 14: F.b[2][1][0] = lds_tr(pb1 + 16384); break;
            case 15: F.b[2][1][1] = lds_tr(pb1 + 16384 + 512); break;
            case 16: if (LOCAL) F.h = lds_b128(pa + 16384 - WM.value * 8192); break;
            default: break;
        }
    };
    auto mm = [](f32x4& c, bf16x8 a, bf16x8 b) { asm volatile("v_mfma_f32_16x16x32_bf16 %0, %1, %2, %0" : "+v"(c) : "v"(a), "v"(b)); };
    auto half = [&](auto WM, const Frags& C, Frags& N, bool doread, lds_cptr pa, lds_cptr pb0, lds_cptr pb1, auto tail, int G0 = 0, int G1 = 3) {
#pragma unroll
        for (int G = G0; G < G1; ++G) {
            const bf16x8 b0 = cat8(C.b[G][0][0], C.b[G][0][1]), b1 = cat8(C.b[G][1][0], C.b[G][1][1]);
#pragma unroll
            for (int t = 0; t < 4; ++t) {
                const int n = (G - G0) * 8 + t * 2;
                if (G < 2) mm(acc[G][t][0], b0, C.a[t]); else mm(acc[G][t][0], C.a[t], b0);
                SBAR();
                if (doread) rd(WM, N, pa, pb0, pb1, n);
                tail(n);
                SBAR();
                if (G < 2) mm(acc[G][t][1], b1, C.a[t]); else mm(acc[G][t][1], C.a[t], b1);
                SBAR();
                if (doread) rd(WM, N, pa, pb0, pb1, n + 1);
                tail(n + 1);
                SBAR();
            }
        }
        if (LOCAL) {
            if (WM.value == 0) { mm(acch[0], cat8(C.b[1][0][0], C.b[1][0][1]), C.h); mm(acch[1], cat8(C.b[1][1][0], C.b[1][1][1]), C.h); }
            else               { mm(acch[0], C.h, cat8(C.b[2][0][0], C.b[2][0][1])); mm(acch[1], C.h, cat8(C.b[2][1][0], C.b[2][1][1])); }
            SBAR();
        }
    };

    Frags F0, F1;
    f32x4 cq[2], ck[2], mu4[5], rs4[5]; float cv[2], mu[5], rs[5];
    u32x4 qfr[4], kfr[4];
    u32x4 va[2][2];
    f32x4 mwin, mfirst;
    typedef __attribute__((address_space(3))) u32x4* lds_u4;
    const bool negpos = (wm == 0) && (panel == 0);
    VM_BAR(0);
    unsigned cm01 = 0, cm23 = 0;
    lds_cptr ka[2] = {sm, sm}, vo2[2] = {sm, sm};
    lds_u4 ex = (lds_u4)(smw + OFF_EX + wn * 6144) + lane;
    lds_cptr s1 = sm + C::OFF_MEAN + wm * 256 + l15 * 4, s4 = sm + C::OFF_MEAN + wm * 256 + g * 16;
    lds_cptr c1 = sm + C::OFF_CS + wn * 128 + l15 * 4, c4 = sm + C::OFF_CS + wn * 128 + g * 16;
    asm volatile("" : "+v"(s1), "+v"(s4), "+v"(c1), "+v"(c4));
    if (LOCAL) {
        unsigned keepv = negpos ? __float_as_uint(NEG_MASK) : 0u;
        asm volatile("" : "+s"(keepv));
        u32x4 mw, mf;
        unsigned prv[4];
#pragma unroll
        for (int r = 0; r < 4; ++r) {
            const int d = l15 - (4 * g + r);
            unsigned out = (unsigned)(((d << 28) >> 31));
            prv[r] = (unsigned)(d >> 31);
            asm volatile("" : "+v"(out), "+v"(prv[r]));
            mw[r] = out & 0xff800000u;
            mf[r] = (out & 0xff800000u) | (~out & keepv);
        }
        const lds_cptr e0 = sm + OFF_EX + wn * 6144;
        ka[0] = e0 + (wm ? lane : 256 + (lane ^ 8)) * 16;         ka[1] = e0 + (wm ? 64 + lane : 256 + lane) * 16;
        vo2[0] = e0 + (wm ? 128 + lane : 320 + (lane ^ 32)) * 16; vo2[1] = e0 + (wm ? 192 + lane : 320 + lane) * 16;
        cm01 = ~((prv[0] >> 16) | (prv[1] << 16)); cm23 = ~((prv[2] >> 16) | (prv[3] << 16));
        mwin = __builtin_bit_cast(f32x4, mw); mfirst = __builtin_bit_cast(f32x4, mf);
        asm volatile("" : "+v"(mwin), "+v"(mfirst), "+v"(cm01), "+v"(cm23), "+v"(ka[0]), "+v"(ka[1]), "+v"(vo2[0]), "+v"(vo2[1]), "+v"(ex));
    }
    typedef const __attribute__((address_space(3))) f32x4* lds_f4; typedef const __attribute__((address_space(3))) float* lds_f1;
    auto mainloop = [&](auto WM) {
#pragma unroll
        for (int a = 0; a < 3; ++a)
            asm volatile("" : "+v"(acc[a][0][0]), "+v"(acc[a][0][1]), "+v"(acc[a][1][0]), "+v"(acc[a][1][1]), "+v"(acc[a][2][0]), "+v"(acc[a][2][1]),
                         "+v"(acc[a][3][0]), "+v"(acc[a][3][1]));
        int ty = lane_id();
        const int l15 = ty & 15, g = ty >> 4;
        const int swA = (l15 >> 1) & 7;
        const int q4 = l15 >> 2, p4 = l15 & 3, fk = ((q4 >> 1) & 1) | ((g & 1) << 1);
        lds_cptr pA0 = sm + wm * 8192 + l15 * 128 + ((0 + g) ^ swA) * 16, pA1 = sm + wm * 8192 + l15 * 128 + ((4 + g) ^ swA) * 16;
        lds_cptr pB0 = sm + ABYTES + (8 * g + q4) * 128 + (((2 * wn + 0) ^ fk) * 32) + p4 * 8;
        lds_cptr pB1 = sm + ABYTES + (8 * g + q4) * 128 + (((2 * wn + 1) ^ fk) * 32) + p4 * 8;
        asm volatile("" : "+v"(acch[0]), "+v"(acch[1]), "+v"(pA0), "+v"(pA1), "+v"(pB0), "+v"(pB1));
        slow_warm((unsigned)(uintptr_t)smem + OFF_DUMP, W, w, ty);
        BAR();
        STAMP(LOCAL ? 1 : 7);
        BAR();
#pragma unroll
        for (int n = 0; n < 17; ++n) rd(WM, F0, pA0, pB0, pB1, n);
        WAIT_LGKM();
        int off = 0;
        lds_cptr a = pA1, b0 = pB0, b1 = pB1;
#pragma unroll 3
        for (int t = 0; t < NT - 1; ++t) {
            half(WM, F0, F1, true, a, b0 + 4096, b1 + 4096, [&](int n) {
                if (n == 17) { off = (off == 2 * STAGE) ? 0 : off + STAGE; asm volatile("" : "+s"(off)); }
                if (n == 19) { b0 = pB0 + off; asm volatile("" : "+v"(b0)); }
                if (n == 20) { a = pA0 + off; asm volatile("" : "+v"(a)); }
                if (n == 21) { b1 = pB1 + off; asm volatile("" : "+v"(b1)); }
            });
            WAIT_LGKM_BAR();
            half(WM, F1, F0, true, a, b0, b1, [&](int n) {
                if (n == 19) { a = pA1 + off; asm volatile("" : "+v"(a)); }
                if (n == 22) __builtin_amdgcn_s_waitcnt(0xC07F);
            });
        }
        half(WM, F0, F1, true, a, b0 + 4096, b1 + 4096, [](int) {});
        WAIT_LGKM_BAR();
        half(WM, F1, F0, false, sm, sm, sm, [&](int n) {
            constexpr int RS = C::OFF_RSTD - C::OFF_MEAN, H = 512 - WM.value * 256;
            if (n < 2) { cq[n] = *(lds_f4)(c4 + n * 64); ck[n] = *(lds_f4)(c4 + 256 + n * 64); }
            else if (n < 6) { mu[n - 2] = *(lds_f1)(s1 + (n - 2) * 64); rs[n - 2] = *(lds_f1)(s1 + RS + (n - 2) * 64); }
            else if (n < 10) { mu4[n - 6] = *(lds_f4)(s4 + (n - 6) * 64); rs4[n - 6] = *(lds_f4)(s4 + RS + (n - 6) * 64); }
            else if (n == 10) { cv[0] = *(lds_f1)(c1 + 512); cv[1] = *(lds_f1)(c1 + 512 + 64); }
            else if (LOCAL && n == 11) {
                if (WM.value == 0) { mu[4] = *(lds_f1)(s1 + H); rs[4] = *(lds_f1)(s1 + RS + H); }
                else { mu4[4] = *(lds_f4)(s4 + H); rs4[4] = *(lds_f4)(s4 + RS + H); }
            } else if (n == (LOCAL ? 22 : 15)) __builtin_amdgcn_s_waitcnt(0xC07F);
        }, LOCAL ? 0 : 1);
        asm volatile("s_nop 7");
        SBAR();
        if (LOCAL) {
            auto fix = [&](int a, int t) {
#pragma unroll
                for (int f = 0; f < 2; ++f)
                    acc[a][t][f] = (a == 2) ? rs4[t] * (acc[2][t][f] + mu4[t] * cv[f]) : rs[t] * (acc[a][t][f] + mu[t] * (a ? ck[f] : cq[f]));
            };
            auto pk = [&](const f32x4 (&v)[2]) { return u32x4{pack_bf16(v[0][0], v[0][1]), pack_bf16(v[0][2], v[0][3]), pack_bf16(v[1][0], v[1][1]), pack_bf16(v[1][2], v[1][3])}; };
            if (!ident) {
                fix(1, 1); fix(1, 3); fix(2, 1); fix(2, 3);
#pragma unroll
                for (int f = 0; f < 2; ++f)
                    acch[f] = (WM.value == 0) ? rs[4] * (acch[f] + mu[4] * ck[f]) : rs4[4] * (acch[f] + mu4[4] * cv[f]);
            }
            auto pkv = [&](int t) {
#pragma unroll
                for (int f = 0; f < 2; ++f) {
                    va[t >> 1][f][(t & 1) * 2] = pack_bf16(acc[2][t][f][0], acc[2][t][f][1]);
                    va[t >> 1][f][(t & 1) * 2 + 1] = pack_bf16(acc[2][t][f][2], acc[2][t][f][3]);
                }
            };
            kfr[1] = pk(acc[1][1]); kfr[3] = pk(acc[1][3]); pkv(1); pkv(3);
            u32x4 hf = pk(acch);
            if (WM.value == 0) {
                ex[0] = kfr[1]; ex[64] = kfr[3];
#pragma unroll
                for (int u = 0; u < 2; ++u)
                    asm volatile("ds_write_b64 %0, %1 offset:%3\n\tds_write_b64 %0, %2 offset:%3+8" :: "v"(ex), "v"(u32x2{va[u][0][2], va[u][0][3]}),
                                 "v"(u32x2{va[u][1][2], va[u][1][3]}), "n"(2048 + u * 1024) : "memory");
                if (negpos) hf = u32x4{0, 0, 0, 0};
            }
            ex[256 + WM.value * 64] = hf;
            SBAR();
            if (!ident) { fix(0, 0); fix(0, 1); fix(0, 2); fix(0, 3); fix(1, 0); fix(1, 2); fix(2, 0); fix(2, 2); }
#pragma unroll
            for (int t = 0; t < 4; ++t) qfr[t] = pk(acc[0][t]);
            kfr[0] = pk(acc[1][0]); kfr[2] = pk(acc[1][2]); pkv(0); pkv(2);
            asm volatile("" : "+v"(qfr[0]), "+v"(qfr[1]), "+v"(qfr[2]), "+v"(qfr[3]), "+v"(kfr[0]), "+v"(kfr[2]),
                         "+v"(va[0][0]), "+v"(va[0][1]), "+v"(va[1][0]), "+v"(va[1][1]));
        }
    };
    const int head = ct * 2 + wn;
    unsigned qo = 0, ko = 0, vo = 0;
    if (LOCAL) {
        qo = (unsigned)((((panel * 8 + wm * 4) * 32 + head) * 64 + lane) * 16);
        asm volatile("" : "+v"(qo));
    } else {
        ko = (unsigned)((wm * 8 + (g & 1)) * 1024 + ((l15 & 7) + ((l15 & 8) << 1) + 32 * (g >> 1)) * 16);
        vo = (unsigned)((wm * 8 + 2 + (g >> 1)) * 1024 + (l15 + 32 * (g & 1)) * 16);
        asm volatile("" : "+v"(ko), "+v"(vo));
    }
    if (LOCAL && wm) mainloop(IC<1>{}); else mainloop(IC<0>{});

    if (!LOCAL) {
        auto pk = [&](f32x4 a, f32x4 b) { return u32x4{pack_bf16(a[0], a[1]), pack_bf16(a[2], a[3]), pack_bf16(b[0], b[1]), pack_bf16(b[2], b[3])}; };
        char* kvp = (char*)kvf + (size_t)(head * 16 + panel) * 16384;
        lds_ptr own = smw + wn * 16384;
        SBAR();
#pragma unroll
        for (int t = 0; t < 4; ++t) {
            const u32x4 kk = pk(rs[t] * (acc[1][t][0] + mu[t] * ck[0]), rs[t] * (acc[1][t][1] + mu[t] * ck[1]));
            if (t & 1) st16o<128>(kvp + (t >> 1) * 4096, ko, kk); else st16o<0>(kvp + (t >> 1) * 4096, ko, kk);
            kfr[t] = kk;
            SBAR();
            const int u = t >> 1, f = t & 1;
            const u32x4 vv = pk(rs4[2 * u] * (acc[2][2 * u][f] + mu4[2 * u] * cv[f]), rs4[2 * u + 1] * (acc[2][2 * u + 1][f] + mu4[2 * u + 1] * cv[f]));
            if (f) st16o<256>(kvp + u * 4096, vo, vv); else st16o<0>(kvp + u * 4096, vo, vv);
            qfr[t] = vv;
            SBAR();
        }
        half(IC<0>{}, F1, F0, false, sm, sm, sm, [&](int n) {
            const int t = n >> 1;
            if (n & 1) *(lds_u4)(own + vo + (t >> 1) * 4096 + (t & 1) * 256) = qfr[t];
            else       *(lds_u4)(own + ko + (t >> 1) * 4096 + (t & 1) * 128) = kfr[t];
        }, 0, 1);
        asm volatile("s_nop 7");
        SBAR();
#pragma unroll
        for (int u = 0; u < 2; ++u) {
            const float r0 = rs[2 * u] * QSCALE, r1 = rs[2 * u + 1] * QSCALE;
            const u32x4 qa = pk(r0 * (acc[0][2 * u][0] + mu[2 * u] * cq[0]), r0 * (acc[0][2 * u][1] + mu[2 * u] * cq[1]));
            const u32x4 qb = pk(r1 * (acc[0][2 * u + 1][0] + mu[2 * u + 1] * cq[0]), r1 * (acc[0][2 * u + 1][1] + mu[2 * u + 1] * cq[1]));
#pragma unroll
            for (int e = 0; e < 4; ++e) {
                const auto x = __builtin_amdgcn_permlane16_swap(qa[e], qb[e], false, false);
                qo4[2 * u][e] = x[0]; qo4[2 * u + 1][e] = x[1];
            }
        }
        return;
    }
    {
        const f32x4 zero4 = {0, 0, 0, 0};
        auto bf8 = [](u32x4 v) { return __builtin_bit_cast(bf16x8, v); };
        f32x4 sp[4], sc[4];
        sc[1] = MFMA16(bf8(kfr[1]), bf8(qfr[1]), mwin); sp[1] = MFMA16(bf8(kfr[0]), bf8(qfr[1]), mwin);
        sc[3] = MFMA16(bf8(kfr[3]), bf8(qfr[3]), mwin); sp[3] = MFMA16(bf8(kfr[2]), bf8(qfr[3]), mwin);
        asm volatile("" : "+v"(sc[1]), "+v"(sp[1]), "+v"(sc[3]), "+v"(sp[3]));
        WAIT_LGKM_BAR();
        const bf16x8 kp[2] = {lds_b128(ka[0]), lds_b128(ka[1])};
        u32x2 vp[2][2];
#pragma unroll
        for (int u = 0; u < 2; ++u)
            asm volatile("ds_read_b64 %0, %2\n\tds_read_b64 %1, %2 offset:8" : "=&v"(vp[u][0]), "=&v"(vp[u][1]) : "v"(vo2[u]) : "memory");
        sc[0] = MFMA16(bf8(kfr[0]), bf8(qfr[0]), mwin); sc[2] = MFMA16(bf8(kfr[2]), bf8(qfr[2]), mwin);
        SBAR();
        float x[4][4], m[4];
        unsigned a[4][2];
        auto logits = [&](int t0, int st) {
#pragma unroll
            for (int t = t0; t < 4; t += 2)
#pragma unroll
                for (int h = 0; h < 2; ++h) {
                    if (st == 0) {
                        const unsigned uc = pack_bf16(sc[t][2 * h], sc[t][2 * h + 1]), up = pack_bf16(sp[t][2 * h], sp[t][2 * h + 1]);
                        asm("v_bfi_b32 %0, %1, %2, %3" : "=v"(a[t][h]) : "v"(h ? cm23 : cm01), "v"(uc), "v"(up));
                    } else if (st == 1) { x[t][2 * h] = bf_lo(a[t][h]) * ATT_SCALE; x[t][2 * h + 1] = bf_hi(a[t][h]) * ATT_SCALE; }
                    else if (st == 2) a[t][h] = pack_bf16(x[t][2 * h], x[t][2 * h + 1]);
                    else { x[t][2 * h] = bf_lo(a[t][h]); x[t][2 * h + 1] = bf_hi(a[t][h]); }
                }
            SBAR();
        };
        logits(1, 0); logits(1, 1);
        sp[0] = MFMA16(kp[0], bf8(qfr[0]), mfirst); sp[2] = MFMA16(kp[1], bf8(qfr[2]), mfirst);
        SBAR();
        logits(1, 2); logits(1, 3);
        logits(0, 0); logits(0, 1); logits(0, 2); logits(0, 3);
        auto across = [&](float (&v)[4], auto op, auto fin) {
            const auto p = __builtin_amdgcn_permlane16_swap(__float_as_uint(v[0]), __float_as_uint(v[1]), false, false);
            const auto q = __builtin_amdgcn_permlane16_swap(__float_as_uint(v[2]), __float_as_uint(v[3]), false, false);
            const auto r = __builtin_amdgcn_permlane32_swap(__float_as_uint(op(__uint_as_float(p[0]), __uint_as_float(p[1]))),
                                                            __float_as_uint(op(__uint_as_float(q[0]), __uint_as_float(q[1]))), false, false);
            const unsigned z = __float_as_uint(fin(op(__uint_as_float(r[0]), __uint_as_float(r[1]))));
            const auto u = __builtin_amdgcn_permlane32_swap(z, z, false, false);
            const auto e = __builtin_amdgcn_permlane16_swap(u[0], u[0], false, false), f = __builtin_amdgcn_permlane16_swap(u[1], u[1], false, false);
            v[0] = __uint_as_float(e[0]); v[1] = __uint_as_float(e[1]); v[2] = __uint_as_float(f[0]); v[3] = __uint_as_float(f[1]);
        };
#pragma unroll
        for (int t = 0; t < 4; ++t) m[t] = amax3(x[t][0], x[t][1], x[t][2]);
        asm("v_max_f32 %0, %0, %4\n\tv_max_f32 %1, %1, %5\n\tv_max_f32 %2, %2, %6\n\tv_max_f32 %3, %3, %7\n\ts_nop 0"
            : "+v"(m[0]), "+v"(m[1]), "+v"(m[2]), "+v"(m[3]) : "v"(x[0][3]), "v"(x[1][3]), "v"(x[2][3]), "v"(x[3][3]));
        across(m, [](float p, float q) { float r; asm("v_max_f32 %0, %1, %2\n\ts_nop 0" : "=v"(r) : "v"(p), "v"(q)); return r; },
               [](float p) { return p * -LOG2E; });
        SBAR();
#pragma unroll
        for (int t = 0; t < 4; ++t)
#pragma unroll
            for (int r = 0; r < 4; ++r) x[t][r] = __builtin_fmaf(x[t][r], LOG2E, m[t]);
        SBAR();
#pragma unroll
        for (int t = 0; t < 4; ++t)
#pragma unroll
            for (int r = 0; r < 4; ++r) x[t][r] = __builtin_amdgcn_exp2f(x[t][r]);
        SBAR();
#pragma unroll
        for (int t = 0; t < 4; ++t) m[t] = (x[t][0] + x[t][1]) + (x[t][2] + x[t][3]);
        asm volatile("s_waitcnt lgkmcnt(0)" : "+v"(vp[0][0]), "+v"(vp[0][1]), "+v"(vp[1][0]), "+v"(vp[1][1]) :: "memory");
        u32x4 ve[2][2];
#pragma unroll
        for (int u = 0; u < 2; ++u)
#pragma unroll
            for (int f = 0; f < 2; ++f) ve[u][f] = u32x4{vp[u][f][0], vp[u][f][1], va[u][f][0], va[u][f][1]};
        across(m, [](float p, float q) { return p + q; }, [](float p) { return __builtin_amdgcn_rcpf(p); });
        asm volatile("" : "+v"(ve[0][0]), "+v"(ve[0][1]), "+v"(ve[1][0]), "+v"(ve[1][1]));
        SBAR();
#pragma unroll
        for (int t = 0; t < 4; ++t)
#pragma unroll
            for (int r = 0; r < 4; ++r) x[t][r] *= m[t];
        SBAR();
#pragma unroll
        for (int t = 0; t < 4; ++t)
#pragma unroll
            for (int h = 0; h < 2; ++h) a[t][h] = pack_bf16(x[t][2 * h], x[t][2 * h + 1]);
        SBAR();
        f32x4 o0[4], o1[4];
#pragma unroll
        for (int t = 0; t < 4; ++t) {
            const bf16x8 pb = bf8(u32x4{a[t][0] & ~cm01, a[t][1] & ~cm23, a[t][0] & cm01, a[t][1] & cm23});
            o0[t] = MFMA16(bf8((t & 1) ? va[t >> 1][0] : ve[t >> 1][0]), pb, zero4);
            o1[t] = MFMA16(bf8((t & 1) ? va[t >> 1][1] : ve[t >> 1][1]), pb, zero4);
        }
        SBAR();
#pragma unroll
        for (int t = 0; t < 4; ++t)
            st16(ofrag + t * 16384, qo, u32x4{pack_bf16(o0[t][0], o0[t][1]), pack_bf16(o0[t][2], o0[t][3]),
                                                 pack_bf16(o1[t][0], o1[t][1]), pack_bf16(o1[t][2], o1[t][3])});
    }
}

DI int out_row(int panel, bool localrows, int T, int a) {
    return localrows ? (panel * 128 + (T >> 2) * 64 + 2 * (16 * (T & 1) + a) + ((T >> 1) & 1)) : (panel * 128 + T * 16 + a);
}
DI int out_swz(int k) { return ((k >> 1) & 1) | (k & 4); }
DI unsigned wo_off(int w, int lane) {
    const int kl = lane >> 3;
    unsigned v = (unsigned)(w * 8 + kl) * 2048u + (unsigned)(ids().co * 128 + ((lane & 7) ^ out_swz(kl)) * 16);
    asm volatile("" : "+v"(v));
    return v;
}
DI void wo_load(const void* Wo, unsigned voff, int i0, int i1, unsigned lds0, int w) {
#pragma unroll
    for (int i = i0; i < i1; ++i) glds16((const char*)Wo + (size_t)i * 65536, voff, lds0 + (w + 4 * i) * 1024);
}
DI unsigned res_off(bool localrows, bool tiled, int lane) {
    const Ids I = ids();
    unsigned v = (unsigned)(I.po * 262144 + I.co * (tiled ? 16384 : 128) + out_row(0, localrows, 0, lane >> 3) * (tiled ? 128 : 2048) +
                            (((lane & 7) ^ (lane >> 3)) * 16));
    asm volatile("" : "+v"(v));
    return v;
}
DI void res_load(u32x4 (&res)[6], const void* Xin, bool localrows, bool tiled, unsigned off, int w) {
#pragma unroll
    for (int i = 0; i < 6; ++i) {
        const int j = min(w + 3 * i, 15);
        asm volatile("global_load_dwordx4 %0, %1, %2 sc1" : "=&v"(res[i]) : "v"(off), "s"((const char*)Xin + out_row(0, localrows, j >> 1, (j & 1) * 8) * (tiled ? 128 : 2048)) : "memory");
    }
}
DI void out_handoff(char* smem, int xoff, u32x4 (&res)[6], int w, char* ws, int set, u64 tag, int lane, u32x4 (&c)[8], int mat) {
    const Ids I = ids();
    u64 land;
    lds_ptr dst = (lds_ptr)smem + xoff + w * 1024 + lane * 16;
    asm volatile("" : "+v"(dst));
    if (w == 3) __builtin_amdgcn_s_setprio(3);
    signal_poll<false, false>(w - 3, ws, flag_off(set, I.pq, lane & 15, I.cq), flag_off(set, I.po, I.co, lane & 15), 0xffffull, tag, land);
    if (w < 3) {
        asm volatile("s_waitcnt vmcnt(0)" : "+v"(res[0]), "+v"(res[1]), "+v"(res[2]), "+v"(res[3]), "+v"(res[4]), "+v"(res[5]) :: "memory");
#pragma unroll
        for (int i = 0; i < 5; ++i) *(__attribute__((address_space(3))) u32x4*)(dst + i * 3072) = res[i];
        if (w == 0) *(__attribute__((address_space(3))) u32x4*)(dst + 15360) = res[5];
        if (mat >= 0) colsum_store(c, ws, mat, w, lane);
    }
    WAIT_LGKM_BAR();
    asm volatile("s_waitcnt vmcnt(0)" : "+v"(land) :: "memory");
    if (w < 3 && mat >= 0) flag_store(ws, (mat ? 10 : 13) + w, I.cq, I.pq, tag, lane);
    __builtin_amdgcn_s_setprio(0);
}

template <bool AFTER_LOCAL>
DI void out_compute(char* smem, int w, const __hip_bfloat16* __restrict__ ofrag,
                    __hip_bfloat16* __restrict__ Xout, float* __restrict__ statp SP_PARAM) {
    constexpr int XOFF = AFTER_LOCAL ? 147456 : 98304;
    STAMP(AFTER_LOCAL ? 3 : 10);
    VM_BAR(0);
    const int lane = lane_id();
    asm volatile("" : "+s"(w));
    const Ids I = ids();
    const int panel = I.po, ct = I.co;
    const int l15 = lane & 15, g = lane >> 4;
    const bool tiled = AFTER_LOCAL || statp != nullptr;
    const int rstr = tiled ? 128 : 2048;
    const int q4 = l15 >> 2, p4 = l15 & 3;
    const int krow = 4 * g + q4;
    const int sw = out_swz(krow);
    const lds_cptr sm = (lds_cptr)smem;
    lds_cptr bw[4], xo[4];
#pragma unroll
    for (int t = 0; t < 4; ++t) {
        bw[t] = sm + krow * 128 + ((((t >> 1) * 4 + (p4 >> 1) * 2 + (t & 1)) ^ sw) * 16) + (p4 & 1) * 8;
        xo[t] = sm + XOFF + (w * 32 + l15) * 128 + ((((t >> 1) * 4 + (g >> 1) * 2 + (t & 1)) ^ (l15 & 7)) * 16) + (g & 1) * 8;
    }
    unsigned so[2], sto, ao[2];
#pragma unroll
    for (int r2 = 0; r2 < 2; ++r2) {
        so[r2] = (unsigned)(panel * 262144 + ct * (tiled ? 16384 : 128) + out_row(0, AFTER_LOCAL, 2 * w + r2, l15 & 7) * rstr + (l15 >> 3) * 64 + g * 16);
        ao[r2] = (unsigned)((panel * 8 + 2 * w + r2) * 32768 + lane * 16);
    }
    sto = (unsigned)(((panel * 16 + ct) * 128 + 2 * w * 16 + l15) * 8);
    asm volatile("" : "+v"(bw[0]), "+v"(bw[1]), "+v"(bw[2]), "+v"(bw[3]), "+v"(xo[0]), "+v"(xo[1]), "+v"(xo[2]), "+v"(xo[3]),
                 "+v"(so[0]), "+v"(so[1]), "+v"(sto), "+v"(ao[0]), "+v"(ao[1]));
    bf16x8 ones;
    {
        unsigned o1; asm volatile("v_mov_b32 %0, 0x3f803f80" : "=v"(o1));
        ones = __builtin_bit_cast(bf16x8, (u32x4{o1, o1, o1, o1}));
        asm volatile("" : "+v"(ones));
    }
    const __amdgpu_buffer_rsrc_t ra = __builtin_amdgcn_make_buffer_rsrc((void*)ofrag, 0, 4 << 20, 0x00020000);
    asm volatile("" :: "s"(ra));
    u32x32 fr;
    BAR();
    STAMP(AFTER_LOCAL ? 4 : 11);
    u32x32 aw0, aw1;
    asm volatile(".irp j,0,1,2,3\n\tbuffer_load_dwordx4 v[64+8*\\j:67+8*\\j], %2, %4, 0 offen offset:1024*\\j sc1\n\t"
                 "buffer_load_dwordx4 v[68+8*\\j:71+8*\\j], %3, %4, 0 offen offset:1024*\\j sc1\n\t.endr\n\t"
                 ".irp j,0,1\n\tbuffer_load_dwordx4 v[96+8*\\j:99+8*\\j], %2, %4, %5 offen offset:1024*\\j sc1\n\t"
                 "buffer_load_dwordx4 v[100+8*\\j:103+8*\\j], %3, %4, %5 offen offset:1024*\\j sc1\n\t.endr"
                 : "=&{v[64:95]}"(aw0), "=&{v[96:127]}"(aw1) : "v"(ao[0]), "v"(ao[1]), "s"(ra), "s"(4096) : "memory");
    f32x16 acc[2];
#pragma unroll
    for (int r2 = 0; r2 < 2; ++r2)
#pragma unroll
        for (int c = 0; c < 4; ++c) {
            const u32x2 xr = *(const __attribute__((address_space(3))) u32x2*)(xo[c] + r2 * 2048);
            acc[r2][4 * c] = bf_lo(xr[0]); acc[r2][4 * c + 1] = bf_hi(xr[0]); acc[r2][4 * c + 2] = bf_lo(xr[1]); acc[r2][4 * c + 3] = bf_hi(xr[1]);
        }
    asm volatile(
        ".macro OUT_MM r, t, j\n\t"
        "v_mfma_f32_16x16x32_bf16 v[16*\\r+4*\\t:16*\\r+4*\\t+3], v[32+16*((\\j)&1)+4*\\t:35+16*((\\j)&1)+4*\\t], v[64+8*\\j+4*\\r:67+8*\\j+4*\\r], v[16*\\r+4*\\t:16*\\r+4*\\t+3]\n\t"
        ".endm\n\t"
        ".macro OUT_RD t, hi, j, on\n\t"
        ".if \\on\n\tds_read_b64_tr_b16 v[32+16*((\\j)&1)+4*\\t+2*\\hi:33+16*((\\j)&1)+4*\\t+2*\\hi], v[128+\\t] offset:((\\j)&7)*4096+2048*\\hi\n\t.endif\n\t"
        ".endm\n\t"
        ".macro OUT_UP t, on\n\t.if \\on\n\tv_add_u32 v[128+\\t], s97, v[128+\\t]\n\t.endif\n\t.endm\n\t"
        ".macro OUT_STEP j, ld, rd, up, vm, lg\n\t"
        "OUT_MM 0, 0, \\j\n\tOUT_RD 3, 1, \\j+1, 1\n\tOUT_UP 0, \\up\n\t"
        ".if \\ld && (((\\j+6)&3) == 0)\n\ts_addk_i32 s96, 0x1000\n\t.endif\n\t"
        "OUT_MM 1, 0, \\j\n\tOUT_RD 0, 0, \\j+2, \\rd\n\t"
        ".if \\ld\n\tbuffer_load_dwordx4 v[64+8*((\\j+6)&7):67+8*((\\j+6)&7)], %[a0], %[ra], s96 offen offset:1024*((\\j+6)&3) sc1\n\t.endif\n\t"
        "OUT_MM 0, 1, \\j\n\tOUT_RD 0, 1, \\j+2, \\rd\n\tOUT_UP 1, \\up\n\t"
        "OUT_MM 1, 1, \\j\n\tOUT_RD 1, 0, \\j+2, \\rd\n\t"
        "OUT_MM 0, 2, \\j\n\tOUT_RD 1, 1, \\j+2, \\rd\n\tOUT_UP 2, \\up\n\t"
        "OUT_MM 1, 2, \\j\n\tOUT_RD 2, 0, \\j+2, \\rd\n\t"
        ".if \\ld\n\tbuffer_load_dwordx4 v[68+8*((\\j+6)&7):71+8*((\\j+6)&7)], %[a1], %[ra], s96 offen offset:1024*((\\j+6)&3) sc1\n\t.endif\n\t"
        "OUT_MM 0, 3, \\j\n\tOUT_RD 2, 1, \\j+2, \\rd\n\tOUT_UP 3, \\up\n\t"
        "OUT_MM 1, 3, \\j\n\tOUT_RD 3, 0, \\j+2, \\rd\n\t"
        "s_waitcnt vmcnt(\\vm) lgkmcnt(\\lg)\n\t"
        ".endm\n\t"
        ".macro OUT_PACK y, c\n\t"
        ".irp i,0,1,2,3,4,5,6,7\n\tv_cvt_pk_bf16_f32 v[\\y+\\i], v[\\c+2*\\i], v[\\c+2*\\i+1]\n\t.endr\n\t"
        ".irp i,0,1,4,5\n\tv_permlane16_swap_b32 v[\\y+\\i], v[\\y+\\i+2]\n\t.endr\n\t"
        ".irp i,0,1,2,3\n\tv_mov_b32 v[\\y+8+\\i], v[\\y+\\i]\n\t.endr\n\t"
        "v_mfma_f32_16x16x32_bf16 v[\\y+12:\\y+15], v[\\y:\\y+3], v[\\y:\\y+3], 0\n\t"
        "v_mov_b32_dpp v[\\y+8], v[\\y+4] row_ror:8 row_mask:0xf bank_mask:0xc\n\tv_mov_b32_dpp v[\\y+9], v[\\y+5] row_ror:8 row_mask:0xf bank_mask:0xc\n\t"
        "v_mfma_f32_16x16x32_bf16 v[\\y+16:\\y+19], %[one], v[\\y:\\y+3], 0\n\t"
        "v_mov_b32_dpp v[\\y+10], v[\\y+6] row_ror:8 row_mask:0xf bank_mask:0xc\n\tv_mov_b32_dpp v[\\y+11], v[\\y+7] row_ror:8 row_mask:0xf bank_mask:0xc\n\t"
        "v_mfma_f32_16x16x32_bf16 v[\\y+12:\\y+15], v[\\y+4:\\y+7], v[\\y+4:\\y+7], v[\\y+12:\\y+15]\n\t"
        ".endm\n\t"
        ".macro OUT_REST y\n\t"
        "v_mfma_f32_16x16x32_bf16 v[\\y+16:\\y+19], %[one], v[\\y+4:\\y+7], v[\\y+16:\\y+19]\n\t"
        ".irp i,0,1,2,3\n\tv_mov_b32_dpp v[\\y+4+\\i], v[\\y+\\i] row_ror:8 row_mask:0xf bank_mask:0x3\n\t.endr\n\t"
        ".endm\n\t"
        ".macro OUT_DIAG y\n\t"
        "v_cndmask_b32 v[\\y+12], v[\\y+12], v[\\y+13], %[m0]\n\tv_cndmask_b32 v[\\y+14], v[\\y+14], v[\\y+15], %[m0]\n\t"
        "v_cndmask_b32 v[\\y+17], v[\\y+12], v[\\y+14], %[m1]\n\t"
        ".endm\n\t"
        ".irp t,0,1,2,3\n\tOUT_RD \\t, 0, 0, 1\n\tOUT_RD \\t, 1, 0, 1\n\t.endr\n\t"
        ".irp t,0,1,2\n\tOUT_RD \\t, 0, 1, 1\n\tOUT_RD \\t, 1, 1, 1\n\t.endr\n\tOUT_RD 3, 0, 1, 1\n\t"
        "s_movk_i32 s96, 0x1000\n\ts_mov_b32 s97, 0x8000\n\t"
        "s_waitcnt vmcnt(10) lgkmcnt(0)\n"
        ".Lout_loop_%=:\n\t"
        "OUT_STEP 0, 1, 1, 0, 10, 7\n\tOUT_STEP 1, 1, 1, 0, 10, 7\n\tOUT_STEP 2, 1, 1, 0, 10, 7\n\tOUT_STEP 3, 1, 1, 0, 10, 7\n\t"
        "OUT_STEP 4, 1, 1, 0, 10, 7\n\t"
        "s_cmpk_eq_u32 s96, 0x6000\n\ts_cselect_b32 s97, %[last], s97\n\t"
        "OUT_STEP 5, 1, 1, 0, 10, 7\n\tOUT_STEP 6, 1, 1, 1, 10, 7\n\t"
        "OUT_STEP 7, 1, 1, 0, 10, 7\n\t"
        "s_cmpk_lg_u32 s96, 0x7000\n\ts_cbranch_scc1 .Lout_loop_%=\n\t"
        "OUT_STEP 0, 1, 1, 0, 10, 7\n\tOUT_STEP 1, 1, 1, 0, 10, 7\n\tOUT_STEP 2, 0, 1, 0, 8, 7\n\tOUT_STEP 3, 0, 1, 0, 6, 7\n\t"
        "OUT_STEP 4, 0, 1, 0, 4, 7\n\tOUT_STEP 5, 0, 1, 0, 2, 7\n\tOUT_STEP 6, 0, 0, 0, 0, 0\n\t"
        ".irp r,0,1\n\t.irp t,0,1,2,3\n\tOUT_MM \\r, \\t, 7\n\t.endr\n\t.endr\n\t"
        "s_nop 0\n\t"
        "OUT_PACK 64, 0\n\tglobal_store_dwordx4 %[s0], v[72:75], %[xa] sc1\n\tOUT_REST 64\n\tglobal_store_dwordx4 %[s0], v[68:71], %[xb] sc1\n\t"
        "OUT_PACK 84, 16\n\tglobal_store_dwordx4 %[s1], v[92:95], %[xa] sc1\n\tOUT_REST 84\n\tglobal_store_dwordx4 %[s1], v[88:91], %[xb] sc1\n\t"
        "s_cmp_eq_u64 %[st], 0\n\ts_cbranch_scc1 .Lout_end_%=\n\t"
        "OUT_DIAG 64\n\tOUT_DIAG 84\n\t"
        "s_mov_b64 exec, %[md]\n\t"
        "global_store_dwordx2 %[so], v[80:81], %[st] sc1\n\tglobal_store_dwordx2 %[so], v[100:101], %[st] offset:128 sc1\n\t"
        "s_mov_b64 exec, -1\n"
        ".Lout_end_%=:\n\t"
        "s_nop 1\n\t"
        ".purgem OUT_MM\n\t.purgem OUT_RD\n\t.purgem OUT_UP\n\t.purgem OUT_STEP\n\t.purgem OUT_PACK\n\t.purgem OUT_REST\n\t.purgem OUT_DIAG"
        : "+{v[0:15]}"(acc[0]), "+{v[16:31]}"(acc[1]), "+{v[64:95]}"(aw0), "+{v[96:127]}"(aw1),
          "+{v128}"(bw[0]), "+{v129}"(bw[1]), "+{v130}"(bw[2]), "+{v131}"(bw[3]), "=&{v[32:63]}"(fr)
        : [a0] "v"(ao[0]), [a1] "v"(ao[1]), [ra] "s"(ra), [last] "s"(AFTER_LOCAL ? 32768 : 65536), [one] "v"(ones), [s0] "v"(so[0]), [s1] "v"(so[1]),
          [xa] "s"(Xout), [xb] "s"((char*)Xout + (AFTER_LOCAL ? 16 : 8) * rstr), [st] "s"(statp), [so] "v"(sto),
          [m0] "s"(0xaaaaaaaaaaaaaaaaull), [m1] "s"(0xccccccccccccccccull), [md] "s"(0xf0000f0000f0000full)
        : "memory", "scc", "s96", "s97");
    STAMP(AFTER_LOCAL ? 5 : 12);
}

#define ROWSUM(p0, p1, c) __builtin_amdgcn_smfmac_f32_16x16x64_bf16(sel, __builtin_bit_cast(bf16v_t, __builtin_shufflevector(p0, p1, 0, 1, 2, 3, 4, 5, 6, 7)), c, sidx, 0, 0)
constexpr int ATT_STAGE = 32768;
typedef __attribute__((ext_vector_type(8))) unsigned u32x8;

DI void attn_loader(char* smem, int w, char* ws, int set, u64 tag, const __hip_bfloat16* __restrict__ kvf, const void* Wo, const void* Xin,
                    const void* Wcs SP_PARAM) {
    const int lane = lane_id();
    asm volatile("" : "+s"(w));
    const Ids I = ids();
    const int panel = I.pq, ct = I.cq;
    const unsigned lds0 = (unsigned)(uintptr_t)smem;
    const char* kvh = (const char*)kvf + (size_t)(ct * 2 + (w >> 1)) * 262144 + (w & 1) * 4096;
    const unsigned voff = (unsigned)(lane * 16);
    auto order = [&](int st) { return ((panel & 8) + st - ((st - 1 < (panel & 7)) ? 1 : 0)) & 15; };
    auto load = [&](int st) {
        const char* src = kvh + (size_t)order(st) * 16384;
        const unsigned dst = lds0 + (w >> 1) * 16384 + (w & 1) * 4096 + (st & 3) * ATT_STAGE;
#pragma unroll
        for (int u = 0; u < 2; ++u)
            asm volatile("s_mov_b32 m0, %0\n\ts_nop 0\n\tglobal_load_lds_dwordx4 %1, %2 sc1\n\tglobal_load_lds_dwordx4 %1, %2 offset:1024 sc1\n\t"
                         "global_load_lds_dwordx4 %1, %2 offset:2048 sc1\n\tglobal_load_lds_dwordx4 %1, %2 offset:3072 sc1"
                         :: "s"(dst + u * 8192), "v"(voff), "s"(src + u * 8192) : "memory");
    };
    const unsigned wv = wo_off(w, lane), rv = res_off(false, true, lane), cv = colsum_off(w, lane);
    const unsigned poff = flag_off(set, ct, panel, lane & 15);
    u64 land;
    __builtin_amdgcn_s_setprio(3);
    BAR();
    signal_poll<false, true>(w, ws, flag_off(set, ct, lane & 15, panel), poff, 1ull << order(1), tag, land);
    STAMP(9);
    load(1);
    asm volatile("global_load_dwordx2 %0, %1, %2 sc1\n\ts_waitcnt vmcnt(0)" : "+v"(land) : "v"(poff), "s"(ws + WS_FLAG) : "memory");
    STAMP(10);
    BAR();
    if (!__all(land == tag)) flag_poll(ws, poff, tag);
    load(2); load(3);
    __builtin_amdgcn_s_setprio(0);
    STAMP(11);
    VM_BAR(8); load(4);
    VM_BAR(0); load(5); load(6);
    VM_BAR(0); load(7); load(8);
    u32x4 res[6], ca[8];
    colsum_load(ca, w < 3 ? Wcs : nullptr, cv);
#pragma unroll 1
    for (int st = 6; st < 12; st += 2) {
        VM_BAR(0);
        load(st + 3); load(st + 4);
        if (st == 8) wo_load(Wo, wv, 24, 32, lds0 + 32768, w);
    }
    VM_BAR(0); load(15); wo_load(Wo, wv, 0, 8, lds0, w);
    res_load(res, Xin, false, true, rv, w);
    VM_BAR(0); wo_load(Wo, wv, 8, 24, lds0, w);
    STAMP(12);
    out_handoff(smem, 98304, res, w, ws, set + 1, tag, lane, ca, Wcs ? 0 : -1);
}

DI void attn_compute(char* smem, int w, const u32x4 (&q4)[4], const __hip_bfloat16* __restrict__ kvf, __hip_bfloat16* __restrict__ ofrag SP_PARAM) {
    const int lane = lane_id();
    asm volatile("" : "+s"(w));
    const int wm = w & 1, wn = w >> 1;
    const Ids I = ids();
    const int panel = I.pq, head = I.cq * 2 + wn;
    const int l15 = lane & 15;
    const unsigned se = ((l15 & 2) == 0 && (l15 & 4) == 0) ? 0x3f803f80u : 0u, so = ((l15 & 2) == 0 && (l15 & 4) != 0) ? 0x3f803f80u : 0u;
    const bf8_t sel = __builtin_bit_cast(bf8_t, u32x4{se, se, so, so});
    const int sidx = (l15 & 1) ? (int)0xEEEEEEEEu : 0x44444444;
    const lds_cptr sm = (lds_cptr)smem + wn * 16384 + lane * 16;
    const f32x16 z16 = {0, 0, 0, 0, 0, 0, 0, 0, 0, 0, 0, 0, 0, 0, 0, 0};
    const bf16x8 qA0 = __builtin_bit_cast(bf16x8, q4[0]), qA1 = __builtin_bit_cast(bf16x8, q4[1]);
    const bf16x8 qB0 = __builtin_bit_cast(bf16x8, q4[2]), qB1 = __builtin_bit_cast(bf16x8, q4[3]);
    f32x16 oA, oB, nmA, nmB, sA, sB;
    f32x4 lsA, lsB;
    u32x8 pB;
    bf16x8 v11;
    asm volatile(".irp r,106,108,110,112,134,136,138,140,170,172\n\tv_mov_b64 v[\\r:\\r+1], 0\n\t.endr"
                 : "={v[106:113]}"(pB), "={v[134:137]}"(lsA), "={v[138:141]}"(lsB), "={v[170:173]}"(v11));
    WAIT_LGKM_BAR();
    STAMP(8);
    bf16x8 k00 = lds_b128(sm), k10 = lds_b128(sm + 1024), k01 = lds_b128(sm + 4096), k11 = lds_b128(sm + 4096 + 1024);
    bf16x8 v00 = lds_b128(sm + 2048), v10 = lds_b128(sm + 3072), v01 = lds_b128(sm + 4096 + 2048);
    float ob;
    {
        const u32x4 vu = __builtin_bit_cast(u32x4, v00);
        unsigned vm = max(max(vu[0] & 0x7fff0000u, vu[1] & 0x7fff0000u), max(vu[2] & 0x7fff0000u, vu[3] & 0x7fff0000u));
        vm = min(vm, (unsigned)__builtin_amdgcn_update_dpp(0, (int)vm, 0xB1, 0xF, 0xF, true));
        vm = min(vm, (unsigned)__builtin_amdgcn_update_dpp(0, (int)vm, 0x4E, 0xF, 0xF, true));
        vm = min(vm, (unsigned)__builtin_amdgcn_update_dpp(0, (int)vm, 0x141, 0xF, 0xF, true));
        vm = min(vm, (unsigned)__builtin_amdgcn_update_dpp(0, (int)vm, 0x140, 0xF, 0xF, true));
        const auto vs = __builtin_amdgcn_permlane16_swap(vm, vm, false, false);
        ob = __uint_as_float(min(vs[0], vs[1])) * 0.015625f;
        sA = MFMA32(k00, qA0, z16); sB = MFMA32(k00, qB0, z16);
        sA = MFMA32(k10, qA1, sA); sB = MFMA32(k10, qB1, sB);
        SBAR(); VM_BAR(0); SBAR();
        float mA = sA[0], mB = sB[0];
#pragma unroll
        for (int r = 1; r < 16; ++r) { mA = fmaxf(mA, sA[r]); mB = fmaxf(mB, sB[r]); }
        mA = max_h(mA) + 16.f; mB = max_h(mB) + 16.f;
#pragma unroll
        for (int r = 0; r < 16; ++r) { nmA[r] = -mA; nmB[r] = -mB; sA[r] -= mA; oA[r] = ob; oB[r] = ob; }
    }
    const unsigned a0 = (unsigned)(uintptr_t)sm;
    unsigned dst = (unsigned)((((panel * 8 + wm * 4 + ((lane & 31) >> 4)) * 32 + head) * 64 + (lane & 15) + (lane >> 5) * 16) * 16);
    asm volatile("" : "+v"(dst));
    asm volatile(
        ".macro ATT_RD dst, y\n\t"
        "ds_read_b128 v[\\dst:\\dst+3], v[175+(((\\y)&0x1ffff)>>16)] offset:(\\y)&0xffff\n\t"
        ".endm\n\t"
        ".macro ATT_E4 c\n\t"
        "v_exp_f32 v[\\c], v[\\c]\n\tv_exp_f32 v[\\c+1], v[\\c+1]\n\tv_exp_f32 v[\\c+2], v[\\c+2]\n\tv_exp_f32 v[\\c+3], v[\\c+3]\n\t"
        ".endm\n\t"
        ".macro ATT_C4 p, c\n\t"
        "v_cvt_pk_bf16_f32 v[\\p], v[\\c], v[\\c+1]\n\tv_cvt_pk_bf16_f32 v[\\p+1], v[\\c+2], v[\\c+3]\n\t"
        "v_cvt_pk_bf16_f32 v[\\p+2], v[\\c+4], v[\\c+5]\n\tv_cvt_pk_bf16_f32 v[\\p+3], v[\\c+6], v[\\c+7]\n\t"
        ".endm\n\t"
        ".macro ATT_STEP cur, nxt, ka, kb, q, nm, oc, od, lsd, pc, pd, vd1, vc0, r0, y0, r1, y1, bar, rd\n\t"
        "v_mfma_f32_32x32x16_bf16 v[\\nxt:\\nxt+15], v[\\ka:\\ka+3], v[\\q:\\q+3], v[\\nm:\\nm+15]\n\t"
        "ATT_E4 \\cur\n\t"
        "v_mfma_f32_32x32x16_bf16 v[\\nxt:\\nxt+15], v[\\kb:\\kb+3], v[\\q+4:\\q+7], v[\\nxt:\\nxt+15]\n\t"
        "ATT_E4 \\cur+4\n\t"
        "v_mfma_f32_32x32x16_bf16 v[\\od:\\od+15], v[\\vd1:\\vd1+3], v[\\pd+4:\\pd+7], v[\\od:\\od+15]\n\t"
        "s_waitcnt lgkmcnt(0)\n\t"
        "ATT_C4 \\pc, \\cur\n\t"
        "v_exp_f32 v[\\cur+8], v[\\cur+8]\n\tv_exp_f32 v[\\cur+9], v[\\cur+9]\n\t"
        "v_mfma_f32_32x32x16_bf16 v[\\oc:\\oc+15], v[\\vc0:\\vc0+3], v[\\pc:\\pc+3], v[\\oc:\\oc+15]\n\t"
        ".if \\bar\n\ts_barrier\n\t.endif\n\t"
        "ATT_E4 \\cur+10\n\t"
        "v_smfmac_f32_16x16x64_bf16 v[\\lsd:\\lsd+3], v[130:133], v[\\pd:\\pd+7], v174\n\t"
        ".if \\rd & 4\n\tATT_RD 142, 32768\n\tATT_RD 150, 33792\n\tATT_RD 158, 34816\n\t.endif\n\t"
        ".if \\rd & 8\n\tATT_RD \\r1-8, \\y1-1024\n\t.endif\n\t"
        ".if \\rd & 1\n\tATT_RD \\r0, \\y0\n\t.endif\n\t"
        ".if \\rd & 2\n\tATT_RD \\r1, \\y1\n\t.endif\n\t"
        "v_exp_f32 v[\\cur+14], v[\\cur+14]\n\tv_exp_f32 v[\\cur+15], v[\\cur+15]\n\t"
        "ATT_C4 \\pc+4, \\cur+8\n\t"
        ".if \\rd & 4\n\ts_waitcnt lgkmcnt(3)\n\t.endif\n\t"
        ".endm\n\t"
        ".macro ATT_TILE p, y1, y2, bar, ra=3, rb=3\n\t"
        "ATT_STEP 66, 82, 142+\\p, 150+\\p, 122, 50, 2, 18, 138, 98, 106, 170-\\p, 158+\\p, 170-\\p, \\y1+3072, 142+\\p, \\y2, \\bar&1, \\ra\n\t"
        "ATT_STEP 82, 66, 146-\\p, 154-\\p, 114, 34, 18, 2, 134, 106, 98, 166+\\p, 158+\\p, 158+\\p, \\y2+2048, 150+\\p, \\y2+1024, \\bar&2, \\rb\n\t"
        ".endm\n\t"
        ".macro ATT_STAGE b, bar\n\t"
        "ATT_TILE 0, \\b+4096, \\b+8192, 0\n\t"
        "ATT_TILE 4, \\b+8192, \\b+12288, 0\n\t"
        ".if \\bar\n\tATT_TILE 0, \\b+12288, \\b+32768, 2, 1, 11\n\t.else\n\tATT_TILE 0, \\b+12288, \\b+32768, 0\n\t.endif\n\t"
        "ATT_TILE 4, \\b+32768, \\b+36864, 0\n\t"
        ".endm\n\t"
        "v_add_u32 v176, 0x10000, v175\n\t"
        "s_mov_b32 s96, 4\n\t"
        "s_waitcnt lgkmcnt(0)\n\t"
        "ATT_TILE 0, 4096, 8192, 0\n\tATT_TILE 4, 8192, 12288, 0\n\tATT_TILE 0, 12288, 0, 0, 1, 0\n\tATT_TILE 4, 32768, 36864, 1, 7, 3\n\t"
        "ATT_STAGE 32768, 1\n\t"
        "s_branch .Latt_entry_%=\n\t"
        ".p2align 6\n"
        ".Latt_loop_%=:\n\t"
        "ATT_STAGE 0, 1\n\tATT_STAGE 32768, 0\n"
        ".Latt_entry_%=:\n\t"
        "ATT_STAGE 65536, 1\n\t"
        "s_sub_u32 s96, s96, 1\n\t"
        "ATT_STAGE 98304, 0\n\t"
        "s_cmp_lg_u32 s96, 0\n\t"
        "s_cbranch_scc1 .Latt_loop_%=\n\t"
        "s_nop 1\n\t"
        "v_mfma_f32_32x32x16_bf16 v[18:33], v[170:173], v[110:113], v[18:33]\n\t"
        "v_smfmac_f32_16x16x64_bf16 v[138:141], v[130:133], v[106:113], v174\n\t"
        "s_waitcnt lgkmcnt(0)\n\ts_nop 10\n\t"
        ".purgem ATT_RD\n\t.purgem ATT_E4\n\t.purgem ATT_C4\n\t.purgem ATT_STEP\n\t.purgem ATT_TILE\n\t.purgem ATT_STAGE"
        : "+{v[2:17]}"(oA), "+{v[18:33]}"(oB), "+{v[66:81]}"(sA), "+{v[106:113]}"(pB), "+{v[134:137]}"(lsA), "+{v[138:141]}"(lsB),
          "+{v[142:145]}"(k00), "+{v[146:149]}"(k01), "+{v[150:153]}"(k10), "+{v[154:157]}"(k11),
          "+{v[158:161]}"(v00), "+{v[162:165]}"(v01), "+{v[166:169]}"(v10), "+{v[170:173]}"(v11)
        : "{v[34:49]}"(nmA), "{v[50:65]}"(nmB), "{v[114:117]}"(qA0), "{v[118:121]}"(qA1), "{v[122:125]}"(qB0), "{v[126:129]}"(qB1),
          "{v[130:133]}"(sel), "{v174}"(sidx), "{v175}"(a0)
        : "memory", "scc", "s96", "v176", "v82", "v83", "v84", "v85", "v86", "v87", "v88", "v89", "v90", "v91", "v92", "v93", "v94", "v95",
          "v96", "v97", "v98", "v99", "v100", "v101", "v102", "v103", "v104", "v105");
    STAMP(9);
    float lA = lsA[0] + lsA[1], lB = lsB[0] + lsB[1];
    auto store = [&] {
#pragma unroll
    for (int u = 0; u < 2; ++u) {
        const float inv = __builtin_amdgcn_rcpf(u ? lB : lA), sub = -ob * inv;
        const f32x16 e = (u ? oB : oA) * inv + sub;
        st16o<0>((char*)ofrag + u * 65536, dst, u32x4{pack_bf16(e[0], e[1]), pack_bf16(e[2], e[3]), pack_bf16(e[8], e[9]), pack_bf16(e[10], e[11])});
        st16o<512>((char*)ofrag + u * 65536, dst, u32x4{pack_bf16(e[4], e[5]), pack_bf16(e[6], e[7]), pack_bf16(e[12], e[13]), pack_bf16(e[14], e[15])});
    }
    };
    store();
    {
        const f32x16 os = oA + oB;
        const float osum = (((os[0] + os[1]) + (os[2] + os[3])) + ((os[4] + os[5]) + (os[6] + os[7]))) +
                           (((os[8] + os[9]) + (os[10] + os[11])) + ((os[12] + os[13]) + (os[14] + os[15])));
        const bool bad = !(fmaxf(lA, lB) < 1.0e24f) || !(lA > 1.0e-24f) || !(lB > 1.0e-24f) || !(fabsf(osum) < 3.0e38f);
                if (__any(bad)) {
            const __amdgpu_buffer_rsrc_t rk = __builtin_amdgcn_make_buffer_rsrc((char*)kvf + (size_t)head * 262144, 0, 262144, 0x00020000);
            oA = z16; oB = z16; lA = 0.f; lB = 0.f; ob = 0.f;
            float mA = -INFINITY, mB = -INFINITY;
#pragma unroll 1
            for (int t = 0; t < 64; ++t) {
                bf16x8 f[4];
#pragma unroll
                for (int i = 0; i < 4; ++i) f[i] = __builtin_bit_cast(bf16x8, __builtin_amdgcn_raw_buffer_load_b128(rk, lane * 16 + i * 1024, t * 4096, 16));
                auto upd = [&](bf16x8 q0, bf16x8 q1, f32x16& o, float& l, float& m) {
                    f32x16 s = MFMA32(f[0], q0, z16); s = MFMA32(f[1], q1, s);
                    float tm = s[0];
#pragma unroll
                    for (int r = 1; r < 16; ++r) tm = fmaxf(tm, s[r]);
                    tm = max_h(tm);
                    const float mn = fmaxf(m, tm);
                    const float alpha = __builtin_amdgcn_exp2f(m - mn);
                    m = mn;
#pragma unroll
                    for (int r = 0; r < 16; ++r) { s[r] = __builtin_amdgcn_exp2f(s[r] - mn); o[r] *= alpha; }
                    const u32x4 p0 = {pack_bf16(s[0], s[1]), pack_bf16(s[2], s[3]), pack_bf16(s[4], s[5]), pack_bf16(s[6], s[7])};
                    const u32x4 p1 = {pack_bf16(s[8], s[9]), pack_bf16(s[10], s[11]), pack_bf16(s[12], s[13]), pack_bf16(s[14], s[15])};
                    f32x4 t4 = {0, 0, 0, 0};
                    o = MFMA32(f[2], __builtin_bit_cast(bf16x8, p0), o);
                    o = MFMA32(f[3], __builtin_bit_cast(bf16x8, p1), o);
                    t4 = ROWSUM(p0, p1, t4);
                    l = l * alpha + (t4[0] + t4[1]);
                };
                upd(qA0, qA1, oA, lA, mA);
                upd(qB0, qB1, oB, lB, mB);
            }
            store();
        }
    }
}

DI void ln_row(const u32x4 (&v)[2], __hip_bfloat16* __restrict__ Y, unsigned off) {
    float s1 = 0.f, s2 = 0.f;
#pragma unroll
    for (int e = 0; e < 8; ++e) s1 = dot2bf(v[e >> 2][e & 3], 0x3f803f80u, s1);
    const float mean = wave_sum(s1) * (1.f / 1024.f);
#pragma unroll
    for (int e = 0; e < 8; ++e) {
        const float d0 = bf_lo(v[e >> 2][e & 3]) - mean, d1 = bf_hi(v[e >> 2][e & 3]) - mean;
        s2 += d0 * d0 + d1 * d1;
    }
    const float mb = round_bf16(mean);
    const float rden = __builtin_amdgcn_rcpf(round_bf16(__builtin_amdgcn_sqrtf(round_bf16(round_bf16(wave_sum(s2) * (1.f / 1024.f)) + 1e-5f))));
#pragma unroll
    for (int hh = 0; hh < 2; ++hh) {
        u32x4 y;
#pragma unroll
        for (int e = 0; e < 4; ++e) {
            const unsigned d = pack_bf16(bf_lo(v[hh][e]) - mb, bf_hi(v[hh][e]) - mb);
            y[e] = pack_bf16(bf_lo(d) * rden, bf_hi(d) * rden);
        }
        st16((char*)Y + hh * 131072, off, y);
    }
}

extern "C" __global__ __launch_bounds__(512, 2) void mega_kernel(const __hip_bfloat16* x, const __hip_bfloat16* wqkvA,
                                                            const __hip_bfloat16* woA, const __hip_bfloat16* wqkvB,
                                                            const __hip_bfloat16* woB, __hip_bfloat16* out,
                                                            char* ws) {
    char* const smem = smem_base;
    typedef __hip_bfloat16 bf;
    const int w8 = __builtin_amdgcn_readfirstlane(threadIdx.x >> 6);
    if (w8 == 7 && (blockIdx.x >> 3) < 10) code_touch(blockIdx.x >> 3, threadIdx.x & 63);
    asm volatile("" :: "s"(x), "s"(wqkvA), "s"(woA), "s"(wqkvB), "s"(woB), "s"(out), "s"(ws));
    STAMP_DECL; STAMPR(14); STAMP(0);
    u64 tag;
    {
        const int lane = threadIdx.x & 63;
        const Ids I = ids();
        const unsigned off = (unsigned)((I.po * 128 + I.co * 8 + w8) * 2048 + lane * 16);
        u32x4 v[2];
        u32x2 ep;
        asm volatile("global_load_dwordx4 %0, %3, %4\n\tglobal_load_dwordx4 %1, %3, %4 offset:1024\n\tglobal_load_dwordx2 %2, %5, %6 sc1"
                     : "=&v"(v[0]), "=&v"(v[1]), "=&v"(ep) : "v"(off), "s"(x), "v"(0), "s"(ws + WS_EPOCH) : "memory");
        if (w8 == 7) {
            const int l = lane & 15, k = l % 5;
            const unsigned o = (l >= 10 || k == 3) ? flag_off(l, I.cq, I.pq, 0) : (k == 0 || k == 2) ? flag_off(l, I.pq, I.cq, 0) : flag_off(l, I.po, I.co, 0);
            touch_at(ws + WS_FLAG, o);
        } else if (w8 >= 4) touch(wqkvA, (w8 - 4) * 256 + blockIdx.x, lane);
        if (w8 >= 4) asm volatile("s_waitcnt vmcnt(2)" ::: "memory"); else asm volatile("s_waitcnt vmcnt(1)" ::: "memory");
        asm volatile("" : "+v"(v[0]), "+v"(v[1]));
        ln_row(v, (bf*)(ws + WS_Y0), (unsigned)(I.po * 262144 + (lane >> 3) * 16384 + (I.co * 8 + w8) * 128 + (lane & 7) * 16));
        if (w8 >= 4) asm volatile("s_waitcnt vmcnt(3)" ::: "memory"); else asm volatile("s_waitcnt vmcnt(2)" ::: "memory");
        asm volatile("" : "+v"(ep));
        tag = (((u64)(unsigned)__builtin_amdgcn_readfirstlane(ep[1]) << 32) | (unsigned)__builtin_amdgcn_readfirstlane(ep[0])) + 1;
    }
    if (w8 >= 4) {
        int w = w8 & 3;
#pragma unroll 1
        for (int it = 0; it < 2; ++it) {
            const int lane = lane_id();
            asm volatile("" : "+s"(x), "+s"(wqkvA), "+s"(woA), "+s"(wqkvB), "+s"(woB), "+s"(ws), "+s"(w));
            const unsigned lds0 = (unsigned)(uintptr_t)smem;
            u32x4 cb[8];
            const unsigned wv = wo_off(w, lane), rv = res_off(true, it, lane), cv = colsum_off(w, lane);
            qkv_loader<true>(smem, w, ws, it * 5, tag, (const bf*)(ws + (it ? WS_X2 : WS_Y0)), wqkvA, (const float*)(ws + WS_ST + 262144),
                             ws + WS_CSP, it == 0, woA, [&] {
                if (w < 3) {
                    colsum_load(cb, wqkvB, cv);
                    asm volatile("s_waitcnt vmcnt(8)" ::: "memory");
                } else {
                    touch(woA, blockIdx.x, lane);
                    asm volatile("s_waitcnt vmcnt(1)" ::: "memory");
                }
            }, [&](int part) {
                if (part == 0) wo_load(woA, wv, 29, 32, lds0, w); else wo_load(woA, wv, 25, 29, lds0, w);
            } SP_ARG);
            STAMP(3);
            u32x4 res[6];
            res_load(res, it ? (const void*)(ws + WS_X2) : x, true, it, rv, w);
            if (it) __builtin_amdgcn_s_sleep(8);
            wo_load(woA, wv, 0, 7, lds0, w);
            BAR();
            wo_load(woA, wv, 7, 25, lds0, w);
            STAMP(4);
            out_handoff(smem, 147456, res, w, ws, it * 5 + 1, tag, lane, cb, it ? -1 : 1);
            STAMP(5);
            qkv_loader<false>(smem, w, ws, it * 5 + 2, tag, (const bf*)(ws + (it ? WS_X3 : WS_X1)), wqkvB, (const float*)(ws + WS_ST + it * 524288), ws + WS_CSP + 196608,
                              false, nullptr, [] {}, [](int) {} SP_ARG);
            STAMP(8);
            attn_loader(smem, w, ws, it * 5 + 3, tag, (const bf*)(ws + WS_KVF + it * 8 * MBy), woB, ws + (it ? WS_X3 : WS_X1),
                        it ? nullptr : wqkvA SP_ARG);
            STAMP(13); STAMPR(15); STAMP_FLUSH(it);
        }
        if (blockIdx.x == 0 && w == 0 && lane_id() == 0) __hip_atomic_store((g_u64ptr)(ws + WS_EPOCH), tag, __ATOMIC_RELAXED, __HIP_MEMORY_SCOPE_AGENT);
    } else {
        __builtin_amdgcn_s_setprio(2);
#pragma unroll 1
        for (int it = 0; it < 2; ++it) {
            u32x4 q4[4];
            asm volatile("" : "+s"(ws), "+s"(wqkvA), "+s"(wqkvB));
            qkv_compute<true>(smem, w8, wqkvA, q4, nullptr, (bf*)(ws + WS_OF + it * 8 * MBy), it == 0 SP_ARG);
            STAMP(2);
            out_compute<true>(smem, w8, (const bf*)(ws + WS_OF + it * 8 * MBy),
                              (bf*)(ws + (it ? WS_X3 : WS_X1)), (float*)(ws + WS_ST + it * 524288) SP_ARG);
            STAMP(6);
            qkv_compute<false>(smem, w8, wqkvB, q4, (bf*)(ws + WS_KVF + it * 8 * MBy), nullptr, false SP_ARG);
            attn_compute(smem, w8, q4, (const bf*)(ws + WS_KVF + it * 8 * MBy), (bf*)(ws + WS_OF + (it * 8 + 4) * MBy) SP_ARG);
            out_compute<false>(smem, w8, (const bf*)(ws + WS_OF + (it * 8 + 4) * MBy),
                               it ? out : (bf*)(ws + WS_X2), it ? nullptr : (float*)(ws + WS_ST + 262144) SP_ARG);
            STAMP(13); STAMPR(15); STAMP_FLUSH(it);
        }
    }
}

extern "C" __global__ void zz_code_end_kernel() {}

extern "C" void kernel_launch(void* const* d_in, const int* in_sizes, int n_in, void* d_out, int out_size, void* d_ws,
                              size_t ws_size, hipStream_t stream) {
    typedef __hip_bfloat16 bf;
    (void)hipFuncSetAttribute((const void*)mega_kernel, hipFuncAttributeMaxDynamicSharedMemorySize, CH_LDS);
    mega_kernel<<<256, 512, CH_LDS, stream>>>((const bf*)d_in[0], (const bf*)d_in[1], (const bf*)d_in[2], (const bf*)d_in[4], (const bf*)d_in[5],
                                              (bf*)d_out, (char*)d_ws);
}
```

```cpp
#include <hip/hip_runtime.h>
#include <hip/hip_bf16.h>
#include <stdint.h>

typedef __attribute__((ext_vector_type(8))) short bf16x8;
typedef __attribute__((ext_vector_type(4))) short s16x4;
typedef __attribute__((ext_vector_type(4))) float f32x4;
typedef __attribute__((ext_vector_type(2))) float f32x2;
typedef __attribute__((ext_vector_type(16))) float f32x16;
typedef __attribute__((ext_vector_type(4))) unsigned u32x4;
typedef __attribute__((ext_vector_type(2))) unsigned u32x2;
typedef __attribute__((ext_vector_type(2))) __bf16 bf16x2_t;
typedef __attribute__((ext_vector_type(8))) __bf16 bf8_t;
typedef __attribute__((ext_vector_type(16))) __bf16 bf16v_t;
typedef __attribute__((address_space(3))) const char* lds_cptr;
typedef __attribute__((address_space(3))) char* lds_ptr;

#define DI __device__ __forceinline__
#define MFMA16(a, b, c) __builtin_amdgcn_mfma_f32_16x16x32_bf16((a), (b), (c), 0, 0, 0)
#define MFMA32(a, b, c) __builtin_amdgcn_mfma_f32_32x32x16_bf16((a), (b), (c), 0, 0, 0)
#define SBAR() __builtin_amdgcn_sched_barrier(0)
#define STAMP(i) do { if ((i) == 1 || (i) == 0) { unsigned long long t_; __builtin_amdgcn_sched_barrier(0); asm volatile("s_memrealtime %0\n\ts_waitcnt lgkmcnt(0)" : "=s"(t_) :: "memory"); __builtin_amdgcn_sched_barrier(0); stv_[(i) ? 1 : 0] = (unsigned)t_; } } while (0)
#define STAMPR(i)
#define STAMP_DECL unsigned stv_[3] = {0, 0, 0}
#define STAMP_FLUSH(it) do { if ((it) == 0) stv_[2] = stv_[1]; else if (w8 < 4) { const unsigned lim = 12000u + 3u * (stv_[2] - stv_[0]); while ((unsigned)__builtin_amdgcn_s_memrealtime() - stv_[0] < lim) __builtin_amdgcn_s_sleep(2); } } while (0)
#define SP_PARAM , unsigned (&stv_)[3]
#define SP_ARG , stv_
typedef unsigned long long u64;
typedef __attribute__((ext_vector_type(32))) unsigned u32x32;

constexpr float QSCALE = 0.17677669529663687f * 1.4426950408889634f;
constexpr float ATT_SCALE = 0.17677669529663687f;
constexpr float LOG2E = 1.4426950408889634f;
constexpr float NEG_MASK = -1e9f / ATT_SCALE;

DI int lane_id() { int l; asm volatile("v_mbcnt_lo_u32_b32 %0, -1, 0\n\tv_mbcnt_hi_u32_b32 %0, -1, %0" : "=v"(l)); return l; }
DI unsigned pack_bf16(float lo, float hi) {
    f32x2 v = {lo, hi};
    bf16x2_t r = __builtin_convertvector(v, bf16x2_t);
    return __builtin_bit_cast(unsigned, r);
}
DI float bf_lo(unsigned u) { return __uint_as_float(u << 16); }
DI float bf_hi(unsigned u) { return __uint_as_float(u & 0xffff0000u); }
DI float round_bf16(float x) { return bf_lo(pack_bf16(x, 0.f)); }
DI void ln_stats(float sum, float sumsq, float& mean_out, float& rstd_out) {
    float mean;
    asm("v_mul_f32 %0, 0x3a800000, %1" : "=v"(mean) : "v"(sum));
    const float var = fmaxf(__builtin_fmaf(-mean, mean, sumsq * (1.f / 1024.f)), 0.f);
    mean_out = round_bf16(mean);
    rstd_out = __builtin_amdgcn_rcpf(round_bf16(__builtin_amdgcn_sqrtf(round_bf16(round_bf16(var) + 1e-5f))));
}

DI void glds16(const void* sbase, unsigned voff, unsigned ldsaddr) {
    asm volatile("s_mov_b32 m0, %0\n\ts_nop 0\n\tglobal_load_lds_dwordx4 %1, %2"
                 :: "s"(ldsaddr), "v"(voff), "s"(sbase) : "memory");
}
DI void glds16c(const void* sbase, unsigned voff, unsigned ldsaddr) {
    asm volatile("s_mov_b32 m0, %0\n\ts_nop 0\n\tglobal_load_lds_dwordx4 %1, %2 sc1"
                 :: "s"(ldsaddr), "v"(voff), "s"(sbase) : "memory");
}
DI void st16(void* sbase, unsigned voff, u32x4 v) {
    asm volatile("global_store_dwordx4 %0, %1, %2 sc1\n\ts_nop 1" :: "v"(voff), "v"(v), "s"(sbase) : "memory");
}
template <int OFF> DI void st16o(void* sbase, unsigned voff, u32x4 v) {
    asm volatile("global_store_dwordx4 %0, %1, %2 offset:%3 sc1\n\ts_nop 1" :: "v"(voff), "v"(v), "s"(sbase), "n"(OFF) : "memory");
}
DI bf16x8 lds_b128(lds_cptr p) { return *(const __attribute__((address_space(3))) bf16x8*)p; }
DI s16x4 lds_tr(lds_cptr p) {
    return __builtin_amdgcn_ds_read_tr16_b64_v4i16((__attribute__((address_space(3))) s16x4*)p);
}
DI bf16x8 cat8(s16x4 a, s16x4 b) { return __builtin_shufflevector(a, b, 0, 1, 2, 3, 4, 5, 6, 7); }
DI float dot2bf(unsigned a, unsigned b, float c) {
    return __builtin_amdgcn_fdot2_f32_bf16(__builtin_bit_cast(bf16x2_t, a), __builtin_bit_cast(bf16x2_t, b), c, false);
}
DI float sum_g(float v) {
    auto a = __builtin_amdgcn_permlane16_swap(__float_as_uint(v), __float_as_uint(v), false, false);
    v = __uint_as_float(a[0]) + __uint_as_float(a[1]);
    auto b = __builtin_amdgcn_permlane32_swap(__float_as_uint(v), __float_as_uint(v), false, false);
    return __uint_as_float(b[0]) + __uint_as_float(b[1]);
}
DI float wave_sum(float v) {
    v += __builtin_bit_cast(float, __builtin_amdgcn_update_dpp(0, __builtin_bit_cast(int, v), 0xB1, 0xF, 0xF, true));
    v += __builtin_bit_cast(float, __builtin_amdgcn_update_dpp(0, __builtin_bit_cast(int, v), 0x4E, 0xF, 0xF, true));
    v += __builtin_bit_cast(float, __builtin_amdgcn_update_dpp(0, __builtin_bit_cast(int, v), 0x141, 0xF, 0xF, true));
    v += __builtin_bit_cast(float, __builtin_amdgcn_update_dpp(0, __builtin_bit_cast(int, v), 0x140, 0xF, 0xF, true));
    return sum_g(v);
}
DI float amax3(float a, float b, float c) { float r; asm("v_max3_f32 %0, %1, %2, %3" : "=v"(r) : "v"(a), "v"(b), "v"(c)); return r; }
DI float max2f(float a, float b) { return __builtin_amdgcn_fmed3f(a, b, INFINITY); }
DI float max_h(float v) {
    auto b = __builtin_amdgcn_permlane32_swap(__float_as_uint(v), __float_as_uint(v), false, false);
    return max2f(__uint_as_float(b[0]), __uint_as_float(b[1]));
}

constexpr unsigned OFF_DUMP = 145408;
extern __shared__ __attribute__((aligned(16))) char smem_base[];
DI void touch_at(const void* p, unsigned off) {
    asm volatile("s_mov_b32 m0, %0\n\ts_nop 0\n\tglobal_load_lds_dword %1, %2"
                 :: "s"((unsigned)(uintptr_t)smem_base + OFF_DUMP), "v"(off), "s"(p) : "memory");
}
DI void touch(const void* p, int slice, int lane) { touch_at(p, (unsigned)(slice * 64 + lane) * 128u); }
DI void code_touch(int slice, int lane) {
    unsigned long long c0, c1;
    asm volatile("s_getpc_b64 s[96:97]\n\ts_add_u32 s96, s96, mega_kernel@rel32@lo+4\n\ts_addc_u32 s97, s97, mega_kernel@rel32@hi+12\n\ts_mov_b64 %0, s[96:97]\n\t"
                 "s_getpc_b64 s[96:97]\n\ts_add_u32 s96, s96, zz_code_end_kernel@rel32@lo+4\n\ts_addc_u32 s97, s97, zz_code_end_kernel@rel32@hi+12\n\ts_mov_b64 %1, s[96:97]"
                 : "=s"(c0), "=s"(c1) :: "s96", "s97", "scc");
    touch_at((const void*)c0, min((unsigned)(slice * 64 + lane) * 128u, (unsigned)(c1 - c0)));
}

#define BAR() asm volatile("s_barrier" ::: "memory")
#define WAIT_LGKM() do { SBAR(); __builtin_amdgcn_s_waitcnt(0xC07F); SBAR(); } while (0)
#define WAIT_LGKM_BAR() do { SBAR(); __builtin_amdgcn_s_waitcnt(0xC07F); asm volatile("s_barrier" ::: "memory"); SBAR(); } while (0)
#define VM_BAR(n) asm volatile("s_waitcnt vmcnt(" #n ")\n\ts_barrier" ::: "memory")
constexpr int CH_LDS = 163840;
typedef __attribute__((address_space(3))) unsigned* lds_uptr;
typedef __attribute__((address_space(1))) u64* g_u64ptr;

constexpr size_t MBy = 1 << 20;
constexpr size_t WS_X3 = 0, WS_X2 = 4 * MBy, WS_X1 = 8 * MBy, WS_OF = 12 * MBy  , WS_KVF = 36 * MBy  ,
                 WS_ST = 52 * MBy  , WS_CSP = 54 * MBy  , WS_FLAG = 55 * MBy  ,
                 WS_EPOCH = WS_CSP + 524288, WS_Y0 = 56 * MBy;
DI unsigned flag_off(int set, int group, int consumer, int producer) { return (unsigned)(set * 65536 + group * 4096 + consumer * 256 + producer * 8); }
DI void flag_store(char* ws, int set, int group, int producer, u64 tag, int lane) {
    asm volatile("" : "+s"(tag));
    if (lane < 16) __hip_atomic_store((g_u64ptr)(ws + WS_FLAG + flag_off(set, group, lane, producer)), tag, __ATOMIC_RELAXED, __HIP_MEMORY_SCOPE_AGENT);
}
DI void flag_poll(const char* ws, unsigned off, u64 tag) {
    asm volatile("" : "+s"(tag));
#pragma unroll 1
    for (int spin = 0; spin < (1 << 16); ++spin) {
        const u64 v = __hip_atomic_load((g_u64ptr)(ws + WS_FLAG + off), __ATOMIC_RELAXED, __HIP_MEMORY_SCOPE_AGENT);
        if (__all(v == tag)) break;
        __builtin_amdgcn_s_sleep(1);
    }
    asm volatile("" ::: "memory");
}
template <bool DRAIN, bool ALLPOLL>
DI void signal_poll(int follower, const char* ws, unsigned soff, unsigned poff, u64 pmask, u64 tag, u64& land) {
    u64 tagv, zero;
    asm volatile("s_not_b64 s[96:97], %[ts]\n\tv_mov_b64 %[tag], %[ts]\n\tv_mov_b64 %[zero], 0\n\tv_mov_b64 %[land], s[96:97]\n\t"
                 ".if %[dr]\n\ts_waitcnt vmcnt(0)\n\t.endif\n\ts_barrier\n\t"
                 "s_cmp_lg_u32 %[fl], 0\n\ts_cbranch_scc1 .Lsig_fol_%=\n\t"
                 "s_mov_b64 exec, 0xffff\n\t"
                 "global_store_dwordx2 %[so], %[tag], %[base] sc1\n"
                 ".if %[ap]\n.Lsig_fol_%=:\n.endif\n\t"
                 "s_mov_b64 exec, %[pm]\n\t"
                 "s_movk_i32 s96, 0x7fff\n"
                 ".Lsig_loop_%=:\n\t"
                 "global_atomic_or_x2 %[land], %[po], %[zero], %[base] sc0 sc1\n\t"
                 "s_sleep 1\n\t"
                 "s_waitcnt vmcnt(3)\n\t"
                 "v_cmp_ne_u64 vcc, %[land], %[tag]\n\t"
                 "s_sub_u32 s96, s96, 1\n\t"
                 "s_cbranch_scc1 .Lsig_out_%=\n\t"
                 "s_cbranch_vccnz .Lsig_loop_%=\n"
                 ".Lsig_out_%=:\n\t"
                 "s_mov_b64 exec, -1\n"
                 ".if !%[ap]\n.Lsig_fol_%=:\n.endif"
                 : [land] "=&v"(land), [tag] "=&v"(tagv), [zero] "=&v"(zero)
                 : [fl] "s"(follower), [so] "v"(soff), [po] "v"(poff), [ts] "s"(tag), [base] "s"(ws + WS_FLAG), [pm] "s"(pmask), [ap] "n"(ALLPOLL ? 1 : 0), [dr] "n"(DRAIN ? 1 : 0)
                 : "memory", "scc", "vcc", "s96", "s97");
}
struct Ids { int pq, cq, po, co; };
DI Ids ids() {
    int bid = blockIdx.x; asm volatile("" : "+s"(bid));
    const int xcd = bid & 7, idx = bid >> 3;
    int pq, cq, po, co;
#define IDS_BODY pq = (xcd & 1) * 8 + (idx & 7); cq = (xcd >> 1) * 4 + (idx >> 3); po = (xcd & 3) * 4 + (idx & 3); co = (xcd >> 2) * 8 + (idx >> 2);
    IDS_BODY
    return Ids{pq, cq, po, co};
}

DI void slow_warm(unsigned dump, const void* W, int w, int lane) {
    int bid = blockIdx.x; asm volatile("" : "+s"(bid));
    const int xcd = bid & 7, idx = bid >> 3;
    const int sc = (3 - (int)((uintptr_t)W >> 7)) & 7;
    if ((sc >> 2) != ((xcd >> 1) & 1)) return;
    const char* src = (const char*)W + (idx + 32 * w) * 49152 + (sc + (xcd >> 2) * 8) * 128;
#pragma unroll
    for (int G = 0; G < 3; ++G) glds16(src + G * 2048, (unsigned)((lane >> 3) * 6144 + (lane & 7) * 16), dump);
}

template <int V> struct IC { static constexpr int value = V; };
template <bool LOCAL> struct QC {
    static constexpr int AROWS = LOCAL ? 144 : 128;
    static constexpr int ABYTES = AROWS * 128;
    static constexpr int STAGE = ABYTES + 24576;
    static constexpr int OFF_MEAN = 143360;
    static constexpr int OFF_RSTD = OFF_MEAN + 576;
    static constexpr int OFF_CS = OFF_RSTD + 576;
};
constexpr int OFF_EX = 131072;

DI unsigned colsum_off(int G, int lane) {
    const Ids I = ids();
    unsigned off = (unsigned)(I.pq * 64 + (lane >> 3)) * 6144u + (unsigned)(G * 2048 + I.cq * 128 + (lane & 7) * 16);
    asm volatile("" : "+v"(off));
    return off;
}
DI void colsum_load(u32x4 (&c)[8], const void* W, unsigned off) {
#define CS_NEXT "s_add_u32 s96, s96, 0xc000\n\ts_addc_u32 s97, s97, 0\n\t"
    asm volatile("s_cmp_eq_u64 %9, 0\n\ts_cbranch_scc1 .Lcs_skip_%=\n\ts_mov_b64 s[96:97], %9\n\t"
                 "global_load_dwordx4 %0, %8, s[96:97]\n\t" CS_NEXT "global_load_dwordx4 %1, %8, s[96:97]\n\t" CS_NEXT
                 "global_load_dwordx4 %2, %8, s[96:97]\n\t" CS_NEXT "global_load_dwordx4 %3, %8, s[96:97]\n\t" CS_NEXT
                 "global_load_dwordx4 %4, %8, s[96:97]\n\t" CS_NEXT "global_load_dwordx4 %5, %8, s[96:97]\n\t" CS_NEXT
                 "global_load_dwordx4 %6, %8, s[96:97]\n\t" CS_NEXT "global_load_dwordx4 %7, %8, s[96:97]\n"
                 ".Lcs_skip_%=:"
                 : "=&v"(c[0]), "=&v"(c[1]), "=&v"(c[2]), "=&v"(c[3]), "=&v"(c[4]), "=&v"(c[5]), "=&v"(c[6]), "=&v"(c[7])
                 : "v"(off), "s"(W) : "memory", "scc", "s96", "s97");
}
DI void colsum_store(u32x4 (&c)[8], char* ws, int mat, int G, int lane) {
    asm volatile("" : "+v"(c[0]), "+v"(c[1]), "+v"(c[2]), "+v"(c[3]), "+v"(c[4]), "+v"(c[5]), "+v"(c[6]), "+v"(c[7]));
    const Ids I = ids();
    const int m = lane & 15;
    const unsigned one = (m & 1) ? 0xbf800000u : 0xbf80u;
    const u32x4 sl = {(m >> 1) == 0 ? one : 0u, (m >> 1) == 1 ? one : 0u, (m >> 1) == 2 ? one : 0u, (m >> 1) == 3 ? one : 0u};
    f32x4 d = {0, 0, 0, 0};
#pragma unroll
    for (int i = 0; i < 8; ++i) d = MFMA16(__builtin_bit_cast(bf16x8, sl), __builtin_bit_cast(bf16x8, c[i]), d);
#pragma unroll
    for (int r = 0; r < 4; ++r)
        d[r] += __int_as_float(__builtin_amdgcn_update_dpp(0, __float_as_int(d[r]), 0x128, 0xF, 0xF, true));
    if (lane < 32 && (lane & 8) == 0)
        st16(ws + WS_CSP + mat * 196608, (unsigned)((I.cq * 16 + I.pq) * 768 + G * 256 + (lane & 7) * 32 + (lane >> 4) * 16), __builtin_bit_cast(u32x4, d));

}

template <bool LOCAL>
struct WFirst {
    unsigned v[2];
    DI WFirst(int w, int ct, int lane) {
#pragma unroll
        for (int i = 0; i < 2; ++i) {
            const int k = 8 * i + (lane >> 3), pos = lane & 7;
            v[i] = (unsigned)k * 6144u + (unsigned)(w * 1024 + ct * 64 + ((pos >> 1) ^ (((k >> 1) & 1) | (i << 1))) * 16 + (pos & 1) * 8) * 2u;
        }
        asm volatile("" : "+v"(v[0]), "+v"(v[1]));
    }
    DI void issue(const void* W, unsigned lds0, int w, int i0, int i1) const {
#pragma unroll
        for (int i = i0; i < i1; ++i)
            glds16((const char*)W + (size_t)(i >> 1) * (16 * 6144), v[i & 1], lds0 + (i >> 3) * QC<LOCAL>::STAGE + QC<LOCAL>::ABYTES + (w * 8 + (i & 7)) * 1024);
    }
};

template <bool LOCAL, class F, class F2>
DI void qkv_loader(char* smem, int w, char* ws, int set, u64 tag, const __hip_bfloat16* __restrict__ X, const __hip_bfloat16* __restrict__ W,
                   const float* __restrict__ statp, const char* __restrict__ csp, bool ident, const void* nextW, F tail, F2 mid SP_PARAM) {
    typedef QC<LOCAL> C;
    constexpr int NT = 16;
    const int lane = lane_id();
    asm volatile("" : "+s"(w));
    const int idx = blockIdx.x >> 3;
    const Ids I = ids();
    const int panel = I.pq, ct = I.cq;
    const int R0 = panel * 128;
    const unsigned lds0 = (unsigned)(uintptr_t)smem;
    auto grow = [&](int i) -> int {
        if (!LOCAL) return R0 + i;
        if (i < 128) { const int t = (i >> 4) & 3, a = i & 15; return R0 + (i & 64) + 2 * (16 * (t & 1) + a) + (t >> 1); }
        const int a = i & 15; const int r = R0 - 16 + 2 * (a & 7) + (a >> 3);
        return r < 0 ? 0 : r;
    };
    unsigned voffA[5], voffB[6];
#pragma unroll
    for (int i = 0; i < 6; ++i) {
        const int j = w + 4 * i, G = j >> 3, k = 8 * (j & 7) + (lane >> 3);
        const int pos = lane & 7, f = ((k >> 1) & 1) | (((k >> 3) & 1) << 1);
        const int seg = (pos >> 1) ^ f;
        voffB[i] = (unsigned)k * 6144u + (unsigned)(G * 1024 + ct * 64 + seg * 16 + (pos & 1) * 8) * 2u;
    }
    auto issueW = [&](int t, int buf) {
        const char* wb = (const char*)W + (size_t)t * (64 * 6144);
        const unsigned sb = lds0 + buf * C::STAGE;
#pragma unroll
        for (int i = 0; i < 6; ++i) glds16(wb, voffB[i], sb + C::ABYTES + (w + 4 * i) * 1024);
    };
    asm volatile("" : "+v"(voffB[0]), "+v"(voffB[1]), "+v"(voffB[2]), "+v"(voffB[3]), "+v"(voffB[4]), "+v"(voffB[5]));
    const WFirst<LOCAL> first(w, ct, lane);
    const int g = lane >> 4, r = lane & 15, pos = 32 * w + 2 * r + (g >> 1);
    const int lr = !LOCAL ? (pos & 64) + 2 * (pos & 31) + ((pos >> 5) & 1)
                          : (w == 0 && (g & 1)) ? 128 + (r & 7) + 8 * (g >> 1) : (pos & 64) + ((pos & 1) * 2 + ((pos >> 5) & 1)) * 16 + ((pos >> 1) & 15);
    unsigned so = (unsigned)(panel * 16384 + g * 1024 + w * 256 + r * 16), ho = (unsigned)(g * 1024 + (panel ? panel * 16384 - 15488 + (r & 7) * 16 : 0));
    unsigned co = (unsigned)((ct * 16 + g) * 768 + w * 192 + r * 12);
    lds_ptr mp = (lds_ptr)smem + C::OFF_MEAN + lr * 4, cp = (lds_ptr)smem + C::OFF_CS + w * 192 + r * 12 + ((g & 1) ? 8 : g * 2);
    unsigned fo = flag_off((LOCAL ? 13 : 10) + min(g, 2), ct, panel, r);
    asm volatile("" : "+v"(so), "+v"(ho), "+v"(co), "+v"(mp), "+v"(cp), "+v"(fo));
#pragma unroll
    for (int i = 0; i < 5; ++i) {
        const int j = (i < 4) ? (w + 4 * i) : (16 + (w & 1));
        const int row = 8 * j + (lane >> 3);
        const int c = (lane & 7) ^ ((row >> 1) & 7);
        const int gr = grow(row < C::AROWS ? row : 0);
        voffA[i] = (unsigned)((gr >> 7) * 262144 + (gr & 127) * 128 + c * 16);
        asm volatile("" : "+v"(voffA[i]));
    }
    auto issueA = [&](int t, int buf) {
        const char* xa = (const char*)X + t * 16384;
        const unsigned sb = lds0 + buf * C::STAGE;
        if (LOCAL && w < 2) glds16c(xa, voffA[4], sb + (16 + w) * 1024);
#pragma unroll
        for (int i = 0; i < 4; ++i) glds16c(xa, voffA[i], sb + (w + 4 * i) * 1024);
    };
    u64 land;
    if (w == 3) __builtin_amdgcn_s_setprio(3);
    signal_poll<LOCAL, false>(w - 3, ws, flag_off(set, I.po, lane & 15, I.co),
                              flag_off(set, (lane & 16) && panel ? panel - 1 : panel, ct, lane & 15),
                              !LOCAL ? 0xffffull : ident ? 0xc000ffffull : 0xffffffffull, tag, land);
    if (w < 3) first.issue(W, lds0, w, 0, 12);
    BAR();
    STAMP(LOCAL ? 1 : 6);
    issueA(0, 0);
    issueA(1, 1);
    if (w < 3) { first.issue(W, lds0, w, 12, 16); asm volatile("s_waitcnt vmcnt(8)" ::: "memory"); }
    else asm volatile("s_waitcnt vmcnt(4)" ::: "memory");
    BAR();
    asm volatile("" :: "v"(land));
    __builtin_amdgcn_s_setprio(0);
    STAMP(LOCAL ? 2 : 7);
    issueW(2, 2);
    issueA(2, 2);
    VM_BAR(10);
    asm volatile("global_load_dwordx2 %0, %1, %2 sc1" : "=&v"(land) : "v"(fo), "s"(ws + WS_FLAG) : "memory");
    issueA(3, 0); issueW(3, 0);
    int buf = 1;
#pragma unroll 1
    for (int t = 1; t < NT - 3; ++t) {
        VM_BAR(10);
        issueA(t + 3, buf); issueW(t + 3, buf);
        if (t == 10 && idx < 3) code_touch(idx * 4 + w, lane);
        if (LOCAL && t == 11)
            glds16(nextW, (unsigned)((idx + 32 * w) * 8 + (lane >> 3)) * 2048u +
                   (unsigned)(((I.co & 8) + ((3 - (int)((uintptr_t)nextW >> 7)) & 7)) * 128 + (lane & 7) * 16), lds0 + OFF_DUMP);
        buf = (buf == 2) ? 0 : buf + 1;
    }
    typedef __attribute__((ext_vector_type(3))) float f32x3;
    const bool halo = LOCAL && w == 0;
    asm volatile("" : "+v"(land));
    if (!ident) {
        if (!__all(land == tag)) flag_poll(ws, fo, tag);
        f32x3 cs[4];
        f32x4 sv[4], hv[4];
        typedef __attribute__((address_space(3))) float* lds_f;
        auto add = [](float a, float b) { float r; asm("v_add_f32 %0, %1, %2" : "=v"(r) : "v"(a), "v"(b)); return r; };
        auto fold32 = [](float a, float b) { asm("s_nop 1\n\tv_permlane32_swap_b32 %0, %1\n\tv_add_f32 %0, %0, %1" : "+v"(a), "+v"(b)); return a; };
        auto fold16 = [](float a, float b) { asm("s_nop 1\n\tv_permlane16_swap_b32 %0, %1\n\tv_add_f32 %0, %0, %1" : "+v"(a), "+v"(b)); return a; };
        auto sum4 = [&](auto (&v)[4], int e) { return add(add(v[0][e], v[1][e]), add(v[2][e], v[3][e])); };
#pragma unroll
        for (int i = 0; i < 4; ++i) asm volatile("global_load_dwordx3 %0, %1, %2 sc1" : "=&v"(cs[i]) : "v"(co), "s"(csp + i * 3072) : "memory");
        VM_BAR(14);
#pragma unroll
        for (int i = 0; i < 4; ++i) asm volatile("global_load_dwordx4 %0, %1, %2 sc1" : "=&v"(sv[i]) : "v"(so), "s"((const char*)statp + i * 4096) : "memory");
        if (LOCAL)
            asm volatile("s_cmp_lg_u32 %9, 0\n\ts_cbranch_scc1 .Lhv_skip_%=\n\t"
                         "global_load_dwordx4 %0, %4, %5 sc1\n\tglobal_load_dwordx4 %1, %4, %6 sc1\n\t"
                         "global_load_dwordx4 %2, %4, %7 sc1\n\tglobal_load_dwordx4 %3, %4, %8 sc1\n"
                         ".Lhv_skip_%=:" : "=&v"(hv[0]), "=&v"(hv[1]), "=&v"(hv[2]), "=&v"(hv[3])
                         : "v"(ho), "s"(statp), "s"(statp + 1024), "s"(statp + 2048), "s"(statp + 3072), "s"(w) : "memory", "scc");
        if (halo) asm volatile("s_waitcnt vmcnt(8)" ::: "memory"); else asm volatile("s_waitcnt vmcnt(4)" ::: "memory");
        asm volatile("" : "+v"(cs[0]), "+v"(cs[1]), "+v"(cs[2]), "+v"(cs[3]));
        const float c2 = sum4(cs, 2);
        *(lds_f)cp = fold16(fold32(sum4(cs, 0), sum4(cs, 1)), fold32(c2, c2));
        BAR();
        asm volatile("s_waitcnt vmcnt(0)" : "+v"(sv[0]), "+v"(sv[1]), "+v"(sv[2]), "+v"(sv[3]) :: "memory");
        mid(0);
        const float s1 = fold32(sum4(sv, 0), sum4(sv, 2)), s2 = fold32(sum4(sv, 1), sum4(sv, 3));
        float h1, h2;
        asm("v_mov_b32 %0, %2\n\tv_mov_b32 %1, %3" : "=&v"(h1), "=&v"(h2) : "v"(s1), "v"(s2));
        if (halo) {
            asm volatile("" : "+v"(hv[0]), "+v"(hv[1]), "+v"(hv[2]), "+v"(hv[3]));
            const float u0 = sum4(hv, 0), u1 = sum4(hv, 1), u2 = sum4(hv, 2), u3 = sum4(hv, 3);
            h1 = fold32(u0, panel ? u2 : u0); h2 = fold32(u1, panel ? u3 : u1);
        }
        float mean, rstd;
        ln_stats(fold16(s1, h1), fold16(s2, h2), mean, rstd);
        *(lds_f)mp = mean; *(lds_f)(mp + (C::OFF_RSTD - C::OFF_MEAN)) = rstd;
    } else { VM_BAR(10); tail(); BAR(); mid(0); }
    mid(1);
    WAIT_LGKM_BAR();
}

template <bool LOCAL>
DI void qkv_compute(char* smem, int w, const void* W, u32x4 (&qo4)[4], __hip_bfloat16* __restrict__ kvf, __hip_bfloat16* __restrict__ ofrag, bool ident SP_PARAM) {
    typedef QC<LOCAL> C;
    constexpr int NT = 16, ABYTES = C::ABYTES, STAGE = C::STAGE;
    const int lane = lane_id();
    asm volatile("" : "+s"(w));
    const int wm = w & 1, wn = w >> 1;
    const Ids I = ids();
    const int panel = I.pq, ct = I.cq;
    const int l15 = lane & 15, g = lane >> 4;
    const lds_cptr sm = (lds_cptr)smem;
    lds_ptr smw = (lds_ptr)smem;

    f32x4 acc[3][4][2];
    f32x4 acch[2];
    f32x2 z2; asm volatile("v_mov_b64 %0, 0" : "=v"(z2));
    const f32x4 z4 = {z2[0], z2[1], z2[0], z2[1]};
#pragma unroll
    for (int a = 0; a < 3; ++a)
#pragma unroll
        for (int t = 0; t < 4; ++t) { acc[a][t][0] = z4; acc[a][t][1] = z4; }
    acch[0] = z4; acch[1] = z4;

    struct Frags { bf16x8 a[4]; bf16x8 h; s16x4 b[3][2][2]; };
    auto rd = [&](auto WM, Frags& F, lds_cptr pa, lds_cptr pb0, lds_cptr pb1, int n) {
        switch (n) {
            case 0: F.b[0][0][0] = lds_tr(pb0); break;
            case 1: F.b[0][0][1] = lds_tr(pb0 + 512); break;
            case 2: F.a[0] = lds_b128(pa); break;
            case 3: F.b[0][1][0] = lds_tr(pb1); break;
            case 4: F.b[0][1][1] = lds_tr(pb1 + 512); break;
            case 5: F.a[1] = lds_b128(pa + 2048); break;
            case 6: F.a[2] = lds_b128(pa + 4096); break;
            case 7: F.a[3] = lds_b128(pa + 6144); break;
            case 8: F.b[1][0][0] = lds_tr(pb0 + 8192); break;
            case 9: F.b[1][0][1] = lds_tr(pb0 + 8192 + 512); break;
            case 10: F.b[1][1][0] = lds_tr(pb1 + 8192); break;
            case 11: F.b[1][1][1] = lds_tr(pb1 + 8192 + 512); break;
            case 12: F.b[2][0][0] = lds_tr(pb0 + 16384); break;
            case 13: F.b[2][0][1] = lds_tr(pb0 + 16384 + 512); break;
            case 14: F.b[2][1][0] = lds_tr(pb1 + 16384); break;
            case 15: F.b[2][1][1] = lds_tr(pb1 + 16384 + 512); break;
            case 16: if (LOCAL) F.h = lds_b128(pa + 16384 - WM.value * 8192); break;
            default: break;
        }
    };
    auto mm = [](f32x4& c, bf16x8 a, bf16x8 b) { asm volatile("v_mfma_f32_16x16x32_bf16 %0, %1, %2, %0" : "+v"(c) : "v"(a), "v"(b)); };
    auto half = [&](auto WM, const Frags& C, Frags& N, bool doread, lds_cptr pa, lds_cptr pb0, lds_cptr pb1, auto tail, int G0 = 0, int G1 = 3) {
#pragma unroll
        for (int G = G0; G < G1; ++G) {
            const bf16x8 b0 = cat8(C.b[G][0][0], C.b[G][0][1]), b1 = cat8(C.b[G][1][0], C.b[G][1][1]);
#pragma unroll
            for (int t = 0; t < 4; ++t) {
                const int n = (G - G0) * 8 + t * 2;
                if (G < 2) mm(acc[G][t][0], b0, C.a[t]); else mm(acc[G][t][0], C.a[t], b0);
                SBAR();
                if (doread) rd(WM, N, pa, pb0, pb1, n);
                tail(n);
                SBAR();
                if (G < 2) mm(acc[G][t][1], b1, C.a[t]); else mm(acc[G][t][1], C.a[t], b1);
                SBAR();
                if (doread) rd(WM, N, pa, pb0, pb1, n + 1);
                tail(n + 1);
                SBAR();
            }
        }
        if (LOCAL) {
            if (WM.value == 0) { mm(acch[0], cat8(C.b[1][0][0], C.b[1][0][1]), C.h); mm(acch[1], cat8(C.b[1][1][0], C.b[1][1][1]), C.h); }
            else               { mm(acch[0], C.h, cat8(C.b[2][0][0], C.b[2][0][1])); mm(acch[1], C.h, cat8(C.b[2][1][0], C.b[2][1][1])); }
            SBAR();
        }
    };

    Frags F0, F1;
    f32x4 cq[2], ck[2], mu4[5], rs4[5]; float cv[2], mu[5], rs[5];
    u32x4 qfr[4], kfr[4];
    u32x4 va[2][2];
    f32x4 mwin, mfirst;
    typedef __attribute__((address_space(3))) u32x4* lds_u4;
    const bool negpos = (wm == 0) && (panel == 0);
    VM_BAR(0);
    unsigned cm01 = 0, cm23 = 0;
    lds_cptr ka[2] = {sm, sm}, vo2[2] = {sm, sm};
    lds_u4 ex = (lds_u4)(smw + OFF_EX + wn * 6144) + lane;
    lds_cptr s1 = sm + C::OFF_MEAN + wm * 256 + l15 * 4, s4 = sm + C::OFF_MEAN + wm * 256 + g * 16;
    lds_cptr c1 = sm + C::OFF_CS + wn * 128 + l15 * 4, c4 = sm + C::OFF_CS + wn * 128 + g * 16;
    asm volatile("" : "+v"(s1), "+v"(s4), "+v"(c1), "+v"(c4));
    if (LOCAL) {
        unsigned keepv = negpos ? __float_as_uint(NEG_MASK) : 0u;
        asm volatile("" : "+s"(keepv));
        u32x4 mw, mf;
        unsigned prv[4];
#pragma unroll
        for (int r = 0; r < 4; ++r) {
            const int d = l15 - (4 * g + r);
            unsigned out = (unsigned)(((d << 28) >> 31));
            prv[r] = (unsigned)(d >> 31);
            asm volatile("" : "+v"(out), "+v"(prv[r]));
            mw[r] = out & 0xff800000u;
            mf[r] = (out & 0xff800000u) | (~out & keepv);
        }
        const lds_cptr e0 = sm + OFF_EX + wn * 6144;
        ka[0] = e0 + (wm ? lane : 256 + (lane ^ 8)) * 16;         ka[1] = e0 + (wm ? 64 + lane : 256 + lane) * 16;
        vo2[0] = e0 + (wm ? 128 + lane : 320 + (lane ^ 32)) * 16; vo2[1] = e0 + (wm ? 192 + lane : 320 + lane) * 16;
        cm01 = ~((prv[0] >> 16) | (prv[1] << 16)); cm23 = ~((prv[2] >> 16) | (prv[3] << 16));
        mwin = __builtin_bit_cast(f32x4, mw); mfirst = __builtin_bit_cast(f32x4, mf);
        asm volatile("" : "+v"(mwin), "+v"(mfirst), "+v"(cm01), "+v"(cm23), "+v"(ka[0]), "+v"(ka[1]), "+v"(vo2[0]), "+v"(vo2[1]), "+v"(ex));
    }
    typedef const __attribute__((address_space(3))) f32x4* lds_f4; typedef const __attribute__((address_space(3))) float* lds_f1;
    auto mainloop = [&](auto WM) {
#pragma unroll
        for (int a = 0; a < 3; ++a)
            asm volatile("" : "+v"(acc[a][0][0]), "+v"(acc[a][0][1]), "+v"(acc[a][1][0]), "+v"(acc[a][1][1]), "+v"(acc[a][2][0]), "+v"(acc[a][2][1]),
                         "+v"(acc[a][3][0]), "+v"(acc[a][3][1]));
        int ty = lane_id();
        const int l15 = ty & 15, g = ty >> 4;
        const int swA = (l15 >> 1) & 7;
        const int q4 = l15 >> 2, p4 = l15 & 3, fk = ((q4 >> 1) & 1) | ((g & 1) << 1);
        lds_cptr pA0 = sm + wm * 8192 + l15 * 128 + ((0 + g) ^ swA) * 16, pA1 = sm + wm * 8192 + l15 * 128 + ((4 + g) ^ swA) * 16;
        lds_cptr pB0 = sm + ABYTES + (8 * g + q4) * 128 + (((2 * wn + 0) ^ fk) * 32) + p4 * 8;
        lds_cptr pB1 = sm + ABYTES + (8 * g + q4) * 128 + (((2 * wn + 1) ^ fk) * 32) + p4 * 8;
        asm volatile("" : "+v"(acch[0]), "+v"(acch[1]), "+v"(pA0), "+v"(pA1), "+v"(pB0), "+v"(pB1));
        slow_warm((unsigned)(uintptr_t)smem + OFF_DUMP, W, w, ty);
        BAR();
        STAMP(LOCAL ? 1 : 7);
        BAR();
#pragma unroll
        for (int n = 0; n < 17; ++n) rd(WM, F0, pA0, pB0, pB1, n);
        WAIT_LGKM();
        int off = 0;
        lds_cptr a = pA1, b0 = pB0, b1 = pB1;
#pragma unroll 3
        for (int t = 0; t < NT - 1; ++t) {
            half(WM, F0, F1, true, a, b0 + 4096, b1 + 4096, [&](int n) {
                if (n == 17) { off = (off == 2 * STAGE) ? 0 : off + STAGE; asm volatile("" : "+s"(off)); }
                if (n == 19) { b0 = pB0 + off; asm volatile("" : "+v"(b0)); }
                if (n == 20) { a = pA0 + off; asm volatile("" : "+v"(a)); }
                if (n == 21) { b1 = pB1 + off; asm volatile("" : "+v"(b1)); }
            });
            WAIT_LGKM_BAR();
            half(WM, F1, F0, true, a, b0, b1, [&](int n) {
                if (n == 19) { a = pA1 + off; asm volatile("" : "+v"(a)); }
                if (n == 22) __builtin_amdgcn_s_waitcnt(0xC07F);
            });
        }
        half(WM, F0, F1, true, a, b0 + 4096, b1 + 4096, [](int) {});
        WAIT_LGKM_BAR();
        half(WM, F1, F0, false, sm, sm, sm, [&](int n) {
            constexpr int RS = C::OFF_RSTD - C::OFF_MEAN, H = 512 - WM.value * 256;
            if (n < 2) { cq[n] = *(lds_f4)(c4 + n * 64); ck[n] = *(lds_f4)(c4 + 256 + n * 64); }
            else if (n < 6) { mu[n - 2] = *(lds_f1)(s1 + (n - 2) * 64); rs[n - 2] = *(lds_f1)(s1 + RS + (n - 2) * 64); }
            else if (n < 10) { mu4[n - 6] = *(lds_f4)(s4 + (n - 6) * 64); rs4[n - 6] = *(lds_f4)(s4 + RS + (n - 6) * 64); }
            else if (n == 10) { cv[0] = *(lds_f1)(c1 + 512); cv[1] = *(lds_f1)(c1 + 512 + 64); }
            else if (LOCAL && n == 11) {
                if (WM.value == 0) { mu[4] = *(lds_f1)(s1 + H); rs[4] = *(lds_f1)(s1 + RS + H); }
                else { mu4[4] = *(lds_f4)(s4 + H); rs4[4] = *(lds_f4)(s4 + RS + H); }
            } else if (n == (LOCAL ? 22 : 15)) __builtin_amdgcn_s_waitcnt(0xC07F);
        }, LOCAL ? 0 : 1);
        asm volatile("s_nop 7");
        SBAR();
        if (LOCAL) {
            auto fix = [&](int a, int t) {
#pragma unroll
                for (int f = 0; f < 2; ++f)
                    acc[a][t][f] = (a == 2) ? rs4[t] * (acc[2][t][f] + mu4[t] * cv[f]) : rs[t] * (acc[a][t][f] + mu[t] * (a ? ck[f] : cq[f]));
            };
            auto pk = [&](const f32x4 (&v)[2]) { return u32x4{pack_bf16(v[0][0], v[0][1]), pack_bf16(v[0][2], v[0][3]), pack_bf16(v[1][0], v[1][1]), pack_bf16(v[1][2], v[1][3])}; };
            if (!ident) {
                fix(1, 1); fix(1, 3); fix(2, 1); fix(2, 3);
#pragma unroll
                for (int f = 0; f < 2; ++f)
                    acch[f] = (WM.value == 0) ? rs[4] * (acch[f] + mu[4] * ck[f]) : rs4[4] * (acch[f] + mu4[4] * cv[f]);
            }
            auto pkv = [&](int t) {
#pragma unroll
                for (int f = 0; f < 2; ++f) {
                    va[t >> 1][f][(t & 1) * 2] = pack_bf16(acc[2][t][f][0], acc[2][t][f][1]);
                    va[t >> 1][f][(t & 1) * 2 + 1] = pack_bf16(acc[2][t][f][2], acc[2][t][f][3]);
                }
            };
            kfr[1] = pk(acc[1][1]); kfr[3] = pk(acc[1][3]); pkv(1); pkv(3);
            u32x4 hf = pk(acch);
            if (WM.value == 0) {
                ex[0] = kfr[1]; ex[64] = kfr[3];
#pragma unroll
                for (int u = 0; u < 2; ++u)
                    asm volatile("ds_write_b64 %0, %1 offset:%3\n\tds_write_b64 %0, %2 offset:%3+8" :: "v"(ex), "v"(u32x2{va[u][0][2], va[u][0][3]}),
                                 "v"(u32x2{va[u][1][2], va[u][1][3]}), "n"(2048 + u * 1024) : "memory");
                if (negpos) hf = u32x4{0, 0, 0, 0};
            }
            ex[256 + WM.value * 64] = hf;
            SBAR();
            if (!ident) { fix(0, 0); fix(0, 1); fix(0, 2); fix(0, 3); fix(1, 0); fix(1, 2); fix(2, 0); fix(2, 2); }
#pragma unroll
            for (int t = 0; t < 4; ++t) qfr[t] = pk(acc[0][t]);
            kfr[0] = pk(acc[1][0]); kfr[2] = pk(acc[1][2]); pkv(0); pkv(2);
            asm volatile("" : "+v"(qfr[0]), "+v"(qfr[1]), "+v"(qfr[2]), "+v"(qfr[3]), "+v"(kfr[0]), "+v"(kfr[2]),
                         "+v"(va[0][0]), "+v"(va[0][1]), "+v"(va[1][0]), "+v"(va[1][1]));
        }
    };
    const int head = ct * 2 + wn;
    unsigned qo = 0, ko = 0, vo = 0;
    if (LOCAL) {
        qo = (unsigned)((((panel * 8 + wm * 4) * 32 + head) * 64 + lane) * 16);
        asm volatile("" : "+v"(qo));
    } else {
        ko = (unsigned)((wm * 8 + (g & 1)) * 1024 + ((l15 & 7) + ((l15 & 8) << 1) + 32 * (g >> 1)) * 16);
        vo = (unsigned)((wm * 8 + 2 + (g >> 1)) * 1024 + (l15 + 32 * (g & 1)) * 16);
        asm volatile("" : "+v"(ko), "+v"(vo));
    }
    if (LOCAL && wm) mainloop(IC<1>{}); else mainloop(IC<0>{});

    if (!LOCAL) {
        auto pk = [&](f32x4 a, f32x4 b) { return u32x4{pack_bf16(a[0], a[1]), pack_bf16(a[2], a[3]), pack_bf16(b[0], b[1]), pack_bf16(b[2], b[3])}; };
        char* kvp = (char*)kvf + (size_t)(head * 16 + panel) * 16384;
        lds_ptr own = smw + wn * 16384;
        SBAR();
#pragma unroll
        for (int t = 0; t < 4; ++t) {
            const u32x4 kk = pk(rs[t] * (acc[1][t][0] + mu[t] * ck[0]), rs[t] * (acc[1][t][1] + mu[t] * ck[1]));
            if (t & 1) st16o<128>(kvp + (t >> 1) * 4096, ko, kk); else st16o<0>(kvp + (t >> 1) * 4096, ko, kk);
            kfr[t] = kk;
            SBAR();
            const int u = t >> 1, f = t & 1;
            const u32x4 vv = pk(rs4[2 * u] * (acc[2][2 * u][f] + mu4[2 * u] * cv[f]), rs4[2 * u + 1] * (acc[2][2 * u + 1][f] + mu4[2 * u + 1] * cv[f]));
            if (f) st16o<256>(kvp + u * 4096, vo, vv); else st16o<0>(kvp + u * 4096, vo, vv);
            qfr[t] = vv;
            SBAR();
        }
        half(IC<0>{}, F1, F0, false, sm, sm, sm, [&](int n) {
            const int t = n >> 1;
            if (n & 1) *(lds_u4)(own + vo + (t >> 1) * 4096 + (t & 1) * 256) = qfr[t];
            else       *(lds_u4)(own + ko + (t >> 1) * 4096 + (t & 1) * 128) = kfr[t];
        }, 0, 1);
        asm volatile("s_nop 7");
        SBAR();
#pragma unroll
        for (int u = 0; u < 2; ++u) {
            const float r0 = rs[2 * u] * QSCALE, r1 = rs[2 * u + 1] * QSCALE;
            const u32x4 qa = pk(r0 * (acc[0][2 * u][0] + mu[2 * u] * cq[0]), r0 * (acc[0][2 * u][1] + mu[2 * u] * cq[1]));
            const u32x4 qb = pk(r1 * (acc[0][2 * u + 1][0] + mu[2 * u + 1] * cq[0]), r1 * (acc[0][2 * u + 1][1] + mu[2 * u + 1] * cq[1]));
#pragma unroll
            for (int e = 0; e < 4; ++e) {
                const auto x = __builtin_amdgcn_permlane16_swap(qa[e], qb[e], false, false);
                qo4[2 * u][e] = x[0]; qo4[2 * u + 1][e] = x[1];
            }
        }
        return;
    }
    {
        const f32x4 zero4 = {0, 0, 0, 0};
        auto bf8 = [](u32x4 v) { return __builtin_bit_cast(bf16x8, v); };
        f32x4 sp[4], sc[4];
        sc[1] = MFMA16(bf8(kfr[1]), bf8(qfr[1]), mwin); sp[1] = MFMA16(bf8(kfr[0]), bf8(qfr[1]), mwin);
        sc[3] = MFMA16(bf8(kfr[3]), bf8(qfr[3]), mwin); sp[3] = MFMA16(bf8(kfr[2]), bf8(qfr[3]), mwin);
        asm volatile("" : "+v"(sc[1]), "+v"(sp[1]), "+v"(sc[3]), "+v"(sp[3]));
        WAIT_LGKM_BAR();
        const bf16x8 kp[2] = {lds_b128(ka[0]), lds_b128(ka[1])};
        u32x2 vp[2][2];
#pragma unroll
        for (int u = 0; u < 2; ++u)
            asm volatile("ds_read_b64 %0, %2\n\tds_read_b64 %1, %2 offset:8" : "=&v"(vp[u][0]), "=&v"(vp[u][1]) : "v"(vo2[u]) : "memory");
        sc[0] = MFMA16(bf8(kfr[0]), bf8(qfr[0]), mwin); sc[2] = MFMA16(bf8(kfr[2]), bf8(qfr[2]), mwin);
        SBAR();
        float x[4][4], m[4];
        unsigned a[4][2];
        auto logits = [&](int t0, int st) {
#pragma unroll
            for (int t = t0; t < 4; t += 2)
#pragma unroll
                for (int h = 0; h < 2; ++h) {
                    if (st == 0) {
                        const unsigned uc = pack_bf16(sc[t][2 * h], sc[t][2 * h + 1]), up = pack_bf16(sp[t][2 * h], sp[t][2 * h + 1]);
                        asm("v_bfi_b32 %0, %1, %2, %3" : "=v"(a[t][h]) : "v"(h ? cm23 : cm01), "v"(uc), "v"(up));
                    } else if (st == 1) { x[t][2 * h] = bf_lo(a[t][h]) * ATT_SCALE; x[t][2 * h + 1] = bf_hi(a[t][h]) * ATT_SCALE; }
                    else if (st == 2) a[t][h] = pack_bf16(x[t][2 * h], x[t][2 * h + 1]);
                    else { x[t][2 * h] = bf_lo(a[t][h]); x[t][2 * h + 1] = bf_hi(a[t][h]); }
                }
            SBAR();
        };
        logits(1, 0); logits(1, 1);
        sp[0] = MFMA16(kp[0], bf8(qfr[0]), mfirst); sp[2] = MFMA16(kp[1], bf8(qfr[2]), mfirst);
        SBAR();
        logits(1, 2); logits(1, 3);
        logits(0, 0); logits(0, 1); logits(0, 2); logits(0, 3);
        auto across = [&](float (&v)[4], auto op, auto fin) {
            const auto p = __builtin_amdgcn_permlane16_swap(__float_as_uint(v[0]), __float_as_uint(v[1]), false, false);
            const auto q = __builtin_amdgcn_permlane16_swap(__float_as_uint(v[2]), __float_as_uint(v[3]), false, false);
            const auto r = __builtin_amdgcn_permlane32_swap(__float_as_uint(op(__uint_as_float(p[0]), __uint_as_float(p[1]))),
                                                            __float_as_uint(op(__uint_as_float(q[0]), __uint_as_float(q[1]))), false, false);
            const unsigned z = __float_as_uint(fin(op(__uint_as_float(r[0]), __uint_as_float(r[1]))));
            const auto u = __builtin_amdgcn_permlane32_swap(z, z, false, false);
            const auto e = __builtin_amdgcn_permlane16_swap(u[0], u[0], false, false), f = __builtin_amdgcn_permlane16_swap(u[1], u[1], false, false);
            v[0] = __uint_as_float(e[0]); v[1] = __uint_as_float(e[1]); v[2] = __uint_as_float(f[0]); v[3] = __uint_as_float(f[1]);
        };
#pragma unroll
        for (int t = 0; t < 4; ++t) m[t] = amax3(x[t][0], x[t][1], x[t][2]);
        asm("v_max_f32 %0, %0, %4\n\tv_max_f32 %1, %1, %5\n\tv_max_f32 %2, %2, %6\n\tv_max_f32 %3, %3, %7\n\ts_nop 0"
            : "+v"(m[0]), "+v"(m[1]), "+v"(m[2]), "+v"(m[3]) : "v"(x[0][3]), "v"(x[1][3]), "v"(x[2][3]), "v"(x[3][3]));
        across(m, [](float p, float q) { float r; asm("v_max_f32 %0, %1, %2\n\ts_nop 0" : "=v"(r) : "v"(p), "v"(q)); return r; },
               [](float p) { return p * -LOG2E; });
        SBAR();
#pragma unroll
        for (int t = 0; t < 4; ++t)
#pragma unroll
            for (int r = 0; r < 4; ++r) x[t][r] = __builtin_fmaf(x[t][r], LOG2E, m[t]);
        SBAR();
#pragma unroll
        for (int t = 0; t < 4; ++t)
#pragma unroll
            for (int r = 0; r < 4; ++r) x[t][r] = __builtin_amdgcn_exp2f(x[t][r]);
        SBAR();
#pragma unroll
        for (int t = 0; t < 4; ++t) m[t] = (x[t][0] + x[t][1]) + (x[t][2] + x[t][3]);
        asm volatile("s_waitcnt lgkmcnt(0)" : "+v"(vp[0][0]), "+v"(vp[0][1]), "+v"(vp[1][0]), "+v"(vp[1][1]) :: "memory");
        u32x4 ve[2][2];
#pragma unroll
        for (int u = 0; u < 2; ++u)
#pragma unroll
            for (int f = 0; f < 2; ++f) ve[u][f] = u32x4{vp[u][f][0], vp[u][f][1], va[u][f][0], va[u][f][1]};
        across(m, [](float p, float q) { return p + q; }, [](float p) { return __builtin_amdgcn_rcpf(p); });
        asm volatile("" : "+v"(ve[0][0]), "+v"(ve[0][1]), "+v"(ve[1][0]), "+v"(ve[1][1]));
        SBAR();
#pragma unroll
        for (int t = 0; t < 4; ++t)
#pragma unroll
            for (int r = 0; r < 4; ++r) x[t][r] *= m[t];
        SBAR();
#pragma unroll
        for (int t = 0; t < 4; ++t)
#pragma unroll
            for (int h = 0; h < 2; ++h) a[t][h] = pack_bf16(x[t][2 * h], x[t][2 * h + 1]);
        SBAR();
        f32x4 o0[4], o1[4];
#pragma unroll
        for (int t = 0; t < 4; ++t) {
            const bf16x8 pb = bf8(u32x4{a[t][0] & ~cm01, a[t][1] & ~cm23, a[t][0] & cm01, a[t][1] & cm23});
            o0[t] = MFMA16(bf8((t & 1) ? va[t >> 1][0] : ve[t >> 1][0]), pb, zero4);
            o1[t] = MFMA16(bf8((t & 1) ? va[t >> 1][1] : ve[t >> 1][1]), pb, zero4);
        }
        SBAR();
#pragma unroll
        for (int t = 0; t < 4; ++t)
            st16(ofrag + t * 16384, qo, u32x4{pack_bf16(o0[t][0], o0[t][1]), pack_bf16(o0[t][2], o0[t][3]),
                                                 pack_bf16(o1[t][0], o1[t][1]), pack_bf16(o1[t][2], o1[t][3])});
    }
}

DI int out_row(int panel, bool localrows, int T, int a) {
    return localrows ? (panel * 128 + (T >> 2) * 64 + 2 * (16 * (T & 1) + a) + ((T >> 1) & 1)) : (panel * 128 + T * 16 + a);
}
DI int out_swz(int k) { return ((k >> 1) & 1) | (k & 4); }
DI unsigned wo_off(int w, int lane) {
    const int kl = lane >> 3;
    unsigned v = (unsigned)(w * 8 + kl) * 2048u + (unsigned)(ids().co * 128 + ((lane & 7) ^ out_swz(kl)) * 16);
    asm volatile("" : "+v"(v));
    return v;
}
DI void wo_load(const void* Wo, unsigned voff, int i0, int i1, unsigned lds0, int w) {
#pragma unroll
    for (int i = i0; i < i1; ++i) glds16((const char*)Wo + (size_t)i * 65536, voff, lds0 + (w + 4 * i) * 1024);
}
DI unsigned res_off(bool localrows, bool tiled, int lane) {
    const Ids I = ids();
    unsigned v = (unsigned)(I.po * 262144 + I.co * (tiled ? 16384 : 128) + out_row(0, localrows, 0, lane >> 3) * (tiled ? 128 : 2048) +
                            (((lane & 7) ^ (lane >> 3)) * 16));
    asm volatile("" : "+v"(v));
    return v;
}
DI void res_load(u32x4 (&res)[6], const void* Xin, bool localrows, bool tiled, unsigned off, int w) {
#pragma unroll
    for (int i = 0; i < 6; ++i) {
        const int j = min(w + 3 * i, 15);
        asm volatile("global_load_dwordx4 %0, %1, %2 sc1" : "=&v"(res[i]) : "v"(off), "s"((const char*)Xin + out_row(0, localrows, j >> 1, (j & 1) * 8) * (tiled ? 128 : 2048)) : "memory");
    }
}
DI void out_handoff(char* smem, int xoff, u32x4 (&res)[6], int w, char* ws, int set, u64 tag, int lane, u32x4 (&c)[8], int mat) {
    const Ids I = ids();
    u64 land;
    lds_ptr dst = (lds_ptr)smem + xoff + w * 1024 + lane * 16;
    asm volatile("" : "+v"(dst));
    if (w == 3) __builtin_amdgcn_s_setprio(3);
    signal_poll<false, false>(w - 3, ws, flag_off(set, I.pq, lane & 15, I.cq), flag_off(set, I.po, I.co, lane & 15), 0xffffull, tag, land);
    if (w < 3) {
        asm volatile("s_waitcnt vmcnt(0)" : "+v"(res[0]), "+v"(res[1]), "+v"(res[2]), "+v"(res[3]), "+v"(res[4]), "+v"(res[5]) :: "memory");
#pragma unroll
        for (int i = 0; i < 5; ++i) *(__attribute__((address_space(3))) u32x4*)(dst + i * 3072) = res[i];
        if (w == 0) *(__attribute__((address_space(3))) u32x4*)(dst + 15360) = res[5];
        if (mat >= 0) colsum_store(c, ws, mat, w, lane);
    }
    WAIT_LGKM_BAR();
    asm volatile("s_waitcnt vmcnt(0)" : "+v"(land) :: "memory");
    if (w < 3 && mat >= 0) flag_store(ws, (mat ? 10 : 13) + w, I.cq, I.pq, tag, lane);
    __builtin_amdgcn_s_setprio(0);
}

template <bool AFTER_LOCAL>
DI void out_compute(char* smem, int w, const __hip_bfloat16* __restrict__ ofrag,
                    __hip_bfloat16* __restrict__ Xout, float* __restrict__ statp SP_PARAM) {
    constexpr int XOFF = AFTER_LOCAL ? 147456 : 98304;
    STAMP(AFTER_LOCAL ? 3 : 10);
    VM_BAR(0);
    const int lane = lane_id();
    asm volatile("" : "+s"(w));
    const Ids I = ids();
    const int panel = I.po, ct = I.co;
    const int l15 = lane & 15, g = lane >> 4;
    const bool tiled = AFTER_LOCAL || statp != nullptr;
    const int rstr = tiled ? 128 : 2048;
    const int q4 = l15 >> 2, p4 = l15 & 3;
    const int krow = 4 * g + q4;
    const int sw = out_swz(krow);
    const lds_cptr sm = (lds_cptr)smem;
    lds_cptr bw[4], xo[4];
#pragma unroll
    for (int t = 0; t < 4; ++t) {
        bw[t] = sm + krow * 128 + ((((t >> 1) * 4 + (p4 >> 1) * 2 + (t & 1)) ^ sw) * 16) + (p4 & 1) * 8;
        xo[t] = sm + XOFF + (w * 32 + l15) * 128 + ((((t >> 1) * 4 + (g >> 1) * 2 + (t & 1)) ^ (l15 & 7)) * 16) + (g & 1) * 8;
    }
    unsigned so[2], sto, ao[2];
#pragma unroll
    for (int r2 = 0; r2 < 2; ++r2) {
        so[r2] = (unsigned)(panel * 262144 + ct * (tiled ? 16384 : 128) + out_row(0, AFTER_LOCAL, 2 * w + r2, l15 & 7) * rstr + (l15 >> 3) * 64 + g * 16);
        ao[r2] = (unsigned)((panel * 8 + 2 * w + r2) * 32768 + lane * 16);
    }
    sto = (unsigned)(((panel * 16 + ct) * 128 + 2 * w * 16 + l15) * 8);
    asm volatile("" : "+v"(bw[0]), "+v"(bw[1]), "+v"(bw[2]), "+v"(bw[3]), "+v"(xo[0]), "+v"(xo[1]), "+v"(xo[2]), "+v"(xo[3]),
                 "+v"(so[0]), "+v"(so[1]), "+v"(sto), "+v"(ao[0]), "+v"(ao[1]));
    bf16x8 ones;
    {
        unsigned o1; asm volatile("v_mov_b32 %0, 0x3f803f80" : "=v"(o1));
        ones = __builtin_bit_cast(bf16x8, (u32x4{o1, o1, o1, o1}));
        asm volatile("" : "+v"(ones));
    }
    const __amdgpu_buffer_rsrc_t ra = __builtin_amdgcn_make_buffer_rsrc((void*)ofrag, 0, 4 << 20, 0x00020000);
    asm volatile("" :: "s"(ra));
    u32x32 fr;
    BAR();
    STAMP(AFTER_LOCAL ? 4 : 11);
    u32x32 aw0, aw1;
    asm volatile(".irp j,0,1,2,3\n\tbuffer_load_dwordx4 v[64+8*\\j:67+8*\\j], %2, %4, 0 offen offset:1024*\\j sc1\n\t"
                 "buffer_load_dwordx4 v[68+8*\\j:71+8*\\j], %3, %4, 0 offen offset:1024*\\j sc1\n\t.endr\n\t"
                 ".irp j,0,1\n\tbuffer_load_dwordx4 v[96+8*\\j:99+8*\\j], %2, %4, %5 offen offset:1024*\\j sc1\n\t"
                 "buffer_load_dwordx4 v[100+8*\\j:103+8*\\j], %3, %4, %5 offen offset:1024*\\j sc1\n\t.endr"
                 : "=&{v[64:95]}"(aw0), "=&{v[96:127]}"(aw1) : "v"(ao[0]), "v"(ao[1]), "s"(ra), "s"(4096) : "memory");
    f32x16 acc[2];
#pragma unroll
    for (int r2 = 0; r2 < 2; ++r2)
#pragma unroll
        for (int c = 0; c < 4; ++c) {
            const u32x2 xr = *(const __attribute__((address_space(3))) u32x2*)(xo[c] + r2 * 2048);
            acc[r2][4 * c] = bf_lo(xr[0]); acc[r2][4 * c + 1] = bf_hi(xr[0]); acc[r2][4 * c + 2] = bf_lo(xr[1]); acc[r2][4 * c + 3] = bf_hi(xr[1]);
        }
    asm volatile(
        ".macro OUT_MM r, t, j\n\t"
        "v_mfma_f32_16x16x32_bf16 v[16*\\r+4*\\t:16*\\r+4*\\t+3], v[32+16*((\\j)&1)+4*\\t:35+16*((\\j)&1)+4*\\t], v[64+8*\\j+4*\\r:67+8*\\j+4*\\r], v[16*\\r+4*\\t:16*\\r+4*\\t+3]\n\t"
        ".endm\n\t"
        ".macro OUT_RD t, hi, j, on\n\t"
        ".if \\on\n\tds_read_b64_tr_b16 v[32+16*((\\j)&1)+4*\\t+2*\\hi:33+16*((\\j)&1)+4*\\t+2*\\hi], v[128+\\t] offset:((\\j)&7)*4096+2048*\\hi\n\t.endif\n\t"
        ".endm\n\t"
        ".macro OUT_UP t, on\n\t.if \\on\n\tv_add_u32 v[128+\\t], s97, v[128+\\t]\n\t.endif\n\t.endm\n\t"
        ".macro OUT_STEP j, ld, rd, up, vm, lg\n\t"
        "OUT_MM 0, 0, \\j\n\tOUT_RD 3, 1, \\j+1, 1\n\tOUT_UP 0, \\up\n\t"
        ".if \\ld && (((\\j+6)&3) == 0)\n\ts_addk_i32 s96, 0x1000\n\t.endif\n\t"
        "OUT_MM 1, 0, \\j\n\tOUT_RD 0, 0, \\j+2, \\rd\n\t"
        ".if \\ld\n\tbuffer_load_dwordx4 v[64+8*((\\j+6)&7):67+8*((\\j+6)&7)], %[a0], %[ra], s96 offen offset:1024*((\\j+6)&3) sc1\n\t.endif\n\t"
        "OUT_MM 0, 1, \\j\n\tOUT_RD 0, 1, \\j+2, \\rd\n\tOUT_UP 1, \\up\n\t"
        "OUT_MM 1, 1, \\j\n\tOUT_RD 1, 0, \\j+2, \\rd\n\t"
        "OUT_MM 0, 2, \\j\n\tOUT_RD 1, 1, \\j+2, \\rd\n\tOUT_UP 2, \\up\n\t"
        "OUT_MM 1, 2, \\j\n\tOUT_RD 2, 0, \\j+2, \\rd\n\t"
        ".if \\ld\n\tbuffer_load_dwordx4 v[68+8*((\\j+6)&7):71+8*((\\j+6)&7)], %[a1], %[ra], s96 offen offset:1024*((\\j+6)&3) sc1\n\t.endif\n\t"
        "OUT_MM 0, 3, \\j\n\tOUT_RD 2, 1, \\j+2, \\rd\n\tOUT_UP 3, \\up\n\t"
        "OUT_MM 1, 3, \\j\n\tOUT_RD 3, 0, \\j+2, \\rd\n\t"
        "s_waitcnt vmcnt(\\vm) lgkmcnt(\\lg)\n\t"
        ".endm\n\t"
        ".macro OUT_PACK y, c\n\t"
        ".irp i,0,1,2,3,4,5,6,7\n\tv_cvt_pk_bf16_f32 v[\\y+\\i], v[\\c+2*\\i], v[\\c+2*\\i+1]\n\t.endr\n\t"
        ".irp i,0,1,4,5\n\tv_permlane16_swap_b32 v[\\y+\\i], v[\\y+\\i+2]\n\t.endr\n\t"
        ".irp i,0,1,2,3\n\tv_mov_b32 v[\\y+8+\\i], v[\\y+\\i]\n\t.endr\n\t"
        "v_mfma_f32_16x16x32_bf16 v[\\y+12:\\y+15], v[\\y:\\y+3], v[\\y:\\y+3], 0\n\t"
        "v_mov_b32_dpp v[\\y+8], v[\\y+4] row_ror:8 row_mask:0xf bank_mask:0xc\n\tv_mov_b32_dpp v[\\y+9], v[\\y+5] row_ror:8 row_mask:0xf bank_mask:0xc\n\t"
        "v_mfma_f32_16x16x32_bf16 v[\\y+16:\\y+19], %[one], v[\\y:\\y+3], 0\n\t"
        "v_mov_b32_dpp v[\\y+10], v[\\y+6] row_ror:8 row_mask:0xf bank_mask:0xc\n\tv_mov_b32_dpp v[\\y+11], v[\\y+7] row_ror:8 row_mask:0xf bank_mask:0xc\n\t"
        "v_mfma_f32_16x16x32_bf16 v[\\y+12:\\y+15], v[\\y+4:\\y+7], v[\\y+4:\\y+7], v[\\y+12:\\y+15]\n\t"
        ".endm\n\t"
        ".macro OUT_REST y\n\t"
        "v_mfma_f32_16x16x32_bf16 v[\\y+16:\\y+19], %[one], v[\\y+4:\\y+7], v[\\y+16:\\y+19]\n\t"
        ".irp i,0,1,2,3\n\tv_mov_b32_dpp v[\\y+4+\\i], v[\\y+\\i] row_ror:8 row_mask:0xf bank_mask:0x3\n\t.endr\n\t"
        ".endm\n\t"
        ".macro OUT_DIAG y\n\t"
        "v_cndmask_b32 v[\\y+12], v[\\y+12], v[\\y+13], %[m0]\n\tv_cndmask_b32 v[\\y+14], v[\\y+14], v[\\y+15], %[m0]\n\t"
        "v_cndmask_b32 v[\\y+17], v[\\y+12], v[\\y+14], %[m1]\n\t"
        ".endm\n\t"
        ".irp t,0,1,2,3\n\tOUT_RD \\t, 0, 0, 1\n\tOUT_RD \\t, 1, 0, 1\n\t.endr\n\t"
        ".irp t,0,1,2\n\tOUT_RD \\t, 0, 1, 1\n\tOUT_RD \\t, 1, 1, 1\n\t.endr\n\tOUT_RD 3, 0, 1, 1\n\t"
        "s_movk_i32 s96, 0x1000\n\ts_mov_b32 s97, 0x8000\n\t"
        "s_waitcnt vmcnt(10) lgkmcnt(0)\n"
        ".Lout_loop_%=:\n\t"
        "OUT_STEP 0, 1, 1, 0, 10, 7\n\tOUT_STEP 1, 1, 1, 0, 10, 7\n\tOUT_STEP 2, 1, 1, 0, 10, 7\n\tOUT_STEP 3, 1, 1, 0, 10, 7\n\t"
        "OUT_STEP 4, 1, 1, 0, 10, 7\n\t"
        "s_cmpk_eq_u32 s96, 0x6000\n\ts_cselect_b32 s97, %[last], s97\n\t"
        "OUT_STEP 5, 1, 1, 0, 10, 7\n\tOUT_STEP 6, 1, 1, 1, 10, 7\n\t"
        "OUT_STEP 7, 1, 1, 0, 10, 7\n\t"
        "s_cmpk_lg_u32 s96, 0x7000\n\ts_cbranch_scc1 .Lout_loop_%=\n\t"
        "OUT_STEP 0, 1, 1, 0, 10, 7\n\tOUT_STEP 1, 1, 1, 0, 10, 7\n\tOUT_STEP 2, 0, 1, 0, 8, 7\n\tOUT_STEP 3, 0, 1, 0, 6, 7\n\t"
        "OUT_STEP 4, 0, 1, 0, 4, 7\n\tOUT_STEP 5, 0, 1, 0, 2, 7\n\tOUT_STEP 6, 0, 0, 0, 0, 0\n\t"
        ".irp r,0,1\n\t.irp t,0,1,2,3\n\tOUT_MM \\r, \\t, 7\n\t.endr\n\t.endr\n\t"
        "s_nop 0\n\t"
        "OUT_PACK 64, 0\n\tglobal_store_dwordx4 %[s0], v[72:75], %[xa] sc1\n\tOUT_REST 64\n\tglobal_store_dwordx4 %[s0], v[68:71], %[xb] sc1\n\t"
        "OUT_PACK 84, 16\n\tglobal_store_dwordx4 %[s1], v[92:95], %[xa] sc1\n\tOUT_REST 84\n\tglobal_store_dwordx4 %[s1], v[88:91], %[xb] sc1\n\t"
        "s_cmp_eq_u64 %[st], 0\n\ts_cbranch_scc1 .Lout_end_%=\n\t"
        "OUT_DIAG 64\n\tOUT_DIAG 84\n\t"
        "s_mov_b64 exec, %[md]\n\t"
        "global_store_dwordx2 %[so], v[80:81], %[st] sc1\n\tglobal_store_dwordx2 %[so], v[100:101], %[st] offset:128 sc1\n\t"
        "s_mov_b64 exec, -1\n"
        ".Lout_end_%=:\n\t"
        "s_nop 1\n\t"
        ".purgem OUT_MM\n\t.purgem OUT_RD\n\t.purgem OUT_UP\n\t.purgem OUT_STEP\n\t.purgem OUT_PACK\n\t.purgem OUT_REST\n\t.purgem OUT_DIAG"
        : "+{v[0:15]}"(acc[0]), "+{v[16:31]}"(acc[1]), "+{v[64:95]}"(aw0), "+{v[96:127]}"(aw1),
          "+{v128}"(bw[0]), "+{v129}"(bw[1]), "+{v130}"(bw[2]), "+{v131}"(bw[3]), "=&{v[32:63]}"(fr)
        : [a0] "v"(ao[0]), [a1] "v"(ao[1]), [ra] "s"(ra), [last] "s"(AFTER_LOCAL ? 32768 : 65536), [one] "v"(ones), [s0] "v"(so[0]), [s1] "v"(so[1]),
          [xa] "s"(Xout), [xb] "s"((char*)Xout + (AFTER_LOCAL ? 16 : 8) * rstr), [st] "s"(statp), [so] "v"(sto),
          [m0] "s"(0xaaaaaaaaaaaaaaaaull), [m1] "s"(0xccccccccccccccccull), [md] "s"(0xf0000f0000f0000full)
        : "memory", "scc", "s96", "s97");
    STAMP(AFTER_LOCAL ? 5 : 12);
}

#define ROWSUM(p0, p1, c) __builtin_amdgcn_smfmac_f32_16x16x64_bf16(sel, __builtin_bit_cast(bf16v_t, __builtin_shufflevector(p0, p1, 0, 1, 2, 3, 4, 5, 6, 7)), c, sidx, 0, 0)
constexpr int ATT_STAGE = 32768;
typedef __attribute__((ext_vector_type(8))) unsigned u32x8;

DI void attn_loader(char* smem, int w, char* ws, int set, u64 tag, const __hip_bfloat16* __restrict__ kvf, const void* Wo, const void* Xin,
                    const void* Wcs SP_PARAM) {
    const int lane = lane_id();
    asm volatile("" : "+s"(w));
    const Ids I = ids();
    const int panel = I.pq, ct = I.cq;
    const unsigned lds0 = (unsigned)(uintptr_t)smem;
    const char* kvh = (const char*)kvf + (size_t)(ct * 2 + (w >> 1)) * 262144 + (w & 1) * 4096;
    const unsigned voff = (unsigned)(lane * 16);
    auto order = [&](int st) { return ((panel & 8) + st - ((st - 1 < (panel & 7)) ? 1 : 0)) & 15; };
    auto load = [&](int st) {
        const char* src = kvh + (size_t)order(st) * 16384;
        const unsigned dst = lds0 + (w >> 1) * 16384 + (w & 1) * 4096 + (st & 3) * ATT_STAGE;
#pragma unroll
        for (int u = 0; u < 2; ++u)
            asm volatile("s_mov_b32 m0, %0\n\ts_nop 0\n\tglobal_load_lds_dwordx4 %1, %2 sc1\n\tglobal_load_lds_dwordx4 %1, %2 offset:1024 sc1\n\t"
                         "global_load_lds_dwordx4 %1, %2 offset:2048 sc1\n\tglobal_load_lds_dwordx4 %1, %2 offset:3072 sc1"
                         :: "s"(dst + u * 8192), "v"(voff), "s"(src + u * 8192) : "memory");
    };
    const unsigned wv = wo_off(w, lane), rv = res_off(false, true, lane), cv = colsum_off(w, lane);
    const unsigned poff = flag_off(set, ct, panel, lane & 15);
    u64 land;
    __builtin_amdgcn_s_setprio(3);
    BAR();
    signal_poll<false, true>(w, ws, flag_off(set, ct, lane & 15, panel), poff, 1ull << order(1), tag, land);
    STAMP(9);
    load(1);
    asm volatile("global_load_dwordx2 %0, %1, %2 sc1\n\ts_waitcnt vmcnt(0)" : "+v"(land) : "v"(poff), "s"(ws + WS_FLAG) : "memory");
    STAMP(10);
    BAR();
    if (!__all(land == tag)) flag_poll(ws, poff, tag);
    load(2); load(3);
    __builtin_amdgcn_s_setprio(0);
    STAMP(11);
    VM_BAR(8); load(4);
    VM_BAR(0); load(5); load(6);
    VM_BAR(0); load(7); load(8);
    u32x4 res[6], ca[8];
    colsum_load(ca, w < 3 ? Wcs : nullptr, cv);
#pragma unroll 1
    for (int st = 6; st < 12; st += 2) {
        VM_BAR(0);
        load(st + 3); load(st + 4);
        if (st == 8) wo_load(Wo, wv, 24, 32, lds0 + 32768, w);
    }
    VM_BAR(0); load(15); wo_load(Wo, wv, 0, 8, lds0, w);
    res_load(res, Xin, false, true, rv, w);
    VM_BAR(0); wo_load(Wo, wv, 8, 24, lds0, w);
    STAMP(12);
    out_handoff(smem, 98304, res, w, ws, set + 1, tag, lane, ca, Wcs ? 0 : -1);
}

DI void attn_compute(char* smem, int w, const u32x4 (&q4)[4], const __hip_bfloat16* __restrict__ kvf, __hip_bfloat16* __restrict__ ofrag SP_PARAM) {
    const int lane = lane_id();
    asm volatile("" : "+s"(w));
    const int wm = w & 1, wn = w >> 1;
    const Ids I = ids();
    const int panel = I.pq, head = I.cq * 2 + wn;
    const int l15 = lane & 15;
    const unsigned se = ((l15 & 2) == 0 && (l15 & 4) == 0) ? 0x3f803f80u : 0u, so = ((l15 & 2) == 0 && (l15 & 4) != 0) ? 0x3f803f80u : 0u;
    const bf8_t sel = __builtin_bit_cast(bf8_t, u32x4{se, se, so, so});
    const int sidx = (l15 & 1) ? (int)0xEEEEEEEEu : 0x44444444;
    const lds_cptr sm = (lds_cptr)smem + wn * 16384 + lane * 16;
    const f32x16 z16 = {0, 0, 0, 0, 0, 0, 0, 0, 0, 0, 0, 0, 0, 0, 0, 0};
    const bf16x8 qA0 = __builtin_bit_cast(bf16x8, q4[0]), qA1 = __builtin_bit_cast(bf16x8, q4[1]);
    const bf16x8 qB0 = __builtin_bit_cast(bf16x8, q4[2]), qB1 = __builtin_bit_cast(bf16x8, q4[3]);
    f32x16 oA, oB, nmA, nmB, sA, sB;
    f32x4 lsA, lsB;
    u32x8 pB;
    bf16x8 v11;
    asm volatile(".irp r,106,108,110,112,134,136,138,140,170,172\n\tv_mov_b64 v[\\r:\\r+1], 0\n\t.endr"
                 : "={v[106:113]}"(pB), "={v[134:137]}"(lsA), "={v[138:141]}"(lsB), "={v[170:173]}"(v11));
    WAIT_LGKM_BAR();
    STAMP(8);
    bf16x8 k00 = lds_b128(sm), k10 = lds_b128(sm + 1024), k01 = lds_b128(sm + 4096), k11 = lds_b128(sm + 4096 + 1024);
    bf16x8 v00 = lds_b128(sm + 2048), v10 = lds_b128(sm + 3072), v01 = lds_b128(sm + 4096 + 2048);
    float ob;
    {
        const u32x4 vu = __builtin_bit_cast(u32x4, v00);
        unsigned vm = max(max(vu[0] & 0x7fff0000u, vu[1] & 0x7fff0000u), max(vu[2] & 0x7fff0000u, vu[3] & 0x7fff0000u));
        vm = min(vm, (unsigned)__builtin_amdgcn_update_dpp(0, (int)vm, 0xB1, 0xF, 0xF, true));
        vm = min(vm, (unsigned)__builtin_amdgcn_update_dpp(0, (int)vm, 0x4E, 0xF, 0xF, true));
        vm = min(vm, (unsigned)__builtin_amdgcn_update_dpp(0, (int)vm, 0x141, 0xF, 0xF, true));
        vm = min(vm, (unsigned)__builtin_amdgcn_update_dpp(0, (int)vm, 0x140, 0xF, 0xF, true));
        const auto vs = __builtin_amdgcn_permlane16_swap(vm, vm, false, false);
        ob = __uint_as_float(min(vs[0], vs[1])) * 0.015625f;
        sA = MFMA32(k00, qA0, z16); sB = MFMA32(k00, qB0, z16);
        sA = MFMA32(k10, qA1, sA); sB = MFMA32(k10, qB1, sB);
        SBAR(); VM_BAR(0); SBAR();
        float mA = sA[0], mB = sB[0];
#pragma unroll
        for (int r = 1; r < 16; ++r) { mA = fmaxf(mA, sA[r]); mB = fmaxf(mB, sB[r]); }
        mA = max_h(mA) + 16.f; mB = max_h(mB) + 16.f;
#pragma unroll
        for (int r = 0; r < 16; ++r) { nmA[r] = -mA; nmB[r] = -mB; sA[r] -= mA; oA[r] = ob; oB[r] = ob; }
    }
    const unsigned a0 = (unsigned)(uintptr_t)sm;
    unsigned dst = (unsigned)((((panel * 8 + wm * 4 + ((lane & 31) >> 4)) * 32 + head) * 64 + (lane & 15) + (lane >> 5) * 16) * 16);
    asm volatile("" : "+v"(dst));
    asm volatile(
        ".macro ATT_RD dst, y\n\t"
        "ds_read_b128 v[\\dst:\\dst+3], v[175+(((\\y)&0x1ffff)>>16)] offset:(\\y)&0xffff\n\t"
        ".endm\n\t"
        ".macro ATT_E4 c\n\t"
        "v_exp_f32 v[\\c], v[\\c]\n\tv_exp_f32 v[\\c+1], v[\\c+1]\n\tv_exp_f32 v[\\c+2], v[\\c+2]\n\tv_exp_f32 v[\\c+3], v[\\c+3]\n\t"
        ".endm\n\t"
        ".macro ATT_C4 p, c\n\t"
        "v_cvt_pk_bf16_f32 v[\\p], v[\\c], v[\\c+1]\n\tv_cvt_pk_bf16_f32 v[\\p+1], v[\\c+2], v[\\c+3]\n\t"
        "v_cvt_pk_bf16_f32 v[\\p+2], v[\\c+4], v[\\c+5]\n\tv_cvt_pk_bf16_f32 v[\\p+3], v[\\c+6], v[\\c+7]\n\t"
        ".endm\n\t"
        ".macro ATT_STEP cur, nxt, ka, kb, q, nm, oc, od, lsd, pc, pd, vd1, vc0, r0, y0, r1, y1, bar, rd\n\t"
        "v_mfma_f32_32x32x16_bf16 v[\\nxt:\\nxt+15], v[\\ka:\\ka+3], v[\\q:\\q+3], v[\\nm:\\nm+15]\n\t"
        "ATT_E4 \\cur\n\t"
        "v_mfma_f32_32x32x16_bf16 v[\\nxt:\\nxt+15], v[\\kb:\\kb+3], v[\\q+4:\\q+7], v[\\nxt:\\nxt+15]\n\t"
        "ATT_E4 \\cur+4\n\t"
        "v_mfma_f32_32x32x16_bf16 v[\\od:\\od+15], v[\\vd1:\\vd1+3], v[\\pd+4:\\pd+7], v[\\od:\\od+15]\n\t"
        "s_waitcnt lgkmcnt(0)\n\t"
        "ATT_C4 \\pc, \\cur\n\t"
        "v_exp_f32 v[\\cur+8], v[\\cur+8]\n\tv_exp_f32 v[\\cur+9], v[\\cur+9]\n\t"
        "v_mfma_f32_32x32x16_bf16 v[\\oc:\\oc+15], v[\\vc0:\\vc0+3], v[\\pc:\\pc+3], v[\\oc:\\oc+15]\n\t"
        ".if \\bar\n\ts_barrier\n\t.endif\n\t"
        "ATT_E4 \\cur+10\n\t"
        "v_smfmac_f32_16x16x64_bf16 v[\\lsd:\\lsd+3], v[130:133], v[\\pd:\\pd+7], v174\n\t"
        ".if \\rd & 4\n\tATT_RD 142, 32768\n\tATT_RD 150, 33792\n\tATT_RD 158, 34816\n\t.endif\n\t"
        ".if \\rd & 8\n\tATT_RD \\r1-8, \\y1-1024\n\t.endif\n\t"
        ".if \\rd & 1\n\tATT_RD \\r0, \\y0\n\t.endif\n\t"
        ".if \\rd & 2\n\tATT_RD \\r1, \\y1\n\t.endif\n\t"
        "v_exp_f32 v[\\cur+14], v[\\cur+14]\n\tv_exp_f32 v[\\cur+15], v[\\cur+15]\n\t"
        "ATT_C4 \\pc+4, \\cur+8\n\t"
        ".if \\rd & 4\n\ts_waitcnt lgkmcnt(3)\n\t.endif\n\t"
        ".endm\n\t"
        ".macro ATT_TILE p, y1, y2, bar, ra=3, rb=3\n\t"
        "ATT_STEP 66, 82, 142+\\p, 150+\\p, 122, 50, 2, 18, 138, 98, 106, 170-\\p, 158+\\p, 170-\\p, \\y1+3072, 142+\\p, \\y2, \\bar&1, \\ra\n\t"
        "ATT_STEP 82, 66, 146-\\p, 154-\\p, 114, 34, 18, 2, 134, 106, 98, 166+\\p, 158+\\p, 158+\\p, \\y2+2048, 150+\\p, \\y2+1024, \\bar&2, \\rb\n\t"
        ".endm\n\t"
        ".macro ATT_STAGE b, bar\n\t"
        "ATT_TILE 0, \\b+4096, \\b+8192, 0\n\t"
        "ATT_TILE 4, \\b+8192, \\b+12288, 0\n\t"
        ".if \\bar\n\tATT_TILE 0, \\b+12288, \\b+32768, 2, 1, 11\n\t.else\n\tATT_TILE 0, \\b+12288, \\b+32768, 0\n\t.endif\n\t"
        "ATT_TILE 4, \\b+32768, \\b+36864, 0\n\t"
        ".endm\n\t"
        "v_add_u32 v176, 0x10000, v175\n\t"
        "s_mov_b32 s96, 4\n\t"
        "s_waitcnt lgkmcnt(0)\n\t"
        "ATT_TILE 0, 4096, 8192, 0\n\tATT_TILE 4, 8192, 12288, 0\n\tATT_TILE 0, 12288, 0, 0, 1, 0\n\tATT_TILE 4, 32768, 36864, 1, 7, 3\n\t"
        "ATT_STAGE 32768, 1\n\t"
        "s_branch .Latt_entry_%=\n\t"
        ".p2align 6\n"
        ".Latt_loop_%=:\n\t"
        "ATT_STAGE 0, 1\n\tATT_STAGE 32768, 0\n"
        ".Latt_entry_%=:\n\t"
        "ATT_STAGE 65536, 1\n\t"
        "s_sub_u32 s96, s96, 1\n\t"
        "ATT_STAGE 98304, 0\n\t"
        "s_cmp_lg_u32 s96, 0\n\t"
        "s_cbranch_scc1 .Latt_loop_%=\n\t"
        "s_nop 1\n\t"
        "v_mfma_f32_32x32x16_bf16 v[18:33], v[170:173], v[110:113], v[18:33]\n\t"
        "v_smfmac_f32_16x16x64_bf16 v[138:141], v[130:133], v[106:113], v174\n\t"
        "s_waitcnt lgkmcnt(0)\n\ts_nop 10\n\t"
        ".purgem ATT_RD\n\t.purgem ATT_E4\n\t.purgem ATT_C4\n\t.purgem ATT_STEP\n\t.purgem ATT_TILE\n\t.purgem ATT_STAGE"
        : "+{v[2:17]}"(oA), "+{v[18:33]}"(oB), "+{v[66:81]}"(sA), "+{v[106:113]}"(pB), "+{v[134:137]}"(lsA), "+{v[138:141]}"(lsB),
          "+{v[142:145]}"(k00), "+{v[146:149]}"(k01), "+{v[150:153]}"(k10), "+{v[154:157]}"(k11),
          "+{v[158:161]}"(v00), "+{v[162:165]}"(v01), "+{v[166:169]}"(v10), "+{v[170:173]}"(v11)
        : "{v[34:49]}"(nmA), "{v[50:65]}"(nmB), "{v[114:117]}"(qA0), "{v[118:121]}"(qA1), "{v[122:125]}"(qB0), "{v[126:129]}"(qB1),
          "{v[130:133]}"(sel), "{v174}"(sidx), "{v175}"(a0)
        : "memory", "scc", "s96", "v176", "v82", "v83", "v84", "v85", "v86", "v87", "v88", "v89", "v90", "v91", "v92", "v93", "v94", "v95",
          "v96", "v97", "v98", "v99", "v100", "v101", "v102", "v103", "v104", "v105");
    STAMP(9);
    float lA = lsA[0] + lsA[1], lB = lsB[0] + lsB[1];
    auto store = [&] {
#pragma unroll
    for (int u = 0; u < 2; ++u) {
        const float inv = __builtin_amdgcn_rcpf(u ? lB : lA), sub = -ob * inv;
        const f32x16 e = (u ? oB : oA) * inv + sub;
        st16o<0>((char*)ofrag + u * 65536, dst, u32x4{pack_bf16(e[0], e[1]), pack_bf16(e[2], e[3]), pack_bf16(e[8], e[9]), pack_bf16(e[10], e[11])});
        st16o<512>((char*)ofrag + u * 65536, dst, u32x4{pack_bf16(e[4], e[5]), pack_bf16(e[6], e[7]), pack_bf16(e[12], e[13]), pack_bf16(e[14], e[15])});
    }
    };
    store();
    {
        const f32x16 os = oA + oB;
        const float osum = (((os[0] + os[1]) + (os[2] + os[3])) + ((os[4] + os[5]) + (os[6] + os[7]))) +
                           (((os[8] + os[9]) + (os[10] + os[11])) + ((os[12] + os[13]) + (os[14] + os[15])));
        const bool bad = !(fmaxf(lA, lB) < 1.0e24f) || !(lA > 1.0e-24f) || !(lB > 1.0e-24f) || !(fabsf(osum) < 3.0e38f);
                if (__any(bad)) {
            const __amdgpu_buffer_rsrc_t rk = __builtin_amdgcn_make_buffer_rsrc((char*)kvf + (size_t)head * 262144, 0, 262144, 0x00020000);
            oA = z16; oB = z16; lA = 0.f; lB = 0.f; ob = 0.f;
            float mA = -INFINITY, mB = -INFINITY;
#pragma unroll 1
            for (int t = 0; t < 64; ++t) {
                bf16x8 f[4];
#pragma unroll
                for (int i = 0; i < 4; ++i) f[i] = __builtin_bit_cast(bf16x8, __builtin_amdgcn_raw_buffer_load_b128(rk, lane * 16 + i * 1024, t * 4096, 16));
                auto upd = [&](bf16x8 q0, bf16x8 q1, f32x16& o, float& l, float& m) {
                    f32x16 s = MFMA32(f[0], q0, z16); s = MFMA32(f[1], q1, s);
                    float tm = s[0];
#pragma unroll
                    for (int r = 1; r < 16; ++r) tm = fmaxf(tm, s[r]);
                    tm = max_h(tm);
                    const float mn = fmaxf(m, tm);
                    const float alpha = __builtin_amdgcn_exp2f(m - mn);
                    m = mn;
#pragma unroll
                    for (int r = 0; r < 16; ++r) { s[r] = __builtin_amdgcn_exp2f(s[r] - mn); o[r] *= alpha; }
                    const u32x4 p0 = {pack_bf16(s[0], s[1]), pack_bf16(s[2], s[3]), pack_bf16(s[4], s[5]), pack_bf16(s[6], s[7])};
                    const u32x4 p1 = {pack_bf16(s[8], s[9]), pack_bf16(s[10], s[11]), pack_bf16(s[12], s[13]), pack_bf16(s[14], s[15])};
                    f32x4 t4 = {0, 0, 0, 0};
                    o = MFMA32(f[2], __builtin_bit_cast(bf16x8, p0), o);
                    o = MFMA32(f[3], __builtin_bit_cast(bf16x8, p1), o);
                    t4 = ROWSUM(p0, p1, t4);
                    l = l * alpha + (t4[0] + t4[1]);
                };
                upd(qA0, qA1, oA, lA, mA);
                upd(qB0, qB1, oB, lB, mB);
            }
            store();
        }
    }
}

DI void ln_row(const u32x4 (&v)[2], __hip_bfloat16* __restrict__ Y, unsigned off) {
    float s1 = 0.f, s2 = 0.f;
#pragma unroll
    for (int e = 0; e < 8; ++e) s1 = dot2bf(v[e >> 2][e & 3], 0x3f803f80u, s1);
    const float mean = wave_sum(s1) * (1.f / 1024.f);
#pragma unroll
    for (int e = 0; e < 8; ++e) {
        const float d0 = bf_lo(v[e >> 2][e & 3]) - mean, d1 = bf_hi(v[e >> 2][e & 3]) - mean;
        s2 += d0 * d0 + d1 * d1;
    }
    const float mb = round_bf16(mean);
    const float rden = __builtin_amdgcn_rcpf(round_bf16(__builtin_amdgcn_sqrtf(round_bf16(round_bf16(wave_sum(s2) * (1.f / 1024.f)) + 1e-5f))));
#pragma unroll
    for (int hh = 0; hh < 2; ++hh) {
        u32x4 y;
#pragma unroll
        for (int e = 0; e < 4; ++e) {
            const unsigned d = pack_bf16(bf_lo(v[hh][e]) - mb, bf_hi(v[hh][e]) - mb);
            y[e] = pack_bf16(bf_lo(d) * rden, bf_hi(d) * rden);
        }
        st16((char*)Y + hh * 131072, off, y);
    }
}

extern "C" __global__ __launch_bounds__(512, 2) void mega_kernel(const __hip_bfloat16* x, const __hip_bfloat16* wqkvA,
                                                            const __hip_bfloat16* woA, const __hip_bfloat16* wqkvB,
                                                            const __hip_bfloat16* woB, __hip_bfloat16* out,
                                                            char* ws) {
    char* const smem = smem_base;
    typedef __hip_bfloat16 bf;
    const int w8 = __builtin_amdgcn_readfirstlane(threadIdx.x >> 6);
    if (w8 == 7 && (blockIdx.x >> 3) < 10) code_touch(blockIdx.x >> 3, threadIdx.x & 63);
    asm volatile("" :: "s"(x), "s"(wqkvA), "s"(woA), "s"(wqkvB), "s"(woB), "s"(out), "s"(ws));
    STAMP_DECL; STAMPR(14); STAMP(0);
    u64 tag;
    {
        const int lane = threadIdx.x & 63;
        const Ids I = ids();
        const unsigned off = (unsigned)((I.po * 128 + I.co * 8 + w8) * 2048 + lane * 16);
        u32x4 v[2];
        u32x2 ep;
        asm volatile("global_load_dwordx4 %0, %3, %4\n\tglobal_load_dwordx4 %1, %3, %4 offset:1024\n\tglobal_load_dwordx2 %2, %5, %6 sc1"
                     : "=&v"(v[0]), "=&v"(v[1]), "=&v"(ep) : "v"(off), "s"(x), "v"(0), "s"(ws + WS_EPOCH) : "memory");
        if (w8 == 7) {
            const int l = lane & 15, k = l % 5;
            const unsigned o = (l >= 10 || k == 3) ? flag_off(l, I.cq, I.pq, 0) : (k == 0 || k == 2) ? flag_off(l, I.pq, I.cq, 0) : flag_off(l, I.po, I.co, 0);
            touch_at(ws + WS_FLAG, o);
        } else if (w8 >= 4) touch(wqkvA, (w8 - 4) * 256 + blockIdx.x, lane);
        if (w8 >= 4) asm volatile("s_waitcnt vmcnt(2)" ::: "memory"); else asm volatile("s_waitcnt vmcnt(1)" ::: "memory");
        asm volatile("" : "+v"(v[0]), "+v"(v[1]));
        ln_row(v, (bf*)(ws + WS_Y0), (unsigned)(I.po * 262144 + (lane >> 3) * 16384 + (I.co * 8 + w8) * 128 + (lane & 7) * 16));
        if (w8 >= 4) asm volatile("s_waitcnt vmcnt(3)" ::: "memory"); else asm volatile("s_waitcnt vmcnt(2)" ::: "memory");
        asm volatile("" : "+v"(ep));
        tag = (((u64)(unsigned)__builtin_amdgcn_readfirstlane(ep[1]) << 32) | (unsigned)__builtin_amdgcn_readfirstlane(ep[0])) + 1;
    }
    if (w8 >= 4) {
        int w = w8 & 3;
#pragma unroll 1
        for (int it = 0; it < 2; ++it) {
            const int lane = lane_id();
            asm volatile("" : "+s"(x), "+s"(wqkvA), "+s"(woA), "+s"(wqkvB), "+s"(woB), "+s"(ws), "+s"(w));
            const unsigned lds0 = (unsigned)(uintptr_t)smem;
            u32x4 cb[8];
            const unsigned wv = wo_off(w, lane), rv = res_off(true, it, lane), cv = colsum_off(w, lane);
            qkv_loader<true>(smem, w, ws, it * 5, tag, (const bf*)(ws + (it ? WS_X2 : WS_Y0)), wqkvA, (const float*)(ws + WS_ST + 262144),
                             ws + WS_CSP, it == 0, woA, [&] {
                if (w < 3) {
                    colsum_load(cb, wqkvB, cv);
                    asm volatile("s_waitcnt vmcnt(8)" ::: "memory");
                } else {
                    touch(woA, blockIdx.x, lane);
                    asm volatile("s_waitcnt vmcnt(1)" ::: "memory");
                }
            }, [&](int part) {
                if (part == 0) wo_load(woA, wv, 29, 32, lds0, w); else wo_load(woA, wv, 25, 29, lds0, w);
            } SP_ARG);
            STAMP(3);
            u32x4 res[6];
            res_load(res, it ? (const void*)(ws + WS_X2) : x, true, it, rv, w);
            if (it) __builtin_amdgcn_s_sleep(8);
            wo_load(woA, wv, 0, 7, lds0, w);
            BAR();
            wo_load(woA, wv, 7, 25, lds0, w);
            STAMP(4);
            out_handoff(smem, 147456, res, w, ws, it * 5 + 1, tag, lane, cb, it ? -1 : 1);
            STAMP(5);
            qkv_loader<false>(smem, w, ws, it * 5 + 2, tag, (const bf*)(ws + (it ? WS_X3 : WS_X1)), wqkvB, (const float*)(ws + WS_ST + it * 524288), ws + WS_CSP + 196608,
                              false, nullptr, [] {}, [](int) {} SP_ARG);
            STAMP(8);
            attn_loader(smem, w, ws, it * 5 + 3, tag, (const bf*)(ws + WS_KVF + it * 8 * MBy), woB, ws + (it ? WS_X3 : WS_X1),
                        it ? nullptr : wqkvA SP_ARG);
            STAMP(13); STAMPR(15); STAMP_FLUSH(it);
        }
        if (blockIdx.x == 0 && w == 0 && lane_id() == 0) __hip_atomic_store((g_u64ptr)(ws + WS_EPOCH), tag, __ATOMIC_RELAXED, __HIP_MEMORY_SCOPE_AGENT);
    } else {
        __builtin_amdgcn_s_setprio(2);
#pragma unroll 1
        for (int it = 0; it < 2; ++it) {
            u32x4 q4[4];
            asm volatile("" : "+s"(ws), "+s"(wqkvA), "+s"(wqkvB));
            qkv_compute<true>(smem, w8, wqkvA, q4, nullptr, (bf*)(ws + WS_OF + it * 8 * MBy), it == 0 SP_ARG);
            STAMP(2);
            out_compute<true>(smem, w8, (const bf*)(ws + WS_OF + it * 8 * MBy),
                              (bf*)(ws + (it ? WS_X3 : WS_X1)), (float*)(ws + WS_ST + it * 524288) SP_ARG);
            STAMP(6);
            qkv_compute<false>(smem, w8, wqkvB, q4, (bf*)(ws + WS_KVF + it * 8 * MBy), nullptr, false SP_ARG);
            attn_compute(smem, w8, q4, (const bf*)(ws + WS_KVF + it * 8 * MBy), (bf*)(ws + WS_OF + (it * 8 + 4) * MBy) SP_ARG);
            out_compute<false>(smem, w8, (const bf*)(ws + WS_OF + (it * 8 + 4) * MBy),
                               it ? out : (bf*)(ws + WS_X2), it ? nullptr : (float*)(ws + WS_ST + 262144) SP_ARG);
            STAMP(13); STAMPR(15); STAMP_FLUSH(it);
        }
    }
}

extern "C" __global__ void zz_code_end_kernel() {}

extern "C" void kernel_launch(void* const* d_in, const int* in_sizes, int n_in, void* d_out, int out_size, void* d_ws,
                              size_t ws_size, hipStream_t stream) {
    typedef __hip_bfloat16 bf;
    (void)hipFuncSetAttribute((const void*)mega_kernel, hipFuncAttributeMaxDynamicSharedMemorySize, CH_LDS);
    mega_kernel<<<256, 512, CH_LDS, stream>>>((const bf*)d_in[0], (const bf*)d_in[1], (const bf*)d_in[2], (const bf*)d_in[4], (const bf*)d_in[5],
                                              (bf*)d_out, (char*)d_ws);
}
```

```cpp
#include <hip/hip_runtime.h>
#include <hip/hip_bf16.h>
#include <stdint.h>

typedef __attribute__((ext_vector_type(8))) short bf16x8;
typedef __attribute__((ext_vector_type(4))) short s16x4;
typedef __attribute__((ext_vector_type(4))) float f32x4;
typedef __attribute__((ext_vector_type(2))) float f32x2;
typedef __attribute__((ext_vector_type(16))) float f32x16;
typedef __attribute__((ext_vector_type(4))) unsigned u32x4;
typedef __attribute__((ext_vector_type(2))) unsigned u32x2;
typedef __attribute__((ext_vector_type(2))) __bf16 bf16x2_t;
typedef __attribute__((ext_vector_type(8))) __bf16 bf8_t;
typedef __attribute__((ext_vector_type(16))) __bf16 bf16v_t;
typedef __attribute__((address_space(3))) const char* lds_cptr;
typedef __attribute__((address_space(3))) char* lds_ptr;

#define DI __device__ __forceinline__
#define MFMA16(a, b, c) __builtin_amdgcn_mfma_f32_16x16x32_bf16((a), (b), (c), 0, 0, 0)
#define MFMA32(a, b, c) __builtin_amdgcn_mfma_f32_32x32x16_bf16((a), (b), (c), 0, 0, 0)
#define SBAR() __builtin_amdgcn_sched_barrier(0)
#define STAMP(i)
#define STAMPR(i)
#define STAMP_DECL
#define STAMP_FLUSH(it)
#define SP_PARAM
#define SP_ARG
typedef unsigned long long u64;
#define FT0 1
#define FTS 3
#define NF 12
typedef __attribute__((ext_vector_type(32))) unsigned u32x32;
#define B2POS 0
#define WO2 7
#define WSL0 0
#define WSL1 8
#define WO1 7
#define WO0 3
#define SPRIO 3
#define QUNR 3
#define NTOUCH 3

constexpr float QSCALE = 0.17677669529663687f * 1.4426950408889634f;
constexpr float ATT_SCALE = 0.17677669529663687f;
constexpr float LOG2E = 1.4426950408889634f;
constexpr float NEG_MASK = -1e9f / ATT_SCALE;

DI int lane_id() { int l; asm volatile("v_mbcnt_lo_u32_b32 %0, -1, 0\n\tv_mbcnt_hi_u32_b32 %0, -1, %0" : "=v"(l)); return l; }
DI unsigned pack_bf16(float lo, float hi) {
    f32x2 v = {lo, hi};
    bf16x2_t r = __builtin_convertvector(v, bf16x2_t);
    return __builtin_bit_cast(unsigned, r);
}
DI float bf_lo(unsigned u) { return __uint_as_float(u << 16); }
DI float bf_hi(unsigned u) { return __uint_as_float(u & 0xffff0000u); }
DI float round_bf16(float x) { return bf_lo(pack_bf16(x, 0.f)); }
DI void ln_stats(float sum, float sumsq, float& mean_out, float& rstd_out) {
    float mean;
    asm("v_mul_f32 %0, 0x3a800000, %1" : "=v"(mean) : "v"(sum));
    const float var = fmaxf(__builtin_fmaf(-mean, mean, sumsq * (1.f / 1024.f)), 0.f);
    mean_out = round_bf16(mean);
    rstd_out = __builtin_amdgcn_rcpf(round_bf16(__builtin_amdgcn_sqrtf(round_bf16(round_bf16(var) + 1e-5f))));
}

DI void glds16(const void* sbase, unsigned voff, unsigned ldsaddr) {
    asm volatile("s_mov_b32 m0, %0\n\ts_nop 0\n\tglobal_load_lds_dwordx4 %1, %2"
                 :: "s"(ldsaddr), "v"(voff), "s"(sbase) : "memory");
}
DI void glds16c(const void* sbase, unsigned voff, unsigned ldsaddr) {
    asm volatile("s_mov_b32 m0, %0\n\ts_nop 0\n\tglobal_load_lds_dwordx4 %1, %2 sc1"
                 :: "s"(ldsaddr), "v"(voff), "s"(sbase) : "memory");
}
DI void st16(void* sbase, unsigned voff, u32x4 v) {
    asm volatile("global_store_dwordx4 %0, %1, %2 sc1\n\ts_nop 1" :: "v"(voff), "v"(v), "s"(sbase) : "memory");
}
template <int OFF> DI void st16o(void* sbase, unsigned voff, u32x4 v) {
    asm volatile("global_store_dwordx4 %0, %1, %2 offset:%3 sc1\n\ts_nop 1" :: "v"(voff), "v"(v), "s"(sbase), "n"(OFF) : "memory");
}
DI bf16x8 lds_b128(lds_cptr p) { return *(const __attribute__((address_space(3))) bf16x8*)p; }
DI s16x4 lds_tr(lds_cptr p) {
    return __builtin_amdgcn_ds_read_tr16_b64_v4i16((__attribute__((address_space(3))) s16x4*)p);
}
DI bf16x8 cat8(s16x4 a, s16x4 b) { return __builtin_shufflevector(a, b, 0, 1, 2, 3, 4, 5, 6, 7); }
DI float dot2bf(unsigned a, unsigned b, float c) {
    return __builtin_amdgcn_fdot2_f32_bf16(__builtin_bit_cast(bf16x2_t, a), __builtin_bit_cast(bf16x2_t, b), c, false);
}
DI float sum_g(float v) {
    auto a = __builtin_amdgcn_permlane16_swap(__float_as_uint(v), __float_as_uint(v), false, false);
    v = __uint_as_float(a[0]) + __uint_as_float(a[1]);
    auto b = __builtin_amdgcn_permlane32_swap(__float_as_uint(v), __float_as_uint(v), false, false);
    return __uint_as_float(b[0]) + __uint_as_float(b[1]);
}
DI float wave_sum(float v) {
    v += __builtin_bit_cast(float, __builtin_amdgcn_update_dpp(0, __builtin_bit_cast(int, v), 0xB1, 0xF, 0xF, true));
    v += __builtin_bit_cast(float, __builtin_amdgcn_update_dpp(0, __builtin_bit_cast(int, v), 0x4E, 0xF, 0xF, true));
    v += __builtin_bit_cast(float, __builtin_amdgcn_update_dpp(0, __builtin_bit_cast(int, v), 0x141, 0xF, 0xF, true));
    v += __builtin_bit_cast(float, __builtin_amdgcn_update_dpp(0, __builtin_bit_cast(int, v), 0x140, 0xF, 0xF, true));
    return sum_g(v);
}
DI float amax3(float a, float b, float c) { float r; asm("v_max3_f32 %0, %1, %2, %3" : "=v"(r) : "v"(a), "v"(b), "v"(c)); return r; }
DI float max2f(float a, float b) { return __builtin_amdgcn_fmed3f(a, b, INFINITY); }
DI float max_h(float v) {
    auto b = __builtin_amdgcn_permlane32_swap(__float_as_uint(v), __float_as_uint(v), false, false);
    return max2f(__uint_as_float(b[0]), __uint_as_float(b[1]));
}

template <int OFF> DI void st8o(void* sbase, unsigned voff, f32x2 v) {
    asm volatile("global_store_dwordx2 %0, %1, %2 offset:%3 sc1\n\ts_nop 1" :: "v"(voff), "v"(v), "s"(sbase), "n"(OFF) : "memory");
}
constexpr unsigned OFF_DUMP = 145408;
extern __shared__ __attribute__((aligned(16))) char smem_base[];
DI void touch_at(const void* p, unsigned off) {
    asm volatile("s_mov_b32 m0, %0\n\ts_nop 0\n\tglobal_load_lds_dword %1, %2"
                 :: "s"((unsigned)(uintptr_t)smem_base + OFF_DUMP), "v"(off), "s"(p) : "memory");
}
DI void touch(const void* p, int slice, int lane) { touch_at(p, (unsigned)(slice * 64 + lane) * 128u); }
DI void code_touch(int slice, int lane) {
    unsigned long long c0, c1;
    asm volatile("s_getpc_b64 s[96:97]\n\ts_add_u32 s96, s96, mega_kernel@rel32@lo+4\n\ts_addc_u32 s97, s97, mega_kernel@rel32@hi+12\n\ts_mov_b64 %0, s[96:97]\n\t"
                 "s_getpc_b64 s[96:97]\n\ts_add_u32 s96, s96, zz_code_end_kernel@rel32@lo+4\n\ts_addc_u32 s97, s97, zz_code_end_kernel@rel32@hi+12\n\ts_mov_b64 %1, s[96:97]"
                 : "=s"(c0), "=s"(c1) :: "s96", "s97", "scc");
    touch_at((const void*)c0, min((unsigned)(slice * 64 + lane) * 128u, (unsigned)(c1 - c0) + 1408u));
}

#define BAR() asm volatile("s_barrier" ::: "memory")
#define WAIT_LGKM() do { SBAR(); __builtin_amdgcn_s_waitcnt(0xC07F); SBAR(); } while (0)
#define WAIT_LGKM_BAR() do { SBAR(); __builtin_amdgcn_s_waitcnt(0xC07F); asm volatile("s_barrier" ::: "memory"); SBAR(); } while (0)
#define VM_BAR(n) asm volatile("s_waitcnt vmcnt(" #n ")\n\ts_barrier" ::: "memory")
constexpr int CH_LDS = 163840;
typedef __attribute__((address_space(3))) unsigned* lds_uptr;
typedef __attribute__((address_space(1))) u64* g_u64ptr;

constexpr size_t MBy = 1 << 20;
constexpr size_t WS_X3 = 0, WS_X2 = 4 * MBy, WS_X1 = 8 * MBy, WS_OF = 12 * MBy  , WS_KVF = 36 * MBy  ,
                 WS_ST = 52 * MBy  , WS_CSP = 54 * MBy  , WS_FLAG = 55 * MBy  ,
                 WS_EPOCH = WS_CSP + 524288, WS_Y0 = 56 * MBy;
DI unsigned flag_off(int set, int group, int consumer, int producer) { return (unsigned)(set * 65536 + group * 4096 + consumer * 256 + producer * 8); }
DI void flag_store(char* ws, int set, int group, int producer, u64 tag, int lane) {
    asm volatile("" : "+s"(tag));
    if (lane < 16) __hip_atomic_store((g_u64ptr)(ws + WS_FLAG + flag_off(set, group, lane, producer)), tag, __ATOMIC_RELAXED, __HIP_MEMORY_SCOPE_AGENT);
}
DI void flag_poll(const char* ws, unsigned off, u64 tag) {
    asm volatile("" : "+s"(tag));
#pragma unroll 1
    for (int spin = 0; spin < (1 << 16); ++spin) {
        const u64 v = __hip_atomic_load((g_u64ptr)(ws + WS_FLAG + off), __ATOMIC_RELAXED, __HIP_MEMORY_SCOPE_AGENT);
        if (__all(v == tag)) break;
        __builtin_amdgcn_s_sleep(1);
    }
    asm volatile("" ::: "memory");
}
#define POLL_INS "global_atomic_or_x2 %[land], %[po], %[zero], %[base] sc0 sc1"
template <bool DRAIN, bool ALLPOLL>
DI void signal_poll(int follower, const char* ws, unsigned soff, unsigned poff, u64 pmask, u64 tag, u64& land) {
    u64 tagv, zero;
    asm volatile("s_not_b64 s[96:97], %[ts]\n\tv_mov_b64 %[tag], %[ts]\n\tv_mov_b64 %[zero], 0\n\tv_mov_b64 %[land], s[96:97]\n\t"
                 ".if %[dr]\n\ts_waitcnt vmcnt(0)\n\t.endif\n\ts_barrier\n\t"
                 "s_cmp_lg_u32 %[fl], 0\n\ts_cbranch_scc1 .Lsig_fol_%=\n\t"
                 "s_mov_b64 exec, 0xffff\n\t"
                 "global_store_dwordx2 %[so], %[tag], %[base] sc1\n"
                 ".if %[ap]\n.Lsig_fol_%=:\n.endif\n\t"
                 "s_mov_b64 exec, %[pm]\n\t"
                 "s_movk_i32 s96, 0x7fff\n"
                 ".Lsig_loop_%=:\n\t"
                 POLL_INS "\n\t"
                 "s_sleep 1\n\t"
                 "s_waitcnt vmcnt(3)\n\t"
                 "v_cmp_ne_u64 vcc, %[land], %[tag]\n\t"
                 "s_sub_u32 s96, s96, 1\n\t"
                 "s_cbranch_scc1 .Lsig_out_%=\n\t"
                 "s_cbranch_vccnz .Lsig_loop_%=\n"
                 ".Lsig_out_%=:\n\t"
                 "s_mov_b64 exec, -1\n"
                 ".if !%[ap]\n.Lsig_fol_%=:\n.endif"
                 : [land] "=&v"(land), [tag] "=&v"(tagv), [zero] "=&v"(zero)
                 : [fl] "s"(follower), [so] "v"(soff), [po] "v"(poff), [ts] "s"(tag), [base] "s"(ws + WS_FLAG), [pm] "s"(pmask), [ap] "n"(ALLPOLL ? 1 : 0), [dr] "n"(DRAIN ? 1 : 0)
                 : "memory", "scc", "vcc", "s96", "s97");
}
struct Ids { int pq, cq, po, co; };
DI Ids ids() {
    int bid = blockIdx.x; asm volatile("" : "+s"(bid));
    const int xcd = bid & 7, idx = bid >> 3;
    int pq, cq, po, co;
#define IDS_BODY pq = (xcd & 1) * 8 + (idx & 7); cq = (xcd >> 1) * 4 + (idx >> 3); po = (xcd & 3) * 4 + (idx & 3); co = (xcd >> 2) * 8 + (idx >> 2);
    IDS_BODY
    return Ids{pq, cq, po, co};
}

DI void slow_warm(unsigned dump, const void* W, int w, int lane) {
    int bid = blockIdx.x; asm volatile("" : "+s"(bid));
    const int xcd = bid & 7, idx = bid >> 3;
    const int sc = (3 - (int)((uintptr_t)W >> 7)) & 7;
    if ((sc >> 2) != ((xcd >> 1) & 1)) return;
    const char* src = (const char*)W + (idx + 32 * w) * 49152 + (sc + (xcd >> 2) * 8) * 128;
#pragma unroll
    for (int G = 0; G < 3; ++G) glds16(src + G * 2048, (unsigned)((lane >> 3) * 6144 + (lane & 7) * 16), dump);
}

template <int V> struct IC { static constexpr int value = V; };
template <bool LOCAL> struct QC {
    static constexpr int AROWS = LOCAL ? 144 : 128;
    static constexpr int ABYTES = AROWS * 128;
    static constexpr int STAGE = ABYTES + 24576;
    static constexpr int OFF_MEAN = 143360;
    static constexpr int OFF_RSTD = OFF_MEAN + 576;
    static constexpr int OFF_CS = OFF_RSTD + 576;
};
constexpr int OFF_EX = 131072;

DI unsigned colsum_off(int G, int lane) {
    const Ids I = ids();
    unsigned off = (unsigned)(I.pq * 64 + (lane >> 3)) * 6144u + (unsigned)(G * 2048 + I.cq * 128 + (lane & 7) * 16);
    asm volatile("" : "+v"(off));
    return off;
}
DI void colsum_load(u32x4 (&c)[8], const void* W, unsigned off) {
#define CS_NEXT "s_add_u32 s96, s96, 0xc000\n\ts_addc_u32 s97, s97, 0\n\t"
    asm volatile("s_cmp_eq_u64 %9, 0\n\ts_cbranch_scc1 .Lcs_skip_%=\n\ts_mov_b64 s[96:97], %9\n\t"
                 "global_load_dwordx4 %0, %8, s[96:97]\n\t" CS_NEXT "global_load_dwordx4 %1, %8, s[96:97]\n\t" CS_NEXT
                 "global_load_dwordx4 %2, %8, s[96:97]\n\t" CS_NEXT "global_load_dwordx4 %3, %8, s[96:97]\n\t" CS_NEXT
                 "global_load_dwordx4 %4, %8, s[96:97]\n\t" CS_NEXT "global_load_dwordx4 %5, %8, s[96:97]\n\t" CS_NEXT
                 "global_load_dwordx4 %6, %8, s[96:97]\n\t" CS_NEXT "global_load_dwordx4 %7, %8, s[96:97]\n"
                 ".Lcs_skip_%=:"
                 : "=&v"(c[0]), "=&v"(c[1]), "=&v"(c[2]), "=&v"(c[3]), "=&v"(c[4]), "=&v"(c[5]), "=&v"(c[6]), "=&v"(c[7])
                 : "v"(off), "s"(W) : "memory", "scc", "s96", "s97");
}
DI void colsum_store(u32x4 (&c)[8], char* ws, int mat, int G, int lane) {
    asm volatile("" : "+v"(c[0]), "+v"(c[1]), "+v"(c[2]), "+v"(c[3]), "+v"(c[4]), "+v"(c[5]), "+v"(c[6]), "+v"(c[7]));
    const Ids I = ids();
    const int m = lane & 15;
    const unsigned one = (m & 1) ? 0xbf800000u : 0xbf80u;
    const u32x4 sl = {(m >> 1) == 0 ? one : 0u, (m >> 1) == 1 ? one : 0u, (m >> 1) == 2 ? one : 0u, (m >> 1) == 3 ? one : 0u};
    f32x4 d = {0, 0, 0, 0};
#pragma unroll
    for (int i = 0; i < 8; ++i) d = MFMA16(__builtin_bit_cast(bf16x8, sl), __builtin_bit_cast(bf16x8, c[i]), d);
#pragma unroll
    for (int r = 0; r < 4; ++r)
        d[r] += __int_as_float(__builtin_amdgcn_update_dpp(0, __float_as_int(d[r]), 0x128, 0xF, 0xF, true));
    if (lane < 32 && (lane & 8) == 0)
        st16(ws + WS_CSP + mat * 196608, (unsigned)((I.cq * 16 + I.pq) * 768 + G * 256 + (lane & 7) * 32 + (lane >> 4) * 16), __builtin_bit_cast(u32x4, d));

}

template <bool LOCAL>
struct WFirst {
    unsigned v[2];
    DI WFirst(int w, int ct, int lane) {
#pragma unroll
        for (int i = 0; i < 2; ++i) {
            const int k = 8 * i + (lane >> 3), pos = lane & 7;
            v[i] = (unsigned)k * 6144u + (unsigned)(w * 1024 + ct * 64 + ((pos >> 1) ^ (((k >> 1) & 1) | (i << 1))) * 16 + (pos & 1) * 8) * 2u;
        }
        asm volatile("" : "+v"(v[0]), "+v"(v[1]));
    }
    DI void issue(const void* W, unsigned lds0, int w, int i0, int i1) const {
#pragma unroll
        for (int i = i0; i < i1; ++i)
            glds16((const char*)W + (size_t)(i >> 1) * (16 * 6144), v[i & 1], lds0 + (i >> 3) * QC<LOCAL>::STAGE + QC<LOCAL>::ABYTES + (w * 8 + (i & 7)) * 1024);
    }
};

template <bool LOCAL, class F, class F2>
DI void qkv_loader(char* smem, int w, char* ws, int set, u64 tag, const __hip_bfloat16* __restrict__ X, const __hip_bfloat16* __restrict__ W,
                   const float* __restrict__ statp, const char* __restrict__ csp, bool ident, const void* nextW, F tail, F2 mid SP_PARAM) {
    typedef QC<LOCAL> C;
    constexpr int NT = 16;
    const int lane = lane_id();
    asm volatile("" : "+s"(w));
    const int idx = blockIdx.x >> 3;
    const Ids I = ids();
    const int panel = I.pq, ct = I.cq;
    const int R0 = panel * 128;
    const unsigned lds0 = (unsigned)(uintptr_t)smem;
    auto grow = [&](int i) -> int {
        if (!LOCAL) return R0 + i;
        if (i < 128) { const int t = (i >> 4) & 3, a = i & 15; return R0 + (i & 64) + 2 * (16 * (t & 1) + a) + (t >> 1); }
        const int a = i & 15; const int r = R0 - 16 + 2 * (a & 7) + (a >> 3);
        return r < 0 ? 0 : r;
    };
    unsigned voffA[5], voffB[6];
#pragma unroll
    for (int i = 0; i < 6; ++i) {
        const int j = w + 4 * i, G = j >> 3, k = 8 * (j & 7) + (lane >> 3);
        const int pos = lane & 7, f = ((k >> 1) & 1) | (((k >> 3) & 1) << 1);
        const int seg = (pos >> 1) ^ f;
        voffB[i] = (unsigned)k * 6144u + (unsigned)(G * 1024 + ct * 64 + seg * 16 + (pos & 1) * 8) * 2u;
    }
    auto issueW = [&](int t, int buf) {
        const char* wb = (const char*)W + (size_t)t * (64 * 6144);
        const unsigned sb = lds0 + buf * C::STAGE;
#pragma unroll
        for (int i = 0; i < 6; ++i) glds16(wb, voffB[i], sb + C::ABYTES + (w + 4 * i) * 1024);
    };
    asm volatile("" : "+v"(voffB[0]), "+v"(voffB[1]), "+v"(voffB[2]), "+v"(voffB[3]), "+v"(voffB[4]), "+v"(voffB[5]));
    const WFirst<LOCAL> first(w, ct, lane);
    const int g = lane >> 4, r = lane & 15, pos = 32 * w + 2 * r + (g >> 1);
    const int lr = !LOCAL ? (pos & 64) + 2 * (pos & 31) + ((pos >> 5) & 1)
                          : (w == 0 && (g & 1)) ? 128 + (r & 7) + 8 * (g >> 1) : (pos & 64) + ((pos & 1) * 2 + ((pos >> 5) & 1)) * 16 + ((pos >> 1) & 15);
    unsigned so = (unsigned)(panel * 16384 + g * 1024 + w * 256 + r * 16), ho = (unsigned)(g * 1024 + (panel ? panel * 16384 - 15488 + (r & 7) * 16 : 0));
    unsigned co = (unsigned)((ct * 16 + g) * 768 + w * 192 + r * 12);
    lds_ptr mp = (lds_ptr)smem + C::OFF_MEAN + lr * 4, cp = (lds_ptr)smem + C::OFF_CS + w * 192 + r * 12 + ((g & 1) ? 8 : g * 2);
    unsigned fo = flag_off((LOCAL ? 13 : 10) + min(g, 2), ct, panel, r);
    asm volatile("" : "+v"(so), "+v"(ho), "+v"(co), "+v"(mp), "+v"(cp), "+v"(fo));
#pragma unroll
    for (int i = 0; i < 5; ++i) {
        const int j = (i < 4) ? (w + 4 * i) : (16 + (w & 1));
        const int row = 8 * j + (lane >> 3);
        const int c = (lane & 7) ^ ((row >> 1) & 7);
        const int gr = grow(row < C::AROWS ? row : 0);
        voffA[i] = (unsigned)((gr >> 7) * 262144 + (gr & 127) * 128 + c * 16);
        asm volatile("" : "+v"(voffA[i]));
    }
    auto issueA = [&](int t, int buf) {
        const char* xa = (const char*)X + t * 16384;
        const unsigned sb = lds0 + buf * C::STAGE;
        if (LOCAL && w < 2) glds16c(xa, voffA[4], sb + (16 + w) * 1024);
#pragma unroll
        for (int i = 0; i < 4; ++i) glds16c(xa, voffA[i], sb + (w + 4 * i) * 1024);
    };
    u64 land;
    if (w == 3) __builtin_amdgcn_s_setprio(3);
    signal_poll<LOCAL, false>(w - 3, ws, flag_off(set, I.po, lane & 15, I.co),
                              flag_off(set, (lane & 16) && panel ? panel - 1 : panel, ct, lane & 15),
                              !LOCAL ? 0xffffull : ident ? 0xc000ffffull : 0xffffffffull, tag, land);
    if (w < 3) first.issue(W, lds0, w, 0, NF);
    BAR();
    STAMP(LOCAL ? 1 : 6);
    issueA(0, 0);
    issueA(1, 1);
    if (w < 3) { first.issue(W, lds0, w, NF, 16); asm volatile("s_waitcnt vmcnt(%0)" :: "n"(20 - NF) : "memory"); }
    else asm volatile("s_waitcnt vmcnt(4)" ::: "memory");
    BAR();
    asm volatile("" :: "v"(land));
    __builtin_amdgcn_s_setprio(0);
    STAMP(LOCAL ? 2 : 7);
    issueW(2, 2);
    issueA(2, 2);
    VM_BAR(10);
    asm volatile("global_load_dwordx2 %0, %1, %2 sc1" : "=&v"(land) : "v"(fo), "s"(ws + WS_FLAG) : "memory");
    issueA(3, 0); issueW(3, 0);
    int buf = 1;
#pragma unroll 1
    for (int t = 1; t < NT - 3; ++t) {
        VM_BAR(10);
        issueA(t + 3, buf); issueW(t + 3, buf);
        if (t == 10 && idx < 3) code_touch(idx * 4 + w, lane);
        if (LOCAL && t == 11)
            glds16(nextW, (unsigned)((idx + 32 * w) * 8 + (lane >> 3)) * 2048u +
                   (unsigned)(((I.co & 8) + ((3 - (int)((uintptr_t)nextW >> 7)) & 7)) * 128 + (lane & 7) * 16), lds0 + OFF_DUMP);
        buf = (buf == 2) ? 0 : buf + 1;
    }
    typedef __attribute__((ext_vector_type(3))) float f32x3;
    const bool halo = LOCAL && w == 0;
    asm volatile("" : "+v"(land));
    if (!ident) {
        if (!__all(land == tag)) flag_poll(ws, fo, tag);
        f32x3 cs[4];
        f32x4 sv[4], hv[4];
        typedef __attribute__((address_space(3))) float* lds_f;
        auto add = [](float a, float b) { float r; asm("v_add_f32 %0, %1, %2" : "=v"(r) : "v"(a), "v"(b)); return r; };
        auto fold32 = [](float a, float b) { asm("s_nop 1\n\tv_permlane32_swap_b32 %0, %1\n\tv_add_f32 %0, %0, %1" : "+v"(a), "+v"(b)); return a; };
        auto fold16 = [](float a, float b) { asm("s_nop 1\n\tv_permlane16_swap_b32 %0, %1\n\tv_add_f32 %0, %0, %1" : "+v"(a), "+v"(b)); return a; };
        auto sum4 = [&](auto (&v)[4], int e) { return add(add(v[0][e], v[1][e]), add(v[2][e], v[3][e])); };
#pragma unroll
        for (int i = 0; i < 4; ++i) asm volatile("global_load_dwordx3 %0, %1, %2 sc1" : "=&v"(cs[i]) : "v"(co), "s"(csp + i * 3072) : "memory");
        VM_BAR(14);
#pragma unroll
        for (int i = 0; i < 4; ++i) asm volatile("global_load_dwordx4 %0, %1, %2 sc1" : "=&v"(sv[i]) : "v"(so), "s"((const char*)statp + i * 4096) : "memory");
        if (LOCAL)
            asm volatile("s_cmp_lg_u32 %9, 0\n\ts_cbranch_scc1 .Lhv_skip_%=\n\t"
                         "global_load_dwordx4 %0, %4, %5 sc1\n\tglobal_load_dwordx4 %1, %4, %6 sc1\n\t"
                         "global_load_dwordx4 %2, %4, %7 sc1\n\tglobal_load_dwordx4 %3, %4, %8 sc1\n"
                         ".Lhv_skip_%=:" : "=&v"(hv[0]), "=&v"(hv[1]), "=&v"(hv[2]), "=&v"(hv[3])
                         : "v"(ho), "s"(statp), "s"(statp + 1024), "s"(statp + 2048), "s"(statp + 3072), "s"(w) : "memory", "scc");
        if (halo) asm volatile("s_waitcnt vmcnt(8)" ::: "memory"); else asm volatile("s_waitcnt vmcnt(4)" ::: "memory");
        asm volatile("" : "+v"(cs[0]), "+v"(cs[1]), "+v"(cs[2]), "+v"(cs[3]));
        const float c2 = sum4(cs, 2);
        *(lds_f)cp = fold16(fold32(sum4(cs, 0), sum4(cs, 1)), fold32(c2, c2));
        BAR();
        asm volatile("s_waitcnt vmcnt(0)" : "+v"(sv[0]), "+v"(sv[1]), "+v"(sv[2]), "+v"(sv[3]) :: "memory");
        mid(0);
        const float s1 = fold32(sum4(sv, 0), sum4(sv, 2)), s2 = fold32(sum4(sv, 1), sum4(sv, 3));
        float h1, h2;
        asm("v_mov_b32 %0, %2\n\tv_mov_b32 %1, %3" : "=&v"(h1), "=&v"(h2) : "v"(s1), "v"(s2));
        if (halo) {
            asm volatile("" : "+v"(hv[0]), "+v"(hv[1]), "+v"(hv[2]), "+v"(hv[3]));
            const float u0 = sum4(hv, 0), u1 = sum4(hv, 1), u2 = sum4(hv, 2), u3 = sum4(hv, 3);
            h1 = fold32(u0, panel ? u2 : u0); h2 = fold32(u1, panel ? u3 : u1);
        }
        float mean, rstd;
        ln_stats(fold16(s1, h1), fold16(s2, h2), mean, rstd);
        *(lds_f)mp = mean; *(lds_f)(mp + (C::OFF_RSTD - C::OFF_MEAN)) = rstd;
    } else { VM_BAR(10); tail(); BAR(); mid(0); }
    mid(1);
    WAIT_LGKM_BAR();
}

template <bool LOCAL>
DI void qkv_compute(char* smem, int w, const void* W, u32x4 (&qo4)[4], __hip_bfloat16* __restrict__ kvf, __hip_bfloat16* __restrict__ ofrag, bool ident,
                    const void* far SP_PARAM) {
    typedef QC<LOCAL> C;
    constexpr int NT = 16, ABYTES = C::ABYTES, STAGE = C::STAGE;
    const int lane = lane_id();
    asm volatile("" : "+s"(w));
    const int wm = w & 1, wn = w >> 1;
    const Ids I = ids();
    const int panel = I.pq, ct = I.cq;
    const int l15 = lane & 15, g = lane >> 4;
    const lds_cptr sm = (lds_cptr)smem;
    lds_ptr smw = (lds_ptr)smem;

    f32x4 acc[3][4][2];
    f32x4 acch[2];
    f32x2 z2; asm volatile("v_mov_b64 %0, 0" : "=v"(z2));
    const f32x4 z4 = {z2[0], z2[1], z2[0], z2[1]};
#pragma unroll
    for (int a = 0; a < 3; ++a)
#pragma unroll
        for (int t = 0; t < 4; ++t) { acc[a][t][0] = z4; acc[a][t][1] = z4; }
    acch[0] = z4; acch[1] = z4;

    struct Frags { bf16x8 a[4]; bf16x8 h; s16x4 b[3][2][2]; };
    auto rd = [&](auto WM, Frags& F, lds_cptr pa, lds_cptr pb0, lds_cptr pb1, int n) {
        switch (n) {
            case 0: F.b[0][0][0] = lds_tr(pb0); break;
            case 1: F.b[0][0][1] = lds_tr(pb0 + 512); break;
            case 2: F.a[0] = lds_b128(pa); break;
            case 3: F.b[0][1][0] = lds_tr(pb1); break;
            case 4: F.b[0][1][1] = lds_tr(pb1 + 512); break;
            case 5: F.a[1] = lds_b128(pa + 2048); break;
            case 6: F.a[2] = lds_b128(pa + 4096); break;
            case 7: F.a[3] = lds_b128(pa + 6144); break;
            case 8: F.b[1][0][0] = lds_tr(pb0 + 8192); break;
            case 9: F.b[1][0][1] = lds_tr(pb0 + 8192 + 512); break;
            case 10: F.b[1][1][0] = lds_tr(pb1 + 8192); break;
            case 11: F.b[1][1][1] = lds_tr(pb1 + 8192 + 512); break;
            case 12: F.b[2][0][0] = lds_tr(pb0 + 16384); break;
            case 13: F.b[2][0][1] = lds_tr(pb0 + 16384 + 512); break;
            case 14: F.b[2][1][0] = lds_tr(pb1 + 16384); break;
            case 15: F.b[2][1][1] = lds_tr(pb1 + 16384 + 512); break;
            case 16: if (LOCAL) F.h = lds_b128(pa + 16384 - WM.value * 8192); break;
            default: break;
        }
    };
    auto mm = [](f32x4& c, bf16x8 a, bf16x8 b) { asm volatile("v_mfma_f32_16x16x32_bf16 %0, %1, %2, %0" : "+v"(c) : "v"(a), "v"(b)); };
    auto half = [&](auto WM, const Frags& C, Frags& N, bool doread, lds_cptr pa, lds_cptr pb0, lds_cptr pb1, auto tail, int G0 = 0, int G1 = 3) {
#pragma unroll
        for (int G = G0; G < G1; ++G) {
            const bf16x8 b0 = cat8(C.b[G][0][0], C.b[G][0][1]), b1 = cat8(C.b[G][1][0], C.b[G][1][1]);
#pragma unroll
            for (int t = 0; t < 4; ++t) {
                const int n = (G - G0) * 8 + t * 2;
                if (G < 2) mm(acc[G][t][0], b0, C.a[t]); else mm(acc[G][t][0], C.a[t], b0);
                SBAR();
                if (doread) rd(WM, N, pa, pb0, pb1, n);
                tail(n);
                SBAR();
                if (G < 2) mm(acc[G][t][1], b1, C.a[t]); else mm(acc[G][t][1], C.a[t], b1);
                SBAR();
                if (doread) rd(WM, N, pa, pb0, pb1, n + 1);
                tail(n + 1);
                SBAR();
            }
        }
        if (LOCAL) {
            if (WM.value == 0) { mm(acch[0], cat8(C.b[1][0][0], C.b[1][0][1]), C.h); mm(acch[1], cat8(C.b[1][1][0], C.b[1][1][1]), C.h); }
            else               { mm(acch[0], C.h, cat8(C.b[2][0][0], C.b[2][0][1])); mm(acch[1], C.h, cat8(C.b[2][1][0], C.b[2][1][1])); }
            SBAR();
        }
    };

    Frags F0, F1;
    f32x4 cq[2], ck[2], mu4[5], rs4[5]; float cv[2], mu[5], rs[5];
    u32x4 qfr[4], kfr[4];
    u32x4 va[2][2];
    f32x4 mwin, mfirst;
    typedef __attribute__((address_space(3))) u32x4* lds_u4;
    const bool negpos = (wm == 0) && (panel == 0);
    VM_BAR(0);
    unsigned cm01 = 0, cm23 = 0;
    lds_cptr ka[2] = {sm, sm}, vo2[2] = {sm, sm};
    lds_u4 ex = (lds_u4)(smw + OFF_EX + wn * 6144) + lane;
    lds_cptr s1 = sm + C::OFF_MEAN + wm * 256 + l15 * 4, s4 = sm + C::OFF_MEAN + wm * 256 + g * 16;
    lds_cptr c1 = sm + C::OFF_CS + wn * 128 + l15 * 4, c4 = sm + C::OFF_CS + wn * 128 + g * 16;
    asm volatile("" : "+v"(s1), "+v"(s4), "+v"(c1), "+v"(c4));
    if (LOCAL) {
        unsigned keepv = negpos ? __float_as_uint(NEG_MASK) : 0u;
        asm volatile("" : "+s"(keepv));
        u32x4 mw, mf;
        unsigned prv[4];
#pragma unroll
        for (int r = 0; r < 4; ++r) {
            const int d = l15 - (4 * g + r);
            unsigned out = (unsigned)(((d << 28) >> 31));
            prv[r] = (unsigned)(d >> 31);
            asm volatile("" : "+v"(out), "+v"(prv[r]));
            mw[r] = out & 0xff800000u;
            mf[r] = (out & 0xff800000u) | (~out & keepv);
        }
        const lds_cptr e0 = sm + OFF_EX + wn * 6144;
        ka[0] = e0 + (wm ? lane : 256 + (lane ^ 8)) * 16;         ka[1] = e0 + (wm ? 64 + lane : 256 + lane) * 16;
        vo2[0] = e0 + (wm ? 128 + lane : 320 + (lane ^ 32)) * 16; vo2[1] = e0 + (wm ? 192 + lane : 320 + lane) * 16;
        cm01 = ~((prv[0] >> 16) | (prv[1] << 16)); cm23 = ~((prv[2] >> 16) | (prv[3] << 16));
        mwin = __builtin_bit_cast(f32x4, mw); mfirst = __builtin_bit_cast(f32x4, mf);
        asm volatile("" : "+v"(mwin), "+v"(mfirst), "+v"(cm01), "+v"(cm23), "+v"(ka[0]), "+v"(ka[1]), "+v"(vo2[0]), "+v"(vo2[1]), "+v"(ex));
    }
    typedef const __attribute__((address_space(3))) f32x4* lds_f4; typedef const __attribute__((address_space(3))) float* lds_f1;
    auto mainloop = [&](auto WM) {
#pragma unroll
        for (int a = 0; a < 3; ++a)
            asm volatile("" : "+v"(acc[a][0][0]), "+v"(acc[a][0][1]), "+v"(acc[a][1][0]), "+v"(acc[a][1][1]), "+v"(acc[a][2][0]), "+v"(acc[a][2][1]),
                         "+v"(acc[a][3][0]), "+v"(acc[a][3][1]));
        int ty = lane_id();
        const int l15 = ty & 15, g = ty >> 4;
        const int swA = (l15 >> 1) & 7;
        const int q4 = l15 >> 2, p4 = l15 & 3, fk = ((q4 >> 1) & 1) | ((g & 1) << 1);
        lds_cptr pA0 = sm + wm * 8192 + l15 * 128 + ((0 + g) ^ swA) * 16, pA1 = sm + wm * 8192 + l15 * 128 + ((4 + g) ^ swA) * 16;
        lds_cptr pB0 = sm + ABYTES + (8 * g + q4) * 128 + (((2 * wn + 0) ^ fk) * 32) + p4 * 8;
        lds_cptr pB1 = sm + ABYTES + (8 * g + q4) * 128 + (((2 * wn + 1) ^ fk) * 32) + p4 * 8;
        asm volatile("" : "+v"(acch[0]), "+v"(acch[1]), "+v"(pA0), "+v"(pA1), "+v"(pB0), "+v"(pB1));
        unsigned fo = (unsigned)(blockIdx.x * 64 + ty) * 128u;
        asm volatile("" : "+v"(fo));
        slow_warm((unsigned)(uintptr_t)smem + OFF_DUMP, W, w, ty);
        BAR();
        STAMP(LOCAL ? 1 : 7);
        BAR();
#pragma unroll
        for (int n = 0; n < 17; ++n) rd(WM, F0, pA0, pB0, pB1, n);
        WAIT_LGKM();
        int off = 0;
        const int ft = ident ? FT0 + FTS * w : -1;
        lds_cptr a = pA1, b0 = pB0, b1 = pB1;
#pragma unroll QUNR
        for (int t = 0; t < NT - 1; ++t) {
            half(WM, F0, F1, true, a, b0 + 4096, b1 + 4096, [&](int n) {
                if (n == 17) { off = (off == 2 * STAGE) ? 0 : off + STAGE; asm volatile("" : "+s"(off)); }
                if (n == 19) { b0 = pB0 + off; asm volatile("" : "+v"(b0)); }
                if (n == 20) { a = pA0 + off; asm volatile("" : "+v"(a)); }
                if (n == 21) { b1 = pB1 + off; asm volatile("" : "+v"(b1)); }
            });
            WAIT_LGKM_BAR();
            half(WM, F1, F0, true, a, b0, b1, [&](int n) {
                if (LOCAL && n == 17)
                    asm volatile("s_cmp_lg_u32 %0, %1\n\ts_cbranch_scc0 .Lft_do_%=\n.Lft_back_%=:\n\t.subsection 1\n.Lft_do_%=:\n\t"
                                 "s_mov_b32 m0, %2\n\ts_nop 0\n\tglobal_load_lds_dword %3, %4\n\ts_branch .Lft_back_%=\n\t.subsection 0"
                                 :: "s"(t), "s"(ft), "s"((unsigned)(uintptr_t)smem + OFF_DUMP), "v"(fo), "s"(far) : "memory", "scc");
                if (n == 19) { a = pA1 + off; asm volatile("" : "+v"(a)); }
                if (n == 22) __builtin_amdgcn_s_waitcnt(0xC07F);
            });
        }
        half(WM, F0, F1, true, a, b0 + 4096, b1 + 4096, [](int) {});
        WAIT_LGKM_BAR();
        half(WM, F1, F0, false, sm, sm, sm, [&](int n) {
            constexpr int RS = C::OFF_RSTD - C::OFF_MEAN, H = 512 - WM.value * 256;
            if (n < 2) { cq[n] = *(lds_f4)(c4 + n * 64); ck[n] = *(lds_f4)(c4 + 256 + n * 64); }
            else if (n < 6) { mu[n - 2] = *(lds_f1)(s1 + (n - 2) * 64); rs[n - 2] = *(lds_f1)(s1 + RS + (n - 2) * 64); }
            else if (n < 10) { mu4[n - 6] = *(lds_f4)(s4 + (n - 6) * 64); rs4[n - 6] = *(lds_f4)(s4 + RS + (n - 6) * 64); }
            else if (n == 10) { cv[0] = *(lds_f1)(c1 + 512); cv[1] = *(lds_f1)(c1 + 512 + 64); }
            else if (LOCAL && n == 11) {
                if (WM.value == 0) { mu[4] = *(lds_f1)(s1 + H); rs[4] = *(lds_f1)(s1 + RS + H); }
                else { mu4[4] = *(lds_f4)(s4 + H); rs4[4] = *(lds_f4)(s4 + RS + H); }
            } else if (n == (LOCAL ? 22 : 15)) __builtin_amdgcn_s_waitcnt(0xC07F);
        }, LOCAL ? 0 : 1);
        asm volatile("s_nop 7");
        SBAR();
        if (LOCAL) {
            auto fix = [&](int a, int t) {
#pragma unroll
                for (int f = 0; f < 2; ++f)
                    acc[a][t][f] = (a == 2) ? rs4[t] * (acc[2][t][f] + mu4[t] * cv[f]) : rs[t] * (acc[a][t][f] + mu[t] * (a ? ck[f] : cq[f]));
            };
            auto pk = [&](const f32x4 (&v)[2]) { return u32x4{pack_bf16(v[0][0], v[0][1]), pack_bf16(v[0][2], v[0][3]), pack_bf16(v[1][0], v[1][1]), pack_bf16(v[1][2], v[1][3])}; };
            if (!ident) {
                fix(1, 1); fix(1, 3); fix(2, 1); fix(2, 3);
#pragma unroll
                for (int f = 0; f < 2; ++f)
                    acch[f] = (WM.value == 0) ? rs[4] * (acch[f] + mu[4] * ck[f]) : rs4[4] * (acch[f] + mu4[4] * cv[f]);
            }
            auto pkv = [&](int t) {
#pragma unroll
                for (int f = 0; f < 2; ++f) {
                    va[t >> 1][f][(t & 1) * 2] = pack_bf16(acc[2][t][f][0], acc[2][t][f][1]);
                    va[t >> 1][f][(t & 1) * 2 + 1] = pack_bf16(acc[2][t][f][2], acc[2][t][f][3]);
                }
            };
            kfr[1] = pk(acc[1][1]); kfr[3] = pk(acc[1][3]); pkv(1); pkv(3);
            u32x4 hf = pk(acch);
            if (WM.value == 0) {
                ex[0] = kfr[1]; ex[64] = kfr[3];
#pragma unroll
                for (int u = 0; u < 2; ++u)
                    asm volatile("ds_write_b64 %0, %1 offset:%3\n\tds_write_b64 %0, %2 offset:%3+8" :: "v"(ex), "v"(u32x2{va[u][0][2], va[u][0][3]}),
                                 "v"(u32x2{va[u][1][2], va[u][1][3]}), "n"(2048 + u * 1024) : "memory");
                if (negpos) hf = u32x4{0, 0, 0, 0};
            }
            ex[256 + WM.value * 64] = hf;
            SBAR();
            if (!ident) { fix(0, 0); fix(0, 1); fix(0, 2); fix(0, 3); fix(1, 0); fix(1, 2); fix(2, 0); fix(2, 2); }
#pragma unroll
            for (int t = 0; t < 4; ++t) qfr[t] = pk(acc[0][t]);
            kfr[0] = pk(acc[1][0]); kfr[2] = pk(acc[1][2]); pkv(0); pkv(2);
            asm volatile("" : "+v"(qfr[0]), "+v"(qfr[1]), "+v"(qfr[2]), "+v"(qfr[3]), "+v"(kfr[0]), "+v"(kfr[2]),
                         "+v"(va[0][0]), "+v"(va[0][1]), "+v"(va[1][0]), "+v"(va[1][1]));
        }
    };
    const int head = ct * 2 + wn;
    unsigned qo = 0, ko = 0, vo = 0;
    if (LOCAL) {
        qo = (unsigned)((((panel * 8 + wm * 4) * 32 + head) * 64 + lane) * 16);
        asm volatile("" : "+v"(qo));
    } else {
        ko = (unsigned)((wm * 8 + (g & 1)) * 1024 + ((l15 & 7) + ((l15 & 8) << 1) + 32 * (g >> 1)) * 16);
        vo = (unsigned)((wm * 8 + 2 + (g >> 1)) * 1024 + (l15 + 32 * (g & 1)) * 16);
        asm volatile("" : "+v"(ko), "+v"(vo));
    }
    if (LOCAL && wm) mainloop(IC<1>{}); else mainloop(IC<0>{});

    if (!LOCAL) {
        auto pk = [&](f32x4 a, f32x4 b) { return u32x4{pack_bf16(a[0], a[1]), pack_bf16(a[2], a[3]), pack_bf16(b[0], b[1]), pack_bf16(b[2], b[3])}; };
        char* kvp = (char*)kvf + (size_t)(head * 16 + panel) * 16384;
        lds_ptr own = smw + wn * 16384;
        SBAR();
#pragma unroll
        for (int t = 0; t < 4; ++t) {
            const u32x4 kk = pk(rs[t] * (acc[1][t][0] + mu[t] * ck[0]), rs[t] * (acc[1][t][1] + mu[t] * ck[1]));
            if (t & 1) st16o<128>(kvp + (t >> 1) * 4096, ko, kk); else st16o<0>(kvp + (t >> 1) * 4096, ko, kk);
            kfr[t] = kk;
            SBAR();
            const int u = t >> 1, f = t & 1;
            const u32x4 vv = pk(rs4[2 * u] * (acc[2][2 * u][f] + mu4[2 * u] * cv[f]), rs4[2 * u + 1] * (acc[2][2 * u + 1][f] + mu4[2 * u + 1] * cv[f]));
            if (f) st16o<256>(kvp + u * 4096, vo, vv); else st16o<0>(kvp + u * 4096, vo, vv);
            qfr[t] = vv;
            SBAR();
        }
        half(IC<0>{}, F1, F0, false, sm, sm, sm, [&](int n) {
            const int t = n >> 1;
            if (n & 1) *(lds_u4)(own + vo + (t >> 1) * 4096 + (t & 1) * 256) = qfr[t];
            else       *(lds_u4)(own + ko + (t >> 1) * 4096 + (t & 1) * 128) = kfr[t];
        }, 0, 1);
        asm volatile("s_nop 7");
        SBAR();
#pragma unroll
        for (int u = 0; u < 2; ++u) {
            const float r0 = rs[2 * u] * QSCALE, r1 = rs[2 * u + 1] * QSCALE;
            const u32x4 qa = pk(r0 * (acc[0][2 * u][0] + mu[2 * u] * cq[0]), r0 * (acc[0][2 * u][1] + mu[2 * u] * cq[1]));
            const u32x4 qb = pk(r1 * (acc[0][2 * u + 1][0] + mu[2 * u + 1] * cq[0]), r1 * (acc[0][2 * u + 1][1] + mu[2 * u + 1] * cq[1]));
#pragma unroll
            for (int e = 0; e < 4; ++e) {
                const auto x = __builtin_amdgcn_permlane16_swap(qa[e], qb[e], false, false);
                qo4[2 * u][e] = x[0]; qo4[2 * u + 1][e] = x[1];
            }
        }
        return;
    }
    {
        const f32x4 zero4 = {0, 0, 0, 0};
        auto bf8 = [](u32x4 v) { return __builtin_bit_cast(bf16x8, v); };
        f32x4 sp[4], sc[4];
        sc[1] = MFMA16(bf8(kfr[1]), bf8(qfr[1]), mwin); sp[1] = MFMA16(bf8(kfr[0]), bf8(qfr[1]), mwin);
        sc[3] = MFMA16(bf8(kfr[3]), bf8(qfr[3]), mwin); sp[3] = MFMA16(bf8(kfr[2]), bf8(qfr[3]), mwin);
        asm volatile("" : "+v"(sc[1]), "+v"(sp[1]), "+v"(sc[3]), "+v"(sp[3]));
        WAIT_LGKM_BAR();
        const bf16x8 kp[2] = {lds_b128(ka[0]), lds_b128(ka[1])};
        u32x2 vp[2][2];
#pragma unroll
        for (int u = 0; u < 2; ++u)
            asm volatile("ds_read_b64 %0, %2\n\tds_read_b64 %1, %2 offset:8" : "=&v"(vp[u][0]), "=&v"(vp[u][1]) : "v"(vo2[u]) : "memory");
        sc[0] = MFMA16(bf8(kfr[0]), bf8(qfr[0]), mwin); sc[2] = MFMA16(bf8(kfr[2]), bf8(qfr[2]), mwin);
        SBAR();
        float x[4][4], m[4];
        unsigned a[4][2];
        auto logits = [&](int t0, int st) {
#pragma unroll
            for (int t = t0; t < 4; t += 2)
#pragma unroll
                for (int h = 0; h < 2; ++h) {
                    if (st == 0) {
                        const unsigned uc = pack_bf16(sc[t][2 * h], sc[t][2 * h + 1]), up = pack_bf16(sp[t][2 * h], sp[t][2 * h + 1]);
                        asm("v_bfi_b32 %0, %1, %2, %3" : "=v"(a[t][h]) : "v"(h ? cm23 : cm01), "v"(uc), "v"(up));
                    } else if (st == 1) { x[t][2 * h] = bf_lo(a[t][h]) * ATT_SCALE; x[t][2 * h + 1] = bf_hi(a[t][h]) * ATT_SCALE; }
                    else if (st == 2) a[t][h] = pack_bf16(x[t][2 * h], x[t][2 * h + 1]);
                    else { x[t][2 * h] = bf_lo(a[t][h]); x[t][2 * h + 1] = bf_hi(a[t][h]); }
                }
            SBAR();
        };
        logits(1, 0); logits(1, 1);
        sp[0] = MFMA16(kp[0], bf8(qfr[0]), mfirst); sp[2] = MFMA16(kp[1], bf8(qfr[2]), mfirst);
        SBAR();
        logits(1, 2); logits(1, 3);
        logits(0, 0); logits(0, 1); logits(0, 2); logits(0, 3);
        auto across = [&](float (&v)[4], auto op, auto fin) {
            const auto p = __builtin_amdgcn_permlane16_swap(__float_as_uint(v[0]), __float_as_uint(v[1]), false, false);
            const auto q = __builtin_amdgcn_permlane16_swap(__float_as_uint(v[2]), __float_as_uint(v[3]), false, false);
            const auto r = __builtin_amdgcn_permlane32_swap(__float_as_uint(op(__uint_as_float(p[0]), __uint_as_float(p[1]))),
                                                            __float_as_uint(op(__uint_as_float(q[0]), __uint_as_float(q[1]))), false, false);
            const unsigned z = __float_as_uint(fin(op(__uint_as_float(r[0]), __uint_as_float(r[1]))));
            const auto u = __builtin_amdgcn_permlane32_swap(z, z, false, false);
            const auto e = __builtin_amdgcn_permlane16_swap(u[0], u[0], false, false), f = __builtin_amdgcn_permlane16_swap(u[1], u[1], false, false);
            v[0] = __uint_as_float(e[0]); v[1] = __uint_as_float(e[1]); v[2] = __uint_as_float(f[0]); v[3] = __uint_as_float(f[1]);
        };
#pragma unroll
        for (int t = 0; t < 4; ++t) m[t] = amax3(x[t][0], x[t][1], x[t][2]);
        asm("v_max_f32 %0, %0, %4\n\tv_max_f32 %1, %1, %5\n\tv_max_f32 %2, %2, %6\n\tv_max_f32 %3, %3, %7\n\ts_nop 0"
            : "+v"(m[0]), "+v"(m[1]), "+v"(m[2]), "+v"(m[3]) : "v"(x[0][3]), "v"(x[1][3]), "v"(x[2][3]), "v"(x[3][3]));
        across(m, [](float p, float q) { float r; asm("v_max_f32 %0, %1, %2\n\ts_nop 0" : "=v"(r) : "v"(p), "v"(q)); return r; },
               [](float p) { return p * -LOG2E; });
        SBAR();
#pragma unroll
        for (int t = 0; t < 4; ++t)
#pragma unroll
            for (int r = 0; r < 4; ++r) x[t][r] = __builtin_fmaf(x[t][r], LOG2E, m[t]);
        SBAR();
#pragma unroll
        for (int t = 0; t < 4; ++t)
#pragma unroll
            for (int r = 0; r < 4; ++r) x[t][r] = __builtin_amdgcn_exp2f(x[t][r]);
        SBAR();
#pragma unroll
        for (int t = 0; t < 4; ++t) m[t] = (x[t][0] + x[t][1]) + (x[t][2] + x[t][3]);
        asm volatile("s_waitcnt lgkmcnt(0)" : "+v"(vp[0][0]), "+v"(vp[0][1]), "+v"(vp[1][0]), "+v"(vp[1][1]) :: "memory");
        u32x4 ve[2][2];
#pragma unroll
        for (int u = 0; u < 2; ++u)
#pragma unroll
            for (int f = 0; f < 2; ++f) ve[u][f] = u32x4{vp[u][f][0], vp[u][f][1], va[u][f][0], va[u][f][1]};
        across(m, [](float p, float q) { return p + q; }, [](float p) { return __builtin_amdgcn_rcpf(p); });
        asm volatile("" : "+v"(ve[0][0]), "+v"(ve[0][1]), "+v"(ve[1][0]), "+v"(ve[1][1]));
        SBAR();
#pragma unroll
        for (int t = 0; t < 4; ++t)
#pragma unroll
            for (int r = 0; r < 4; ++r) x[t][r] *= m[t];
        SBAR();
#pragma unroll
        for (int t = 0; t < 4; ++t)
#pragma unroll
            for (int h = 0; h < 2; ++h) a[t][h] = pack_bf16(x[t][2 * h], x[t][2 * h + 1]);
        SBAR();
        f32x4 o0[4], o1[4];
#pragma unroll
        for (int t = 0; t < 4; ++t) {
            const bf16x8 pb = bf8(u32x4{a[t][0] & ~cm01, a[t][1] & ~cm23, a[t][0] & cm01, a[t][1] & cm23});
            o0[t] = MFMA16(bf8((t & 1) ? va[t >> 1][0] : ve[t >> 1][0]), pb, zero4);
            o1[t] = MFMA16(bf8((t & 1) ? va[t >> 1][1] : ve[t >> 1][1]), pb, zero4);
        }
        SBAR();
#pragma unroll
        for (int t = 0; t < 4; ++t)
            st16(ofrag + t * 16384, qo, u32x4{pack_bf16(o0[t][0], o0[t][1]), pack_bf16(o0[t][2], o0[t][3]),
                                                 pack_bf16(o1[t][0], o1[t][1]), pack_bf16(o1[t][2], o1[t][3])});
    }
}

DI int out_row(int panel, bool localrows, int T, int a) {
    return localrows ? (panel * 128 + (T >> 2) * 64 + 2 * (16 * (T & 1) + a) + ((T >> 1) & 1)) : (panel * 128 + T * 16 + a);
}
DI int out_swz(int k) { return ((k >> 1) & 1) | (k & 4); }
DI unsigned wo_off(int w, int lane) {
    const int kl = lane >> 3;
    unsigned v = (unsigned)(w * 8 + kl) * 2048u + (unsigned)(ids().co * 128 + ((lane & 7) ^ out_swz(kl)) * 16);
    asm volatile("" : "+v"(v));
    return v;
}
DI void wo_load(const void* Wo, unsigned voff, int i0, int i1, unsigned lds0, int w) {
#pragma unroll
    for (int i = i0; i < i1; ++i) glds16((const char*)Wo + (size_t)i * 65536, voff, lds0 + (w + 4 * i) * 1024);
}
DI unsigned res_off(bool localrows, bool tiled, int lane) {
    const Ids I = ids();
    unsigned v = (unsigned)(I.po * 262144 + I.co * (tiled ? 16384 : 128) + out_row(0, localrows, 0, lane >> 3) * (tiled ? 128 : 2048) +
                            (((lane & 7) ^ (lane >> 3)) * 16));
    asm volatile("" : "+v"(v));
    return v;
}
DI void res_load(u32x4 (&res)[6], const void* Xin, bool localrows, bool tiled, unsigned off, int w) {
#pragma unroll
    for (int i = 0; i < 6; ++i) {
        const int j = min(w + 3 * i, 15);
        asm volatile("global_load_dwordx4 %0, %1, %2 sc1" : "=&v"(res[i]) : "v"(off), "s"((const char*)Xin + out_row(0, localrows, j >> 1, (j & 1) * 8) * (tiled ? 128 : 2048)) : "memory");
    }
}
DI void out_handoff(char* smem, int xoff, u32x4 (&res)[6], int w, char* ws, int set, u64 tag, int lane, u32x4 (&c)[8], int mat) {
    const Ids I = ids();
    u64 land;
    lds_ptr dst = (lds_ptr)smem + xoff + w * 1024 + lane * 16;
    asm volatile("" : "+v"(dst));
    if (w == 3) __builtin_amdgcn_s_setprio(3);
    signal_poll<false, false>(w - 3, ws, flag_off(set, I.pq, lane & 15, I.cq), flag_off(set, I.po, I.co, lane & 15), 0xffffull, tag, land);
    if (w < 3) {
        asm volatile("s_waitcnt vmcnt(0)" : "+v"(res[0]), "+v"(res[1]), "+v"(res[2]), "+v"(res[3]), "+v"(res[4]), "+v"(res[5]) :: "memory");
#pragma unroll
        for (int i = 0; i < 5; ++i) *(__attribute__((address_space(3))) u32x4*)(dst + i * 3072) = res[i];
        if (w == 0) *(__attribute__((address_space(3))) u32x4*)(dst + 15360) = res[5];
        if (mat >= 0) colsum_store(c, ws, mat, w, lane);
    }
    WAIT_LGKM_BAR();
    asm volatile("s_waitcnt vmcnt(0)" : "+v"(land) :: "memory");
    if (w < 3 && mat >= 0) flag_store(ws, (mat ? 10 : 13) + w, I.cq, I.pq, tag, lane);
    __builtin_amdgcn_s_setprio(0);
}

template <bool AFTER_LOCAL>
DI void out_compute(char* smem, int w, const __hip_bfloat16* __restrict__ ofrag,
                    __hip_bfloat16* __restrict__ Xout, float* __restrict__ statp SP_PARAM) {
    constexpr int XOFF = AFTER_LOCAL ? 147456 : 98304;
    STAMP(AFTER_LOCAL ? 3 : 10);
    VM_BAR(0);
    const int lane = lane_id();
    asm volatile("" : "+s"(w));
    const Ids I = ids();
    const int panel = I.po, ct = I.co;
    const int l15 = lane & 15, g = lane >> 4;
    const bool tiled = AFTER_LOCAL || statp != nullptr;
    const int rstr = tiled ? 128 : 2048;
    const int q4 = l15 >> 2, p4 = l15 & 3;
    const int krow = 4 * g + q4;
    const int sw = out_swz(krow);
    const lds_cptr sm = (lds_cptr)smem;
    lds_cptr bw[4], xo[4];
#pragma unroll
    for (int t = 0; t < 4; ++t) {
        bw[t] = sm + krow * 128 + ((((t >> 1) * 4 + (p4 >> 1) * 2 + (t & 1)) ^ sw) * 16) + (p4 & 1) * 8;
        xo[t] = sm + XOFF + (w * 32 + l15) * 128 + ((((t >> 1) * 4 + (g >> 1) * 2 + (t & 1)) ^ (l15 & 7)) * 16) + (g & 1) * 8;
    }
    unsigned so[2], sto, ao[2];
#pragma unroll
    for (int r2 = 0; r2 < 2; ++r2) {
        so[r2] = (unsigned)(panel * 262144 + ct * (tiled ? 16384 : 128) + out_row(0, AFTER_LOCAL, 2 * w + r2, l15 & 7) * rstr + (l15 >> 3) * 64 + g * 16);
        ao[r2] = (unsigned)((panel * 8 + 2 * w + r2) * 32768 + lane * 16);
    }
    sto = (unsigned)(((panel * 16 + ct) * 128 + 2 * w * 16 + l15) * 8);
    asm volatile("" : "+v"(bw[0]), "+v"(bw[1]), "+v"(bw[2]), "+v"(bw[3]), "+v"(xo[0]), "+v"(xo[1]), "+v"(xo[2]), "+v"(xo[3]),
                 "+v"(so[0]), "+v"(so[1]), "+v"(sto), "+v"(ao[0]), "+v"(ao[1]));
    bf16x8 ones;
    {
        unsigned o1; asm volatile("v_mov_b32 %0, 0x3f803f80" : "=v"(o1));
        ones = __builtin_bit_cast(bf16x8, (u32x4{o1, o1, o1, o1}));
        asm volatile("" : "+v"(ones));
    }
    const __amdgpu_buffer_rsrc_t ra = __builtin_amdgcn_make_buffer_rsrc((void*)ofrag, 0, 4 << 20, 0x00020000);
    asm volatile("" :: "s"(ra));
    u32x32 fr;
    BAR();
    STAMP(AFTER_LOCAL ? 4 : 11);
    u32x32 aw0, aw1;
    asm volatile(".irp j,0,1,2,3\n\tbuffer_load_dwordx4 v[64+8*\\j:67+8*\\j], %2, %4, 0 offen offset:1024*\\j sc1\n\t"
                 "buffer_load_dwordx4 v[68+8*\\j:71+8*\\j], %3, %4, 0 offen offset:1024*\\j sc1\n\t.endr\n\t"
                 ".irp j,0,1\n\tbuffer_load_dwordx4 v[96+8*\\j:99+8*\\j], %2, %4, %5 offen offset:1024*\\j sc1\n\t"
                 "buffer_load_dwordx4 v[100+8*\\j:103+8*\\j], %3, %4, %5 offen offset:1024*\\j sc1\n\t.endr"
                 : "=&{v[64:95]}"(aw0), "=&{v[96:127]}"(aw1) : "v"(ao[0]), "v"(ao[1]), "s"(ra), "s"(4096) : "memory");
    f32x16 acc[2];
#pragma unroll
    for (int r2 = 0; r2 < 2; ++r2)
#pragma unroll
        for (int c = 0; c < 4; ++c) {
            const u32x2 xr = *(const __attribute__((address_space(3))) u32x2*)(xo[c] + r2 * 2048);
            acc[r2][4 * c] = bf_lo(xr[0]); acc[r2][4 * c + 1] = bf_hi(xr[0]); acc[r2][4 * c + 2] = bf_lo(xr[1]); acc[r2][4 * c + 3] = bf_hi(xr[1]);
        }
    asm volatile(
        ".macro OUT_MM r, t, j\n\t"
        "v_mfma_f32_16x16x32_bf16 v[16*\\r+4*\\t:16*\\r+4*\\t+3], v[32+16*((\\j)&1)+4*\\t:35+16*((\\j)&1)+4*\\t], v[64+8*\\j+4*\\r:67+8*\\j+4*\\r], v[16*\\r+4*\\t:16*\\r+4*\\t+3]\n\t"
        ".endm\n\t"
        ".macro OUT_RD t, hi, j, on\n\t"
        ".if \\on\n\tds_read_b64_tr_b16 v[32+16*((\\j)&1)+4*\\t+2*\\hi:33+16*((\\j)&1)+4*\\t+2*\\hi], v[128+\\t] offset:((\\j)&7)*4096+2048*\\hi\n\t.endif\n\t"
        ".endm\n\t"
        ".macro OUT_UP t, on\n\t.if \\on\n\tv_add_u32 v[128+\\t], s97, v[128+\\t]\n\t.endif\n\t.endm\n\t"
        ".macro OUT_STEP j, ld, rd, up, vm, lg\n\t"
        "OUT_MM 0, 0, \\j\n\tOUT_RD 3, 1, \\j+1, 1\n\tOUT_UP 0, \\up\n\t"
        ".if \\ld && (((\\j+6)&3) == 0)\n\ts_addk_i32 s96, 0x1000\n\t.endif\n\t"
        "OUT_MM 1, 0, \\j\n\tOUT_RD 0, 0, \\j+2, \\rd\n\t"
        ".if \\ld\n\tbuffer_load_dwordx4 v[64+8*((\\j+6)&7):67+8*((\\j+6)&7)], %[a0], %[ra], s96 offen offset:1024*((\\j+6)&3) sc1\n\t.endif\n\t"
        "OUT_MM 0, 1, \\j\n\tOUT_RD 0, 1, \\j+2, \\rd\n\tOUT_UP 1, \\up\n\t"
        "OUT_MM 1, 1, \\j\n\tOUT_RD 1, 0, \\j+2, \\rd\n\t"
        "OUT_MM 0, 2, \\j\n\tOUT_RD 1, 1, \\j+2, \\rd\n\tOUT_UP 2, \\up\n\t"
        "OUT_MM 1, 2, \\j\n\tOUT_RD 2, 0, \\j+2, \\rd\n\t"
        ".if \\ld\n\tbuffer_load_dwordx4 v[68+8*((\\j+6)&7):71+8*((\\j+6)&7)], %[a1], %[ra], s96 offen offset:1024*((\\j+6)&3) sc1\n\t.endif\n\t"
        "OUT_MM 0, 3, \\j\n\tOUT_RD 2, 1, \\j+2, \\rd\n\tOUT_UP 3, \\up\n\t"
        "OUT_MM 1, 3, \\j\n\tOUT_RD 3, 0, \\j+2, \\rd\n\t"
        "s_waitcnt vmcnt(\\vm) lgkmcnt(\\lg)\n\t"
        ".endm\n\t"
        ".macro OUT_PACK y, c\n\t"
        ".irp i,0,1,2,3,4,5,6,7\n\tv_cvt_pk_bf16_f32 v[\\y+\\i], v[\\c+2*\\i], v[\\c+2*\\i+1]\n\t.endr\n\t"
        ".irp i,0,1,4,5\n\tv_permlane16_swap_b32 v[\\y+\\i], v[\\y+\\i+2]\n\t.endr\n\t"
        ".irp i,0,1,2,3\n\tv_mov_b32 v[\\y+8+\\i], v[\\y+\\i]\n\t.endr\n\t"
        "v_mfma_f32_16x16x32_bf16 v[\\y+12:\\y+15], v[\\y:\\y+3], v[\\y:\\y+3], 0\n\t"
        "v_mov_b32_dpp v[\\y+8], v[\\y+4] row_ror:8 row_mask:0xf bank_mask:0xc\n\tv_mov_b32_dpp v[\\y+9], v[\\y+5] row_ror:8 row_mask:0xf bank_mask:0xc\n\t"
        "v_mfma_f32_16x16x32_bf16 v[\\y+16:\\y+19], %[one], v[\\y:\\y+3], 0\n\t"
        "v_mov_b32_dpp v[\\y+10], v[\\y+6] row_ror:8 row_mask:0xf bank_mask:0xc\n\tv_mov_b32_dpp v[\\y+11], v[\\y+7] row_ror:8 row_mask:0xf bank_mask:0xc\n\t"
        "v_mfma_f32_16x16x32_bf16 v[\\y+12:\\y+15], v[\\y+4:\\y+7], v[\\y+4:\\y+7], v[\\y+12:\\y+15]\n\t"
        ".endm\n\t"
        ".macro OUT_REST y\n\t"
        "v_mfma_f32_16x16x32_bf16 v[\\y+16:\\y+19], %[one], v[\\y+4:\\y+7], v[\\y+16:\\y+19]\n\t"
        ".irp i,0,1,2,3\n\tv_mov_b32_dpp v[\\y+4+\\i], v[\\y+\\i] row_ror:8 row_mask:0xf bank_mask:0x3\n\t.endr\n\t"
        ".endm\n\t"
        ".macro OUT_DIAG y\n\t"
        "v_cndmask_b32 v[\\y+12], v[\\y+12], v[\\y+13], %[m0]\n\tv_cndmask_b32 v[\\y+14], v[\\y+14], v[\\y+15], %[m0]\n\t"
        "v_cndmask_b32 v[\\y+17], v[\\y+12], v[\\y+14], %[m1]\n\t"
        ".endm\n\t"
        ".irp t,0,1,2,3\n\tOUT_RD \\t, 0, 0, 1\n\tOUT_RD \\t, 1, 0, 1\n\t.endr\n\t"
        ".irp t,0,1,2\n\tOUT_RD \\t, 0, 1, 1\n\tOUT_RD \\t, 1, 1, 1\n\t.endr\n\tOUT_RD 3, 0, 1, 1\n\t"
        "s_movk_i32 s96, 0x1000\n\ts_mov_b32 s97, 0x8000\n\t"
        "s_waitcnt vmcnt(10) lgkmcnt(0)\n"
        ".Lout_loop_%=:\n\t"
        "OUT_STEP 0, 1, 1, 0, 10, 7\n\tOUT_STEP 1, 1, 1, 0, 10, 7\n\tOUT_STEP 2, 1, 1, 0, 10, 7\n\tOUT_STEP 3, 1, 1, 0, 10, 7\n\t"
        "OUT_STEP 4, 1, 1, 0, 10, 7\n\t"
        "s_cmpk_eq_u32 s96, 0x6000\n\ts_cselect_b32 s97, %[last], s97\n\t"
        "OUT_STEP 5, 1, 1, 0, 10, 7\n\tOUT_STEP 6, 1, 1, 1, 10, 7\n\t"
        "OUT_STEP 7, 1, 1, 0, 10, 7\n\t"
        "s_cmpk_lg_u32 s96, 0x7000\n\ts_cbranch_scc1 .Lout_loop_%=\n\t"
        "OUT_STEP 0, 1, 1, 0, 10, 7\n\tOUT_STEP 1, 1, 1, 0, 10, 7\n\tOUT_STEP 2, 0, 1, 0, 8, 7\n\tOUT_STEP 3, 0, 1, 0, 6, 7\n\t"
        "OUT_STEP 4, 0, 1, 0, 4, 7\n\tOUT_STEP 5, 0, 1, 0, 2, 7\n\tOUT_STEP 6, 0, 0, 0, 0, 0\n\t"
        ".irp r,0,1\n\t.irp t,0,1,2,3\n\tOUT_MM \\r, \\t, 7\n\t.endr\n\t.endr\n\t"
        "s_nop 0\n\t"
        "OUT_PACK 64, 0\n\tglobal_store_dwordx4 %[s0], v[72:75], %[xa] sc1\n\tOUT_REST 64\n\tglobal_store_dwordx4 %[s0], v[68:71], %[xb] sc1\n\t"
        "OUT_PACK 84, 16\n\tglobal_store_dwordx4 %[s1], v[92:95], %[xa] sc1\n\tOUT_REST 84\n\tglobal_store_dwordx4 %[s1], v[88:91], %[xb] sc1\n\t"
        "s_cmp_eq_u64 %[st], 0\n\ts_cbranch_scc1 .Lout_end_%=\n\t"
        "OUT_DIAG 64\n\tOUT_DIAG 84\n\t"
        "s_mov_b64 exec, %[md]\n\t"
        "global_store_dwordx2 %[so], v[80:81], %[st] sc1\n\tglobal_store_dwordx2 %[so], v[100:101], %[st] offset:128 sc1\n\t"
        "s_mov_b64 exec, -1\n"
        ".Lout_end_%=:\n\t"
        "s_nop 1\n\t"
        ".purgem OUT_MM\n\t.purgem OUT_RD\n\t.purgem OUT_UP\n\t.purgem OUT_STEP\n\t.purgem OUT_PACK\n\t.purgem OUT_REST\n\t.purgem OUT_DIAG"
        : "+{v[0:15]}"(acc[0]), "+{v[16:31]}"(acc[1]), "+{v[64:95]}"(aw0), "+{v[96:127]}"(aw1),
          "+{v128}"(bw[0]), "+{v129}"(bw[1]), "+{v130}"(bw[2]), "+{v131}"(bw[3]), "=&{v[32:63]}"(fr)
        : [a0] "v"(ao[0]), [a1] "v"(ao[1]), [ra] "s"(ra), [last] "s"(AFTER_LOCAL ? 32768 : 65536), [one] "v"(ones), [s0] "v"(so[0]), [s1] "v"(so[1]),
          [xa] "s"(Xout), [xb] "s"((char*)Xout + (AFTER_LOCAL ? 16 : 8) * rstr), [st] "s"(statp), [so] "v"(sto),
          [m0] "s"(0xaaaaaaaaaaaaaaaaull), [m1] "s"(0xccccccccccccccccull), [md] "s"(0xf0000f0000f0000full)
        : "memory", "scc", "s96", "s97");
    STAMP(AFTER_LOCAL ? 5 : 12);
}

#define ROWSUM(p0, p1, c) __builtin_amdgcn_smfmac_f32_16x16x64_bf16(sel, __builtin_bit_cast(bf16v_t, __builtin_shufflevector(p0, p1, 0, 1, 2, 3, 4, 5, 6, 7)), c, sidx, 0, 0)
constexpr int ATT_STAGE = 32768;
typedef __attribute__((ext_vector_type(8))) unsigned u32x8;

DI void attn_loader(char* smem, int w, char* ws, int set, u64 tag, const __hip_bfloat16* __restrict__ kvf, const void* Wo, const void* Xin,
                    const void* Wcs SP_PARAM) {
    const int lane = lane_id();
    asm volatile("" : "+s"(w));
    const Ids I = ids();
    const int panel = I.pq, ct = I.cq;
    const unsigned lds0 = (unsigned)(uintptr_t)smem;
    const char* kvh = (const char*)kvf + (size_t)(ct * 2 + (w >> 1)) * 262144 + (w & 1) * 4096;
    const unsigned voff = (unsigned)(lane * 16);
    auto order = [&](int st) { return ((panel & 8) + st - ((st - 1 < (panel & 7)) ? 1 : 0)) & 15; };
    auto load = [&](int st) {
        const char* src = kvh + (size_t)order(st) * 16384;
        const unsigned dst = lds0 + (w >> 1) * 16384 + (w & 1) * 4096 + (st & 3) * ATT_STAGE;
#pragma unroll
        for (int u = 0; u < 2; ++u)
            asm volatile("s_mov_b32 m0, %0\n\ts_nop 0\n\tglobal_load_lds_dwordx4 %1, %2 sc1\n\tglobal_load_lds_dwordx4 %1, %2 offset:1024 sc1\n\t"
                         "global_load_lds_dwordx4 %1, %2 offset:2048 sc1\n\tglobal_load_lds_dwordx4 %1, %2 offset:3072 sc1"
                         :: "s"(dst + u * 8192), "v"(voff), "s"(src + u * 8192) : "memory");
    };
    const unsigned wv = wo_off(w, lane), rv = res_off(false, true, lane), cv = colsum_off(w, lane);
    const unsigned poff = flag_off(set, ct, panel, lane & 15);
    u64 land;
    __builtin_amdgcn_s_setprio(3);
    BAR();
    signal_poll<false, true>(w, ws, flag_off(set, ct, lane & 15, panel), poff, 1ull << order(1), tag, land);
    STAMP(9);
    load(1);
    asm volatile("global_load_dwordx2 %0, %1, %2 sc1\n\ts_waitcnt vmcnt(0)" : "+v"(land) : "v"(poff), "s"(ws + WS_FLAG) : "memory");
    STAMP(10);
    BAR();
    if (!__all(land == tag)) flag_poll(ws, poff, tag);
    load(2); load(3);
    __builtin_amdgcn_s_setprio(0);
    STAMP(11);
    VM_BAR(8); load(4);
    VM_BAR(0); load(5); load(6);
    VM_BAR(0); load(7); load(8);
    u32x4 res[6], ca[8];
    colsum_load(ca, w < 3 ? Wcs : nullptr, cv);
#pragma unroll 1
    for (int st = 6; st < 12; st += 2) {
        VM_BAR(0);
        load(st + 3); load(st + 4);
        if (st == 8) wo_load(Wo, wv, 24, 32, lds0 + 32768, w);
    }
    VM_BAR(0); load(15); wo_load(Wo, wv, 0, 8, lds0, w);
    res_load(res, Xin, false, true, rv, w);
    VM_BAR(0); wo_load(Wo, wv, 8, 24, lds0, w);
    STAMP(12);
    out_handoff(smem, 98304, res, w, ws, set + 1, tag, lane, ca, Wcs ? 0 : -1);
}

DI void attn_compute(char* smem, int w, const u32x4 (&q4)[4], const __hip_bfloat16* __restrict__ kvf, __hip_bfloat16* __restrict__ ofrag SP_PARAM) {
    const int lane = lane_id();
    asm volatile("" : "+s"(w));
    const int wm = w & 1, wn = w >> 1;
    const Ids I = ids();
    const int panel = I.pq, head = I.cq * 2 + wn;
    const int l15 = lane & 15;
    const unsigned se = ((l15 & 2) == 0 && (l15 & 4) == 0) ? 0x3f803f80u : 0u, so = ((l15 & 2) == 0 && (l15 & 4) != 0) ? 0x3f803f80u : 0u;
    const bf8_t sel = __builtin_bit_cast(bf8_t, u32x4{se, se, so, so});
    const int sidx = (l15 & 1) ? (int)0xEEEEEEEEu : 0x44444444;
    const lds_cptr sm = (lds_cptr)smem + wn * 16384 + lane * 16;
    const f32x16 z16 = {0, 0, 0, 0, 0, 0, 0, 0, 0, 0, 0, 0, 0, 0, 0, 0};
    const bf16x8 qA0 = __builtin_bit_cast(bf16x8, q4[0]), qA1 = __builtin_bit_cast(bf16x8, q4[1]);
    const bf16x8 qB0 = __builtin_bit_cast(bf16x8, q4[2]), qB1 = __builtin_bit_cast(bf16x8, q4[3]);
    f32x16 oA, oB, nmA, nmB, sA, sB;
    f32x4 lsA, lsB;
    u32x8 pB;
    bf16x8 v11;
    asm volatile(".irp r,106,108,110,112,134,136,138,140,170,172\n\tv_mov_b64 v[\\r:\\r+1], 0\n\t.endr"
                 : "={v[106:113]}"(pB), "={v[134:137]}"(lsA), "={v[138:141]}"(lsB), "={v[170:173]}"(v11));
    WAIT_LGKM_BAR();
    STAMP(8);
    bf16x8 k00 = lds_b128(sm), k10 = lds_b128(sm + 1024), k01 = lds_b128(sm + 4096), k11 = lds_b128(sm + 4096 + 1024);
    bf16x8 v00 = lds_b128(sm + 2048), v10 = lds_b128(sm + 3072), v01 = lds_b128(sm + 4096 + 2048);
    float ob;
    {
        const u32x4 vu = __builtin_bit_cast(u32x4, v00);
        unsigned vm = max(max(vu[0] & 0x7fff0000u, vu[1] & 0x7fff0000u), max(vu[2] & 0x7fff0000u, vu[3] & 0x7fff0000u));
        vm = min(vm, (unsigned)__builtin_amdgcn_update_dpp(0, (int)vm, 0xB1, 0xF, 0xF, true));
        vm = min(vm, (unsigned)__builtin_amdgcn_update_dpp(0, (int)vm, 0x4E, 0xF, 0xF, true));
        vm = min(vm, (unsigned)__builtin_amdgcn_update_dpp(0, (int)vm, 0x141, 0xF, 0xF, true));
        vm = min(vm, (unsigned)__builtin_amdgcn_update_dpp(0, (int)vm, 0x140, 0xF, 0xF, true));
        const auto vs = __builtin_amdgcn_permlane16_swap(vm, vm, false, false);
        ob = __uint_as_float(min(vs[0], vs[1])) * 0.015625f;
        sA = MFMA32(k00, qA0, z16); sB = MFMA32(k00, qB0, z16);
        sA = MFMA32(k10, qA1, sA); sB = MFMA32(k10, qB1, sB);
        SBAR(); VM_BAR(0); SBAR();
        float mA = sA[0], mB = sB[0];
#pragma unroll
        for (int r = 1; r < 16; ++r) { mA = fmaxf(mA, sA[r]); mB = fmaxf(mB, sB[r]); }
        mA = max_h(mA) + 16.f; mB = max_h(mB) + 16.f;
#pragma unroll
        for (int r = 0; r < 16; ++r) { nmA[r] = -mA; nmB[r] = -mB; sA[r] -= mA; oA[r] = ob; oB[r] = ob; }
    }
    const unsigned a0 = (unsigned)(uintptr_t)sm;
    unsigned dst = (unsigned)((((panel * 8 + wm * 4 + ((lane & 31) >> 4)) * 32 + head) * 64 + (lane & 15) + (lane >> 5) * 16) * 16);
    asm volatile("" : "+v"(dst));
    asm volatile(
        ".macro ATT_RD dst, y\n\t"
        "ds_read_b128 v[\\dst:\\dst+3], v[175+(((\\y)&0x1ffff)>>16)] offset:(\\y)&0xffff\n\t"
        ".endm\n\t"
        ".macro ATT_E4 c\n\t"
        "v_exp_f32 v[\\c], v[\\c]\n\tv_exp_f32 v[\\c+1], v[\\c+1]\n\tv_exp_f32 v[\\c+2], v[\\c+2]\n\tv_exp_f32 v[\\c+3], v[\\c+3]\n\t"
        ".endm\n\t"
        ".macro ATT_C4 p, c\n\t"
        "v_cvt_pk_bf16_f32 v[\\p], v[\\c], v[\\c+1]\n\tv_cvt_pk_bf16_f32 v[\\p+1], v[\\c+2], v[\\c+3]\n\t"
        "v_cvt_pk_bf16_f32 v[\\p+2], v[\\c+4], v[\\c+5]\n\tv_cvt_pk_bf16_f32 v[\\p+3], v[\\c+6], v[\\c+7]\n\t"
        ".endm\n\t"
        ".macro ATT_STEP cur, nxt, ka, kb, q, nm, oc, od, lsd, pc, pd, vd1, vc0, r0, y0, r1, y1, bar, rd\n\t"
        "v_mfma_f32_32x32x16_bf16 v[\\nxt:\\nxt+15], v[\\ka:\\ka+3], v[\\q:\\q+3], v[\\nm:\\nm+15]\n\t"
        "ATT_E4 \\cur\n\t"
        "v_mfma_f32_32x32x16_bf16 v[\\nxt:\\nxt+15], v[\\kb:\\kb+3], v[\\q+4:\\q+7], v[\\nxt:\\nxt+15]\n\t"
        "ATT_E4 \\cur+4\n\t"
        "v_mfma_f32_32x32x16_bf16 v[\\od:\\od+15], v[\\vd1:\\vd1+3], v[\\pd+4:\\pd+7], v[\\od:\\od+15]\n\t"
        "s_waitcnt lgkmcnt(0)\n\t"
        "ATT_C4 \\pc, \\cur\n\t"
        "v_exp_f32 v[\\cur+8], v[\\cur+8]\n\tv_exp_f32 v[\\cur+9], v[\\cur+9]\n\t"
        "v_mfma_f32_32x32x16_bf16 v[\\oc:\\oc+15], v[\\vc0:\\vc0+3], v[\\pc:\\pc+3], v[\\oc:\\oc+15]\n\t"
        ".if \\bar\n\ts_barrier\n\t.endif\n\t"
        "ATT_E4 \\cur+10\n\t"
        "v_smfmac_f32_16x16x64_bf16 v[\\lsd:\\lsd+3], v[130:133], v[\\pd:\\pd+7], v174\n\t"
        ".if \\rd & 4\n\tATT_RD 142, 32768\n\tATT_RD 150, 33792\n\tATT_RD 158, 34816\n\t.endif\n\t"
        ".if \\rd & 8\n\tATT_RD \\r1-8, \\y1-1024\n\t.endif\n\t"
        ".if \\rd & 1\n\tATT_RD \\r0, \\y0\n\t.endif\n\t"
        ".if \\rd & 2\n\tATT_RD \\r1, \\y1\n\t.endif\n\t"
        "v_exp_f32 v[\\cur+14], v[\\cur+14]\n\tv_exp_f32 v[\\cur+15], v[\\cur+15]\n\t"
        "ATT_C4 \\pc+4, \\cur+8\n\t"
        ".if \\rd & 4\n\ts_waitcnt lgkmcnt(3)\n\t.endif\n\t"
        ".endm\n\t"
        ".macro ATT_TILE p, y1, y2, bar, ra=3, rb=3\n\t"
        "ATT_STEP 66, 82, 142+\\p, 150+\\p, 122, 50, 2, 18, 138, 98, 106, 170-\\p, 158+\\p, 170-\\p, \\y1+3072, 142+\\p, \\y2, \\bar&1, \\ra\n\t"
        "ATT_STEP 82, 66, 146-\\p, 154-\\p, 114, 34, 18, 2, 134, 106, 98, 166+\\p, 158+\\p, 158+\\p, \\y2+2048, 150+\\p, \\y2+1024, \\bar&2, \\rb\n\t"
        ".endm\n\t"
        ".macro ATT_STAGE b, bar\n\t"
        "ATT_TILE 0, \\b+4096, \\b+8192, 0\n\t"
        "ATT_TILE 4, \\b+8192, \\b+12288, 0\n\t"
        ".if \\bar\n\tATT_TILE 0, \\b+12288, \\b+32768, 2, 1, 11\n\t.else\n\tATT_TILE 0, \\b+12288, \\b+32768, 0\n\t.endif\n\t"
        "ATT_TILE 4, \\b+32768, \\b+36864, 0\n\t"
        ".endm\n\t"
        "v_add_u32 v176, 0x10000, v175\n\t"
        "s_mov_b32 s96, 4\n\t"
        "s_waitcnt lgkmcnt(0)\n\t"
        "ATT_TILE 0, 4096, 8192, 0\n\tATT_TILE 4, 8192, 12288, 0\n\tATT_TILE 0, 12288, 0, 0, 1, 0\n\tATT_TILE 4, 32768, 36864, 1, 7, 3\n\t"
        "ATT_STAGE 32768, 1\n\t"
        "s_branch .Latt_entry_%=\n\t"
        ".p2align 6\n"
        ".Latt_loop_%=:\n\t"
        "ATT_STAGE 0, 1\n\tATT_STAGE 32768, 0\n"
        ".Latt_entry_%=:\n\t"
        "ATT_STAGE 65536, 1\n\t"
        "s_sub_u32 s96, s96, 1\n\t"
        "ATT_STAGE 98304, 0\n\t"
        "s_cmp_lg_u32 s96, 0\n\t"
        "s_cbranch_scc1 .Latt_loop_%=\n\t"
        "s_nop 1\n\t"
        "v_mfma_f32_32x32x16_bf16 v[18:33], v[170:173], v[110:113], v[18:33]\n\t"
        "v_smfmac_f32_16x16x64_bf16 v[138:141], v[130:133], v[106:113], v174\n\t"
        "s_waitcnt lgkmcnt(0)\n\ts_nop 10\n\t"
        ".purgem ATT_RD\n\t.purgem ATT_E4\n\t.purgem ATT_C4\n\t.purgem ATT_STEP\n\t.purgem ATT_TILE\n\t.purgem ATT_STAGE"
        : "+{v[2:17]}"(oA), "+{v[18:33]}"(oB), "+{v[66:81]}"(sA), "+{v[106:113]}"(pB), "+{v[134:137]}"(lsA), "+{v[138:141]}"(lsB),
          "+{v[142:145]}"(k00), "+{v[146:149]}"(k01), "+{v[150:153]}"(k10), "+{v[154:157]}"(k11),
          "+{v[158:161]}"(v00), "+{v[162:165]}"(v01), "+{v[166:169]}"(v10), "+{v[170:173]}"(v11)
        : "{v[34:49]}"(nmA), "{v[50:65]}"(nmB), "{v[114:117]}"(qA0), "{v[118:121]}"(qA1), "{v[122:125]}"(qB0), "{v[126:129]}"(qB1),
          "{v[130:133]}"(sel), "{v174}"(sidx), "{v175}"(a0)
        : "memory", "scc", "s96", "v176", "v82", "v83", "v84", "v85", "v86", "v87", "v88", "v89", "v90", "v91", "v92", "v93", "v94", "v95",
          "v96", "v97", "v98", "v99", "v100", "v101", "v102", "v103", "v104", "v105");
    STAMP(9);
    float lA = lsA[0] + lsA[1], lB = lsB[0] + lsB[1];
    auto store = [&] {
#pragma unroll
    for (int u = 0; u < 2; ++u) {
        const float inv = __builtin_amdgcn_rcpf(u ? lB : lA), sub = -ob * inv;
        const f32x16 e = (u ? oB : oA) * inv + sub;
        st16o<0>((char*)ofrag + u * 65536, dst, u32x4{pack_bf16(e[0], e[1]), pack_bf16(e[2], e[3]), pack_bf16(e[8], e[9]), pack_bf16(e[10], e[11])});
        st16o<512>((char*)ofrag + u * 65536, dst, u32x4{pack_bf16(e[4], e[5]), pack_bf16(e[6], e[7]), pack_bf16(e[12], e[13]), pack_bf16(e[14], e[15])});
    }
    };
    store();
    {
        const f32x16 os = oA + oB;
        const float osum = (((os[0] + os[1]) + (os[2] + os[3])) + ((os[4] + os[5]) + (os[6] + os[7]))) +
                           (((os[8] + os[9]) + (os[10] + os[11])) + ((os[12] + os[13]) + (os[14] + os[15])));
        const bool bad = !(fmaxf(lA, lB) < 1.0e24f) || !(lA > 1.0e-24f) || !(lB > 1.0e-24f) || !(fabsf(osum) < 3.0e38f);
        if (__any(bad)) {
            const __amdgpu_buffer_rsrc_t rk = __builtin_amdgcn_make_buffer_rsrc((char*)kvf + (size_t)head * 262144, 0, 262144, 0x00020000);
            oA = z16; oB = z16; lA = 0.f; lB = 0.f; ob = 0.f;
            float mA = -INFINITY, mB = -INFINITY;
#pragma unroll 1
            for (int t = 0; t < 64; ++t) {
                bf16x8 f[4];
#pragma unroll
                for (int i = 0; i < 4; ++i) f[i] = __builtin_bit_cast(bf16x8, __builtin_amdgcn_raw_buffer_load_b128(rk, lane * 16 + i * 1024, t * 4096, 16));
                auto upd = [&](bf16x8 q0, bf16x8 q1, f32x16& o, float& l, float& m) {
                    f32x16 s = MFMA32(f[0], q0, z16); s = MFMA32(f[1], q1, s);
                    float tm = s[0];
#pragma unroll
                    for (int r = 1; r < 16; ++r) tm = fmaxf(tm, s[r]);
                    tm = max_h(tm);
                    const float mn = fmaxf(m, tm);
                    const float alpha = __builtin_amdgcn_exp2f(m - mn);
                    m = mn;
#pragma unroll
                    for (int r = 0; r < 16; ++r) { s[r] = __builtin_amdgcn_exp2f(s[r] - mn); o[r] *= alpha; }
                    const u32x4 p0 = {pack_bf16(s[0], s[1]), pack_bf16(s[2], s[3]), pack_bf16(s[4], s[5]), pack_bf16(s[6], s[7])};
                    const u32x4 p1 = {pack_bf16(s[8], s[9]), pack_bf16(s[10], s[11]), pack_bf16(s[12], s[13]), pack_bf16(s[14], s[15])};
                    f32x4 t4 = {0, 0, 0, 0};
                    o = MFMA32(f[2], __builtin_bit_cast(bf16x8, p0), o);
                    o = MFMA32(f[3], __builtin_bit_cast(bf16x8, p1), o);
                    t4 = ROWSUM(p0, p1, t4);
                    l = l * alpha + (t4[0] + t4[1]);
                };
                upd(qA0, qA1, oA, lA, mA);
                upd(qB0, qB1, oB, lB, mB);
            }
            store();
        }
    }
}

DI void ln_row(const u32x4 (&v)[2], __hip_bfloat16* __restrict__ Y, unsigned off) {
    float s1 = 0.f, s2 = 0.f;
#pragma unroll
    for (int e = 0; e < 8; ++e) s1 = dot2bf(v[e >> 2][e & 3], 0x3f803f80u, s1);
    const float mean = wave_sum(s1) * (1.f / 1024.f);
#pragma unroll
    for (int e = 0; e < 8; ++e) {
        const float d0 = bf_lo(v[e >> 2][e & 3]) - mean, d1 = bf_hi(v[e >> 2][e & 3]) - mean;
        s2 += d0 * d0 + d1 * d1;
    }
    const float mb = round_bf16(mean);
    const float rden = __builtin_amdgcn_rcpf(round_bf16(__builtin_amdgcn_sqrtf(round_bf16(round_bf16(wave_sum(s2) * (1.f / 1024.f)) + 1e-5f))));
#pragma unroll
    for (int hh = 0; hh < 2; ++hh) {
        u32x4 y;
#pragma unroll
        for (int e = 0; e < 4; ++e) {
            const unsigned d = pack_bf16(bf_lo(v[hh][e]) - mb, bf_hi(v[hh][e]) - mb);
            y[e] = pack_bf16(bf_lo(d) * rden, bf_hi(d) * rden);
        }
        st16((char*)Y + hh * 131072, off, y);
    }
}

extern "C" __global__ __launch_bounds__(512, 2) void mega_kernel(const __hip_bfloat16* x, const __hip_bfloat16* wqkvA,
                                                            const __hip_bfloat16* woA, const __hip_bfloat16* wqkvB,
                                                            const __hip_bfloat16* woB, __hip_bfloat16* out,
                                                            char* ws) {
    char* const smem = smem_base;
    typedef __hip_bfloat16 bf;
    const int w8 = __builtin_amdgcn_readfirstlane(threadIdx.x >> 6);
    if (w8 == 7 && (blockIdx.x >> 3) < 10) code_touch(blockIdx.x >> 3, threadIdx.x & 63);
    asm volatile("" :: "s"(x), "s"(wqkvA), "s"(woA), "s"(wqkvB), "s"(woB), "s"(out), "s"(ws));
    STAMP_DECL; STAMPR(14); STAMP(0);
    u64 tag;
    {
        const int lane = threadIdx.x & 63;
        const Ids I = ids();
        const unsigned off = (unsigned)((I.po * 128 + I.co * 8 + w8) * 2048 + lane * 16);
        u32x4 v[2];
        asm volatile("global_load_dwordx4 %0, %2, %3\n\tglobal_load_dwordx4 %1, %2, %3 offset:1024" : "=&v"(v[0]), "=&v"(v[1]) : "v"(off), "s"(x) : "memory");
        asm volatile("s_load_dwordx2 %0, %1, 0x0" : "=s"(tag) : "s"(ws + WS_EPOCH) : "memory");
        if (w8 == 7) {
            const int l = lane & 15, k = l % 5;
            const unsigned o = (l >= 10 || k == 3) ? flag_off(l, I.cq, I.pq, 0) : (k == 0 || k == 2) ? flag_off(l, I.pq, I.cq, 0) : flag_off(l, I.po, I.co, 0);
            touch_at(ws + WS_FLAG, o);
        } else if (w8 >= 4 && w8 - 4 < NTOUCH) touch(wqkvA, (w8 - 4) * 256 + blockIdx.x, lane);
        if (w8 == 7 || (w8 >= 4 && w8 - 4 < NTOUCH)) asm volatile("s_waitcnt vmcnt(1)" ::: "memory"); else asm volatile("s_waitcnt vmcnt(0)" ::: "memory");
        asm volatile("" : "+v"(v[0]), "+v"(v[1]));
        ln_row(v, (bf*)(ws + WS_Y0), (unsigned)(I.po * 262144 + (lane >> 3) * 16384 + (I.co * 8 + w8) * 128 + (lane & 7) * 16));
        asm volatile("s_waitcnt lgkmcnt(0)" : "+s"(tag) :: "memory");
        tag += 1;
    }
    if (w8 >= 4) {
        int w = w8 & 3;
#pragma unroll 1
        for (int it = 0; it < 2; ++it) {
            const int lane = lane_id();
            asm volatile("" : "+s"(x), "+s"(wqkvA), "+s"(woA), "+s"(wqkvB), "+s"(woB), "+s"(ws), "+s"(w));
            const unsigned lds0 = (unsigned)(uintptr_t)smem;
            u32x4 cb[8];
            const unsigned wv = wo_off(w, lane), rv = res_off(true, it, lane), cv = colsum_off(w, lane);
            qkv_loader<true>(smem, w, ws, it * 5, tag, (const bf*)(ws + (it ? WS_X2 : WS_Y0)), wqkvA, (const float*)(ws + WS_ST + 262144),
                             ws + WS_CSP, it == 0, woA, [&] {
                if (w < 3) {
                    colsum_load(cb, wqkvB, cv);
                    asm volatile("s_waitcnt vmcnt(8)" ::: "memory");
                } else asm volatile("s_waitcnt vmcnt(0)" ::: "memory");
            }, [&](int part) {
                if (part == 0) wo_load(woA, wv, 32 - WO0, 32, lds0, w); else wo_load(woA, wv, 32 - WO1, 32 - WO0, lds0, w);
            } SP_ARG);
            STAMP(3);
            u32x4 res[6];
            res_load(res, it ? (const void*)(ws + WS_X2) : x, true, it, rv, w);
            if (it) __builtin_amdgcn_s_sleep(WSL1); else __builtin_amdgcn_s_sleep(WSL0);
            wo_load(woA, wv, 0, WO2, lds0, w);
            BAR();
            wo_load(woA, wv, WO2, 32 - WO1, lds0, w);
            STAMP(4);
            out_handoff(smem, 147456, res, w, ws, it * 5 + 1, tag, lane, cb, it ? -1 : 1);
            STAMP(5);
            qkv_loader<false>(smem, w, ws, it * 5 + 2, tag, (const bf*)(ws + (it ? WS_X3 : WS_X1)), wqkvB, (const float*)(ws + WS_ST + it * 524288), ws + WS_CSP + 196608,
                              false, nullptr, [] {}, [](int) {} SP_ARG);
            STAMP(8);
            attn_loader(smem, w, ws, it * 5 + 3, tag, (const bf*)(ws + WS_KVF + it * 8 * MBy), woB, ws + (it ? WS_X3 : WS_X1),
                        it ? nullptr : wqkvA SP_ARG);
            STAMP(13); STAMPR(15); STAMP_FLUSH(it);
        }
        if (blockIdx.x == 0 && w == 0 && lane_id() == 0) __hip_atomic_store((g_u64ptr)(ws + WS_EPOCH), tag, __ATOMIC_RELAXED, __HIP_MEMORY_SCOPE_AGENT);
    } else {
        __builtin_amdgcn_s_setprio(2);
#pragma unroll 1
        for (int it = 0; it < 2; ++it) {
            u32x4 q4[4];
            asm volatile("" : "+s"(ws), "+s"(wqkvA), "+s"(wqkvB), "+s"(woA));
            qkv_compute<true>(smem, w8, wqkvA, q4, nullptr, (bf*)(ws + WS_OF + it * 8 * MBy), it == 0,
                              w8 == 3 ? (const char*)woA : (const char*)wqkvB + w8 * 2097152 SP_ARG);
            STAMP(2);
            out_compute<true>(smem, w8, (const bf*)(ws + WS_OF + it * 8 * MBy),
                              (bf*)(ws + (it ? WS_X3 : WS_X1)), (float*)(ws + WS_ST + it * 524288) SP_ARG);
            STAMP(6);
            qkv_compute<false>(smem, w8, wqkvB, q4, (bf*)(ws + WS_KVF + it * 8 * MBy), nullptr, false, nullptr SP_ARG);
            attn_compute(smem, w8, q4, (const bf*)(ws + WS_KVF + it * 8 * MBy), (bf*)(ws + WS_OF + (it * 8 + 4) * MBy) SP_ARG);
            out_compute<false>(smem, w8, (const bf*)(ws + WS_OF + (it * 8 + 4) * MBy),
                               it ? out : (bf*)(ws + WS_X2), it ? nullptr : (float*)(ws + WS_ST + 262144) SP_ARG);
            STAMP(13); STAMPR(15); STAMP_FLUSH(it);
        }
    }
}

extern "C" __global__ void zz_code_end_kernel() {
    asm volatile(".subsection 2\n\t.fill 256, 4, 0xBF800000\n\t.subsection 0");
}

extern "C" void kernel_launch(void* const* d_in, const int* in_sizes, int n_in, void* d_out, int out_size, void* d_ws,
                              size_t ws_size, hipStream_t stream) {
    typedef __hip_bfloat16 bf;
    (void)hipFuncSetAttribute((const void*)mega_kernel, hipFuncAttributeMaxDynamicSharedMemorySize, CH_LDS);
    mega_kernel<<<256, 512, CH_LDS, stream>>>((const bf*)d_in[0], (const bf*)d_in[1], (const bf*)d_in[2], (const bf*)d_in[4], (const bf*)d_in[5],
                                              (bf*)d_out, (char*)d_ws);
}
```

```cpp
#include <hip/hip_runtime.h>
#include <hip/hip_bf16.h>
#include <stdint.h>

typedef __attribute__((ext_vector_type(8))) short bf16x8;
typedef __attribute__((ext_vector_type(4))) short s16x4;
typedef __attribute__((ext_vector_type(4))) float f32x4;
typedef __attribute__((ext_vector_type(2))) float f32x2;
typedef __attribute__((ext_vector_type(16))) float f32x16;
typedef __attribute__((ext_vector_type(4))) unsigned u32x4;
typedef __attribute__((ext_vector_type(2))) unsigned u32x2;
typedef __attribute__((ext_vector_type(2))) __bf16 bf16x2_t;
typedef __attribute__((ext_vector_type(8))) __bf16 bf8_t;
typedef __attribute__((ext_vector_type(16))) __bf16 bf16v_t;
typedef __attribute__((address_space(3))) const char* lds_cptr;
typedef __attribute__((address_space(3))) char* lds_ptr;

#define DI __device__ __forceinline__
#define MFMA16(a, b, c) __builtin_amdgcn_mfma_f32_16x16x32_bf16((a), (b), (c), 0, 0, 0)
#define MFMA32(a, b, c) __builtin_amdgcn_mfma_f32_32x32x16_bf16((a), (b), (c), 0, 0, 0)
#define SBAR() __builtin_amdgcn_sched_barrier(0)
#define STAMP(i)
#define STAMPR(i)
#define STAMP_DECL
#define STAMP_FLUSH(it)
#define SP_PARAM
#define SP_ARG
typedef unsigned long long u64;
typedef __attribute__((ext_vector_type(32))) unsigned u32x32;

constexpr float QSCALE = 0.17677669529663687f * 1.4426950408889634f;
constexpr float ATT_SCALE = 0.17677669529663687f;
constexpr float LOG2E = 1.4426950408889634f;
constexpr float NEG_MASK = -1e9f / ATT_SCALE;

DI int lane_id() { int l; asm volatile("v_mbcnt_lo_u32_b32 %0, -1, 0\n\tv_mbcnt_hi_u32_b32 %0, -1, %0" : "=v"(l)); return l; }
DI unsigned pack_bf16(float lo, float hi) {
    f32x2 v = {lo, hi};
    bf16x2_t r = __builtin_convertvector(v, bf16x2_t);
    return __builtin_bit_cast(unsigned, r);
}
DI float bf_lo(unsigned u) { return __uint_as_float(u << 16); }
DI float bf_hi(unsigned u) { return __uint_as_float(u & 0xffff0000u); }
DI float round_bf16(float x) { return bf_lo(pack_bf16(x, 0.f)); }
DI void ln_stats(float sum, float sumsq, float& mean_out, float& rstd_out) {
    float mean;
    asm("v_mul_f32 %0, 0x3a800000, %1" : "=v"(mean) : "v"(sum));
    const float var = fmaxf(__builtin_fmaf(-mean, mean, sumsq * (1.f / 1024.f)), 0.f);
    mean_out = round_bf16(mean);
    rstd_out = __builtin_amdgcn_rcpf(round_bf16(__builtin_amdgcn_sqrtf(round_bf16(round_bf16(var) + 1e-5f))));
}

DI void glds16(const void* sbase, unsigned voff, unsigned ldsaddr) {
    asm volatile("s_mov_b32 m0, %0\n\ts_nop 0\n\tglobal_load_lds_dwordx4 %1, %2"
                 :: "s"(ldsaddr), "v"(voff), "s"(sbase) : "memory");
}
DI void glds16c(const void* sbase, unsigned voff, unsigned ldsaddr) {
    asm volatile("s_mov_b32 m0, %0\n\ts_nop 0\n\tglobal_load_lds_dwordx4 %1, %2 sc1"
                 :: "s"(ldsaddr), "v"(voff), "s"(sbase) : "memory");
}
DI void st16(void* sbase, unsigned voff, u32x4 v) {
    asm volatile("global_store_dwordx4 %0, %1, %2 sc1\n\ts_nop 1" :: "v"(voff), "v"(v), "s"(sbase) : "memory");
}
template <int OFF> DI void st16o(void* sbase, unsigned voff, u32x4 v) {
    asm volatile("global_store_dwordx4 %0, %1, %2 offset:%3 sc1\n\ts_nop 1" :: "v"(voff), "v"(v), "s"(sbase), "n"(OFF) : "memory");
}
DI bf16x8 lds_b128(lds_cptr p) { return *(const __attribute__((address_space(3))) bf16x8*)p; }
DI s16x4 lds_tr(lds_cptr p) {
    return __builtin_amdgcn_ds_read_tr16_b64_v4i16((__attribute__((address_space(3))) s16x4*)p);
}
DI bf16x8 cat8(s16x4 a, s16x4 b) { return __builtin_shufflevector(a, b, 0, 1, 2, 3, 4, 5, 6, 7); }
DI float dot2bf(unsigned a, unsigned b, float c) {
    return __builtin_amdgcn_fdot2_f32_bf16(__builtin_bit_cast(bf16x2_t, a), __builtin_bit_cast(bf16x2_t, b), c, false);
}
DI float sum_g(float v) {
    auto a = __builtin_amdgcn_permlane16_swap(__float_as_uint(v), __float_as_uint(v), false, false);
    v = __uint_as_float(a[0]) + __uint_as_float(a[1]);
    auto b = __builtin_amdgcn_permlane32_swap(__float_as_uint(v), __float_as_uint(v), false, false);
    return __uint_as_float(b[0]) + __uint_as_float(b[1]);
}
DI float wave_sum(float v) {
    v += __builtin_bit_cast(float, __builtin_amdgcn_update_dpp(0, __builtin_bit_cast(int, v), 0xB1, 0xF, 0xF, true));
    v += __builtin_bit_cast(float, __builtin_amdgcn_update_dpp(0, __builtin_bit_cast(int, v), 0x4E, 0xF, 0xF, true));
    v += __builtin_bit_cast(float, __builtin_amdgcn_update_dpp(0, __builtin_bit_cast(int, v), 0x141, 0xF, 0xF, true));
    v += __builtin_bit_cast(float, __builtin_amdgcn_update_dpp(0, __builtin_bit_cast(int, v), 0x140, 0xF, 0xF, true));
    return sum_g(v);
}
DI unsigned wave_min(unsigned v) {
    v = min(v, (unsigned)__builtin_amdgcn_update_dpp(0, (int)v, 0xB1, 0xF, 0xF, true));
    v = min(v, (unsigned)__builtin_amdgcn_update_dpp(0, (int)v, 0x4E, 0xF, 0xF, true));
    v = min(v, (unsigned)__builtin_amdgcn_update_dpp(0, (int)v, 0x141, 0xF, 0xF, true));
    v = min(v, (unsigned)__builtin_amdgcn_update_dpp(0, (int)v, 0x140, 0xF, 0xF, true));
    const auto a = __builtin_amdgcn_permlane16_swap(v, v, false, false);
    v = min(a[0], a[1]);
    const auto b = __builtin_amdgcn_permlane32_swap(v, v, false, false);
    return min(b[0], b[1]);
}
DI float amax3(float a, float b, float c) { float r; asm("v_max3_f32 %0, %1, %2, %3" : "=v"(r) : "v"(a), "v"(b), "v"(c)); return r; }
DI float max2f(float a, float b) { return __builtin_amdgcn_fmed3f(a, b, INFINITY); }
DI float max_h(float v) {
    auto b = __builtin_amdgcn_permlane32_swap(__float_as_uint(v), __float_as_uint(v), false, false);
    return max2f(__uint_as_float(b[0]), __uint_as_float(b[1]));
}

constexpr unsigned OFF_DUMP = 145408;
extern __shared__ __attribute__((aligned(16))) char smem_base[];
DI void touch_at(const void* p, unsigned off) {
    asm volatile("s_mov_b32 m0, %0\n\ts_nop 0\n\tglobal_load_lds_dword %1, %2"
                 :: "s"((unsigned)(uintptr_t)smem_base + OFF_DUMP), "v"(off), "s"(p) : "memory");
}
DI void touch(const void* p, int slice, int lane) { touch_at(p, (unsigned)(slice * 64 + lane) * 128u); }
DI void code_touch(int slice, int lane) {
    unsigned long long c0, c1;
    asm volatile("s_getpc_b64 s[96:97]\n\ts_add_u32 s96, s96, mega_kernel@rel32@lo+4\n\ts_addc_u32 s97, s97, mega_kernel@rel32@hi+12\n\ts_mov_b64 %0, s[96:97]\n\t"
                 "s_getpc_b64 s[96:97]\n\ts_add_u32 s96, s96, zz_code_end_kernel@rel32@lo+4\n\ts_addc_u32 s97, s97, zz_code_end_kernel@rel32@hi+12\n\ts_mov_b64 %1, s[96:97]"
                 : "=s"(c0), "=s"(c1) :: "s96", "s97", "scc");
    touch_at((const void*)c0, min((unsigned)(slice * 64 + lane) * 128u, (unsigned)(c1 - c0)));
}

#define BAR() asm volatile("s_barrier" ::: "memory")
#define WAIT_LGKM() do { SBAR(); __builtin_amdgcn_s_waitcnt(0xC07F); SBAR(); } while (0)
#define WAIT_LGKM_BAR() do { SBAR(); __builtin_amdgcn_s_waitcnt(0xC07F); asm volatile("s_barrier" ::: "memory"); SBAR(); } while (0)
#define VM_BAR(n) asm volatile("s_waitcnt vmcnt(" #n ")\n\ts_barrier" ::: "memory")
constexpr int CH_LDS = 163840;
typedef __attribute__((address_space(3))) unsigned* lds_uptr;
typedef __attribute__((address_space(1))) u64* g_u64ptr;

constexpr size_t MBy = 1 << 20;
constexpr size_t WS_X3 = 0, WS_X2 = 4 * MBy, WS_X1 = 8 * MBy, WS_OF = 12 * MBy  , WS_KVF = 36 * MBy  ,
                 WS_ST = 52 * MBy  , WS_CSP = 54 * MBy  , WS_FLAG = 55 * MBy  ,
                 WS_WN = WS_CSP + 458752  , WS_EPOCH = WS_CSP + 524288, WS_Y0 = 56 * MBy;
DI unsigned flag_off(int set, int group, int consumer, int producer) { return (unsigned)(set * 65536 + group * 4096 + consumer * 256 + producer * 8); }
DI void flag_store(char* ws, int set, int group, int producer, u64 tag, int lane) {
    asm volatile("" : "+s"(tag));
    if (lane < 16) __hip_atomic_store((g_u64ptr)(ws + WS_FLAG + flag_off(set, group, lane, producer)), tag, __ATOMIC_RELAXED, __HIP_MEMORY_SCOPE_AGENT);
}
DI void flag_poll(const char* ws, unsigned off, u64 tag) {
    asm volatile("" : "+s"(tag));
#pragma unroll 1
    for (int spin = 0; spin < (1 << 16); ++spin) {
        const u64 v = __hip_atomic_load((g_u64ptr)(ws + WS_FLAG + off), __ATOMIC_RELAXED, __HIP_MEMORY_SCOPE_AGENT);
        if (__all(v == tag)) break;
        __builtin_amdgcn_s_sleep(1);
    }
    asm volatile("" ::: "memory");
}
template <bool DRAIN, bool ALLPOLL>
DI void signal_poll(int follower, const char* ws, unsigned soff, unsigned poff, u64 pmask, u64 tag, u64& land) {
    u64 tagv, zero;
    asm volatile("s_not_b64 s[96:97], %[ts]\n\tv_mov_b64 %[tag], %[ts]\n\tv_mov_b64 %[zero], 0\n\tv_mov_b64 %[land], s[96:97]\n\t"
                 ".if %[dr]\n\ts_waitcnt vmcnt(0)\n\t.endif\n\ts_barrier\n\t"
                 "s_cmp_lg_u32 %[fl], 0\n\ts_cbranch_scc1 .Lsig_fol_%=\n\t"
                 "s_mov_b64 exec, 0xffff\n\t"
                 "global_store_dwordx2 %[so], %[tag], %[base] sc1\n"
                 ".if %[ap]\n.Lsig_fol_%=:\n.endif\n\t"
                 "s_mov_b64 exec, %[pm]\n\t"
                 "s_movk_i32 s96, 0x7fff\n"
                 ".Lsig_loop_%=:\n\t"
                 "global_atomic_or_x2 %[land], %[po], %[zero], %[base] sc0 sc1\n\t"
                 "s_sleep 1\n\t"
                 "s_waitcnt vmcnt(3)\n\t"
                 "v_cmp_ne_u64 vcc, %[land], %[tag]\n\t"
                 "s_sub_u32 s96, s96, 1\n\t"
                 "s_cbranch_scc1 .Lsig_out_%=\n\t"
                 "s_cbranch_vccnz .Lsig_loop_%=\n"
                 ".Lsig_out_%=:\n\t"
                 "s_mov_b64 exec, -1\n"
                 ".if !%[ap]\n.Lsig_fol_%=:\n.endif"
                 : [land] "=&v"(land), [tag] "=&v"(tagv), [zero] "=&v"(zero)
                 : [fl] "s"(follower), [so] "v"(soff), [po] "v"(poff), [ts] "s"(tag), [base] "s"(ws + WS_FLAG), [pm] "s"(pmask), [ap] "n"(ALLPOLL ? 1 : 0), [dr] "n"(DRAIN ? 1 : 0)
                 : "memory", "scc", "vcc", "s96", "s97");
}
struct Ids { int pq, cq, po, co; };
DI Ids ids() {
    int bid = blockIdx.x; asm volatile("" : "+s"(bid));
    const int xcd = bid & 7, idx = bid >> 3;
    int pq, cq, po, co;
#define IDS_BODY pq = (xcd & 1) * 8 + (idx & 7); cq = (xcd >> 1) * 4 + (idx >> 3); po = (xcd & 3) * 4 + (idx & 3); co = (xcd >> 2) * 8 + (idx >> 2);
    IDS_BODY
    return Ids{pq, cq, po, co};
}

DI void slow_warm(unsigned dump, const void* W, int w, int lane) {
    int bid = blockIdx.x; asm volatile("" : "+s"(bid));
    const int xcd = bid & 7, idx = bid >> 3;
    const int sc = (3 - (int)((uintptr_t)W >> 7)) & 7;
    if ((sc >> 2) != ((xcd >> 1) & 1)) return;
    const char* src = (const char*)W + (idx + 32 * w) * 49152 + (sc + (xcd >> 2) * 8) * 128;
#pragma unroll
    for (int G = 0; G < 3; ++G) glds16(src + G * 2048, (unsigned)((lane >> 3) * 6144 + (lane & 7) * 16), dump);
}

template <int V> struct IC { static constexpr int value = V; };
template <bool LOCAL> struct QC {
    static constexpr int AROWS = LOCAL ? 144 : 128;
    static constexpr int ABYTES = AROWS * 128;
    static constexpr int STAGE = ABYTES + 24576;
    static constexpr int OFF_MEAN = 143360;
    static constexpr int OFF_RSTD = OFF_MEAN + 576;
    static constexpr int OFF_CS = OFF_RSTD + 576;
};
constexpr int OFF_EX = 131072;

DI unsigned colsum_off(int G, int lane) {
    const Ids I = ids();
    unsigned off = (unsigned)(I.pq * 64 + (lane >> 3)) * 6144u + (unsigned)(G * 2048 + I.cq * 128 + (lane & 7) * 16);
    asm volatile("" : "+v"(off));
    return off;
}
DI void colsum_load(u32x4 (&c)[8], const void* W, unsigned off) {
#define CS_NEXT "s_add_u32 s96, s96, 0xc000\n\ts_addc_u32 s97, s97, 0\n\t"
    asm volatile("s_cmp_eq_u64 %9, 0\n\ts_cbranch_scc1 .Lcs_skip_%=\n\ts_mov_b64 s[96:97], %9\n\t"
                 "global_load_dwordx4 %0, %8, s[96:97]\n\t" CS_NEXT "global_load_dwordx4 %1, %8, s[96:97]\n\t" CS_NEXT
                 "global_load_dwordx4 %2, %8, s[96:97]\n\t" CS_NEXT "global_load_dwordx4 %3, %8, s[96:97]\n\t" CS_NEXT
                 "global_load_dwordx4 %4, %8, s[96:97]\n\t" CS_NEXT "global_load_dwordx4 %5, %8, s[96:97]\n\t" CS_NEXT
                 "global_load_dwordx4 %6, %8, s[96:97]\n\t" CS_NEXT "global_load_dwordx4 %7, %8, s[96:97]\n"
                 ".Lcs_skip_%=:"
                 : "=&v"(c[0]), "=&v"(c[1]), "=&v"(c[2]), "=&v"(c[3]), "=&v"(c[4]), "=&v"(c[5]), "=&v"(c[6]), "=&v"(c[7])
                 : "v"(off), "s"(W) : "memory", "scc", "s96", "s97");
}
DI void colsum_store(u32x4 (&c)[8], char* ws, int mat, int G, int lane) {
    asm volatile("" : "+v"(c[0]), "+v"(c[1]), "+v"(c[2]), "+v"(c[3]), "+v"(c[4]), "+v"(c[5]), "+v"(c[6]), "+v"(c[7]));
    const Ids I = ids();
    const int m = lane & 15;
    const unsigned one = (m & 1) ? 0xbf800000u : 0xbf80u;
    const u32x4 sl = {(m >> 1) == 0 ? one : 0u, (m >> 1) == 1 ? one : 0u, (m >> 1) == 2 ? one : 0u, (m >> 1) == 3 ? one : 0u};
    f32x4 d = {0, 0, 0, 0};
#pragma unroll
    for (int i = 0; i < 8; ++i) d = MFMA16(__builtin_bit_cast(bf16x8, sl), __builtin_bit_cast(bf16x8, c[i]), d);
#pragma unroll
    for (int r = 0; r < 4; ++r)
        d[r] += __int_as_float(__builtin_amdgcn_update_dpp(0, __float_as_int(d[r]), 0x128, 0xF, 0xF, true));
    if (lane < 32 && (lane & 8) == 0)
        st16(ws + WS_CSP + mat * 196608, (unsigned)((I.cq * 16 + I.pq) * 768 + G * 256 + (lane & 7) * 32 + (lane >> 4) * 16), __builtin_bit_cast(u32x4, d));
    typedef __attribute__((ext_vector_type(2))) unsigned short u16x2;
    u16x2 mn = {0x7fff, 0x7fff};
#pragma unroll
    for (int e = 0; e < 4; ++e) {
        u16x2 mx = {0, 0};
#pragma unroll
        for (int i = 0; i < 8; ++i) { const unsigned u = c[i][e] & 0x7fff7fffu; mx = __builtin_elementwise_max(mx, __builtin_bit_cast(u16x2, u)); }
        mn = __builtin_elementwise_min(mn, mx);
    }
    const unsigned wn = wave_min((unsigned)min(mn[0], mn[1])) << 16;
    if (lane == 0) __hip_atomic_store((__attribute__((address_space(1))) unsigned*)(ws + WS_WN + mat * 4096 + blockIdx.x * 16 + G * 4), wn,
                                      __ATOMIC_RELAXED, __HIP_MEMORY_SCOPE_AGENT);

}

template <bool LOCAL>
struct WFirst {
    unsigned v[2];
    DI WFirst(int w, int ct, int lane) {
#pragma unroll
        for (int i = 0; i < 2; ++i) {
            const int k = 8 * i + (lane >> 3), pos = lane & 7;
            v[i] = (unsigned)k * 6144u + (unsigned)(w * 1024 + ct * 64 + ((pos >> 1) ^ (((k >> 1) & 1) | (i << 1))) * 16 + (pos & 1) * 8) * 2u;
        }
        asm volatile("" : "+v"(v[0]), "+v"(v[1]));
    }
    DI void issue(const void* W, unsigned lds0, int w, int i0, int i1) const {
#pragma unroll
        for (int i = i0; i < i1; ++i)
            glds16((const char*)W + (size_t)(i >> 1) * (16 * 6144), v[i & 1], lds0 + (i >> 3) * QC<LOCAL>::STAGE + QC<LOCAL>::ABYTES + (w * 8 + (i & 7)) * 1024);
    }
};

template <bool LOCAL, class F, class F2>
DI void qkv_loader(char* smem, int w, char* ws, int set, u64 tag, const __hip_bfloat16* __restrict__ X, const __hip_bfloat16* __restrict__ W,
                   const float* __restrict__ statp, const char* __restrict__ csp, bool ident, const void* nextW, F tail, F2 mid SP_PARAM) {
    typedef QC<LOCAL> C;
    constexpr int NT = 16;
    const int lane = lane_id();
    asm volatile("" : "+s"(w));
    const int idx = blockIdx.x >> 3;
    const Ids I = ids();
    const int panel = I.pq, ct = I.cq;
    const int R0 = panel * 128;
    const unsigned lds0 = (unsigned)(uintptr_t)smem;
    auto grow = [&](int i) -> int {
        if (!LOCAL) return R0 + i;
        if (i < 128) { const int t = (i >> 4) & 3, a = i & 15; return R0 + (i & 64) + 2 * (16 * (t & 1) + a) + (t >> 1); }
        const int a = i & 15; const int r = R0 - 16 + 2 * (a & 7) + (a >> 3);
        return r < 0 ? 0 : r;
    };
    unsigned voffA[5], voffB[6];
#pragma unroll
    for (int i = 0; i < 6; ++i) {
        const int j = w + 4 * i, G = j >> 3, k = 8 * (j & 7) + (lane >> 3);
        const int pos = lane & 7, f = ((k >> 1) & 1) | (((k >> 3) & 1) << 1);
        const int seg = (pos >> 1) ^ f;
        voffB[i] = (unsigned)k * 6144u + (unsigned)(G * 1024 + ct * 64 + seg * 16 + (pos & 1) * 8) * 2u;
    }
    auto issueW = [&](int t, int buf) {
        const char* wb = (const char*)W + (size_t)t * (64 * 6144);
        const unsigned sb = lds0 + buf * C::STAGE;
#pragma unroll
        for (int i = 0; i < 6; ++i) glds16(wb, voffB[i], sb + C::ABYTES + (w + 4 * i) * 1024);
    };
    asm volatile("" : "+v"(voffB[0]), "+v"(voffB[1]), "+v"(voffB[2]), "+v"(voffB[3]), "+v"(voffB[4]), "+v"(voffB[5]));
    const WFirst<LOCAL> first(w, ct, lane);
    const int g = lane >> 4, r = lane & 15, pos = 32 * w + 2 * r + (g >> 1);
    const int lr = !LOCAL ? (pos & 64) + 2 * (pos & 31) + ((pos >> 5) & 1)
                          : (w == 0 && (g & 1)) ? 128 + (r & 7) + 8 * (g >> 1) : (pos & 64) + ((pos & 1) * 2 + ((pos >> 5) & 1)) * 16 + ((pos >> 1) & 15);
    unsigned so = (unsigned)(panel * 16384 + g * 1024 + w * 256 + r * 16), ho = (unsigned)(g * 1024 + (panel ? panel * 16384 - 15488 + (r & 7) * 16 : 0));
    unsigned co = (unsigned)((ct * 16 + g) * 768 + w * 192 + r * 12);
    lds_ptr mp = (lds_ptr)smem + C::OFF_MEAN + lr * 4, cp = (lds_ptr)smem + C::OFF_CS + w * 192 + r * 12 + ((g & 1) ? 8 : g * 2);
    unsigned fo = flag_off((LOCAL ? 13 : 10) + min(g, 2), ct, panel, r);
    asm volatile("" : "+v"(so), "+v"(ho), "+v"(co), "+v"(mp), "+v"(cp), "+v"(fo));
#pragma unroll
    for (int i = 0; i < 5; ++i) {
        const int j = (i < 4) ? (w + 4 * i) : (16 + (w & 1));
        const int row = 8 * j + (lane >> 3);
        const int c = (lane & 7) ^ ((row >> 1) & 7);
        const int gr = grow(row < C::AROWS ? row : 0);
        voffA[i] = (unsigned)((gr >> 7) * 262144 + (gr & 127) * 128 + c * 16);
        asm volatile("" : "+v"(voffA[i]));
    }
    auto issueA = [&](int t, int buf) {
        const char* xa = (const char*)X + t * 16384;
        const unsigned sb = lds0 + buf * C::STAGE;
        if (LOCAL && w < 2) glds16c(xa, voffA[4], sb + (16 + w) * 1024);
#pragma unroll
        for (int i = 0; i < 4; ++i) glds16c(xa, voffA[i], sb + (w + 4 * i) * 1024);
    };
    u64 land;
    if (w == 3) __builtin_amdgcn_s_setprio(3);
    signal_poll<LOCAL, false>(w - 3, ws, flag_off(set, I.po, lane & 15, I.co),
                              flag_off(set, (lane & 16) && panel ? panel - 1 : panel, ct, lane & 15),
                              !LOCAL ? 0xffffull : ident ? 0xc000ffffull : 0xffffffffull, tag, land);
    if (w < 3) first.issue(W, lds0, w, 0, 12);
    BAR();
    STAMP(LOCAL ? 1 : 6);
    issueA(0, 0);
    issueA(1, 1);
    if (w < 3) { first.issue(W, lds0, w, 12, 16); asm volatile("s_waitcnt vmcnt(8)" ::: "memory"); }
    else asm volatile("s_waitcnt vmcnt(4)" ::: "memory");
    BAR();
    asm volatile("" :: "v"(land));
    __builtin_amdgcn_s_setprio(0);
    STAMP(LOCAL ? 2 : 7);
    issueW(2, 2);
    issueA(2, 2);
    VM_BAR(10);
    asm volatile("global_load_dwordx2 %0, %1, %2 sc1" : "=&v"(land) : "v"(fo), "s"(ws + WS_FLAG) : "memory");
    issueA(3, 0); issueW(3, 0);
    int buf = 1;
#pragma unroll 1
    for (int t = 1; t < NT - 3; ++t) {
        VM_BAR(10);
        issueA(t + 3, buf); issueW(t + 3, buf);
        if (t == 10 && idx < 3) code_touch(idx * 4 + w, lane);
        if (LOCAL && t == 11)
            glds16(nextW, (unsigned)((idx + 32 * w) * 8 + (lane >> 3)) * 2048u +
                   (unsigned)(((I.co & 8) + ((3 - (int)((uintptr_t)nextW >> 7)) & 7)) * 128 + (lane & 7) * 16), lds0 + OFF_DUMP);
        buf = (buf == 2) ? 0 : buf + 1;
    }
    typedef __attribute__((ext_vector_type(3))) float f32x3;
    const bool halo = LOCAL && w == 0;
    asm volatile("" : "+v"(land));
    if (!ident) {
        if (!__all(land == tag)) flag_poll(ws, fo, tag);
        f32x3 cs[4];
        f32x4 sv[4], hv[4];
        typedef __attribute__((address_space(3))) float* lds_f;
        auto add = [](float a, float b) { float r; asm("v_add_f32 %0, %1, %2" : "=v"(r) : "v"(a), "v"(b)); return r; };
        auto fold32 = [](float a, float b) { asm("s_nop 1\n\tv_permlane32_swap_b32 %0, %1\n\tv_add_f32 %0, %0, %1" : "+v"(a), "+v"(b)); return a; };
        auto fold16 = [](float a, float b) { asm("s_nop 1\n\tv_permlane16_swap_b32 %0, %1\n\tv_add_f32 %0, %0, %1" : "+v"(a), "+v"(b)); return a; };
        auto sum4 = [&](auto (&v)[4], int e) { return add(add(v[0][e], v[1][e]), add(v[2][e], v[3][e])); };
#pragma unroll
        for (int i = 0; i < 4; ++i) asm volatile("global_load_dwordx3 %0, %1, %2 sc1" : "=&v"(cs[i]) : "v"(co), "s"(csp + i * 3072) : "memory");
        VM_BAR(14);
#pragma unroll
        for (int i = 0; i < 4; ++i) asm volatile("global_load_dwordx4 %0, %1, %2 sc1" : "=&v"(sv[i]) : "v"(so), "s"((const char*)statp + i * 4096) : "memory");
        if (LOCAL)
            asm volatile("s_cmp_lg_u32 %9, 0\n\ts_cbranch_scc1 .Lhv_skip_%=\n\t"
                         "global_load_dwordx4 %0, %4, %5 sc1\n\tglobal_load_dwordx4 %1, %4, %6 sc1\n\t"
                         "global_load_dwordx4 %2, %4, %7 sc1\n\tglobal_load_dwordx4 %3, %4, %8 sc1\n"
                         ".Lhv_skip_%=:" : "=&v"(hv[0]), "=&v"(hv[1]), "=&v"(hv[2]), "=&v"(hv[3])
                         : "v"(ho), "s"(statp), "s"(statp + 1024), "s"(statp + 2048), "s"(statp + 3072), "s"(w) : "memory", "scc");
        if (halo) asm volatile("s_waitcnt vmcnt(8)" ::: "memory"); else asm volatile("s_waitcnt vmcnt(4)" ::: "memory");
        asm volatile("" : "+v"(cs[0]), "+v"(cs[1]), "+v"(cs[2]), "+v"(cs[3]));
        const float c2 = sum4(cs, 2);
        *(lds_f)cp = fold16(fold32(sum4(cs, 0), sum4(cs, 1)), fold32(c2, c2));
        BAR();
        asm volatile("s_waitcnt vmcnt(0)" : "+v"(sv[0]), "+v"(sv[1]), "+v"(sv[2]), "+v"(sv[3]) :: "memory");
        mid(0);
        const float s1 = fold32(sum4(sv, 0), sum4(sv, 2)), s2 = fold32(sum4(sv, 1), sum4(sv, 3));
        float h1, h2;
        asm("v_mov_b32 %0, %2\n\tv_mov_b32 %1, %3" : "=&v"(h1), "=&v"(h2) : "v"(s1), "v"(s2));
        if (halo) {
            asm volatile("" : "+v"(hv[0]), "+v"(hv[1]), "+v"(hv[2]), "+v"(hv[3]));
            const float u0 = sum4(hv, 0), u1 = sum4(hv, 1), u2 = sum4(hv, 2), u3 = sum4(hv, 3);
            h1 = fold32(u0, panel ? u2 : u0); h2 = fold32(u1, panel ? u3 : u1);
        }
        float mean, rstd;
        ln_stats(fold16(s1, h1), fold16(s2, h2), mean, rstd);
        *(lds_f)mp = mean; *(lds_f)(mp + (C::OFF_RSTD - C::OFF_MEAN)) = rstd;
    } else { VM_BAR(10); tail(); BAR(); mid(0); }
    mid(1);
    WAIT_LGKM_BAR();
}

template <bool LOCAL>
DI void qkv_compute(char* smem, int w, const void* W, u32x4 (&qo4)[4], __hip_bfloat16* __restrict__ kvf, __hip_bfloat16* __restrict__ ofrag, bool ident,
                    const char* statp, const char* wnp SP_PARAM) {
    typedef QC<LOCAL> C;
    constexpr int NT = 16, ABYTES = C::ABYTES, STAGE = C::STAGE;
    const int lane = lane_id();
    asm volatile("" : "+s"(w));
    const int wm = w & 1, wn = w >> 1;
    const Ids I = ids();
    const int panel = I.pq, ct = I.cq;
    const int l15 = lane & 15, g = lane >> 4;
    const lds_cptr sm = (lds_cptr)smem;
    lds_ptr smw = (lds_ptr)smem;

    f32x4 acc[3][4][2];
    f32x4 acch[2];
    float cb;
    f32x4 bias;

    struct Frags { bf16x8 a[4]; bf16x8 h; s16x4 b[3][2][2]; };
    auto rd = [&](auto WM, Frags& F, lds_cptr pa, lds_cptr pb0, lds_cptr pb1, int n) {
        switch (n) {
            case 0: F.b[0][0][0] = lds_tr(pb0); break;
            case 1: F.b[0][0][1] = lds_tr(pb0 + 512); break;
            case 2: F.a[0] = lds_b128(pa); break;
            case 3: F.b[0][1][0] = lds_tr(pb1); break;
            case 4: F.b[0][1][1] = lds_tr(pb1 + 512); break;
            case 5: F.a[1] = lds_b128(pa + 2048); break;
            case 6: F.a[2] = lds_b128(pa + 4096); break;
            case 7: F.a[3] = lds_b128(pa + 6144); break;
            case 8: F.b[1][0][0] = lds_tr(pb0 + 8192); break;
            case 9: F.b[1][0][1] = lds_tr(pb0 + 8192 + 512); break;
            case 10: F.b[1][1][0] = lds_tr(pb1 + 8192); break;
            case 11: F.b[1][1][1] = lds_tr(pb1 + 8192 + 512); break;
            case 12: F.b[2][0][0] = lds_tr(pb0 + 16384); break;
            case 13: F.b[2][0][1] = lds_tr(pb0 + 16384 + 512); break;
            case 14: F.b[2][1][0] = lds_tr(pb1 + 16384); break;
            case 15: F.b[2][1][1] = lds_tr(pb1 + 16384 + 512); break;
            case 16: if (LOCAL) F.h = lds_b128(pa + 16384 - WM.value * 8192); break;
            default: break;
        }
    };
    auto half = [&](auto WM, const Frags& C, Frags& N, bool doread, lds_cptr pa, lds_cptr pb0, lds_cptr pb1, auto tail, int G0 = 0, int G1 = 3,
                    bool first = false) {
        auto mm = [&](f32x4& c, bf16x8 a, bf16x8 b, bool halo = false) {
            if (!first) asm volatile("v_mfma_f32_16x16x32_bf16 %0, %1, %2, %0" : "+v"(c) : "v"(a), "v"(b));
            else if (halo) asm volatile("v_mfma_f32_16x16x32_bf16 %0, %1, %2, 0" : "=&v"(c) : "v"(a), "v"(b));
            else { const f32x4 c0 = bias; asm volatile("v_mfma_f32_16x16x32_bf16 %0, %1, %2, %3" : "=&v"(c) : "v"(a), "v"(b), "v"(c0)); }
        };
#pragma unroll
        for (int G = G0; G < G1; ++G) {
            const bf16x8 b0 = cat8(C.b[G][0][0], C.b[G][0][1]), b1 = cat8(C.b[G][1][0], C.b[G][1][1]);
#pragma unroll
            for (int t = 0; t < 4; ++t) {
                const int n = (G - G0) * 8 + t * 2;
                if (G < 2) mm(acc[G][t][0], b0, C.a[t]); else mm(acc[G][t][0], C.a[t], b0);
                SBAR();
                if (doread) rd(WM, N, pa, pb0, pb1, n);
                tail(n);
                SBAR();
                if (G < 2) mm(acc[G][t][1], b1, C.a[t]); else mm(acc[G][t][1], C.a[t], b1);
                SBAR();
                if (doread) rd(WM, N, pa, pb0, pb1, n + 1);
                tail(n + 1);
                SBAR();
            }
        }
        if (LOCAL) {
            if (WM.value == 0) { mm(acch[0], cat8(C.b[1][0][0], C.b[1][0][1]), C.h, true); mm(acch[1], cat8(C.b[1][1][0], C.b[1][1][1]), C.h, true); }
            else               { mm(acch[0], C.h, cat8(C.b[2][0][0], C.b[2][0][1]), true); mm(acch[1], C.h, cat8(C.b[2][1][0], C.b[2][1][1]), true); }
            SBAR();
        }
    };

    Frags F0, F1;
    f32x4 cq[2], ck[2], mu4[5], rs4[5]; float cv[2], mu[5], rs[5];
    u32x4 qfr[4], kfr[4];
    u32x4 va[2][2];
    f32x4 mwin, mfirst;
    typedef __attribute__((address_space(3))) u32x4* lds_u4;
    const bool negpos = (wm == 0) && (panel == 0);
    VM_BAR(0);
    unsigned cm01 = 0, cm23 = 0;
    lds_cptr ka[2] = {sm, sm}, vo2[2] = {sm, sm};
    lds_u4 ex = (lds_u4)(smw + OFF_EX + wn * 6144) + lane;
    lds_cptr s1 = sm + C::OFF_MEAN + wm * 256 + l15 * 4, s4 = sm + C::OFF_MEAN + wm * 256 + g * 16;
    lds_cptr c1 = sm + C::OFF_CS + wn * 128 + l15 * 4, c4 = sm + C::OFF_CS + wn * 128 + g * 16;
    asm volatile("" : "+v"(s1), "+v"(s4), "+v"(c1), "+v"(c4));
    if (LOCAL) {
        unsigned keepv = negpos ? __float_as_uint(NEG_MASK) : 0u;
        asm volatile("" : "+s"(keepv));
        u32x4 mw, mf;
        unsigned prv[4];
#pragma unroll
        for (int r = 0; r < 4; ++r) {
            const int d = l15 - (4 * g + r);
            unsigned out = (unsigned)(((d << 28) >> 31));
            prv[r] = (unsigned)(d >> 31);
            asm volatile("" : "+v"(out), "+v"(prv[r]));
            mw[r] = out & 0xff800000u;
            mf[r] = (out & 0xff800000u) | (~out & keepv);
        }
        const lds_cptr e0 = sm + OFF_EX + wn * 6144;
        ka[0] = e0 + (wm ? lane : 256 + (lane ^ 8)) * 16;         ka[1] = e0 + (wm ? 64 + lane : 256 + lane) * 16;
        vo2[0] = e0 + (wm ? 128 + lane : 320 + (lane ^ 32)) * 16; vo2[1] = e0 + (wm ? 192 + lane : 320 + lane) * 16;
        cm01 = ~((prv[0] >> 16) | (prv[1] << 16)); cm23 = ~((prv[2] >> 16) | (prv[3] << 16));
        mwin = __builtin_bit_cast(f32x4, mw); mfirst = __builtin_bit_cast(f32x4, mf);
        asm volatile("" : "+v"(mwin), "+v"(mfirst), "+v"(cm01), "+v"(cm23), "+v"(ka[0]), "+v"(ka[1]), "+v"(vo2[0]), "+v"(vo2[1]), "+v"(ex));
    }
    typedef const __attribute__((address_space(3))) f32x4* lds_f4; typedef const __attribute__((address_space(3))) float* lds_f1;
    auto mainloop = [&](auto WM) {
        int ty = lane_id();
        const int l15 = ty & 15, g = ty >> 4;
        const int swA = (l15 >> 1) & 7;
        const int q4 = l15 >> 2, p4 = l15 & 3, fk = ((q4 >> 1) & 1) | ((g & 1) << 1);
        lds_cptr pA0 = sm + wm * 8192 + l15 * 128 + ((0 + g) ^ swA) * 16, pA1 = sm + wm * 8192 + l15 * 128 + ((4 + g) ^ swA) * 16;
        lds_cptr pB0 = sm + ABYTES + (8 * g + q4) * 128 + (((2 * wn + 0) ^ fk) * 32) + p4 * 8;
        lds_cptr pB1 = sm + ABYTES + (8 * g + q4) * 128 + (((2 * wn + 1) ^ fk) * 32) + p4 * 8;
        asm volatile("" : "+v"(pA0), "+v"(pA1), "+v"(pB0), "+v"(pB1));
        slow_warm((unsigned)(uintptr_t)smem + OFF_DUMP, W, w, ty);
        unsigned so = (unsigned)(panel * 16384 + wm * 512 + ty * 8);
        const char *p0 = ident ? (const char*)W : statp, *p1 = ident ? (const char*)W : wnp;
        u32x4 wn;
        asm volatile("global_load_dwordx4 %0, %2, %3 sc1" : "=&v"(wn), "+v"(so) : "v"(0), "s"(p1) : "memory");
        BAR();
        STAMP(LOCAL ? 1 : 7);
        f32x2 sq;
        asm volatile("global_load_dwordx2 %0, %1, %2 sc1" : "=&v"(sq) : "v"(so), "s"(p0) : "memory");
        BAR();
#pragma unroll
        for (int n = 0; n < 17; ++n) rd(WM, F0, pA0, pB0, pB1, n);
        SBAR();
        asm volatile("s_waitcnt vmcnt(0)" : "+v"(sq), "+v"(wn) :: "memory");
        const float d = fmaxf(__builtin_fmaf(sq[0] * (-1.f / 64.f), sq[0], sq[1]), 0.f);
        cb = fminf(__builtin_amdgcn_sqrtf(__uint_as_float(wave_min(__float_as_uint(d)))) * 64.f * __uint_as_float(min(min(wn[0], wn[1]), wn[2])), 1.0e30f);
        if (ident) cb = 0.f;
        bias = f32x4{cb, cb, cb, cb};
        asm volatile("s_nop 1" : "+v"(bias));
        WAIT_LGKM();
        int off = 0;
        lds_cptr a = pA1, b0 = pB0, b1 = pB1;
        auto next = [&](int n) {
            if (n == 17) { off = (off == 2 * STAGE) ? 0 : off + STAGE; asm volatile("" : "+s"(off)); }
            if (n == 19) { b0 = pB0 + off; asm volatile("" : "+v"(b0)); }
            if (n == 20) { a = pA0 + off; asm volatile("" : "+v"(a)); }
            if (n == 21) { b1 = pB1 + off; asm volatile("" : "+v"(b1)); }
        };
        half(WM, F0, F1, true, a, b0 + 4096, b1 + 4096, next, 0, 3, true);
#pragma unroll 3
        for (int t = 0; t < NT - 1; ++t) {
            WAIT_LGKM_BAR();
            half(WM, F1, F0, true, a, b0, b1, [&](int n) {
                if (n == 19) { a = pA1 + off; asm volatile("" : "+v"(a)); }
                if (n == 22) __builtin_amdgcn_s_waitcnt(0xC07F);
            });
            half(WM, F0, F1, true, a, b0 + 4096, b1 + 4096, next);
        }
        WAIT_LGKM_BAR();
        half(WM, F1, F0, false, sm, sm, sm, [&](int n) {
            constexpr int RS = C::OFF_RSTD - C::OFF_MEAN, H = 512 - WM.value * 256;
            if (n < 2) { cq[n] = *(lds_f4)(c4 + n * 64); ck[n] = *(lds_f4)(c4 + 256 + n * 64); }
            else if (n < 6) { mu[n - 2] = *(lds_f1)(s1 + (n - 2) * 64); rs[n - 2] = *(lds_f1)(s1 + RS + (n - 2) * 64); }
            else if (n < 10) { mu4[n - 6] = *(lds_f4)(s4 + (n - 6) * 64); rs4[n - 6] = *(lds_f4)(s4 + RS + (n - 6) * 64); }
            else if (n == 10) { cv[0] = *(lds_f1)(c1 + 512); cv[1] = *(lds_f1)(c1 + 512 + 64); }
            else if (LOCAL && n == 11) {
                if (WM.value == 0) { mu[4] = *(lds_f1)(s1 + H); rs[4] = *(lds_f1)(s1 + RS + H); }
                else { mu4[4] = *(lds_f4)(s4 + H); rs4[4] = *(lds_f4)(s4 + RS + H); }
            } else if (n == (LOCAL ? 22 : 15)) __builtin_amdgcn_s_waitcnt(0xC07F);
        }, LOCAL ? 0 : 1);
        asm volatile("s_nop 7");
        SBAR();
        if (LOCAL) {
            auto fix = [&](int a, int t) {
#pragma unroll
                for (int f = 0; f < 2; ++f)
                    acc[a][t][f] = (a == 2) ? rs4[t] * (acc[2][t][f] + mu4[t] * cv[f]) + rs4[t] * -cb
                                            : rs[t] * (acc[a][t][f] + mu[t] * (a ? ck[f] : cq[f])) + rs[t] * -cb;
            };
            auto pk = [&](const f32x4 (&v)[2]) { return u32x4{pack_bf16(v[0][0], v[0][1]), pack_bf16(v[0][2], v[0][3]), pack_bf16(v[1][0], v[1][1]), pack_bf16(v[1][2], v[1][3])}; };
            if (!ident) {
                fix(1, 1); fix(1, 3); fix(2, 1); fix(2, 3);
#pragma unroll
                for (int f = 0; f < 2; ++f)
                    acch[f] = (WM.value == 0) ? rs[4] * (acch[f] + mu[4] * ck[f]) : rs4[4] * (acch[f] + mu4[4] * cv[f]);
            }
            auto pkv = [&](int t) {
#pragma unroll
                for (int f = 0; f < 2; ++f) {
                    va[t >> 1][f][(t & 1) * 2] = pack_bf16(acc[2][t][f][0], acc[2][t][f][1]);
                    va[t >> 1][f][(t & 1) * 2 + 1] = pack_bf16(acc[2][t][f][2], acc[2][t][f][3]);
                }
            };
            kfr[1] = pk(acc[1][1]); kfr[3] = pk(acc[1][3]); pkv(1); pkv(3);
            u32x4 hf = pk(acch);
            if (WM.value == 0) {
                ex[0] = kfr[1]; ex[64] = kfr[3];
#pragma unroll
                for (int u = 0; u < 2; ++u)
                    asm volatile("ds_write_b64 %0, %1 offset:%3\n\tds_write_b64 %0, %2 offset:%3+8" :: "v"(ex), "v"(u32x2{va[u][0][2], va[u][0][3]}),
                                 "v"(u32x2{va[u][1][2], va[u][1][3]}), "n"(2048 + u * 1024) : "memory");
                if (negpos) hf = u32x4{0, 0, 0, 0};
            }
            ex[256 + WM.value * 64] = hf;
            SBAR();
            if (!ident) { fix(0, 0); fix(0, 1); fix(0, 2); fix(0, 3); fix(1, 0); fix(1, 2); fix(2, 0); fix(2, 2); }
#pragma unroll
            for (int t = 0; t < 4; ++t) qfr[t] = pk(acc[0][t]);
            kfr[0] = pk(acc[1][0]); kfr[2] = pk(acc[1][2]); pkv(0); pkv(2);
            asm volatile("" : "+v"(qfr[0]), "+v"(qfr[1]), "+v"(qfr[2]), "+v"(qfr[3]), "+v"(kfr[0]), "+v"(kfr[2]),
                         "+v"(va[0][0]), "+v"(va[0][1]), "+v"(va[1][0]), "+v"(va[1][1]));
        }
    };
    const int head = ct * 2 + wn;
    unsigned qo = 0, ko = 0, vo = 0;
    if (LOCAL) {
        qo = (unsigned)((((panel * 8 + wm * 4) * 32 + head) * 64 + lane) * 16);
        asm volatile("" : "+v"(qo));
    } else {
        ko = (unsigned)((wm * 8 + (g & 1)) * 1024 + ((l15 & 7) + ((l15 & 8) << 1) + 32 * (g >> 1)) * 16);
        vo = (unsigned)((wm * 8 + 2 + (g >> 1)) * 1024 + (l15 + 32 * (g & 1)) * 16);
        asm volatile("" : "+v"(ko), "+v"(vo));
    }
    if (LOCAL && wm) mainloop(IC<1>{}); else mainloop(IC<0>{});

    if (!LOCAL) {
        auto pk = [&](f32x4 a, f32x4 b) { return u32x4{pack_bf16(a[0], a[1]), pack_bf16(a[2], a[3]), pack_bf16(b[0], b[1]), pack_bf16(b[2], b[3])}; };
        char* kvp = (char*)kvf + (size_t)(head * 16 + panel) * 16384;
        lds_ptr own = smw + wn * 16384;
        SBAR();
#pragma unroll
        for (int t = 0; t < 4; ++t) {
            const u32x4 kk = pk(rs[t] * (acc[1][t][0] + mu[t] * ck[0]) + rs[t] * -cb, rs[t] * (acc[1][t][1] + mu[t] * ck[1]) + rs[t] * -cb);
            if (t & 1) st16o<128>(kvp + (t >> 1) * 4096, ko, kk); else st16o<0>(kvp + (t >> 1) * 4096, ko, kk);
            kfr[t] = kk;
            SBAR();
            const int u = t >> 1, f = t & 1;
            const u32x4 vv = pk(rs4[2 * u] * (acc[2][2 * u][f] + mu4[2 * u] * cv[f]) + rs4[2 * u] * -cb,
                                rs4[2 * u + 1] * (acc[2][2 * u + 1][f] + mu4[2 * u + 1] * cv[f]) + rs4[2 * u + 1] * -cb);
            if (f) st16o<256>(kvp + u * 4096, vo, vv); else st16o<0>(kvp + u * 4096, vo, vv);
            qfr[t] = vv;
            SBAR();
        }
        half(IC<0>{}, F1, F0, false, sm, sm, sm, [&](int n) {
            const int t = n >> 1;
            if (n & 1) *(lds_u4)(own + vo + (t >> 1) * 4096 + (t & 1) * 256) = qfr[t];
            else       *(lds_u4)(own + ko + (t >> 1) * 4096 + (t & 1) * 128) = kfr[t];
        }, 0, 1);
        asm volatile("s_nop 7");
        SBAR();
#pragma unroll
        for (int u = 0; u < 2; ++u) {
            const float r0 = rs[2 * u] * QSCALE, r1 = rs[2 * u + 1] * QSCALE;
            const u32x4 qa = pk(r0 * (acc[0][2 * u][0] + mu[2 * u] * cq[0]) + r0 * -cb, r0 * (acc[0][2 * u][1] + mu[2 * u] * cq[1]) + r0 * -cb);
            const u32x4 qb = pk(r1 * (acc[0][2 * u + 1][0] + mu[2 * u + 1] * cq[0]) + r1 * -cb, r1 * (acc[0][2 * u + 1][1] + mu[2 * u + 1] * cq[1]) + r1 * -cb);
#pragma unroll
            for (int e = 0; e < 4; ++e) {
                const auto x = __builtin_amdgcn_permlane16_swap(qa[e], qb[e], false, false);
                qo4[2 * u][e] = x[0]; qo4[2 * u + 1][e] = x[1];
            }
        }
        return;
    }
    {
        const f32x4 zero4 = {0, 0, 0, 0};
        auto bf8 = [](u32x4 v) { return __builtin_bit_cast(bf16x8, v); };
        f32x4 sp[4], sc[4];
        sc[1] = MFMA16(bf8(kfr[1]), bf8(qfr[1]), mwin); sp[1] = MFMA16(bf8(kfr[0]), bf8(qfr[1]), mwin);
        sc[3] = MFMA16(bf8(kfr[3]), bf8(qfr[3]), mwin); sp[3] = MFMA16(bf8(kfr[2]), bf8(qfr[3]), mwin);
        asm volatile("" : "+v"(sc[1]), "+v"(sp[1]), "+v"(sc[3]), "+v"(sp[3]));
        WAIT_LGKM_BAR();
        const bf16x8 kp[2] = {lds_b128(ka[0]), lds_b128(ka[1])};
        u32x2 vp[2][2];
#pragma unroll
        for (int u = 0; u < 2; ++u)
            asm volatile("ds_read_b64 %0, %2\n\tds_read_b64 %1, %2 offset:8" : "=&v"(vp[u][0]), "=&v"(vp[u][1]) : "v"(vo2[u]) : "memory");
        sc[0] = MFMA16(bf8(kfr[0]), bf8(qfr[0]), mwin); sc[2] = MFMA16(bf8(kfr[2]), bf8(qfr[2]), mwin);
        SBAR();
        float x[4][4], m[4];
        unsigned a[4][2];
        auto logits = [&](int t0, int st) {
#pragma unroll
            for (int t = t0; t < 4; t += 2)
#pragma unroll
                for (int h = 0; h < 2; ++h) {
                    if (st == 0) {
                        const unsigned uc = pack_bf16(sc[t][2 * h], sc[t][2 * h + 1]), up = pack_bf16(sp[t][2 * h], sp[t][2 * h + 1]);
                        asm("v_bfi_b32 %0, %1, %2, %3" : "=v"(a[t][h]) : "v"(h ? cm23 : cm01), "v"(uc), "v"(up));
                    } else if (st == 1) { x[t][2 * h] = bf_lo(a[t][h]) * ATT_SCALE; x[t][2 * h + 1] = bf_hi(a[t][h]) * ATT_SCALE; }
                    else if (st == 2) a[t][h] = pack_bf16(x[t][2 * h], x[t][2 * h + 1]);
                    else { x[t][2 * h] = bf_lo(a[t][h]); x[t][2 * h + 1] = bf_hi(a[t][h]); }
                }
            SBAR();
        };
        logits(1, 0); logits(1, 1);
        sp[0] = MFMA16(kp[0], bf8(qfr[0]), mfirst); sp[2] = MFMA16(kp[1], bf8(qfr[2]), mfirst);
        SBAR();
        logits(1, 2); logits(1, 3);
        logits(0, 0); logits(0, 1); logits(0, 2); logits(0, 3);
        auto across = [&](float (&v)[4], auto op, auto fin) {
            const auto p = __builtin_amdgcn_permlane16_swap(__float_as_uint(v[0]), __float_as_uint(v[1]), false, false);
            const auto q = __builtin_amdgcn_permlane16_swap(__float_as_uint(v[2]), __float_as_uint(v[3]), false, false);
            const auto r = __builtin_amdgcn_permlane32_swap(__float_as_uint(op(__uint_as_float(p[0]), __uint_as_float(p[1]))),
                                                            __float_as_uint(op(__uint_as_float(q[0]), __uint_as_float(q[1]))), false, false);
            const unsigned z = __float_as_uint(fin(op(__uint_as_float(r[0]), __uint_as_float(r[1]))));
            const auto u = __builtin_amdgcn_permlane32_swap(z, z, false, false);
            const auto e = __builtin_amdgcn_permlane16_swap(u[0], u[0], false, false), f = __builtin_amdgcn_permlane16_swap(u[1], u[1], false, false);
            v[0] = __uint_as_float(e[0]); v[1] = __uint_as_float(e[1]); v[2] = __uint_as_float(f[0]); v[3] = __uint_as_float(f[1]);
        };
#pragma unroll
        for (int t = 0; t < 4; ++t) m[t] = amax3(x[t][0], x[t][1], x[t][2]);
        asm("v_max_f32 %0, %0, %4\n\tv_max_f32 %1, %1, %5\n\tv_max_f32 %2, %2, %6\n\tv_max_f32 %3, %3, %7\n\ts_nop 0"
            : "+v"(m[0]), "+v"(m[1]), "+v"(m[2]), "+v"(m[3]) : "v"(x[0][3]), "v"(x[1][3]), "v"(x[2][3]), "v"(x[3][3]));
        across(m, [](float p, float q) { float r; asm("v_max_f32 %0, %1, %2\n\ts_nop 0" : "=v"(r) : "v"(p), "v"(q)); return r; },
               [](float p) { return p * -LOG2E; });
        SBAR();
#pragma unroll
        for (int t = 0; t < 4; ++t)
#pragma unroll
            for (int r = 0; r < 4; ++r) x[t][r] = __builtin_fmaf(x[t][r], LOG2E, m[t]);
        SBAR();
#pragma unroll
        for (int t = 0; t < 4; ++t)
#pragma unroll
            for (int r = 0; r < 4; ++r) x[t][r] = __builtin_amdgcn_exp2f(x[t][r]);
        SBAR();
#pragma unroll
        for (int t = 0; t < 4; ++t) m[t] = (x[t][0] + x[t][1]) + (x[t][2] + x[t][3]);
        asm volatile("s_waitcnt lgkmcnt(0)" : "+v"(vp[0][0]), "+v"(vp[0][1]), "+v"(vp[1][0]), "+v"(vp[1][1]) :: "memory");
        u32x4 ve[2][2];
#pragma unroll
        for (int u = 0; u < 2; ++u)
#pragma unroll
            for (int f = 0; f < 2; ++f) ve[u][f] = u32x4{vp[u][f][0], vp[u][f][1], va[u][f][0], va[u][f][1]};
        across(m, [](float p, float q) { return p + q; }, [](float p) { return __builtin_amdgcn_rcpf(p); });
        asm volatile("" : "+v"(ve[0][0]), "+v"(ve[0][1]), "+v"(ve[1][0]), "+v"(ve[1][1]));
        SBAR();
#pragma unroll
        for (int t = 0; t < 4; ++t)
#pragma unroll
            for (int r = 0; r < 4; ++r) x[t][r] *= m[t];
        SBAR();
#pragma unroll
        for (int t = 0; t < 4; ++t)
#pragma unroll
            for (int h = 0; h < 2; ++h) a[t][h] = pack_bf16(x[t][2 * h], x[t][2 * h + 1]);
        SBAR();
        f32x4 o0[4], o1[4];
#pragma unroll
        for (int t = 0; t < 4; ++t) {
            const bf16x8 pb = bf8(u32x4{a[t][0] & ~cm01, a[t][1] & ~cm23, a[t][0] & cm01, a[t][1] & cm23});
            o0[t] = MFMA16(bf8((t & 1) ? va[t >> 1][0] : ve[t >> 1][0]), pb, zero4);
            o1[t] = MFMA16(bf8((t & 1) ? va[t >> 1][1] : ve[t >> 1][1]), pb, zero4);
        }
        SBAR();
#pragma unroll
        for (int t = 0; t < 4; ++t)
            st16(ofrag + t * 16384, qo, u32x4{pack_bf16(o0[t][0], o0[t][1]), pack_bf16(o0[t][2], o0[t][3]),
                                                 pack_bf16(o1[t][0], o1[t][1]), pack_bf16(o1[t][2], o1[t][3])});
    }
}

DI int out_row(int panel, bool localrows, int T, int a) {
    return localrows ? (panel * 128 + (T >> 2) * 64 + 2 * (16 * (T & 1) + a) + ((T >> 1) & 1)) : (panel * 128 + T * 16 + a);
}
DI int out_swz(int k) { return ((k >> 1) & 1) | (k & 4); }
DI unsigned wo_off(int w, int lane) {
    const int kl = lane >> 3;
    unsigned v = (unsigned)(w * 8 + kl) * 2048u + (unsigned)(ids().co * 128 + ((lane & 7) ^ out_swz(kl)) * 16);
    asm volatile("" : "+v"(v));
    return v;
}
DI void wo_load(const void* Wo, unsigned voff, int i0, int i1, unsigned lds0, int w) {
#pragma unroll
    for (int i = i0; i < i1; ++i) glds16((const char*)Wo + (size_t)i * 65536, voff, lds0 + (w + 4 * i) * 1024);
}
DI unsigned res_off(bool localrows, bool tiled, int lane) {
    const Ids I = ids();
    unsigned v = (unsigned)(I.po * 262144 + I.co * (tiled ? 16384 : 128) + out_row(0, localrows, 0, lane >> 3) * (tiled ? 128 : 2048) +
                            (((lane & 7) ^ (lane >> 3)) * 16));
    asm volatile("" : "+v"(v));
    return v;
}
DI void res_load(u32x4 (&res)[6], const void* Xin, bool localrows, bool tiled, unsigned off, int w) {
#pragma unroll
    for (int i = 0; i < 6; ++i) {
        const int j = min(w + 3 * i, 15);
        asm volatile("global_load_dwordx4 %0, %1, %2 sc1" : "=&v"(res[i]) : "v"(off), "s"((const char*)Xin + out_row(0, localrows, j >> 1, (j & 1) * 8) * (tiled ? 128 : 2048)) : "memory");
    }
}
DI void out_handoff(char* smem, int xoff, u32x4 (&res)[6], int w, char* ws, int set, u64 tag, int lane, u32x4 (&c)[8], int mat) {
    const Ids I = ids();
    u64 land;
    lds_ptr dst = (lds_ptr)smem + xoff + w * 1024 + lane * 16;
    asm volatile("" : "+v"(dst));
    if (w == 3) __builtin_amdgcn_s_setprio(3);
    signal_poll<false, false>(w - 3, ws, flag_off(set, I.pq, lane & 15, I.cq), flag_off(set, I.po, I.co, lane & 15), 0xffffull, tag, land);
    if (w < 3) {
        asm volatile("s_waitcnt vmcnt(0)" : "+v"(res[0]), "+v"(res[1]), "+v"(res[2]), "+v"(res[3]), "+v"(res[4]), "+v"(res[5]) :: "memory");
#pragma unroll
        for (int i = 0; i < 5; ++i) *(__attribute__((address_space(3))) u32x4*)(dst + i * 3072) = res[i];
        if (w == 0) *(__attribute__((address_space(3))) u32x4*)(dst + 15360) = res[5];
        if (mat >= 0) colsum_store(c, ws, mat, w, lane);
    }
    WAIT_LGKM_BAR();
    asm volatile("s_waitcnt vmcnt(0)" : "+v"(land) :: "memory");
    if (w < 3 && mat >= 0) flag_store(ws, (mat ? 10 : 13) + w, I.cq, I.pq, tag, lane);
    __builtin_amdgcn_s_setprio(0);
}

template <bool AFTER_LOCAL>
DI void out_compute(char* smem, int w, const __hip_bfloat16* __restrict__ ofrag,
                    __hip_bfloat16* __restrict__ Xout, float* __restrict__ statp SP_PARAM) {
    constexpr int XOFF = AFTER_LOCAL ? 147456 : 98304;
    STAMP(AFTER_LOCAL ? 3 : 10);
    VM_BAR(0);
    const int lane = lane_id();
    asm volatile("" : "+s"(w));
    const Ids I = ids();
    const int panel = I.po, ct = I.co;
    const int l15 = lane & 15, g = lane >> 4;
    const bool tiled = AFTER_LOCAL || statp != nullptr;
    const int rstr = tiled ? 128 : 2048;
    const int q4 = l15 >> 2, p4 = l15 & 3;
    const int krow = 4 * g + q4;
    const int sw = out_swz(krow);
    const lds_cptr sm = (lds_cptr)smem;
    lds_cptr bw[4], xo[4];
#pragma unroll
    for (int t = 0; t < 4; ++t) {
        bw[t] = sm + krow * 128 + ((((t >> 1) * 4 + (p4 >> 1) * 2 + (t & 1)) ^ sw) * 16) + (p4 & 1) * 8;
        xo[t] = sm + XOFF + (w * 32 + l15) * 128 + ((((t >> 1) * 4 + (g >> 1) * 2 + (t & 1)) ^ (l15 & 7)) * 16) + (g & 1) * 8;
    }
    unsigned so[2], sto, ao[2];
#pragma unroll
    for (int r2 = 0; r2 < 2; ++r2) {
        so[r2] = (unsigned)(panel * 262144 + ct * (tiled ? 16384 : 128) + out_row(0, AFTER_LOCAL, 2 * w + r2, l15 & 7) * rstr + (l15 >> 3) * 64 + g * 16);
        ao[r2] = (unsigned)((panel * 8 + 2 * w + r2) * 32768 + lane * 16);
    }
    sto = (unsigned)(((panel * 16 + ct) * 128 + 2 * w * 16 + l15) * 8);
    asm volatile("" : "+v"(bw[0]), "+v"(bw[1]), "+v"(bw[2]), "+v"(bw[3]), "+v"(xo[0]), "+v"(xo[1]), "+v"(xo[2]), "+v"(xo[3]),
                 "+v"(so[0]), "+v"(so[1]), "+v"(sto), "+v"(ao[0]), "+v"(ao[1]));
    bf16x8 ones;
    {
        unsigned o1; asm volatile("v_mov_b32 %0, 0x3f803f80" : "=v"(o1));
        ones = __builtin_bit_cast(bf16x8, (u32x4{o1, o1, o1, o1}));
        asm volatile("" : "+v"(ones));
    }
    const __amdgpu_buffer_rsrc_t ra = __builtin_amdgcn_make_buffer_rsrc((void*)ofrag, 0, 4 << 20, 0x00020000);
    asm volatile("" :: "s"(ra));
    u32x32 fr;
    BAR();
    STAMP(AFTER_LOCAL ? 4 : 11);
    u32x32 aw0, aw1;
    asm volatile(".irp j,0,1,2,3\n\tbuffer_load_dwordx4 v[64+8*\\j:67+8*\\j], %2, %4, 0 offen offset:1024*\\j sc1\n\t"
                 "buffer_load_dwordx4 v[68+8*\\j:71+8*\\j], %3, %4, 0 offen offset:1024*\\j sc1\n\t.endr\n\t"
                 ".irp j,0,1\n\tbuffer_load_dwordx4 v[96+8*\\j:99+8*\\j], %2, %4, %5 offen offset:1024*\\j sc1\n\t"
                 "buffer_load_dwordx4 v[100+8*\\j:103+8*\\j], %3, %4, %5 offen offset:1024*\\j sc1\n\t.endr"
                 : "=&{v[64:95]}"(aw0), "=&{v[96:127]}"(aw1) : "v"(ao[0]), "v"(ao[1]), "s"(ra), "s"(4096) : "memory");
    f32x16 acc[2];
#pragma unroll
    for (int r2 = 0; r2 < 2; ++r2)
#pragma unroll
        for (int c = 0; c < 4; ++c) {
            const u32x2 xr = *(const __attribute__((address_space(3))) u32x2*)(xo[c] + r2 * 2048);
            acc[r2][4 * c] = bf_lo(xr[0]); acc[r2][4 * c + 1] = bf_hi(xr[0]); acc[r2][4 * c + 2] = bf_lo(xr[1]); acc[r2][4 * c + 3] = bf_hi(xr[1]);
        }
    asm volatile(
        ".macro OUT_MM r, t, j\n\t"
        "v_mfma_f32_16x16x32_bf16 v[16*\\r+4*\\t:16*\\r+4*\\t+3], v[32+16*((\\j)&1)+4*\\t:35+16*((\\j)&1)+4*\\t], v[64+8*\\j+4*\\r:67+8*\\j+4*\\r], v[16*\\r+4*\\t:16*\\r+4*\\t+3]\n\t"
        ".endm\n\t"
        ".macro OUT_RD t, hi, j, on\n\t"
        ".if \\on\n\tds_read_b64_tr_b16 v[32+16*((\\j)&1)+4*\\t+2*\\hi:33+16*((\\j)&1)+4*\\t+2*\\hi], v[128+\\t] offset:((\\j)&7)*4096+2048*\\hi\n\t.endif\n\t"
        ".endm\n\t"
        ".macro OUT_UP t, on\n\t.if \\on\n\tv_add_u32 v[128+\\t], s97, v[128+\\t]\n\t.endif\n\t.endm\n\t"
        ".macro OUT_STEP j, ld, rd, up, vm, lg\n\t"
        "OUT_MM 0, 0, \\j\n\tOUT_RD 3, 1, \\j+1, 1\n\tOUT_UP 0, \\up\n\t"
        ".if \\ld && (((\\j+6)&3) == 0)\n\ts_addk_i32 s96, 0x1000\n\t.endif\n\t"
        "OUT_MM 1, 0, \\j\n\tOUT_RD 0, 0, \\j+2, \\rd\n\t"
        ".if \\ld\n\tbuffer_load_dwordx4 v[64+8*((\\j+6)&7):67+8*((\\j+6)&7)], %[a0], %[ra], s96 offen offset:1024*((\\j+6)&3) sc1\n\t.endif\n\t"
        "OUT_MM 0, 1, \\j\n\tOUT_RD 0, 1, \\j+2, \\rd\n\tOUT_UP 1, \\up\n\t"
        "OUT_MM 1, 1, \\j\n\tOUT_RD 1, 0, \\j+2, \\rd\n\t"
        "OUT_MM 0, 2, \\j\n\tOUT_RD 1, 1, \\j+2, \\rd\n\tOUT_UP 2, \\up\n\t"
        "OUT_MM 1, 2, \\j\n\tOUT_RD 2, 0, \\j+2, \\rd\n\t"
        ".if \\ld\n\tbuffer_load_dwordx4 v[68+8*((\\j+6)&7):71+8*((\\j+6)&7)], %[a1], %[ra], s96 offen offset:1024*((\\j+6)&3) sc1\n\t.endif\n\t"
        "OUT_MM 0, 3, \\j\n\tOUT_RD 2, 1, \\j+2, \\rd\n\tOUT_UP 3, \\up\n\t"
        "OUT_MM 1, 3, \\j\n\tOUT_RD 3, 0, \\j+2, \\rd\n\t"
        "s_waitcnt vmcnt(\\vm) lgkmcnt(\\lg)\n\t"
        ".endm\n\t"
        ".macro OUT_PACK y, c\n\t"
        ".irp i,0,1,2,3,4,5,6,7\n\tv_cvt_pk_bf16_f32 v[\\y+\\i], v[\\c+2*\\i], v[\\c+2*\\i+1]\n\t.endr\n\t"
        ".irp i,0,1,4,5\n\tv_permlane16_swap_b32 v[\\y+\\i], v[\\y+\\i+2]\n\t.endr\n\t"
        ".irp i,0,1,2,3\n\tv_mov_b32 v[\\y+8+\\i], v[\\y+\\i]\n\t.endr\n\t"
        "v_mfma_f32_16x16x32_bf16 v[\\y+12:\\y+15], v[\\y:\\y+3], v[\\y:\\y+3], 0\n\t"
        "v_mov_b32_dpp v[\\y+8], v[\\y+4] row_ror:8 row_mask:0xf bank_mask:0xc\n\tv_mov_b32_dpp v[\\y+9], v[\\y+5] row_ror:8 row_mask:0xf bank_mask:0xc\n\t"
        "v_mfma_f32_16x16x32_bf16 v[\\y+16:\\y+19], %[one], v[\\y:\\y+3], 0\n\t"
        "v_mov_b32_dpp v[\\y+10], v[\\y+6] row_ror:8 row_mask:0xf bank_mask:0xc\n\tv_mov_b32_dpp v[\\y+11], v[\\y+7] row_ror:8 row_mask:0xf bank_mask:0xc\n\t"
        "v_mfma_f32_16x16x32_bf16 v[\\y+12:\\y+15], v[\\y+4:\\y+7], v[\\y+4:\\y+7], v[\\y+12:\\y+15]\n\t"
        ".endm\n\t"
        ".macro OUT_REST y\n\t"
        "v_mfma_f32_16x16x32_bf16 v[\\y+16:\\y+19], %[one], v[\\y+4:\\y+7], v[\\y+16:\\y+19]\n\t"
        ".irp i,0,1,2,3\n\tv_mov_b32_dpp v[\\y+4+\\i], v[\\y+\\i] row_ror:8 row_mask:0xf bank_mask:0x3\n\t.endr\n\t"
        ".endm\n\t"
        ".macro OUT_DIAG y\n\t"
        "v_cndmask_b32 v[\\y+12], v[\\y+12], v[\\y+13], %[m0]\n\tv_cndmask_b32 v[\\y+14], v[\\y+14], v[\\y+15], %[m0]\n\t"
        "v_cndmask_b32 v[\\y+17], v[\\y+12], v[\\y+14], %[m1]\n\t"
        ".endm\n\t"
        ".irp t,0,1,2,3\n\tOUT_RD \\t, 0, 0, 1\n\tOUT_RD \\t, 1, 0, 1\n\t.endr\n\t"
        ".irp t,0,1,2\n\tOUT_RD \\t, 0, 1, 1\n\tOUT_RD \\t, 1, 1, 1\n\t.endr\n\tOUT_RD 3, 0, 1, 1\n\t"
        "s_movk_i32 s96, 0x1000\n\ts_mov_b32 s97, 0x8000\n\t"
        "s_waitcnt vmcnt(10) lgkmcnt(0)\n"
        ".Lout_loop_%=:\n\t"
        "OUT_STEP 0, 1, 1, 0, 10, 7\n\tOUT_STEP 1, 1, 1, 0, 10, 7\n\tOUT_STEP 2, 1, 1, 0, 10, 7\n\tOUT_STEP 3, 1, 1, 0, 10, 7\n\t"
        "OUT_STEP 4, 1, 1, 0, 10, 7\n\t"
        "s_cmpk_eq_u32 s96, 0x6000\n\ts_cselect_b32 s97, %[last], s97\n\t"
        "OUT_STEP 5, 1, 1, 0, 10, 7\n\tOUT_STEP 6, 1, 1, 1, 10, 7\n\t"
        "OUT_STEP 7, 1, 1, 0, 10, 7\n\t"
        "s_cmpk_lg_u32 s96, 0x7000\n\ts_cbranch_scc1 .Lout_loop_%=\n\t"
        "OUT_STEP 0, 1, 1, 0, 10, 7\n\tOUT_STEP 1, 1, 1, 0, 10, 7\n\tOUT_STEP 2, 0, 1, 0, 8, 7\n\tOUT_STEP 3, 0, 1, 0, 6, 7\n\t"
        "OUT_STEP 4, 0, 1, 0, 4, 7\n\tOUT_STEP 5, 0, 1, 0, 2, 7\n\tOUT_STEP 6, 0, 0, 0, 0, 0\n\t"
        ".irp r,0,1\n\t.irp t,0,1,2,3\n\tOUT_MM \\r, \\t, 7\n\t.endr\n\t.endr\n\t"
        "s_nop 0\n\t"
        "OUT_PACK 64, 0\n\tglobal_store_dwordx4 %[s0], v[72:75], %[xa] sc1\n\tOUT_REST 64\n\tglobal_store_dwordx4 %[s0], v[68:71], %[xb] sc1\n\t"
        "OUT_PACK 84, 16\n\tglobal_store_dwordx4 %[s1], v[92:95], %[xa] sc1\n\tOUT_REST 84\n\tglobal_store_dwordx4 %[s1], v[88:91], %[xb] sc1\n\t"
        "s_cmp_eq_u64 %[st], 0\n\ts_cbranch_scc1 .Lout_end_%=\n\t"
        "OUT_DIAG 64\n\tOUT_DIAG 84\n\t"
        "s_mov_b64 exec, %[md]\n\t"
        "global_store_dwordx2 %[so], v[80:81], %[st] sc1\n\tglobal_store_dwordx2 %[so], v[100:101], %[st] offset:128 sc1\n\t"
        "s_mov_b64 exec, -1\n"
        ".Lout_end_%=:\n\t"
        "s_nop 1\n\t"
        ".purgem OUT_MM\n\t.purgem OUT_RD\n\t.purgem OUT_UP\n\t.purgem OUT_STEP\n\t.purgem OUT_PACK\n\t.purgem OUT_REST\n\t.purgem OUT_DIAG"
        : "+{v[0:15]}"(acc[0]), "+{v[16:31]}"(acc[1]), "+{v[64:95]}"(aw0), "+{v[96:127]}"(aw1),
          "+{v128}"(bw[0]), "+{v129}"(bw[1]), "+{v130}"(bw[2]), "+{v131}"(bw[3]), "=&{v[32:63]}"(fr)
        : [a0] "v"(ao[0]), [a1] "v"(ao[1]), [ra] "s"(ra), [last] "s"(AFTER_LOCAL ? 32768 : 65536), [one] "v"(ones), [s0] "v"(so[0]), [s1] "v"(so[1]),
          [xa] "s"(Xout), [xb] "s"((char*)Xout + (AFTER_LOCAL ? 16 : 8) * rstr), [st] "s"(statp), [so] "v"(sto),
          [m0] "s"(0xaaaaaaaaaaaaaaaaull), [m1] "s"(0xccccccccccccccccull), [md] "s"(0xf0000f0000f0000full)
        : "memory", "scc", "s96", "s97");
    STAMP(AFTER_LOCAL ? 5 : 12);
}

#define ROWSUM(p0, p1, c) __builtin_amdgcn_smfmac_f32_16x16x64_bf16(sel, __builtin_bit_cast(bf16v_t, __builtin_shufflevector(p0, p1, 0, 1, 2, 3, 4, 5, 6, 7)), c, sidx, 0, 0)
constexpr int ATT_STAGE = 32768;
typedef __attribute__((ext_vector_type(8))) unsigned u32x8;

DI void attn_loader(char* smem, int w, char* ws, int set, u64 tag, const __hip_bfloat16* __restrict__ kvf, const void* Wo, const void* Xin,
                    const void* Wcs SP_PARAM) {
    const int lane = lane_id();
    asm volatile("" : "+s"(w));
    const Ids I = ids();
    const int panel = I.pq, ct = I.cq;
    const unsigned lds0 = (unsigned)(uintptr_t)smem;
    const char* kvh = (const char*)kvf + (size_t)(ct * 2 + (w >> 1)) * 262144 + (w & 1) * 4096;
    const unsigned voff = (unsigned)(lane * 16);
    auto order = [&](int st) { return ((panel & 8) + st - ((st - 1 < (panel & 7)) ? 1 : 0)) & 15; };
    auto load = [&](int st) {
        const char* src = kvh + (size_t)order(st) * 16384;
        const unsigned dst = lds0 + (w >> 1) * 16384 + (w & 1) * 4096 + (st & 3) * ATT_STAGE;
#pragma unroll
        for (int u = 0; u < 2; ++u)
            asm volatile("s_mov_b32 m0, %0\n\ts_nop 0\n\tglobal_load_lds_dwordx4 %1, %2 sc1\n\tglobal_load_lds_dwordx4 %1, %2 offset:1024 sc1\n\t"
                         "global_load_lds_dwordx4 %1, %2 offset:2048 sc1\n\tglobal_load_lds_dwordx4 %1, %2 offset:3072 sc1"
                         :: "s"(dst + u * 8192), "v"(voff), "s"(src + u * 8192) : "memory");
    };
    const unsigned wv = wo_off(w, lane), rv = res_off(false, true, lane), cv = colsum_off(w, lane);
    const unsigned poff = flag_off(set, ct, panel, lane & 15);
    u64 land;
    __builtin_amdgcn_s_setprio(3);
    BAR();
    signal_poll<false, true>(w, ws, flag_off(set, ct, lane & 15, panel), poff, 1ull << order(1), tag, land);
    STAMP(9);
    load(1);
    asm volatile("global_load_dwordx2 %0, %1, %2 sc1\n\ts_waitcnt vmcnt(0)" : "+v"(land) : "v"(poff), "s"(ws + WS_FLAG) : "memory");
    STAMP(10);
    BAR();
    if (!__all(land == tag)) flag_poll(ws, poff, tag);
    load(2); load(3);
    __builtin_amdgcn_s_setprio(0);
    STAMP(11);
    VM_BAR(8); load(4);
    VM_BAR(0); load(5); load(6);
    VM_BAR(0); load(7); load(8);
    u32x4 res[6], ca[8];
    colsum_load(ca, w < 3 ? Wcs : nullptr, cv);
#pragma unroll 1
    for (int st = 6; st < 12; st += 2) {
        VM_BAR(0);
        load(st + 3); load(st + 4);
        if (st == 8) wo_load(Wo, wv, 24, 32, lds0 + 32768, w);
    }
    VM_BAR(0); load(15); wo_load(Wo, wv, 0, 8, lds0, w);
    res_load(res, Xin, false, true, rv, w);
    VM_BAR(0); wo_load(Wo, wv, 8, 24, lds0, w);
    STAMP(12);
    out_handoff(smem, 98304, res, w, ws, set + 1, tag, lane, ca, Wcs ? 0 : -1);
}

DI void attn_compute(char* smem, int w, const u32x4 (&q4)[4], const __hip_bfloat16* __restrict__ kvf, __hip_bfloat16* __restrict__ ofrag SP_PARAM) {
    const int lane = lane_id();
    asm volatile("" : "+s"(w));
    const int wm = w & 1, wn = w >> 1;
    const Ids I = ids();
    const int panel = I.pq, head = I.cq * 2 + wn;
    const int l15 = lane & 15;
    const unsigned se = ((l15 & 2) == 0 && (l15 & 4) == 0) ? 0x3f803f80u : 0u, so = ((l15 & 2) == 0 && (l15 & 4) != 0) ? 0x3f803f80u : 0u;
    const bf8_t sel = __builtin_bit_cast(bf8_t, u32x4{se, se, so, so});
    const int sidx = (l15 & 1) ? (int)0xEEEEEEEEu : 0x44444444;
    const lds_cptr sm = (lds_cptr)smem + wn * 16384 + lane * 16;
    const f32x16 z16 = {0, 0, 0, 0, 0, 0, 0, 0, 0, 0, 0, 0, 0, 0, 0, 0};
    const bf16x8 qA0 = __builtin_bit_cast(bf16x8, q4[0]), qA1 = __builtin_bit_cast(bf16x8, q4[1]);
    const bf16x8 qB0 = __builtin_bit_cast(bf16x8, q4[2]), qB1 = __builtin_bit_cast(bf16x8, q4[3]);
    f32x16 oA, oB, nmA, nmB, sA, sB;
    f32x4 lsA, lsB;
    u32x8 pB;
    bf16x8 v11;
    asm volatile(".irp r,106,108,110,112,134,136,138,140,170,172\n\tv_mov_b64 v[\\r:\\r+1], 0\n\t.endr"
                 : "={v[106:113]}"(pB), "={v[134:137]}"(lsA), "={v[138:141]}"(lsB), "={v[170:173]}"(v11));
    WAIT_LGKM_BAR();
    STAMP(8);
    bf16x8 k00 = lds_b128(sm), k10 = lds_b128(sm + 1024), k01 = lds_b128(sm + 4096), k11 = lds_b128(sm + 4096 + 1024);
    bf16x8 v00 = lds_b128(sm + 2048), v10 = lds_b128(sm + 3072), v01 = lds_b128(sm + 4096 + 2048);
    float ob;
    {
        const u32x4 vu = __builtin_bit_cast(u32x4, v00);
        const unsigned vm = max(max(vu[0] & 0x7fff0000u, vu[1] & 0x7fff0000u), max(vu[2] & 0x7fff0000u, vu[3] & 0x7fff0000u));
        ob = __uint_as_float(wave_min(vm)) * 0.015625f;
        sA = MFMA32(k00, qA0, z16); sB = MFMA32(k00, qB0, z16);
        sA = MFMA32(k10, qA1, sA); sB = MFMA32(k10, qB1, sB);
        SBAR(); VM_BAR(0); SBAR();
        float mA = sA[0], mB = sB[0];
#pragma unroll
        for (int r = 1; r < 16; ++r) { mA = fmaxf(mA, sA[r]); mB = fmaxf(mB, sB[r]); }
        mA = max_h(mA) + 16.f; mB = max_h(mB) + 16.f;
#pragma unroll
        for (int r = 0; r < 16; ++r) { nmA[r] = -mA; nmB[r] = -mB; sA[r] -= mA; oA[r] = ob; oB[r] = ob; }
    }
    const unsigned a0 = (unsigned)(uintptr_t)sm;
    unsigned dst = (unsigned)((((panel * 8 + wm * 4 + ((lane & 31) >> 4)) * 32 + head) * 64 + (lane & 15) + (lane >> 5) * 16) * 16);
    asm volatile("" : "+v"(dst));
    asm volatile(
        ".macro ATT_RD dst, y\n\t"
        "ds_read_b128 v[\\dst:\\dst+3], v[175+(((\\y)&0x1ffff)>>16)] offset:(\\y)&0xffff\n\t"
        ".endm\n\t"
        ".macro ATT_E4 c\n\t"
        "v_exp_f32 v[\\c], v[\\c]\n\tv_exp_f32 v[\\c+1], v[\\c+1]\n\tv_exp_f32 v[\\c+2], v[\\c+2]\n\tv_exp_f32 v[\\c+3], v[\\c+3]\n\t"
        ".endm\n\t"
        ".macro ATT_C4 p, c\n\t"
        "v_cvt_pk_bf16_f32 v[\\p], v[\\c], v[\\c+1]\n\tv_cvt_pk_bf16_f32 v[\\p+1], v[\\c+2], v[\\c+3]\n\t"
        "v_cvt_pk_bf16_f32 v[\\p+2], v[\\c+4], v[\\c+5]\n\tv_cvt_pk_bf16_f32 v[\\p+3], v[\\c+6], v[\\c+7]\n\t"
        ".endm\n\t"
        ".macro ATT_STEP cur, nxt, ka, kb, q, nm, oc, od, lsd, pc, pd, vd1, vc0, r0, y0, r1, y1, bar, rd\n\t"
        "v_mfma_f32_32x32x16_bf16 v[\\nxt:\\nxt+15], v[\\ka:\\ka+3], v[\\q:\\q+3], v[\\nm:\\nm+15]\n\t"
        "ATT_E4 \\cur\n\t"
        "v_mfma_f32_32x32x16_bf16 v[\\nxt:\\nxt+15], v[\\kb:\\kb+3], v[\\q+4:\\q+7], v[\\nxt:\\nxt+15]\n\t"
        "ATT_E4 \\cur+4\n\t"
        "v_mfma_f32_32x32x16_bf16 v[\\od:\\od+15], v[\\vd1:\\vd1+3], v[\\pd+4:\\pd+7], v[\\od:\\od+15]\n\t"
        "s_waitcnt lgkmcnt(0)\n\t"
        "ATT_C4 \\pc, \\cur\n\t"
        "v_exp_f32 v[\\cur+8], v[\\cur+8]\n\tv_exp_f32 v[\\cur+9], v[\\cur+9]\n\t"
        "v_mfma_f32_32x32x16_bf16 v[\\oc:\\oc+15], v[\\vc0:\\vc0+3], v[\\pc:\\pc+3], v[\\oc:\\oc+15]\n\t"
        ".if \\bar\n\ts_barrier\n\t.endif\n\t"
        "ATT_E4 \\cur+10\n\t"
        "v_smfmac_f32_16x16x64_bf16 v[\\lsd:\\lsd+3], v[130:133], v[\\pd:\\pd+7], v174\n\t"
        ".if \\rd & 4\n\tATT_RD 142, 32768\n\tATT_RD 150, 33792\n\tATT_RD 158, 34816\n\t.endif\n\t"
        ".if \\rd & 8\n\tATT_RD \\r1-8, \\y1-1024\n\t.endif\n\t"
        ".if \\rd & 1\n\tATT_RD \\r0, \\y0\n\t.endif\n\t"
        ".if \\rd & 2\n\tATT_RD \\r1, \\y1\n\t.endif\n\t"
        "v_exp_f32 v[\\cur+14], v[\\cur+14]\n\tv_exp_f32 v[\\cur+15], v[\\cur+15]\n\t"
        "ATT_C4 \\pc+4, \\cur+8\n\t"
        ".if \\rd & 4\n\ts_waitcnt lgkmcnt(3)\n\t.endif\n\t"
        ".endm\n\t"
        ".macro ATT_TILE p, y1, y2, bar, ra=3, rb=3\n\t"
        "ATT_STEP 66, 82, 142+\\p, 150+\\p, 122, 50, 2, 18, 138, 98, 106, 170-\\p, 158+\\p, 170-\\p, \\y1+3072, 142+\\p, \\y2, \\bar&1, \\ra\n\t"
        "ATT_STEP 82, 66, 146-\\p, 154-\\p, 114, 34, 18, 2, 134, 106, 98, 166+\\p, 158+\\p, 158+\\p, \\y2+2048, 150+\\p, \\y2+1024, \\bar&2, \\rb\n\t"
        ".endm\n\t"
        ".macro ATT_STAGE b, bar\n\t"
        "ATT_TILE 0, \\b+4096, \\b+8192, 0\n\t"
        "ATT_TILE 4, \\b+8192, \\b+12288, 0\n\t"
        ".if \\bar\n\tATT_TILE 0, \\b+12288, \\b+32768, 2, 1, 11\n\t.else\n\tATT_TILE 0, \\b+12288, \\b+32768, 0\n\t.endif\n\t"
        "ATT_TILE 4, \\b+32768, \\b+36864, 0\n\t"
        ".endm\n\t"
        "v_add_u32 v176, 0x10000, v175\n\t"
        "s_mov_b32 s96, 4\n\t"
        "s_waitcnt lgkmcnt(0)\n\t"
        "ATT_TILE 0, 4096, 8192, 0\n\tATT_TILE 4, 8192, 12288, 0\n\tATT_TILE 0, 12288, 0, 0, 1, 0\n\tATT_TILE 4, 32768, 36864, 1, 7, 3\n\t"
        "ATT_STAGE 32768, 1\n\t"
        "s_branch .Latt_entry_%=\n\t"
        ".p2align 6\n"
        ".Latt_loop_%=:\n\t"
        "ATT_STAGE 0, 1\n\tATT_STAGE 32768, 0\n"
        ".Latt_entry_%=:\n\t"
        "ATT_STAGE 65536, 1\n\t"
        "s_sub_u32 s96, s96, 1\n\t"
        "ATT_STAGE 98304, 0\n\t"
        "s_cmp_lg_u32 s96, 0\n\t"
        "s_cbranch_scc1 .Latt_loop_%=\n\t"
        "s_nop 1\n\t"
        "v_mfma_f32_32x32x16_bf16 v[18:33], v[170:173], v[110:113], v[18:33]\n\t"
        "v_smfmac_f32_16x16x64_bf16 v[138:141], v[130:133], v[106:113], v174\n\t"
        "s_waitcnt lgkmcnt(0)\n\ts_nop 10\n\t"
        ".purgem ATT_RD\n\t.purgem ATT_E4\n\t.purgem ATT_C4\n\t.purgem ATT_STEP\n\t.purgem ATT_TILE\n\t.purgem ATT_STAGE"
        : "+{v[2:17]}"(oA), "+{v[18:33]}"(oB), "+{v[66:81]}"(sA), "+{v[106:113]}"(pB), "+{v[134:137]}"(lsA), "+{v[138:141]}"(lsB),
          "+{v[142:145]}"(k00), "+{v[146:149]}"(k01), "+{v[150:153]}"(k10), "+{v[154:157]}"(k11),
          "+{v[158:161]}"(v00), "+{v[162:165]}"(v01), "+{v[166:169]}"(v10), "+{v[170:173]}"(v11)
        : "{v[34:49]}"(nmA), "{v[50:65]}"(nmB), "{v[114:117]}"(qA0), "{v[118:121]}"(qA1), "{v[122:125]}"(qB0), "{v[126:129]}"(qB1),
          "{v[130:133]}"(sel), "{v174}"(sidx), "{v175}"(a0)
        : "memory", "scc", "s96", "v176", "v82", "v83", "v84", "v85", "v86", "v87", "v88", "v89", "v90", "v91", "v92", "v93", "v94", "v95",
          "v96", "v97", "v98", "v99", "v100", "v101", "v102", "v103", "v104", "v105");
    STAMP(9);
    float lA = lsA[0] + lsA[1], lB = lsB[0] + lsB[1];
    auto store = [&] {
#pragma unroll
    for (int u = 0; u < 2; ++u) {
        const float inv = __builtin_amdgcn_rcpf(u ? lB : lA), sub = -ob * inv;
        const f32x16 e = (u ? oB : oA) * inv + sub;
        st16o<0>((char*)ofrag + u * 65536, dst, u32x4{pack_bf16(e[0], e[1]), pack_bf16(e[2], e[3]), pack_bf16(e[8], e[9]), pack_bf16(e[10], e[11])});
        st16o<512>((char*)ofrag + u * 65536, dst, u32x4{pack_bf16(e[4], e[5]), pack_bf16(e[6], e[7]), pack_bf16(e[12], e[13]), pack_bf16(e[14], e[15])});
    }
    };
    store();
    {
        const f32x16 os = oA + oB;
        const float osum = (((os[0] + os[1]) + (os[2] + os[3])) + ((os[4] + os[5]) + (os[6] + os[7]))) +
                           (((os[8] + os[9]) + (os[10] + os[11])) + ((os[12] + os[13]) + (os[14] + os[15])));
        const bool bad = !(fmaxf(lA, lB) < 1.0e24f) || !(lA > 1.0e-24f) || !(lB > 1.0e-24f) || !(fabsf(osum) < 3.0e38f);
                if (__any(bad)) {
            const __amdgpu_buffer_rsrc_t rk = __builtin_amdgcn_make_buffer_rsrc((char*)kvf + (size_t)head * 262144, 0, 262144, 0x00020000);
            oA = z16; oB = z16; lA = 0.f; lB = 0.f; ob = 0.f;
            float mA = -INFINITY, mB = -INFINITY;
#pragma unroll 1
            for (int t = 0; t < 64; ++t) {
                bf16x8 f[4];
#pragma unroll
                for (int i = 0; i < 4; ++i) f[i] = __builtin_bit_cast(bf16x8, __builtin_amdgcn_raw_buffer_load_b128(rk, lane * 16 + i * 1024, t * 4096, 16));
                auto upd = [&](bf16x8 q0, bf16x8 q1, f32x16& o, float& l, float& m) {
                    f32x16 s = MFMA32(f[0], q0, z16); s = MFMA32(f[1], q1, s);
                    float tm = s[0];
#pragma unroll
                    for (int r = 1; r < 16; ++r) tm = fmaxf(tm, s[r]);
                    tm = max_h(tm);
                    const float mn = fmaxf(m, tm);
                    const float alpha = __builtin_amdgcn_exp2f(m - mn);
                    m = mn;
#pragma unroll
                    for (int r = 0; r < 16; ++r) { s[r] = __builtin_amdgcn_exp2f(s[r] - mn); o[r] *= alpha; }
                    const u32x4 p0 = {pack_bf16(s[0], s[1]), pack_bf16(s[2], s[3]), pack_bf16(s[4], s[5]), pack_bf16(s[6], s[7])};
                    const u32x4 p1 = {pack_bf16(s[8], s[9]), pack_bf16(s[10], s[11]), pack_bf16(s[12], s[13]), pack_bf16(s[14], s[15])};
                    f32x4 t4 = {0, 0, 0, 0};
                    o = MFMA32(f[2], __builtin_bit_cast(bf16x8, p0), o);
                    o = MFMA32(f[3], __builtin_bit_cast(bf16x8, p1), o);
                    t4 = ROWSUM(p0, p1, t4);
                    l = l * alpha + (t4[0] + t4[1]);
                };
                upd(qA0, qA1, oA, lA, mA);
                upd(qB0, qB1, oB, lB, mB);
            }
            store();
        }
    }
}

DI void ln_row(const u32x4 (&v)[2], __hip_bfloat16* __restrict__ Y, unsigned off) {
    float s1 = 0.f, s2 = 0.f;
#pragma unroll
    for (int e = 0; e < 8; ++e) s1 = dot2bf(v[e >> 2][e & 3], 0x3f803f80u, s1);
    const float mean = wave_sum(s1) * (1.f / 1024.f);
#pragma unroll
    for (int e = 0; e < 8; ++e) {
        const float d0 = bf_lo(v[e >> 2][e & 3]) - mean, d1 = bf_hi(v[e >> 2][e & 3]) - mean;
        s2 += d0 * d0 + d1 * d1;
    }
    const float mb = round_bf16(mean);
    const float rden = __builtin_amdgcn_rcpf(round_bf16(__builtin_amdgcn_sqrtf(round_bf16(round_bf16(wave_sum(s2) * (1.f / 1024.f)) + 1e-5f))));
#pragma unroll
    for (int hh = 0; hh < 2; ++hh) {
        u32x4 y;
#pragma unroll
        for (int e = 0; e < 4; ++e) {
            const unsigned d = pack_bf16(bf_lo(v[hh][e]) - mb, bf_hi(v[hh][e]) - mb);
            y[e] = pack_bf16(bf_lo(d) * rden, bf_hi(d) * rden);
        }
        st16((char*)Y + hh * 131072, off, y);
    }
}

extern "C" __global__ __launch_bounds__(512, 2) void mega_kernel(const __hip_bfloat16* x, const __hip_bfloat16* wqkvA,
                                                            const __hip_bfloat16* woA, const __hip_bfloat16* wqkvB,
                                                            const __hip_bfloat16* woB, __hip_bfloat16* out,
                                                            char* ws) {
    char* const smem = smem_base;
    typedef __hip_bfloat16 bf;
    const int w8 = __builtin_amdgcn_readfirstlane(threadIdx.x >> 6);
    if (w8 == 7 && (blockIdx.x >> 3) < 10) code_touch(blockIdx.x >> 3, threadIdx.x & 63);
    asm volatile("" :: "s"(x), "s"(wqkvA), "s"(woA), "s"(wqkvB), "s"(woB), "s"(out), "s"(ws));
    STAMP_DECL; STAMPR(14); STAMP(0);
    u64 tag;
    {
        const int lane = threadIdx.x & 63;
        const Ids I = ids();
        const unsigned off = (unsigned)((I.po * 128 + I.co * 8 + w8) * 2048 + lane * 16);
        u32x4 v[2];
        u32x2 ep;
        asm volatile("global_load_dwordx4 %0, %3, %4\n\tglobal_load_dwordx4 %1, %3, %4 offset:1024\n\tglobal_load_dwordx2 %2, %5, %6 sc1"
                     : "=&v"(v[0]), "=&v"(v[1]), "=&v"(ep) : "v"(off), "s"(x), "v"(0), "s"(ws + WS_EPOCH) : "memory");
        if (w8 == 7) {
            const int l = lane & 15, k = l % 5;
            const unsigned o = (l >= 10 || k == 3) ? flag_off(l, I.cq, I.pq, 0) : (k == 0 || k == 2) ? flag_off(l, I.pq, I.cq, 0) : flag_off(l, I.po, I.co, 0);
            touch_at(ws + WS_FLAG, o);
        } else if (w8 >= 4) touch(wqkvA, (w8 - 4) * 256 + blockIdx.x, lane);
        if (w8 >= 4) asm volatile("s_waitcnt vmcnt(2)" ::: "memory"); else asm volatile("s_waitcnt vmcnt(1)" ::: "memory");
        asm volatile("" : "+v"(v[0]), "+v"(v[1]));
        ln_row(v, (bf*)(ws + WS_Y0), (unsigned)(I.po * 262144 + (lane >> 3) * 16384 + (I.co * 8 + w8) * 128 + (lane & 7) * 16));
        if (w8 >= 4) asm volatile("s_waitcnt vmcnt(3)" ::: "memory"); else asm volatile("s_waitcnt vmcnt(2)" ::: "memory");
        asm volatile("" : "+v"(ep));
        tag = (((u64)(unsigned)__builtin_amdgcn_readfirstlane(ep[1]) << 32) | (unsigned)__builtin_amdgcn_readfirstlane(ep[0])) + 1;
    }
    if (w8 >= 4) {
        int w = w8 & 3;
#pragma unroll 1
        for (int it = 0; it < 2; ++it) {
            const int lane = lane_id();
            asm volatile("" : "+s"(x), "+s"(wqkvA), "+s"(woA), "+s"(wqkvB), "+s"(woB), "+s"(ws), "+s"(w));
            const unsigned lds0 = (unsigned)(uintptr_t)smem;
            u32x4 cb[8];
            const unsigned wv = wo_off(w, lane), rv = res_off(true, it, lane), cv = colsum_off(w, lane);
            qkv_loader<true>(smem, w, ws, it * 5, tag, (const bf*)(ws + (it ? WS_X2 : WS_Y0)), wqkvA, (const float*)(ws + WS_ST + 262144),
                             ws + WS_CSP, it == 0, woA, [&] {
                if (w < 3) {
                    colsum_load(cb, wqkvB, cv);
                    asm volatile("s_waitcnt vmcnt(8)" ::: "memory");
                } else {
                    touch(woA, blockIdx.x, lane);
                    asm volatile("s_waitcnt vmcnt(1)" ::: "memory");
                }
            }, [&](int part) {
                if (part == 0) wo_load(woA, wv, 29, 32, lds0, w); else wo_load(woA, wv, 25, 29, lds0, w);
            } SP_ARG);
            STAMP(3);
            u32x4 res[6];
            res_load(res, it ? (const void*)(ws + WS_X2) : x, true, it, rv, w);
            if (it) __builtin_amdgcn_s_sleep(8);
            wo_load(woA, wv, 0, 7, lds0, w);
            BAR();
            wo_load(woA, wv, 7, 25, lds0, w);
            STAMP(4);
            out_handoff(smem, 147456, res, w, ws, it * 5 + 1, tag, lane, cb, it ? -1 : 1);
            STAMP(5);
            qkv_loader<false>(smem, w, ws, it * 5 + 2, tag, (const bf*)(ws + (it ? WS_X3 : WS_X1)), wqkvB, (const float*)(ws + WS_ST + it * 524288), ws + WS_CSP + 196608,
                              false, nullptr, [] {}, [](int) {} SP_ARG);
            STAMP(8);
            attn_loader(smem, w, ws, it * 5 + 3, tag, (const bf*)(ws + WS_KVF + it * 8 * MBy), woB, ws + (it ? WS_X3 : WS_X1),
                        it ? nullptr : wqkvA SP_ARG);
            STAMP(13); STAMPR(15); STAMP_FLUSH(it);
        }
        if (blockIdx.x == 0 && w == 0 && lane_id() == 0) __hip_atomic_store((g_u64ptr)(ws + WS_EPOCH), tag, __ATOMIC_RELAXED, __HIP_MEMORY_SCOPE_AGENT);
    } else {
        __builtin_amdgcn_s_setprio(2);
#pragma unroll 1
        for (int it = 0; it < 2; ++it) {
            u32x4 q4[4];
            asm volatile("" : "+s"(ws), "+s"(wqkvA), "+s"(wqkvB));
            qkv_compute<true>(smem, w8, wqkvA, q4, nullptr, (bf*)(ws + WS_OF + it * 8 * MBy), it == 0,
                              ws + WS_ST + 262144, ws + WS_WN + blockIdx.x * 16 SP_ARG);
            STAMP(2);
            out_compute<true>(smem, w8, (const bf*)(ws + WS_OF + it * 8 * MBy),
                              (bf*)(ws + (it ? WS_X3 : WS_X1)), (float*)(ws + WS_ST + it * 524288) SP_ARG);
            STAMP(6);
            qkv_compute<false>(smem, w8, wqkvB, q4, (bf*)(ws + WS_KVF + it * 8 * MBy), nullptr, false,
                               ws + WS_ST + it * 524288, ws + WS_WN + 4096 + blockIdx.x * 16 SP_ARG);
            attn_compute(smem, w8, q4, (const bf*)(ws + WS_KVF + it * 8 * MBy), (bf*)(ws + WS_OF + (it * 8 + 4) * MBy) SP_ARG);
            out_compute<false>(smem, w8, (const bf*)(ws + WS_OF + (it * 8 + 4) * MBy),
                               it ? out : (bf*)(ws + WS_X2), it ? nullptr : (float*)(ws + WS_ST + 262144) SP_ARG);
            STAMP(13); STAMPR(15); STAMP_FLUSH(it);
        }
    }
}

extern "C" __global__ void zz_code_end_kernel() {}

extern "C" void kernel_launch(void* const* d_in, const int* in_sizes, int n_in, void* d_out, int out_size, void* d_ws,
                              size_t ws_size, hipStream_t stream) {
    typedef __hip_bfloat16 bf;
    (void)hipFuncSetAttribute((const void*)mega_kernel, hipFuncAttributeMaxDynamicSharedMemorySize, CH_LDS);
    mega_kernel<<<256, 512, CH_LDS, stream>>>((const bf*)d_in[0], (const bf*)d_in[1], (const bf*)d_in[2], (const bf*)d_in[4], (const bf*)d_in[5],
                                              (bf*)d_out, (char*)d_ws);
}
```

```cpp
#include <hip/hip_runtime.h>
#include <hip/hip_bf16.h>
#include <stdint.h>

typedef __attribute__((ext_vector_type(8))) short bf16x8;
typedef __attribute__((ext_vector_type(4))) short s16x4;
typedef __attribute__((ext_vector_type(4))) float f32x4;
typedef __attribute__((ext_vector_type(2))) float f32x2;
typedef __attribute__((ext_vector_type(16))) float f32x16;
typedef __attribute__((ext_vector_type(4))) unsigned u32x4;
typedef __attribute__((ext_vector_type(2))) unsigned u32x2;
typedef __attribute__((ext_vector_type(2))) __bf16 bf16x2_t;
typedef __attribute__((ext_vector_type(8))) __bf16 bf8_t;
typedef __attribute__((ext_vector_type(16))) __bf16 bf16v_t;
typedef __attribute__((address_space(3))) const char* lds_cptr;
typedef __attribute__((address_space(3))) char* lds_ptr;

#define DI __device__ __forceinline__
#define MFMA16(a, b, c) __builtin_amdgcn_mfma_f32_16x16x32_bf16((a), (b), (c), 0, 0, 0)
#define MFMA32(a, b, c) __builtin_amdgcn_mfma_f32_32x32x16_bf16((a), (b), (c), 0, 0, 0)
#define SBAR() __builtin_amdgcn_sched_barrier(0)
#define STAMP(i)
#define STAMPR(i)
#define STAMP_DECL
#define STAMP_FLUSH(it)
#define SP_PARAM
#define SP_ARG
typedef unsigned long long u64;
#define V3 0
#define ROT 1
#define WA 25
#define WS1 8
typedef __attribute__((ext_vector_type(32))) unsigned u32x32;

constexpr float QSCALE = 0.17677669529663687f * 1.4426950408889634f;
constexpr float ATT_SCALE = 0.17677669529663687f;
constexpr float LOG2E = 1.4426950408889634f;
constexpr float NEG_MASK = -1e9f / ATT_SCALE;

DI int lane_id() { int l; asm volatile("v_mbcnt_lo_u32_b32 %0, -1, 0\n\tv_mbcnt_hi_u32_b32 %0, -1, %0" : "=v"(l)); return l; }
DI unsigned pack_bf16(float lo, float hi) {
    f32x2 v = {lo, hi};
    bf16x2_t r = __builtin_convertvector(v, bf16x2_t);
    return __builtin_bit_cast(unsigned, r);
}
DI float bf_lo(unsigned u) { return __uint_as_float(u << 16); }
DI float bf_hi(unsigned u) { return __uint_as_float(u & 0xffff0000u); }
DI float round_bf16(float x) { return bf_lo(pack_bf16(x, 0.f)); }
DI void ln_stats(float sum, float sumsq, float& mean_out, float& rstd_out) {
    float mean;
    asm("v_mul_f32 %0, 0x3a800000, %1" : "=v"(mean) : "v"(sum));
    const float var = fmaxf(__builtin_fmaf(-mean, mean, sumsq * (1.f / 1024.f)), 0.f);
    mean_out = round_bf16(mean);
    rstd_out = __builtin_amdgcn_rcpf(round_bf16(__builtin_amdgcn_sqrtf(round_bf16(round_bf16(var) + 1e-5f))));
}

DI void glds16(const void* sbase, unsigned voff, unsigned ldsaddr) {
    asm volatile("s_mov_b32 m0, %0\n\ts_nop 0\n\tglobal_load_lds_dwordx4 %1, %2"
                 :: "s"(ldsaddr), "v"(voff), "s"(sbase) : "memory");
}
DI void glds16c(const void* sbase, unsigned voff, unsigned ldsaddr) {
    asm volatile("s_mov_b32 m0, %0\n\ts_nop 0\n\tglobal_load_lds_dwordx4 %1, %2 sc1"
                 :: "s"(ldsaddr), "v"(voff), "s"(sbase) : "memory");
}
DI void st16(void* sbase, unsigned voff, u32x4 v) {
    asm volatile("global_store_dwordx4 %0, %1, %2 sc1\n\ts_nop 1" :: "v"(voff), "v"(v), "s"(sbase) : "memory");
}
template <int OFF> DI void st16o(void* sbase, unsigned voff, u32x4 v) {
    asm volatile("global_store_dwordx4 %0, %1, %2 offset:%3 sc1\n\ts_nop 1" :: "v"(voff), "v"(v), "s"(sbase), "n"(OFF) : "memory");
}
DI bf16x8 lds_b128(lds_cptr p) { return *(const __attribute__((address_space(3))) bf16x8*)p; }
DI s16x4 lds_tr(lds_cptr p) {
    return __builtin_amdgcn_ds_read_tr16_b64_v4i16((__attribute__((address_space(3))) s16x4*)p);
}
DI bf16x8 cat8(s16x4 a, s16x4 b) { return __builtin_shufflevector(a, b, 0, 1, 2, 3, 4, 5, 6, 7); }
DI float dot2bf(unsigned a, unsigned b, float c) {
    return __builtin_amdgcn_fdot2_f32_bf16(__builtin_bit_cast(bf16x2_t, a), __builtin_bit_cast(bf16x2_t, b), c, false);
}
DI float sum_g(float v) {
    auto a = __builtin_amdgcn_permlane16_swap(__float_as_uint(v), __float_as_uint(v), false, false);
    v = __uint_as_float(a[0]) + __uint_as_float(a[1]);
    auto b = __builtin_amdgcn_permlane32_swap(__float_as_uint(v), __float_as_uint(v), false, false);
    return __uint_as_float(b[0]) + __uint_as_float(b[1]);
}
DI float wave_sum(float v) {
    v += __builtin_bit_cast(float, __builtin_amdgcn_update_dpp(0, __builtin_bit_cast(int, v), 0xB1, 0xF, 0xF, true));
    v += __builtin_bit_cast(float, __builtin_amdgcn_update_dpp(0, __builtin_bit_cast(int, v), 0x4E, 0xF, 0xF, true));
    v += __builtin_bit_cast(float, __builtin_amdgcn_update_dpp(0, __builtin_bit_cast(int, v), 0x141, 0xF, 0xF, true));
    v += __builtin_bit_cast(float, __builtin_amdgcn_update_dpp(0, __builtin_bit_cast(int, v), 0x140, 0xF, 0xF, true));
    return sum_g(v);
}
DI unsigned wave_min(unsigned v) {
    v = min(v, (unsigned)__builtin_amdgcn_update_dpp(0, (int)v, 0xB1, 0xF, 0xF, true));
    v = min(v, (unsigned)__builtin_amdgcn_update_dpp(0, (int)v, 0x4E, 0xF, 0xF, true));
    v = min(v, (unsigned)__builtin_amdgcn_update_dpp(0, (int)v, 0x141, 0xF, 0xF, true));
    v = min(v, (unsigned)__builtin_amdgcn_update_dpp(0, (int)v, 0x140, 0xF, 0xF, true));
    const auto a = __builtin_amdgcn_permlane16_swap(v, v, false, false);
    v = min(a[0], a[1]);
    const auto b = __builtin_amdgcn_permlane32_swap(v, v, false, false);
    return min(b[0], b[1]);
}
DI float amax3(float a, float b, float c) { float r; asm("v_max3_f32 %0, %1, %2, %3" : "=v"(r) : "v"(a), "v"(b), "v"(c)); return r; }
DI float max2f(float a, float b) { return __builtin_amdgcn_fmed3f(a, b, INFINITY); }
DI float max_h(float v) {
    auto b = __builtin_amdgcn_permlane32_swap(__float_as_uint(v), __float_as_uint(v), false, false);
    return max2f(__uint_as_float(b[0]), __uint_as_float(b[1]));
}

constexpr unsigned OFF_DUMP = 145408;
extern __shared__ __attribute__((aligned(16))) char smem_base[];
DI void touch_at(const void* p, unsigned off) {
    asm volatile("s_mov_b32 m0, %0\n\ts_nop 0\n\tglobal_load_lds_dword %1, %2"
                 :: "s"((unsigned)(uintptr_t)smem_base + OFF_DUMP), "v"(off), "s"(p) : "memory");
}
DI void touch(const void* p, int slice, int lane) { touch_at(p, (unsigned)(slice * 64 + lane) * 128u); }
DI void code_touch(int slice, int lane) {
    unsigned long long c0, c1;
    asm volatile("s_getpc_b64 s[96:97]\n\ts_add_u32 s96, s96, mega_kernel@rel32@lo+4\n\ts_addc_u32 s97, s97, mega_kernel@rel32@hi+12\n\ts_mov_b64 %0, s[96:97]\n\t"
                 "s_getpc_b64 s[96:97]\n\ts_add_u32 s96, s96, zz_code_end_kernel@rel32@lo+4\n\ts_addc_u32 s97, s97, zz_code_end_kernel@rel32@hi+12\n\ts_mov_b64 %1, s[96:97]"
                 : "=s"(c0), "=s"(c1) :: "s96", "s97", "scc");
    touch_at((const void*)c0, min((unsigned)(slice * 64 + lane) * 128u, (unsigned)(c1 - c0)));
}

#define BAR() asm volatile("s_barrier" ::: "memory")
#define WAIT_LGKM() do { SBAR(); __builtin_amdgcn_s_waitcnt(0xC07F); SBAR(); } while (0)
#define WAIT_LGKM_BAR() do { SBAR(); __builtin_amdgcn_s_waitcnt(0xC07F); asm volatile("s_barrier" ::: "memory"); SBAR(); } while (0)
#define VM_BAR(n) asm volatile("s_waitcnt vmcnt(" #n ")\n\ts_barrier" ::: "memory")
constexpr int CH_LDS = 163840;
typedef __attribute__((address_space(3))) unsigned* lds_uptr;
typedef __attribute__((address_space(1))) u64* g_u64ptr;

constexpr size_t MBy = 1 << 20;
constexpr size_t WS_X3 = 0, WS_X2 = 4 * MBy, WS_X1 = 8 * MBy, WS_OF = 12 * MBy  , WS_KVF = 36 * MBy  ,
                 WS_ST = 52 * MBy  , WS_CSP = 54 * MBy  , WS_FLAG = 55 * MBy  ,
                 WS_WN = WS_CSP + 458752  , WS_EPOCH = WS_CSP + 524288, WS_Y0 = 56 * MBy;
DI unsigned flag_off(int set, int group, int consumer, int producer) { return (unsigned)(set * 65536 + group * 4096 + consumer * 256 + producer * 8); }
DI void flag_store(char* ws, int set, int group, int producer, u64 tag, int lane) {
    asm volatile("" : "+s"(tag));
    if (lane < 16) __hip_atomic_store((g_u64ptr)(ws + WS_FLAG + flag_off(set, group, lane, producer)), tag, __ATOMIC_RELAXED, __HIP_MEMORY_SCOPE_AGENT);
}
DI void flag_poll(const char* ws, unsigned off, u64 tag) {
    asm volatile("" : "+s"(tag));
#pragma unroll 1
    for (int spin = 0; spin < (1 << 16); ++spin) {
        const u64 v = __hip_atomic_load((g_u64ptr)(ws + WS_FLAG + off), __ATOMIC_RELAXED, __HIP_MEMORY_SCOPE_AGENT);
        if (__all(v == tag)) break;
        __builtin_amdgcn_s_sleep(1);
    }
    asm volatile("" ::: "memory");
}
template <bool DRAIN, bool ALLPOLL>
DI void signal_poll(int follower, const char* ws, unsigned soff, unsigned poff, u64 pmask, u64 tag, u64& land) {
    u64 tagv, zero;
    asm volatile("s_not_b64 s[96:97], %[ts]\n\tv_mov_b64 %[tag], %[ts]\n\tv_mov_b64 %[zero], 0\n\tv_mov_b64 %[land], s[96:97]\n\t"
                 ".if %[dr]\n\ts_waitcnt vmcnt(0)\n\t.endif\n\ts_barrier\n\t"
                 "s_cmp_lg_u32 %[fl], 0\n\ts_cbranch_scc1 .Lsig_fol_%=\n\t"
                 "s_mov_b64 exec, 0xffff\n\t"
                 "global_store_dwordx2 %[so], %[tag], %[base] sc1\n"
                 ".if %[ap]\n.Lsig_fol_%=:\n.endif\n\t"
                 "s_mov_b64 exec, %[pm]\n\t"
                 "s_movk_i32 s96, 0x7fff\n"
                 ".Lsig_loop_%=:\n\t"
                 "global_atomic_or_x2 %[land], %[po], %[zero], %[base] sc0 sc1\n\t"
                 "s_sleep 1\n\t"
                 "s_waitcnt vmcnt(3)\n\t"
                 "v_cmp_ne_u64 vcc, %[land], %[tag]\n\t"
                 "s_sub_u32 s96, s96, 1\n\t"
                 "s_cbranch_scc1 .Lsig_out_%=\n\t"
                 "s_cbranch_vccnz .Lsig_loop_%=\n"
                 ".Lsig_out_%=:\n\t"
                 "s_mov_b64 exec, -1\n"
                 ".if !%[ap]\n.Lsig_fol_%=:\n.endif"
                 : [land] "=&v"(land), [tag] "=&v"(tagv), [zero] "=&v"(zero)
                 : [fl] "s"(follower), [so] "v"(soff), [po] "v"(poff), [ts] "s"(tag), [base] "s"(ws + WS_FLAG), [pm] "s"(pmask), [ap] "n"(ALLPOLL ? 1 : 0), [dr] "n"(DRAIN ? 1 : 0)
                 : "memory", "scc", "vcc", "s96", "s97");
}
struct Ids { int pq, cq, po, co; };
DI Ids ids() {
    int bid = blockIdx.x; asm volatile("" : "+s"(bid));
    const int xcd = bid & 7, idx = bid >> 3;
    int pq, cq, po, co;
#define IDS_BODY pq = (xcd & 1) * 8 + (idx & 7); cq = (xcd >> 1) * 4 + (idx >> 3); po = (xcd & 3) * 4 + (idx & 3); co = (xcd >> 2) * 8 + (idx >> 2);
    IDS_BODY
    return Ids{pq, cq, po, co};
}

DI void slow_warm(unsigned dump, const void* W, int w, int lane) {
    int bid = blockIdx.x; asm volatile("" : "+s"(bid));
    const int xcd = bid & 7, idx = bid >> 3;
    const int sc = (3 - (int)((uintptr_t)W >> 7)) & 7;
    if ((sc >> 2) != ((xcd >> 1) & 1)) return;
    const char* src = (const char*)W + (idx + 32 * w) * 49152 + (sc + (xcd >> 2) * 8) * 128;
#pragma unroll
    for (int G = 0; G < 3; ++G) glds16(src + G * 2048, (unsigned)((lane >> 3) * 6144 + (lane & 7) * 16), dump);
}

template <int V> struct IC { static constexpr int value = V; };
template <bool LOCAL> struct QC {
    static constexpr int AROWS = LOCAL ? 144 : 128;
    static constexpr int ABYTES = AROWS * 128;
    static constexpr int STAGE = ABYTES + 24576;
    static constexpr int OFF_MEAN = 143360;
    static constexpr int OFF_RSTD = OFF_MEAN + 576;
    static constexpr int OFF_CS = OFF_RSTD + 576;
};
constexpr int OFF_EX = 131072;

DI unsigned colsum_off(int G, int lane) {
    const Ids I = ids();
    unsigned off = (unsigned)(I.pq * 64 + (lane >> 3)) * 6144u + (unsigned)(G * 2048 + I.cq * 128 + (lane & 7) * 16);
    asm volatile("" : "+v"(off));
    return off;
}
DI void colsum_load(u32x4 (&c)[8], const void* W, unsigned off) {
#define CS_NEXT "s_add_u32 s96, s96, 0xc000\n\ts_addc_u32 s97, s97, 0\n\t"
    asm volatile("s_cmp_eq_u64 %9, 0\n\ts_cbranch_scc1 .Lcs_skip_%=\n\ts_mov_b64 s[96:97], %9\n\t"
                 "global_load_dwordx4 %0, %8, s[96:97]\n\t" CS_NEXT "global_load_dwordx4 %1, %8, s[96:97]\n\t" CS_NEXT
                 "global_load_dwordx4 %2, %8, s[96:97]\n\t" CS_NEXT "global_load_dwordx4 %3, %8, s[96:97]\n\t" CS_NEXT
                 "global_load_dwordx4 %4, %8, s[96:97]\n\t" CS_NEXT "global_load_dwordx4 %5, %8, s[96:97]\n\t" CS_NEXT
                 "global_load_dwordx4 %6, %8, s[96:97]\n\t" CS_NEXT "global_load_dwordx4 %7, %8, s[96:97]\n"
                 ".Lcs_skip_%=:"
                 : "=&v"(c[0]), "=&v"(c[1]), "=&v"(c[2]), "=&v"(c[3]), "=&v"(c[4]), "=&v"(c[5]), "=&v"(c[6]), "=&v"(c[7])
                 : "v"(off), "s"(W) : "memory", "scc", "s96", "s97");
}
DI void colsum_store(u32x4 (&c)[8], char* ws, int mat, int G, int lane) {
    asm volatile("" : "+v"(c[0]), "+v"(c[1]), "+v"(c[2]), "+v"(c[3]), "+v"(c[4]), "+v"(c[5]), "+v"(c[6]), "+v"(c[7]));
    const Ids I = ids();
    const int m = lane & 15;
    const unsigned one = (m & 1) ? 0xbf800000u : 0xbf80u;
    const u32x4 sl = {(m >> 1) == 0 ? one : 0u, (m >> 1) == 1 ? one : 0u, (m >> 1) == 2 ? one : 0u, (m >> 1) == 3 ? one : 0u};
    f32x4 d = {0, 0, 0, 0};
#pragma unroll
    for (int i = 0; i < 8; ++i) d = MFMA16(__builtin_bit_cast(bf16x8, sl), __builtin_bit_cast(bf16x8, c[i]), d);
#pragma unroll
    for (int r = 0; r < 4; ++r)
        d[r] += __int_as_float(__builtin_amdgcn_update_dpp(0, __float_as_int(d[r]), 0x128, 0xF, 0xF, true));
    if (lane < 32 && (lane & 8) == 0)
        st16(ws + WS_CSP + mat * 196608, (unsigned)((I.cq * 16 + I.pq) * 768 + G * 256 + (lane & 7) * 32 + (lane >> 4) * 16), __builtin_bit_cast(u32x4, d));
    typedef __attribute__((ext_vector_type(2))) unsigned short u16x2;
    u16x2 mn = {0x7fff, 0x7fff};
#pragma unroll
    for (int e = 0; e < 4; ++e) {
        u16x2 mx = {0, 0};
#pragma unroll
        for (int i = 0; i < 8; ++i) { const unsigned u = c[i][e] & 0x7fff7fffu; mx = __builtin_elementwise_max(mx, __builtin_bit_cast(u16x2, u)); }
        mn = __builtin_elementwise_min(mn, mx);
    }
    const unsigned wn = wave_min((unsigned)min(mn[0], mn[1])) << 16;
    if (lane == 0) __hip_atomic_store((__attribute__((address_space(1))) unsigned*)(ws + WS_WN + mat * 4096 + blockIdx.x * 16 + G * 4), wn,
                                      __ATOMIC_RELAXED, __HIP_MEMORY_SCOPE_AGENT);

}

template <bool LOCAL>
struct WFirst {
    unsigned v[2];
    DI WFirst(int w, int ct, int lane) {
#pragma unroll
        for (int i = 0; i < 2; ++i) {
            const int k = 8 * i + (lane >> 3), pos = lane & 7;
            v[i] = (unsigned)k * 6144u + (unsigned)(w * 1024 + ct * 64 + ((pos >> 1) ^ (((k >> 1) & 1) | (i << 1))) * 16 + (pos & 1) * 8) * 2u;
        }
        asm volatile("" : "+v"(v[0]), "+v"(v[1]));
    }
    DI void issue(const void* W, unsigned lds0, int w, int i0, int i1) const {
#pragma unroll
        for (int i = i0; i < i1; ++i)
            glds16((const char*)W + (size_t)(i >> 1) * (16 * 6144), v[i & 1], lds0 + (i >> 3) * QC<LOCAL>::STAGE + QC<LOCAL>::ABYTES + (w * 8 + (i & 7)) * 1024);
    }
};

template <bool LOCAL, class F, class F2>
DI void qkv_loader(char* smem, int w, char* ws, int set, u64 tag, const __hip_bfloat16* __restrict__ X, const __hip_bfloat16* __restrict__ W,
                   const float* __restrict__ statp, const char* __restrict__ csp, bool ident, const void* nextW, F tail, F2 mid SP_PARAM) {
    typedef QC<LOCAL> C;
    constexpr int NT = 16;
    const int lane = lane_id();
    asm volatile("" : "+s"(w));
    const int idx = blockIdx.x >> 3;
    const Ids I = ids();
    const int panel = I.pq, ct = I.cq;
    const int R0 = panel * 128;
    const unsigned lds0 = (unsigned)(uintptr_t)smem;
    auto grow = [&](int i) -> int {
        if (!LOCAL) return R0 + i;
        if (i < 128) { const int t = (i >> 4) & 3, a = i & 15; return R0 + (i & 64) + 2 * (16 * (t & 1) + a) + (t >> 1); }
        const int a = i & 15; const int r = R0 - 16 + 2 * (a & 7) + (a >> 3);
        return r < 0 ? 0 : r;
    };
    unsigned voffA[5], voffB[6];
#pragma unroll
    for (int i = 0; i < 6; ++i) {
        const int j = w + 4 * i, G = j >> 3, k = 8 * (j & 7) + (lane >> 3);
        const int pos = lane & 7, f = ((k >> 1) & 1) | (((k >> 3) & 1) << 1);
        const int seg = (pos >> 1) ^ f;
        voffB[i] = (unsigned)k * 6144u + (unsigned)(G * 1024 + ct * 64 + seg * 16 + (pos & 1) * 8) * 2u;
    }
    auto issueW = [&](int t, int buf) {
        const char* wb = (const char*)W + (size_t)t * (64 * 6144);
        const unsigned sb = lds0 + buf * C::STAGE;
#pragma unroll
        for (int i = 0; i < 6; ++i) glds16(wb, voffB[i], sb + C::ABYTES + (w + 4 * i) * 1024);
    };
    asm volatile("" : "+v"(voffB[0]), "+v"(voffB[1]), "+v"(voffB[2]), "+v"(voffB[3]), "+v"(voffB[4]), "+v"(voffB[5]));
    const WFirst<LOCAL> first(w, ct, lane);
    const int g = lane >> 4, r = lane & 15, pos = 32 * w + 2 * r + (g >> 1);
    const int lr = !LOCAL ? (pos & 64) + 2 * (pos & 31) + ((pos >> 5) & 1)
                          : (w == 0 && (g & 1)) ? 128 + (r & 7) + 8 * (g >> 1) : (pos & 64) + ((pos & 1) * 2 + ((pos >> 5) & 1)) * 16 + ((pos >> 1) & 15);
    unsigned so = (unsigned)(panel * 16384 + g * 1024 + w * 256 + r * 16), ho = (unsigned)(g * 1024 + (panel ? panel * 16384 - 15488 + (r & 7) * 16 : 0));
    unsigned co = (unsigned)((ct * 16 + g) * 768 + w * 192 + r * 12);
    lds_ptr mp = (lds_ptr)smem + C::OFF_MEAN + lr * 4, cp = (lds_ptr)smem + C::OFF_CS + w * 192 + r * 12 + ((g & 1) ? 8 : g * 2);
    unsigned fo = flag_off((LOCAL ? 13 : 10) + min(g, 2), ct, panel, r);
    asm volatile("" : "+v"(so), "+v"(ho), "+v"(co), "+v"(mp), "+v"(cp), "+v"(fo));
#pragma unroll
    for (int i = 0; i < 5; ++i) {
        const int j = (i < 4) ? (w + 4 * i) : (16 + (w & 1));
        const int row = 8 * j + (lane >> 3);
        const int c = (lane & 7) ^ ((row >> 1) & 7);
        const int gr = grow(row < C::AROWS ? row : 0);
        voffA[i] = (unsigned)((gr >> 7) * 262144 + (gr & 127) * 128 + c * 16);
        asm volatile("" : "+v"(voffA[i]));
    }
    auto issueA = [&](int t, int buf) {
        const char* xa = (const char*)X + t * 16384;
        const unsigned sb = lds0 + buf * C::STAGE;
        if (LOCAL && w < 2) glds16c(xa, voffA[4], sb + (16 + w) * 1024);
#pragma unroll
        for (int i = 0; i < 4; ++i) glds16c(xa, voffA[i], sb + (w + 4 * i) * 1024);
    };
    u64 land;
    if (w == 3) __builtin_amdgcn_s_setprio(3);
    signal_poll<LOCAL, false>(w - 3, ws, flag_off(set, I.po, lane & 15, I.co),
                              flag_off(set, (lane & 16) && panel ? panel - 1 : panel, ct, lane & 15),
                              !LOCAL ? 0xffffull : ident ? 0xc000ffffull : 0xffffffffull, tag, land);
    if (w < 3) first.issue(W, lds0, w, 0, 12);
    BAR();
    STAMP(LOCAL ? 1 : 6);
    issueA(0, 0);
    issueA(1, 1);
    if (w < 3) { first.issue(W, lds0, w, 12, 16); asm volatile("s_waitcnt vmcnt(8)" ::: "memory"); }
    else asm volatile("s_waitcnt vmcnt(4)" ::: "memory");
    BAR();
    asm volatile("" :: "v"(land));
    __builtin_amdgcn_s_setprio(0);
    STAMP(LOCAL ? 2 : 7);
    issueW(2, 2);
    issueA(2, 2);
    VM_BAR(10);
    asm volatile("global_load_dwordx2 %0, %1, %2 sc1" : "=&v"(land) : "v"(fo), "s"(ws + WS_FLAG) : "memory");
    issueA(3, 0); issueW(3, 0);
    int buf = 1;
#pragma unroll 1
    for (int t = 1; t < NT - 3; ++t) {
        VM_BAR(10);
        issueA(t + 3, buf); issueW(t + 3, buf);
        if (t == 10 && idx < 3) code_touch(idx * 4 + w, lane);
        if (LOCAL && t == 11)
            glds16(nextW, (unsigned)((idx + 32 * w) * 8 + (lane >> 3)) * 2048u +
                   (unsigned)(((I.co & 8) + ((3 - (int)((uintptr_t)nextW >> 7)) & 7)) * 128 + (lane & 7) * 16), lds0 + OFF_DUMP);
        buf = (buf == 2) ? 0 : buf + 1;
    }
    typedef __attribute__((ext_vector_type(3))) float f32x3;
    const bool halo = LOCAL && w == 0;
    asm volatile("" : "+v"(land));
    if (!ident) {
        if (!__all(land == tag)) flag_poll(ws, fo, tag);
        f32x3 cs[4];
        f32x4 sv[4], hv[4];
        typedef __attribute__((address_space(3))) float* lds_f;
        auto add = [](float a, float b) { float r; asm("v_add_f32 %0, %1, %2" : "=v"(r) : "v"(a), "v"(b)); return r; };
        auto fold32 = [](float a, float b) { asm("s_nop 1\n\tv_permlane32_swap_b32 %0, %1\n\tv_add_f32 %0, %0, %1" : "+v"(a), "+v"(b)); return a; };
        auto fold16 = [](float a, float b) { asm("s_nop 1\n\tv_permlane16_swap_b32 %0, %1\n\tv_add_f32 %0, %0, %1" : "+v"(a), "+v"(b)); return a; };
        auto sum4 = [&](auto (&v)[4], int e) { return add(add(v[0][e], v[1][e]), add(v[2][e], v[3][e])); };
#pragma unroll
        for (int i = 0; i < 4; ++i) asm volatile("global_load_dwordx3 %0, %1, %2 sc1" : "=&v"(cs[i]) : "v"(co), "s"(csp + i * 3072) : "memory");
        VM_BAR(14);
#pragma unroll
        for (int i = 0; i < 4; ++i) asm volatile("global_load_dwordx4 %0, %1, %2 sc1" : "=&v"(sv[i]) : "v"(so), "s"((const char*)statp + i * 4096) : "memory");
        if (LOCAL)
            asm volatile("s_cmp_lg_u32 %9, 0\n\ts_cbranch_scc1 .Lhv_skip_%=\n\t"
                         "global_load_dwordx4 %0, %4, %5 sc1\n\tglobal_load_dwordx4 %1, %4, %6 sc1\n\t"
                         "global_load_dwordx4 %2, %4, %7 sc1\n\tglobal_load_dwordx4 %3, %4, %8 sc1\n"
                         ".Lhv_skip_%=:" : "=&v"(hv[0]), "=&v"(hv[1]), "=&v"(hv[2]), "=&v"(hv[3])
                         : "v"(ho), "s"(statp), "s"(statp + 1024), "s"(statp + 2048), "s"(statp + 3072), "s"(w) : "memory", "scc");
        if (halo) asm volatile("s_waitcnt vmcnt(8)" ::: "memory"); else asm volatile("s_waitcnt vmcnt(4)" ::: "memory");
        asm volatile("" : "+v"(cs[0]), "+v"(cs[1]), "+v"(cs[2]), "+v"(cs[3]));
        const float c2 = sum4(cs, 2);
        *(lds_f)cp = fold16(fold32(sum4(cs, 0), sum4(cs, 1)), fold32(c2, c2));
        BAR();
        asm volatile("s_waitcnt vmcnt(0)" : "+v"(sv[0]), "+v"(sv[1]), "+v"(sv[2]), "+v"(sv[3]) :: "memory");
        mid(0);
        const float s1 = fold32(sum4(sv, 0), sum4(sv, 2)), s2 = fold32(sum4(sv, 1), sum4(sv, 3));
        float h1, h2;
        asm("v_mov_b32 %0, %2\n\tv_mov_b32 %1, %3" : "=&v"(h1), "=&v"(h2) : "v"(s1), "v"(s2));
        if (halo) {
            asm volatile("" : "+v"(hv[0]), "+v"(hv[1]), "+v"(hv[2]), "+v"(hv[3]));
            const float u0 = sum4(hv, 0), u1 = sum4(hv, 1), u2 = sum4(hv, 2), u3 = sum4(hv, 3);
            h1 = fold32(u0, panel ? u2 : u0); h2 = fold32(u1, panel ? u3 : u1);
        }
        float mean, rstd;
        ln_stats(fold16(s1, h1), fold16(s2, h2), mean, rstd);
        *(lds_f)mp = mean; *(lds_f)(mp + (C::OFF_RSTD - C::OFF_MEAN)) = rstd;
    } else { VM_BAR(10); tail(); BAR(); mid(0); }
    mid(1);
    WAIT_LGKM_BAR();
}

template <bool LOCAL>
DI void qkv_compute(char* smem, int w, const void* W, u32x4 (&qo4)[4], __hip_bfloat16* __restrict__ kvf, __hip_bfloat16* __restrict__ ofrag, bool ident,
                    const char* statp, const char* wnp SP_PARAM) {
    typedef QC<LOCAL> C;
    constexpr int NT = 16, ABYTES = C::ABYTES, STAGE = C::STAGE;
    const int lane = lane_id();
    asm volatile("" : "+s"(w));
    const int wm = w & 1, wn = w >> 1;
    const Ids I = ids();
    const int panel = I.pq, ct = I.cq;
    const int l15 = lane & 15, g = lane >> 4;
    const lds_cptr sm = (lds_cptr)smem;
    lds_ptr smw = (lds_ptr)smem;

    f32x4 acc[3][4][2];
    f32x4 acch[2];
    float cb;
    f32x4 bias;

    bf16x8 A[2][5][2];
    s16x4 B[3][2][2];
    lds_cptr pa[2], pb[2];
    auto rdA = [&](auto WM, int p, int i) { A[p][i >> 1][i & 1] = lds_b128(pa[i & 1] + ((i >> 1) < 4 ? (i >> 1) * 2048 : 16384 - WM.value * 8192)); };
    auto rdB = [&](int c, int i) { B[c % 3][i >> 1][i & 1] = lds_tr(pb[c & 1] + ((c >> 1) + 1) % 3 * 8192 + (i >> 1) * 4096 + (i & 1) * 512); };
    auto column = [&](auto WM, int p, int c, bool first, auto gap) {
        const int G = ((c >> 1) + 1) % 3, f = c & 1;
        const bool halo = LOCAL && G == 1 + WM.value;
#pragma unroll
        for (int n = 0; n < (halo ? 10 : 8); ++n) {
            const bf16x8 x = A[p][n >> 1][n & 1], y = cat8(B[c % 3][n & 1][0], B[c % 3][n & 1][1]);
            const bf16x8 u = G < 2 ? y : x, v = G < 2 ? x : y;
            f32x4& d = n < 8 ? acc[G][n >> 1][f] : acch[f];
            const f32x4 c0 = bias;
            if (!first || (n & 1)) asm volatile("v_mfma_f32_16x16x32_bf16 %0, %1, %2, %0" : "+v"(d) : "v"(u), "v"(v));
            else if (n == 8) asm volatile("v_mfma_f32_16x16x32_bf16 %0, %1, %2, 0" : "=&v"(d) : "v"(u), "v"(v));
            else asm volatile("v_mfma_f32_16x16x32_bf16 %0, %1, %2, %3" : "=&v"(d) : "v"(u), "v"(v), "v"(c0));
            SBAR();
            gap(n);
            SBAR();
        }
    };

    f32x4 cq[2], ck[2], mu4[5], rs4[5]; float cv[2], mu[5], rs[5];
    u32x4 qfr[4], kfr[4];
    u32x4 va[2][2];
    f32x4 mwin, mfirst;
    typedef __attribute__((address_space(3))) u32x4* lds_u4;
    const bool negpos = (wm == 0) && (panel == 0);
    VM_BAR(0);
    unsigned cm01 = 0, cm23 = 0;
    lds_cptr ka[2] = {sm, sm}, vo2[2] = {sm, sm};
    lds_u4 ex = (lds_u4)(smw + OFF_EX + wn * 6144) + lane;
    lds_cptr s1 = sm + C::OFF_MEAN + wm * 256 + l15 * 4, s4 = sm + C::OFF_MEAN + wm * 256 + g * 16;
    lds_cptr c1 = sm + C::OFF_CS + wn * 128 + l15 * 4, c4 = sm + C::OFF_CS + wn * 128 + g * 16;
    asm volatile("" : "+v"(s1), "+v"(s4), "+v"(c1), "+v"(c4));
    if (LOCAL) {
        unsigned keepv = negpos ? __float_as_uint(NEG_MASK) : 0u;
        asm volatile("" : "+s"(keepv));
        u32x4 mw, mf;
        unsigned prv[4];
#pragma unroll
        for (int r = 0; r < 4; ++r) {
            const int d = l15 - (4 * g + r);
            unsigned out = (unsigned)(((d << 28) >> 31));
            prv[r] = (unsigned)(d >> 31);
            asm volatile("" : "+v"(out), "+v"(prv[r]));
            mw[r] = out & 0xff800000u;
            mf[r] = (out & 0xff800000u) | (~out & keepv);
        }
        const lds_cptr e0 = sm + OFF_EX + wn * 6144;
        ka[0] = e0 + (wm ? lane : 256 + (lane ^ 8)) * 16;         ka[1] = e0 + (wm ? 64 + lane : 256 + lane) * 16;
        vo2[0] = e0 + (wm ? 128 + lane : 320 + (lane ^ 32)) * 16; vo2[1] = e0 + (wm ? 192 + lane : 320 + lane) * 16;
        cm01 = ~((prv[0] >> 16) | (prv[1] << 16)); cm23 = ~((prv[2] >> 16) | (prv[3] << 16));
        mwin = __builtin_bit_cast(f32x4, mw); mfirst = __builtin_bit_cast(f32x4, mf);
        asm volatile("" : "+v"(mwin), "+v"(mfirst), "+v"(cm01), "+v"(cm23), "+v"(ka[0]), "+v"(ka[1]), "+v"(vo2[0]), "+v"(vo2[1]), "+v"(ex));
    }
    typedef const __attribute__((address_space(3))) f32x4* lds_f4; typedef const __attribute__((address_space(3))) float* lds_f1;
    auto mainloop = [&](auto WM) {
        int ty = lane_id();
        const int l15 = ty & 15, g = ty >> 4;
        const int swA = (l15 >> 1) & 7;
        const int q4 = l15 >> 2, p4 = l15 & 3, fk = ((q4 >> 1) & 1) | ((g & 1) << 1);
        lds_cptr pA0 = sm + wm * 8192 + l15 * 128 + ((0 + g) ^ swA) * 16, pA1 = sm + wm * 8192 + l15 * 128 + ((4 + g) ^ swA) * 16;
        lds_cptr pB0 = sm + ABYTES + (8 * g + q4) * 128 + (((2 * wn + 0) ^ fk) * 32) + p4 * 8;
        lds_cptr pB1 = sm + ABYTES + (8 * g + q4) * 128 + (((2 * wn + 1) ^ fk) * 32) + p4 * 8;
        asm volatile("" : "+v"(pA0), "+v"(pA1), "+v"(pB0), "+v"(pB1));
        slow_warm((unsigned)(uintptr_t)smem + OFF_DUMP, W, w, ty);
        unsigned so = (unsigned)(panel * 16384 + wm * 512 + ty * 8);
        const char *p0 = ident ? (const char*)W : statp, *p1 = ident ? (const char*)W : wnp;
        u32x4 wn;
        asm volatile("global_load_dwordx4 %0, %2, %3 sc1" : "=&v"(wn), "+v"(so) : "v"(0), "s"(p1) : "memory");
        BAR();
        STAMP(LOCAL ? 1 : 7);
        f32x2 sq;
        asm volatile("global_load_dwordx2 %0, %1, %2 sc1" : "=&v"(sq) : "v"(so), "s"(p0) : "memory");
        BAR();
        pa[0] = pA0; pa[1] = pA1; pb[0] = pB0; pb[1] = pB1;
#pragma unroll
        for (int i = 0; i < 4; ++i) rdB(0, i);
#pragma unroll
        for (int i = 0; i < 8; ++i) rdA(WM, 0, i);
#pragma unroll
        for (int i = 0; i < 4; ++i) if (LOCAL && WM.value == 0 && i >= 2) rdA(WM, 0, 6 + i); else rdB(1, i);
        SBAR();
        asm volatile("s_waitcnt vmcnt(0)" : "+v"(sq), "+v"(wn) :: "memory");
        const float d = fmaxf(__builtin_fmaf(sq[0] * (-1.f / 64.f), sq[0], sq[1]), 0.f);
        cb = fminf(__builtin_amdgcn_sqrtf(__uint_as_float(wave_min(__float_as_uint(d)))) * 64.f * __uint_as_float(min(min(wn[0], wn[1]), wn[2])), 1.0e30f);
        if (ident) cb = 0.f;
        bias = f32x4{cb, cb, cb, cb};
        asm volatile("s_nop 1" : "+v"(bias));
        WAIT_LGKM();
        int off = 0;
        constexpr bool HK = LOCAL && WM.value == 0;
        auto front = [&](int p, bool first) {
#pragma unroll
            for (int c = 0; c < 4; ++c)
                column(WM, p, c, first, [&](int n) {
                    if (HK && c == 0) { if (n < 2) rdB(1, 2 + n); else if (n < 6) rdB(2, n - 2); }
                    else if (n < 4) rdB(c + 2, n);
                    if (LOCAL && !HK && c == 0 && (n == 4 || n == 5)) rdA(WM, p, 4 + n);
                    if (c == 1 && n == 5) { off = (off == 2 * STAGE) ? 0 : off + STAGE; asm volatile("" : "+s"(off)); }
                    if (c == 2 && n == 4) { pa[0] = pA0 + off; asm volatile("" : "+v"(pa[0])); }
                    if (c == 2 && n == 5) { pa[1] = pA1 + off; asm volatile("" : "+v"(pa[1])); }
                    if (c == 3 && n == 4) { pb[0] = pB0 + off; asm volatile("" : "+v"(pb[0])); }
                    if (c == 3 && n == 5) { pb[1] = pB1 + off; asm volatile("" : "+v"(pb[1])); }
                    if ((c == 0 && n == 3) || ((c == 1 || c == 2 || (HK && c == 0)) && n == 7)) __builtin_amdgcn_s_waitcnt(0xC47F);
                });
            WAIT_LGKM_BAR();
        };
        auto back = [&](int p, bool first) {
            column(WM, p, 4, first, [&](int n) { if (n & 1) rdB(0, n >> 1); else rdA(WM, p ^ 1, n >> 1); });
            column(WM, p, 5, first, [&](int n) {
                if (!(n & 1)) rdA(WM, p ^ 1, 4 + (n >> 1)); else if (HK && n > 4) rdA(WM, p ^ 1, 6 + (n >> 1)); else rdB(1, n >> 1);
                if (n == 7) __builtin_amdgcn_s_waitcnt(0xC87F);
            });
        };
        auto stage = [&](int p, bool first) { front(p, first); back(p, first); };
        stage(0, true);
        front(1, false);
#pragma unroll 1
        for (int t = 1; t < NT - 1; t += 2) { back(1, false); stage(0, false); front(1, false); }
        column(WM, 1, 4, false, [&](int n) {
            constexpr int RS = C::OFF_RSTD - C::OFF_MEAN, H = 512 - WM.value * 256;
            if (n == 0) { ck[0] = *(lds_f4)(c4 + 256); ck[1] = *(lds_f4)(c4 + 320); cv[0] = *(lds_f1)(c1 + 512); cv[1] = *(lds_f1)(c1 + 512 + 64); }
            else if (n < 5) {
                mu[n - 1] = *(lds_f1)(s1 + (n - 1) * 64); rs[n - 1] = *(lds_f1)(s1 + RS + (n - 1) * 64);
                mu4[n - 1] = *(lds_f4)(s4 + (n - 1) * 64); rs4[n - 1] = *(lds_f4)(s4 + RS + (n - 1) * 64);
            } else if (n == 5) {
                if (LOCAL && WM.value == 0) { mu[4] = *(lds_f1)(s1 + H); rs[4] = *(lds_f1)(s1 + RS + H); }
                else if (LOCAL) { mu4[4] = *(lds_f4)(s4 + H); rs4[4] = *(lds_f4)(s4 + RS + H); }
            } else if (n == 6) { cq[0] = *(lds_f4)c4; cq[1] = *(lds_f4)(c4 + 64); }
        });
        if (LOCAL) column(WM, 1, 5, false, [](int n) { if (n == 6) __builtin_amdgcn_s_waitcnt(0xC07F); });
        if (LOCAL) {
            auto fix = [&](int a, int t) {
#pragma unroll
                for (int f = 0; f < 2; ++f)
                    acc[a][t][f] = (a == 2) ? rs4[t] * (acc[2][t][f] + mu4[t] * cv[f]) + rs4[t] * -cb
                                            : rs[t] * (acc[a][t][f] + mu[t] * (a ? ck[f] : cq[f])) + rs[t] * -cb;
            };
            auto pk = [&](const f32x4 (&v)[2]) { return u32x4{pack_bf16(v[0][0], v[0][1]), pack_bf16(v[0][2], v[0][3]), pack_bf16(v[1][0], v[1][1]), pack_bf16(v[1][2], v[1][3])}; };
            if (!ident) {
                fix(1, 1); fix(1, 3); fix(2, 1); fix(2, 3);
#pragma unroll
                for (int f = 0; f < 2; ++f)
                    acch[f] = (WM.value == 0) ? rs[4] * (acch[f] + mu[4] * ck[f]) : rs4[4] * (acch[f] + mu4[4] * cv[f]);
            }
            auto pkv = [&](int t) {
#pragma unroll
                for (int f = 0; f < 2; ++f) {
                    va[t >> 1][f][(t & 1) * 2] = pack_bf16(acc[2][t][f][0], acc[2][t][f][1]);
                    va[t >> 1][f][(t & 1) * 2 + 1] = pack_bf16(acc[2][t][f][2], acc[2][t][f][3]);
                }
            };
            kfr[1] = pk(acc[1][1]); kfr[3] = pk(acc[1][3]); pkv(1); pkv(3);
            u32x4 hf = pk(acch);
            if (WM.value == 0) {
                ex[0] = kfr[1]; ex[64] = kfr[3];
#pragma unroll
                for (int u = 0; u < 2; ++u)
                    asm volatile("ds_write_b64 %0, %1 offset:%3\n\tds_write_b64 %0, %2 offset:%3+8" :: "v"(ex), "v"(u32x2{va[u][0][2], va[u][0][3]}),
                                 "v"(u32x2{va[u][1][2], va[u][1][3]}), "n"(2048 + u * 1024) : "memory");
                if (negpos) hf = u32x4{0, 0, 0, 0};
            }
            ex[256 + WM.value * 64] = hf;
            SBAR();
            if (!ident) { fix(0, 0); fix(0, 1); fix(0, 2); fix(0, 3); fix(1, 0); fix(1, 2); fix(2, 0); fix(2, 2); }
#pragma unroll
            for (int t = 0; t < 4; ++t) qfr[t] = pk(acc[0][t]);
            kfr[0] = pk(acc[1][0]); kfr[2] = pk(acc[1][2]); pkv(0); pkv(2);
            asm volatile("" : "+v"(qfr[0]), "+v"(qfr[1]), "+v"(qfr[2]), "+v"(qfr[3]), "+v"(kfr[0]), "+v"(kfr[2]),
                         "+v"(va[0][0]), "+v"(va[0][1]), "+v"(va[1][0]), "+v"(va[1][1]));
        }
    };
    const int head = ct * 2 + wn;
    unsigned qo = 0, ko = 0, vo = 0;
    if (LOCAL) {
        qo = (unsigned)((((panel * 8 + wm * 4) * 32 + head) * 64 + lane) * 16);
        asm volatile("" : "+v"(qo));
    } else {
        ko = (unsigned)((wm * 8 + (g & 1)) * 1024 + ((l15 & 7) + ((l15 & 8) << 1) + 32 * (g >> 1)) * 16);
        vo = (unsigned)((wm * 8 + 2 + (g >> 1)) * 1024 + (l15 + 32 * (g & 1)) * 16);
        asm volatile("" : "+v"(ko), "+v"(vo));
    }
    if (LOCAL && wm) mainloop(IC<1>{}); else mainloop(IC<0>{});

    if (!LOCAL) {
        auto pk = [&](f32x4 a, f32x4 b) { return u32x4{pack_bf16(a[0], a[1]), pack_bf16(a[2], a[3]), pack_bf16(b[0], b[1]), pack_bf16(b[2], b[3])}; };
        char* kvp = (char*)kvf + (size_t)(head * 16 + panel) * 16384;
        lds_ptr own = smw + wn * 16384;
        SBAR();
#pragma unroll
        for (int t = 0; t < 4; ++t) {
            const u32x4 kk = pk(rs[t] * (acc[1][t][0] + mu[t] * ck[0]) + rs[t] * -cb, rs[t] * (acc[1][t][1] + mu[t] * ck[1]) + rs[t] * -cb);
            if (t & 1) st16o<128>(kvp + (t >> 1) * 4096, ko, kk); else st16o<0>(kvp + (t >> 1) * 4096, ko, kk);
            kfr[t] = kk;
            SBAR();
            const int u = t >> 1, f = t & 1;
            const u32x4 vv = pk(rs4[2 * u] * (acc[2][2 * u][f] + mu4[2 * u] * cv[f]) + rs4[2 * u] * -cb,
                                rs4[2 * u + 1] * (acc[2][2 * u + 1][f] + mu4[2 * u + 1] * cv[f]) + rs4[2 * u + 1] * -cb);
            if (f) st16o<256>(kvp + u * 4096, vo, vv); else st16o<0>(kvp + u * 4096, vo, vv);
            qfr[t] = vv;
            SBAR();
        }
        column(IC<0>{}, 1, 5, false, [&](int n) {
            const int t = n >> 1;
            if (n & 1) *(lds_u4)(own + vo + (t >> 1) * 4096 + (t & 1) * 256) = qfr[t];
            else       *(lds_u4)(own + ko + (t >> 1) * 4096 + (t & 1) * 128) = kfr[t];
        });
        asm volatile("s_nop 7");
        SBAR();
#pragma unroll
        for (int u = 0; u < 2; ++u) {
            const float r0 = rs[2 * u] * QSCALE, r1 = rs[2 * u + 1] * QSCALE;
            const u32x4 qa = pk(r0 * (acc[0][2 * u][0] + mu[2 * u] * cq[0]) + r0 * -cb, r0 * (acc[0][2 * u][1] + mu[2 * u] * cq[1]) + r0 * -cb);
            const u32x4 qb = pk(r1 * (acc[0][2 * u + 1][0] + mu[2 * u + 1] * cq[0]) + r1 * -cb, r1 * (acc[0][2 * u + 1][1] + mu[2 * u + 1] * cq[1]) + r1 * -cb);
#pragma unroll
            for (int e = 0; e < 4; ++e) {
                const auto x = __builtin_amdgcn_permlane16_swap(qa[e], qb[e], false, false);
                qo4[2 * u][e] = x[0]; qo4[2 * u + 1][e] = x[1];
            }
        }
        return;
    }
    {
        const f32x4 zero4 = {0, 0, 0, 0};
        auto bf8 = [](u32x4 v) { return __builtin_bit_cast(bf16x8, v); };
        f32x4 sp[4], sc[4];
        sc[1] = MFMA16(bf8(kfr[1]), bf8(qfr[1]), mwin); sp[1] = MFMA16(bf8(kfr[0]), bf8(qfr[1]), mwin);
        sc[3] = MFMA16(bf8(kfr[3]), bf8(qfr[3]), mwin); sp[3] = MFMA16(bf8(kfr[2]), bf8(qfr[3]), mwin);
        asm volatile("" : "+v"(sc[1]), "+v"(sp[1]), "+v"(sc[3]), "+v"(sp[3]));
        WAIT_LGKM_BAR();
        const bf16x8 kp[2] = {lds_b128(ka[0]), lds_b128(ka[1])};
        u32x2 vp[2][2];
#pragma unroll
        for (int u = 0; u < 2; ++u)
            asm volatile("ds_read_b64 %0, %2\n\tds_read_b64 %1, %2 offset:8" : "=&v"(vp[u][0]), "=&v"(vp[u][1]) : "v"(vo2[u]) : "memory");
        sc[0] = MFMA16(bf8(kfr[0]), bf8(qfr[0]), mwin); sc[2] = MFMA16(bf8(kfr[2]), bf8(qfr[2]), mwin);
        SBAR();
        float x[4][4], m[4];
        unsigned a[4][2];
        auto logits = [&](int t0, int st) {
#pragma unroll
            for (int t = t0; t < 4; t += 2)
#pragma unroll
                for (int h = 0; h < 2; ++h) {
                    if (st == 0) {
                        const unsigned uc = pack_bf16(sc[t][2 * h], sc[t][2 * h + 1]), up = pack_bf16(sp[t][2 * h], sp[t][2 * h + 1]);
                        asm("v_bfi_b32 %0, %1, %2, %3" : "=v"(a[t][h]) : "v"(h ? cm23 : cm01), "v"(uc), "v"(up));
                    } else if (st == 1) { x[t][2 * h] = bf_lo(a[t][h]) * ATT_SCALE; x[t][2 * h + 1] = bf_hi(a[t][h]) * ATT_SCALE; }
                    else if (st == 2) a[t][h] = pack_bf16(x[t][2 * h], x[t][2 * h + 1]);
                    else { x[t][2 * h] = bf_lo(a[t][h]); x[t][2 * h + 1] = bf_hi(a[t][h]); }
                }
            SBAR();
        };
        logits(1, 0); logits(1, 1);
        sp[0] = MFMA16(kp[0], bf8(qfr[0]), mfirst); sp[2] = MFMA16(kp[1], bf8(qfr[2]), mfirst);
        SBAR();
        logits(1, 2); logits(1, 3);
        logits(0, 0); logits(0, 1); logits(0, 2); logits(0, 3);
        auto across = [&](float (&v)[4], auto op, auto fin) {
            const auto p = __builtin_amdgcn_permlane16_swap(__float_as_uint(v[0]), __float_as_uint(v[1]), false, false);
            const auto q = __builtin_amdgcn_permlane16_swap(__float_as_uint(v[2]), __float_as_uint(v[3]), false, false);
            const auto r = __builtin_amdgcn_permlane32_swap(__float_as_uint(op(__uint_as_float(p[0]), __uint_as_float(p[1]))),
                                                            __float_as_uint(op(__uint_as_float(q[0]), __uint_as_float(q[1]))), false, false);
            const unsigned z = __float_as_uint(fin(op(__uint_as_float(r[0]), __uint_as_float(r[1]))));
            const auto u = __builtin_amdgcn_permlane32_swap(z, z, false, false);
            const auto e = __builtin_amdgcn_permlane16_swap(u[0], u[0], false, false), f = __builtin_amdgcn_permlane16_swap(u[1], u[1], false, false);
            v[0] = __uint_as_float(e[0]); v[1] = __uint_as_float(e[1]); v[2] = __uint_as_float(f[0]); v[3] = __uint_as_float(f[1]);
        };
#pragma unroll
        for (int t = 0; t < 4; ++t) m[t] = amax3(x[t][0], x[t][1], x[t][2]);
        asm("v_max_f32 %0, %0, %4\n\tv_max_f32 %1, %1, %5\n\tv_max_f32 %2, %2, %6\n\tv_max_f32 %3, %3, %7\n\ts_nop 0"
            : "+v"(m[0]), "+v"(m[1]), "+v"(m[2]), "+v"(m[3]) : "v"(x[0][3]), "v"(x[1][3]), "v"(x[2][3]), "v"(x[3][3]));
        across(m, [](float p, float q) { float r; asm("v_max_f32 %0, %1, %2\n\ts_nop 0" : "=v"(r) : "v"(p), "v"(q)); return r; },
               [](float p) { return p * -LOG2E; });
        SBAR();
#pragma unroll
        for (int t = 0; t < 4; ++t)
#pragma unroll
            for (int r = 0; r < 4; ++r) x[t][r] = __builtin_fmaf(x[t][r], LOG2E, m[t]);
        SBAR();
#pragma unroll
        for (int t = 0; t < 4; ++t)
#pragma unroll
            for (int r = 0; r < 4; ++r) x[t][r] = __builtin_amdgcn_exp2f(x[t][r]);
        SBAR();
#pragma unroll
        for (int t = 0; t < 4; ++t) m[t] = (x[t][0] + x[t][1]) + (x[t][2] + x[t][3]);
        asm volatile("s_waitcnt lgkmcnt(0)" : "+v"(vp[0][0]), "+v"(vp[0][1]), "+v"(vp[1][0]), "+v"(vp[1][1]) :: "memory");
        u32x4 ve[2][2];
#pragma unroll
        for (int u = 0; u < 2; ++u)
#pragma unroll
            for (int f = 0; f < 2; ++f) ve[u][f] = u32x4{vp[u][f][0], vp[u][f][1], va[u][f][0], va[u][f][1]};
        across(m, [](float p, float q) { return p + q; }, [](float p) { return __builtin_amdgcn_rcpf(p); });
        asm volatile("" : "+v"(ve[0][0]), "+v"(ve[0][1]), "+v"(ve[1][0]), "+v"(ve[1][1]));
        SBAR();
#pragma unroll
        for (int t = 0; t < 4; ++t)
#pragma unroll
            for (int r = 0; r < 4; ++r) x[t][r] *= m[t];
        SBAR();
#pragma unroll
        for (int t = 0; t < 4; ++t)
#pragma unroll
            for (int h = 0; h < 2; ++h) a[t][h] = pack_bf16(x[t][2 * h], x[t][2 * h + 1]);
        SBAR();
        f32x4 o0[4], o1[4];
#pragma unroll
        for (int t = 0; t < 4; ++t) {
            const bf16x8 pb = bf8(u32x4{a[t][0] & ~cm01, a[t][1] & ~cm23, a[t][0] & cm01, a[t][1] & cm23});
            o0[t] = MFMA16(bf8((t & 1) ? va[t >> 1][0] : ve[t >> 1][0]), pb, zero4);
            o1[t] = MFMA16(bf8((t & 1) ? va[t >> 1][1] : ve[t >> 1][1]), pb, zero4);
        }
        SBAR();
#pragma unroll
        for (int t = 0; t < 4; ++t)
            st16(ofrag + t * 16384, qo, u32x4{pack_bf16(o0[t][0], o0[t][1]), pack_bf16(o0[t][2], o0[t][3]),
                                                 pack_bf16(o1[t][0], o1[t][1]), pack_bf16(o1[t][2], o1[t][3])});
    }
}

DI int out_row(int panel, bool localrows, int T, int a) {
    return localrows ? (panel * 128 + (T >> 2) * 64 + 2 * (16 * (T & 1) + a) + ((T >> 1) & 1)) : (panel * 128 + T * 16 + a);
}
DI int out_swz(int k) { return ((k >> 1) & 1) | (k & 4); }
DI unsigned wo_off(int w, int lane) {
    const int kl = lane >> 3;
    unsigned v = (unsigned)(w * 8 + kl) * 2048u + (unsigned)(ids().co * 128 + ((lane & 7) ^ out_swz(kl)) * 16);
    asm volatile("" : "+v"(v));
    return v;
}
DI void wo_load(const void* Wo, unsigned voff, int i0, int i1, unsigned lds0, int w) {
#pragma unroll
    for (int i = i0; i < i1; ++i) glds16((const char*)Wo + (size_t)i * 65536, voff, lds0 + (w + 4 * i) * 1024);
}
DI unsigned res_off(bool localrows, bool tiled, int lane) {
    const Ids I = ids();
    unsigned v = (unsigned)(I.po * 262144 + I.co * (tiled ? 16384 : 128) + out_row(0, localrows, 0, lane >> 3) * (tiled ? 128 : 2048) +
                            (((lane & 7) ^ (lane >> 3)) * 16));
    asm volatile("" : "+v"(v));
    return v;
}
DI void res_load(u32x4 (&res)[6], const void* Xin, bool localrows, bool tiled, unsigned off, int w) {
#pragma unroll
    for (int i = 0; i < 6; ++i) {
        const int j = min(w + 3 * i, 15);
        asm volatile("global_load_dwordx4 %0, %1, %2 sc1" : "=&v"(res[i]) : "v"(off), "s"((const char*)Xin + out_row(0, localrows, j >> 1, (j & 1) * 8) * (tiled ? 128 : 2048)) : "memory");
    }
}
DI void out_handoff(char* smem, int xoff, u32x4 (&res)[6], int w, char* ws, int set, u64 tag, int lane, u32x4 (&c)[8], int mat) {
    const Ids I = ids();
    u64 land;
    lds_ptr dst = (lds_ptr)smem + xoff + w * 1024 + lane * 16;
    asm volatile("" : "+v"(dst));
    if (w == 3) __builtin_amdgcn_s_setprio(3);
    signal_poll<false, false>(w - 3, ws, flag_off(set, I.pq, lane & 15, I.cq), flag_off(set, I.po, I.co, lane & 15), 0xffffull, tag, land);
    if (w < 3) {
        asm volatile("s_waitcnt vmcnt(0)" : "+v"(res[0]), "+v"(res[1]), "+v"(res[2]), "+v"(res[3]), "+v"(res[4]), "+v"(res[5]) :: "memory");
#pragma unroll
        for (int i = 0; i < 5; ++i) *(__attribute__((address_space(3))) u32x4*)(dst + i * 3072) = res[i];
        if (w == 0) *(__attribute__((address_space(3))) u32x4*)(dst + 15360) = res[5];
        if (mat >= 0) colsum_store(c, ws, mat, w, lane);
    }
    WAIT_LGKM_BAR();
    asm volatile("s_waitcnt vmcnt(0)" : "+v"(land) :: "memory");
    if (w < 3 && mat >= 0) flag_store(ws, (mat ? 10 : 13) + w, I.cq, I.pq, tag, lane);
    __builtin_amdgcn_s_setprio(0);
}

template <bool AFTER_LOCAL>
DI void out_compute(char* smem, int w, const __hip_bfloat16* __restrict__ ofrag,
                    __hip_bfloat16* __restrict__ Xout, float* __restrict__ statp SP_PARAM) {
    constexpr int XOFF = AFTER_LOCAL ? 147456 : 98304;
    STAMP(AFTER_LOCAL ? 3 : 10);
    VM_BAR(0);
    const int lane = lane_id();
    asm volatile("" : "+s"(w));
    const Ids I = ids();
    const int panel = I.po, ct = I.co;
    const int l15 = lane & 15, g = lane >> 4;
    const bool tiled = AFTER_LOCAL || statp != nullptr;
    const int rstr = tiled ? 128 : 2048;
    const int q4 = l15 >> 2, p4 = l15 & 3;
    const int krow = 4 * g + q4;
    const int sw = out_swz(krow);
    const lds_cptr sm = (lds_cptr)smem;
    lds_cptr bw[4], xo[4];
#pragma unroll
    for (int t = 0; t < 4; ++t) {
        bw[t] = sm + krow * 128 + ((((t >> 1) * 4 + (p4 >> 1) * 2 + (t & 1)) ^ sw) * 16) + (p4 & 1) * 8;
        xo[t] = sm + XOFF + (w * 32 + l15) * 128 + ((((t >> 1) * 4 + (g >> 1) * 2 + (t & 1)) ^ (l15 & 7)) * 16) + (g & 1) * 8;
    }
    unsigned so[2], sto, ao[2];
#pragma unroll
    for (int r2 = 0; r2 < 2; ++r2) {
        so[r2] = (unsigned)(panel * 262144 + ct * (tiled ? 16384 : 128) + out_row(0, AFTER_LOCAL, 2 * w + r2, l15 & 7) * rstr + (l15 >> 3) * 64 + g * 16);
        ao[r2] = (unsigned)((panel * 8 + 2 * w + r2) * 32768 + lane * 16);
    }
    sto = (unsigned)(((panel * 16 + ct) * 128 + 2 * w * 16 + l15) * 8);
    asm volatile("" : "+v"(bw[0]), "+v"(bw[1]), "+v"(bw[2]), "+v"(bw[3]), "+v"(xo[0]), "+v"(xo[1]), "+v"(xo[2]), "+v"(xo[3]),
                 "+v"(so[0]), "+v"(so[1]), "+v"(sto), "+v"(ao[0]), "+v"(ao[1]));
    bf16x8 ones;
    {
        unsigned o1; asm volatile("v_mov_b32 %0, 0x3f803f80" : "=v"(o1));
        ones = __builtin_bit_cast(bf16x8, (u32x4{o1, o1, o1, o1}));
        asm volatile("" : "+v"(ones));
    }
    const __amdgpu_buffer_rsrc_t ra = __builtin_amdgcn_make_buffer_rsrc((void*)ofrag, 0, 4 << 20, 0x00020000);
    asm volatile("" :: "s"(ra));
    u32x32 fr;
    BAR();
    STAMP(AFTER_LOCAL ? 4 : 11);
    u32x32 aw0, aw1;
    asm volatile(".irp j,0,1,2,3\n\tbuffer_load_dwordx4 v[64+8*\\j:67+8*\\j], %2, %4, 0 offen offset:1024*\\j sc1\n\t"
                 "buffer_load_dwordx4 v[68+8*\\j:71+8*\\j], %3, %4, 0 offen offset:1024*\\j sc1\n\t.endr\n\t"
                 ".irp j,0,1\n\tbuffer_load_dwordx4 v[96+8*\\j:99+8*\\j], %2, %4, %5 offen offset:1024*\\j sc1\n\t"
                 "buffer_load_dwordx4 v[100+8*\\j:103+8*\\j], %3, %4, %5 offen offset:1024*\\j sc1\n\t.endr"
                 : "=&{v[64:95]}"(aw0), "=&{v[96:127]}"(aw1) : "v"(ao[0]), "v"(ao[1]), "s"(ra), "s"(4096) : "memory");
    f32x16 acc[2];
#pragma unroll
    for (int r2 = 0; r2 < 2; ++r2)
#pragma unroll
        for (int c = 0; c < 4; ++c) {
            const u32x2 xr = *(const __attribute__((address_space(3))) u32x2*)(xo[c] + r2 * 2048);
            acc[r2][4 * c] = bf_lo(xr[0]); acc[r2][4 * c + 1] = bf_hi(xr[0]); acc[r2][4 * c + 2] = bf_lo(xr[1]); acc[r2][4 * c + 3] = bf_hi(xr[1]);
        }
    asm volatile(
        ".macro OUT_MM r, t, j\n\t"
        "v_mfma_f32_16x16x32_bf16 v[16*\\r+4*\\t:16*\\r+4*\\t+3], v[32+16*((\\j)&1)+4*\\t:35+16*((\\j)&1)+4*\\t], v[64+8*\\j+4*\\r:67+8*\\j+4*\\r], v[16*\\r+4*\\t:16*\\r+4*\\t+3]\n\t"
        ".endm\n\t"
        ".macro OUT_RD t, hi, j, on\n\t"
        ".if \\on\n\tds_read_b64_tr_b16 v[32+16*((\\j)&1)+4*\\t+2*\\hi:33+16*((\\j)&1)+4*\\t+2*\\hi], v[128+\\t] offset:((\\j)&7)*4096+2048*\\hi\n\t.endif\n\t"
        ".endm\n\t"
        ".macro OUT_UP t, on\n\t.if \\on\n\tv_add_u32 v[128+\\t], s97, v[128+\\t]\n\t.endif\n\t.endm\n\t"
        ".macro OUT_STEP j, ld, rd, up, vm, lg\n\t"
        "OUT_MM 0, 0, \\j\n\tOUT_RD 3, 1, \\j+1, 1\n\tOUT_UP 0, \\up\n\t"
        ".if \\ld && (((\\j+6)&3) == 0)\n\ts_addk_i32 s96, 0x1000\n\t.endif\n\t"
        "OUT_MM 1, 0, \\j\n\tOUT_RD 0, 0, \\j+2, \\rd\n\t"
        ".if \\ld\n\tbuffer_load_dwordx4 v[64+8*((\\j+6)&7):67+8*((\\j+6)&7)], %[a0], %[ra], s96 offen offset:1024*((\\j+6)&3) sc1\n\t.endif\n\t"
        "OUT_MM 0, 1, \\j\n\tOUT_RD 0, 1, \\j+2, \\rd\n\tOUT_UP 1, \\up\n\t"
        "OUT_MM 1, 1, \\j\n\tOUT_RD 1, 0, \\j+2, \\rd\n\t"
        "OUT_MM 0, 2, \\j\n\tOUT_RD 1, 1, \\j+2, \\rd\n\tOUT_UP 2, \\up\n\t"
        "OUT_MM 1, 2, \\j\n\tOUT_RD 2, 0, \\j+2, \\rd\n\t"
        ".if \\ld\n\tbuffer_load_dwordx4 v[68+8*((\\j+6)&7):71+8*((\\j+6)&7)], %[a1], %[ra], s96 offen offset:1024*((\\j+6)&3) sc1\n\t.endif\n\t"
        "OUT_MM 0, 3, \\j\n\tOUT_RD 2, 1, \\j+2, \\rd\n\tOUT_UP 3, \\up\n\t"
        "OUT_MM 1, 3, \\j\n\tOUT_RD 3, 0, \\j+2, \\rd\n\t"
        "s_waitcnt vmcnt(\\vm) lgkmcnt(\\lg)\n\t"
        ".endm\n\t"
        ".macro OUT_PACK y, c\n\t"
        ".irp i,0,1,2,3,4,5,6,7\n\tv_cvt_pk_bf16_f32 v[\\y+\\i], v[\\c+2*\\i], v[\\c+2*\\i+1]\n\t.endr\n\t"
        ".irp i,0,1,4,5\n\tv_permlane16_swap_b32 v[\\y+\\i], v[\\y+\\i+2]\n\t.endr\n\t"
        ".irp i,0,1,2,3\n\tv_mov_b32 v[\\y+8+\\i], v[\\y+\\i]\n\t.endr\n\t"
        "v_mfma_f32_16x16x32_bf16 v[\\y+12:\\y+15], v[\\y:\\y+3], v[\\y:\\y+3], 0\n\t"
        "v_mov_b32_dpp v[\\y+8], v[\\y+4] row_ror:8 row_mask:0xf bank_mask:0xc\n\tv_mov_b32_dpp v[\\y+9], v[\\y+5] row_ror:8 row_mask:0xf bank_mask:0xc\n\t"
        "v_mfma_f32_16x16x32_bf16 v[\\y+16:\\y+19], %[one], v[\\y:\\y+3], 0\n\t"
        "v_mov_b32_dpp v[\\y+10], v[\\y+6] row_ror:8 row_mask:0xf bank_mask:0xc\n\tv_mov_b32_dpp v[\\y+11], v[\\y+7] row_ror:8 row_mask:0xf bank_mask:0xc\n\t"
        "v_mfma_f32_16x16x32_bf16 v[\\y+12:\\y+15], v[\\y+4:\\y+7], v[\\y+4:\\y+7], v[\\y+12:\\y+15]\n\t"
        ".endm\n\t"
        ".macro OUT_REST y\n\t"
        "v_mfma_f32_16x16x32_bf16 v[\\y+16:\\y+19], %[one], v[\\y+4:\\y+7], v[\\y+16:\\y+19]\n\t"
        ".irp i,0,1,2,3\n\tv_mov_b32_dpp v[\\y+4+\\i], v[\\y+\\i] row_ror:8 row_mask:0xf bank_mask:0x3\n\t.endr\n\t"
        ".endm\n\t"
        ".macro OUT_DIAG y\n\t"
        "v_cndmask_b32 v[\\y+12], v[\\y+12], v[\\y+13], %[m0]\n\tv_cndmask_b32 v[\\y+14], v[\\y+14], v[\\y+15], %[m0]\n\t"
        "v_cndmask_b32 v[\\y+17], v[\\y+12], v[\\y+14], %[m1]\n\t"
        ".endm\n\t"
        ".irp t,0,1,2,3\n\tOUT_RD \\t, 0, 0, 1\n\tOUT_RD \\t, 1, 0, 1\n\t.endr\n\t"
        ".irp t,0,1,2\n\tOUT_RD \\t, 0, 1, 1\n\tOUT_RD \\t, 1, 1, 1\n\t.endr\n\tOUT_RD 3, 0, 1, 1\n\t"
        "s_movk_i32 s96, 0x1000\n\ts_mov_b32 s97, 0x8000\n\t"
        "s_waitcnt vmcnt(10) lgkmcnt(0)\n"
        ".Lout_loop_%=:\n\t"
        "OUT_STEP 0, 1, 1, 0, 10, 7\n\tOUT_STEP 1, 1, 1, 0, 10, 7\n\tOUT_STEP 2, 1, 1, 0, 10, 7\n\tOUT_STEP 3, 1, 1, 0, 10, 7\n\t"
        "OUT_STEP 4, 1, 1, 0, 10, 7\n\t"
        "s_cmpk_eq_u32 s96, 0x6000\n\ts_cselect_b32 s97, %[last], s97\n\t"
        "OUT_STEP 5, 1, 1, 0, 10, 7\n\tOUT_STEP 6, 1, 1, 1, 10, 7\n\t"
        "OUT_STEP 7, 1, 1, 0, 10, 7\n\t"
        "s_cmpk_lg_u32 s96, 0x7000\n\ts_cbranch_scc1 .Lout_loop_%=\n\t"
        "OUT_STEP 0, 1, 1, 0, 10, 7\n\tOUT_STEP 1, 1, 1, 0, 10, 7\n\tOUT_STEP 2, 0, 1, 0, 8, 7\n\tOUT_STEP 3, 0, 1, 0, 6, 7\n\t"
        "OUT_STEP 4, 0, 1, 0, 4, 7\n\tOUT_STEP 5, 0, 1, 0, 2, 7\n\tOUT_STEP 6, 0, 0, 0, 0, 0\n\t"
        ".irp r,0,1\n\t.irp t,0,1,2,3\n\tOUT_MM \\r, \\t, 7\n\t.endr\n\t.endr\n\t"
        "s_nop 0\n\t"
        "OUT_PACK 64, 0\n\tglobal_store_dwordx4 %[s0], v[72:75], %[xa] sc1\n\tOUT_REST 64\n\tglobal_store_dwordx4 %[s0], v[68:71], %[xb] sc1\n\t"
        "OUT_PACK 84, 16\n\tglobal_store_dwordx4 %[s1], v[92:95], %[xa] sc1\n\tOUT_REST 84\n\tglobal_store_dwordx4 %[s1], v[88:91], %[xb] sc1\n\t"
        "s_cmp_eq_u64 %[st], 0\n\ts_cbranch_scc1 .Lout_end_%=\n\t"
        "OUT_DIAG 64\n\tOUT_DIAG 84\n\t"
        "s_mov_b64 exec, %[md]\n\t"
        "global_store_dwordx2 %[so], v[80:81], %[st] sc1\n\tglobal_store_dwordx2 %[so], v[100:101], %[st] offset:128 sc1\n\t"
        "s_mov_b64 exec, -1\n"
        ".Lout_end_%=:\n\t"
        "s_nop 1\n\t"
        ".purgem OUT_MM\n\t.purgem OUT_RD\n\t.purgem OUT_UP\n\t.purgem OUT_STEP\n\t.purgem OUT_PACK\n\t.purgem OUT_REST\n\t.purgem OUT_DIAG"
        : "+{v[0:15]}"(acc[0]), "+{v[16:31]}"(acc[1]), "+{v[64:95]}"(aw0), "+{v[96:127]}"(aw1),
          "+{v128}"(bw[0]), "+{v129}"(bw[1]), "+{v130}"(bw[2]), "+{v131}"(bw[3]), "=&{v[32:63]}"(fr)
        : [a0] "v"(ao[0]), [a1] "v"(ao[1]), [ra] "s"(ra), [last] "s"(AFTER_LOCAL ? 32768 : 65536), [one] "v"(ones), [s0] "v"(so[0]), [s1] "v"(so[1]),
          [xa] "s"(Xout), [xb] "s"((char*)Xout + (AFTER_LOCAL ? 16 : 8) * rstr), [st] "s"(statp), [so] "v"(sto),
          [m0] "s"(0xaaaaaaaaaaaaaaaaull), [m1] "s"(0xccccccccccccccccull), [md] "s"(0xf0000f0000f0000full)
        : "memory", "scc", "s96", "s97");
    STAMP(AFTER_LOCAL ? 5 : 12);
}

#define ROWSUM(p0, p1, c) __builtin_amdgcn_smfmac_f32_16x16x64_bf16(sel, __builtin_bit_cast(bf16v_t, __builtin_shufflevector(p0, p1, 0, 1, 2, 3, 4, 5, 6, 7)), c, sidx, 0, 0)
constexpr int ATT_STAGE = 32768;
typedef __attribute__((ext_vector_type(8))) unsigned u32x8;

DI void attn_loader(char* smem, int w, char* ws, int set, u64 tag, const __hip_bfloat16* __restrict__ kvf, const void* Wo, const void* Xin,
                    const void* Wcs SP_PARAM) {
    const int lane = lane_id();
    asm volatile("" : "+s"(w));
    const Ids I = ids();
    const int panel = I.pq, ct = I.cq;
    const unsigned lds0 = (unsigned)(uintptr_t)smem;
    const char* kvh = (const char*)kvf + (size_t)(ct * 2 + (w >> 1)) * 262144 + (w & 1) * 4096;
    const unsigned voff = (unsigned)(lane * 16);
    auto order = [&](int st) { return ((panel & 8) + st - ((st - 1 < (panel & 7)) ? 1 : 0)) & 15; };
    auto load = [&](int st) {
        const char* src = kvh + (size_t)order(st) * 16384;
        const unsigned dst = lds0 + (w >> 1) * 16384 + (w & 1) * 4096 + (st & 3) * ATT_STAGE;
#pragma unroll
        for (int u = 0; u < 2; ++u)
            asm volatile("s_mov_b32 m0, %0\n\ts_nop 0\n\tglobal_load_lds_dwordx4 %1, %2 sc1\n\tglobal_load_lds_dwordx4 %1, %2 offset:1024 sc1\n\t"
                         "global_load_lds_dwordx4 %1, %2 offset:2048 sc1\n\tglobal_load_lds_dwordx4 %1, %2 offset:3072 sc1"
                         :: "s"(dst + u * 8192), "v"(voff), "s"(src + u * 8192) : "memory");
    };
    const unsigned wv = wo_off(w, lane), rv = res_off(false, true, lane), cv = colsum_off(w, lane);
    const unsigned poff = flag_off(set, ct, panel, lane & 15);
    u64 land;
    __builtin_amdgcn_s_setprio(3);
    BAR();
    signal_poll<false, true>(w, ws, flag_off(set, ct, lane & 15, panel), poff, 1ull << order(1), tag, land);
    STAMP(9);
    load(1);
    asm volatile("global_load_dwordx2 %0, %1, %2 sc1\n\ts_waitcnt vmcnt(0)" : "+v"(land) : "v"(poff), "s"(ws + WS_FLAG) : "memory");
    STAMP(10);
    BAR();
    if (!__all(land == tag)) flag_poll(ws, poff, tag);
    load(2); load(3);
    __builtin_amdgcn_s_setprio(0);
    STAMP(11);
    VM_BAR(8); load(4);
    VM_BAR(0); load(5); load(6);
    VM_BAR(0); load(7); load(8);
    u32x4 res[6], ca[8];
    colsum_load(ca, w < 3 ? Wcs : nullptr, cv);
#pragma unroll 1
    for (int st = 6; st < 12; st += 2) {
        VM_BAR(0);
        load(st + 3); load(st + 4);
        if (st == 8) wo_load(Wo, wv, 24, 32, lds0 + 32768, w);
    }
    VM_BAR(0); load(15); wo_load(Wo, wv, 0, 8, lds0, w);
    res_load(res, Xin, false, true, rv, w);
    VM_BAR(0); wo_load(Wo, wv, 8, 24, lds0, w);
    STAMP(12);
    out_handoff(smem, 98304, res, w, ws, set + 1, tag, lane, ca, Wcs ? 0 : -1);
}

DI void attn_compute(char* smem, int w, const u32x4 (&q4)[4], const __hip_bfloat16* __restrict__ kvf, __hip_bfloat16* __restrict__ ofrag SP_PARAM) {
    const int lane = lane_id();
    asm volatile("" : "+s"(w));
    const int wm = w & 1, wn = w >> 1;
    const Ids I = ids();
    const int panel = I.pq, head = I.cq * 2 + wn;
    const int l15 = lane & 15;
    const unsigned se = ((l15 & 2) == 0 && (l15 & 4) == 0) ? 0x3f803f80u : 0u, so = ((l15 & 2) == 0 && (l15 & 4) != 0) ? 0x3f803f80u : 0u;
    const bf8_t sel = __builtin_bit_cast(bf8_t, u32x4{se, se, so, so});
    const int sidx = (l15 & 1) ? (int)0xEEEEEEEEu : 0x44444444;
    const lds_cptr sm = (lds_cptr)smem + wn * 16384 + lane * 16;
    const f32x16 z16 = {0, 0, 0, 0, 0, 0, 0, 0, 0, 0, 0, 0, 0, 0, 0, 0};
    const bf16x8 qA0 = __builtin_bit_cast(bf16x8, q4[0]), qA1 = __builtin_bit_cast(bf16x8, q4[1]);
    const bf16x8 qB0 = __builtin_bit_cast(bf16x8, q4[2]), qB1 = __builtin_bit_cast(bf16x8, q4[3]);
    f32x16 oA, oB, nmA, nmB, sA, sB;
    f32x4 lsA, lsB;
    u32x8 pB;
    bf16x8 v11;
    asm volatile(".irp r,106,108,110,112,134,136,138,140,170,172\n\tv_mov_b64 v[\\r:\\r+1], 0\n\t.endr"
                 : "={v[106:113]}"(pB), "={v[134:137]}"(lsA), "={v[138:141]}"(lsB), "={v[170:173]}"(v11));
    WAIT_LGKM_BAR();
    STAMP(8);
    bf16x8 k00 = lds_b128(sm), k10 = lds_b128(sm + 1024), k01 = lds_b128(sm + 4096), k11 = lds_b128(sm + 4096 + 1024);
    bf16x8 v00 = lds_b128(sm + 2048), v10 = lds_b128(sm + 3072), v01 = lds_b128(sm + 4096 + 2048);
    float ob;
    {
        const u32x4 vu = __builtin_bit_cast(u32x4, v00);
        const unsigned vm = max(max(vu[0] & 0x7fff0000u, vu[1] & 0x7fff0000u), max(vu[2] & 0x7fff0000u, vu[3] & 0x7fff0000u));
        ob = __uint_as_float(wave_min(vm)) * 0.015625f;
        sA = MFMA32(k00, qA0, z16); sB = MFMA32(k00, qB0, z16);
        sA = MFMA32(k10, qA1, sA); sB = MFMA32(k10, qB1, sB);
        SBAR(); VM_BAR(0); SBAR();
        float mA = sA[0], mB = sB[0];
#pragma unroll
        for (int r = 1; r < 16; ++r) { mA = fmaxf(mA, sA[r]); mB = fmaxf(mB, sB[r]); }
        mA = max_h(mA) + 16.f; mB = max_h(mB) + 16.f;
#pragma unroll
        for (int r = 0; r < 16; ++r) { nmA[r] = -mA; nmB[r] = -mB; sA[r] -= mA; oA[r] = ob; oB[r] = ob; }
    }
    const unsigned a0 = (unsigned)(uintptr_t)sm;
    unsigned dst = (unsigned)((((panel * 8 + wm * 4 + ((lane & 31) >> 4)) * 32 + head) * 64 + (lane & 15) + (lane >> 5) * 16) * 16);
    asm volatile("" : "+v"(dst));
    asm volatile(
        ".macro ATT_RD dst, y\n\t"
        "ds_read_b128 v[\\dst:\\dst+3], v[175+(((\\y)&0x1ffff)>>16)] offset:(\\y)&0xffff\n\t"
        ".endm\n\t"
        ".macro ATT_E4 c\n\t"
        "v_exp_f32 v[\\c], v[\\c]\n\tv_exp_f32 v[\\c+1], v[\\c+1]\n\tv_exp_f32 v[\\c+2], v[\\c+2]\n\tv_exp_f32 v[\\c+3], v[\\c+3]\n\t"
        ".endm\n\t"
        ".macro ATT_C4 p, c\n\t"
        "v_cvt_pk_bf16_f32 v[\\p], v[\\c], v[\\c+1]\n\tv_cvt_pk_bf16_f32 v[\\p+1], v[\\c+2], v[\\c+3]\n\t"
        "v_cvt_pk_bf16_f32 v[\\p+2], v[\\c+4], v[\\c+5]\n\tv_cvt_pk_bf16_f32 v[\\p+3], v[\\c+6], v[\\c+7]\n\t"
        ".endm\n\t"
        ".macro ATT_STEP cur, nxt, ka, kb, q, nm, oc, od, lsd, pc, pd, vd1, vc0, r0, y0, r1, y1, bar, rd\n\t"
        "v_mfma_f32_32x32x16_bf16 v[\\nxt:\\nxt+15], v[\\ka:\\ka+3], v[\\q:\\q+3], v[\\nm:\\nm+15]\n\t"
        "ATT_E4 \\cur\n\t"
        "v_mfma_f32_32x32x16_bf16 v[\\nxt:\\nxt+15], v[\\kb:\\kb+3], v[\\q+4:\\q+7], v[\\nxt:\\nxt+15]\n\t"
        "ATT_E4 \\cur+4\n\t"
        "v_mfma_f32_32x32x16_bf16 v[\\od:\\od+15], v[\\vd1:\\vd1+3], v[\\pd+4:\\pd+7], v[\\od:\\od+15]\n\t"
        "s_waitcnt lgkmcnt(0)\n\t"
        "ATT_C4 \\pc, \\cur\n\t"
        "v_exp_f32 v[\\cur+8], v[\\cur+8]\n\tv_exp_f32 v[\\cur+9], v[\\cur+9]\n\t"
        "v_mfma_f32_32x32x16_bf16 v[\\oc:\\oc+15], v[\\vc0:\\vc0+3], v[\\pc:\\pc+3], v[\\oc:\\oc+15]\n\t"
        ".if \\bar\n\ts_barrier\n\t.endif\n\t"
        "ATT_E4 \\cur+10\n\t"
        "v_smfmac_f32_16x16x64_bf16 v[\\lsd:\\lsd+3], v[130:133], v[\\pd:\\pd+7], v174\n\t"
        ".if \\rd & 4\n\tATT_RD 142, 32768\n\tATT_RD 150, 33792\n\tATT_RD 158, 34816\n\t.endif\n\t"
        ".if \\rd & 8\n\tATT_RD \\r1-8, \\y1-1024\n\t.endif\n\t"
        ".if \\rd & 1\n\tATT_RD \\r0, \\y0\n\t.endif\n\t"
        ".if \\rd & 2\n\tATT_RD \\r1, \\y1\n\t.endif\n\t"
        "v_exp_f32 v[\\cur+14], v[\\cur+14]\n\tv_exp_f32 v[\\cur+15], v[\\cur+15]\n\t"
        "ATT_C4 \\pc+4, \\cur+8\n\t"
        ".if \\rd & 4\n\ts_waitcnt lgkmcnt(3)\n\t.endif\n\t"
        ".endm\n\t"
        ".macro ATT_TILE p, y1, y2, bar, ra=3, rb=3\n\t"
        "ATT_STEP 66, 82, 142+\\p, 150+\\p, 122, 50, 2, 18, 138, 98, 106, 170-\\p, 158+\\p, 170-\\p, \\y1+3072, 142+\\p, \\y2, \\bar&1, \\ra\n\t"
        "ATT_STEP 82, 66, 146-\\p, 154-\\p, 114, 34, 18, 2, 134, 106, 98, 166+\\p, 158+\\p, 158+\\p, \\y2+2048, 150+\\p, \\y2+1024, \\bar&2, \\rb\n\t"
        ".endm\n\t"
        ".macro ATT_STAGE b, bar\n\t"
        "ATT_TILE 0, \\b+4096, \\b+8192, 0\n\t"
        "ATT_TILE 4, \\b+8192, \\b+12288, 0\n\t"
        ".if \\bar\n\tATT_TILE 0, \\b+12288, \\b+32768, 2, 1, 11\n\t.else\n\tATT_TILE 0, \\b+12288, \\b+32768, 0\n\t.endif\n\t"
        "ATT_TILE 4, \\b+32768, \\b+36864, 0\n\t"
        ".endm\n\t"
        "v_add_u32 v176, 0x10000, v175\n\t"
        "s_mov_b32 s96, 4\n\t"
        "s_waitcnt lgkmcnt(0)\n\t"
        "ATT_TILE 0, 4096, 8192, 0\n\tATT_TILE 4, 8192, 12288, 0\n\tATT_TILE 0, 12288, 0, 0, 1, 0\n\tATT_TILE 4, 32768, 36864, 1, 7, 3\n\t"
        "ATT_STAGE 32768, 1\n\t"
        "s_branch .Latt_entry_%=\n\t"
        ".p2align 6\n"
        ".Latt_loop_%=:\n\t"
        "ATT_STAGE 0, 1\n\tATT_STAGE 32768, 0\n"
        ".Latt_entry_%=:\n\t"
        "ATT_STAGE 65536, 1\n\t"
        "s_sub_u32 s96, s96, 1\n\t"
        "ATT_STAGE 98304, 0\n\t"
        "s_cmp_lg_u32 s96, 0\n\t"
        "s_cbranch_scc1 .Latt_loop_%=\n\t"
        "s_nop 1\n\t"
        "v_mfma_f32_32x32x16_bf16 v[18:33], v[170:173], v[110:113], v[18:33]\n\t"
        "v_smfmac_f32_16x16x64_bf16 v[138:141], v[130:133], v[106:113], v174\n\t"
        "s_waitcnt lgkmcnt(0)\n\ts_nop 10\n\t"
        ".purgem ATT_RD\n\t.purgem ATT_E4\n\t.purgem ATT_C4\n\t.purgem ATT_STEP\n\t.purgem ATT_TILE\n\t.purgem ATT_STAGE"
        : "+{v[2:17]}"(oA), "+{v[18:33]}"(oB), "+{v[66:81]}"(sA), "+{v[106:113]}"(pB), "+{v[134:137]}"(lsA), "+{v[138:141]}"(lsB),
          "+{v[142:145]}"(k00), "+{v[146:149]}"(k01), "+{v[150:153]}"(k10), "+{v[154:157]}"(k11),
          "+{v[158:161]}"(v00), "+{v[162:165]}"(v01), "+{v[166:169]}"(v10), "+{v[170:173]}"(v11)
        : "{v[34:49]}"(nmA), "{v[50:65]}"(nmB), "{v[114:117]}"(qA0), "{v[118:121]}"(qA1), "{v[122:125]}"(qB0), "{v[126:129]}"(qB1),
          "{v[130:133]}"(sel), "{v174}"(sidx), "{v175}"(a0)
        : "memory", "scc", "s96", "v176", "v82", "v83", "v84", "v85", "v86", "v87", "v88", "v89", "v90", "v91", "v92", "v93", "v94", "v95",
          "v96", "v97", "v98", "v99", "v100", "v101", "v102", "v103", "v104", "v105");
    STAMP(9);
    float lA = lsA[0] + lsA[1], lB = lsB[0] + lsB[1];
    auto store = [&] {
#pragma unroll
    for (int u = 0; u < 2; ++u) {
        const float inv = __builtin_amdgcn_rcpf(u ? lB : lA), sub = -ob * inv;
        const f32x16 e = (u ? oB : oA) * inv + sub;
        st16o<0>((char*)ofrag + u * 65536, dst, u32x4{pack_bf16(e[0], e[1]), pack_bf16(e[2], e[3]), pack_bf16(e[8], e[9]), pack_bf16(e[10], e[11])});
        st16o<512>((char*)ofrag + u * 65536, dst, u32x4{pack_bf16(e[4], e[5]), pack_bf16(e[6], e[7]), pack_bf16(e[12], e[13]), pack_bf16(e[14], e[15])});
    }
    };
    store();
    {
        const f32x16 os = oA + oB;
        const float osum = (((os[0] + os[1]) + (os[2] + os[3])) + ((os[4] + os[5]) + (os[6] + os[7]))) +
                           (((os[8] + os[9]) + (os[10] + os[11])) + ((os[12] + os[13]) + (os[14] + os[15])));
        const bool bad = !(fmaxf(lA, lB) < 1.0e24f) || !(lA > 1.0e-24f) || !(lB > 1.0e-24f) || !(fabsf(osum) < 3.0e38f);
                if (__any(bad)) {
            const __amdgpu_buffer_rsrc_t rk = __builtin_amdgcn_make_buffer_rsrc((char*)kvf + (size_t)head * 262144, 0, 262144, 0x00020000);
            oA = z16; oB = z16; lA = 0.f; lB = 0.f; ob = 0.f;
            float mA = -INFINITY, mB = -INFINITY;
#pragma unroll 1
            for (int t = 0; t < 64; ++t) {
                bf16x8 f[4];
#pragma unroll
                for (int i = 0; i < 4; ++i) f[i] = __builtin_bit_cast(bf16x8, __builtin_amdgcn_raw_buffer_load_b128(rk, lane * 16 + i * 1024, t * 4096, 16));
                auto upd = [&](bf16x8 q0, bf16x8 q1, f32x16& o, float& l, float& m) {
                    f32x16 s = MFMA32(f[0], q0, z16); s = MFMA32(f[1], q1, s);
                    float tm = s[0];
#pragma unroll
                    for (int r = 1; r < 16; ++r) tm = fmaxf(tm, s[r]);
                    tm = max_h(tm);
                    const float mn = fmaxf(m, tm);
                    const float alpha = __builtin_amdgcn_exp2f(m - mn);
                    m = mn;
#pragma unroll
                    for (int r = 0; r < 16; ++r) { s[r] = __builtin_amdgcn_exp2f(s[r] - mn); o[r] *= alpha; }
                    const u32x4 p0 = {pack_bf16(s[0], s[1]), pack_bf16(s[2], s[3]), pack_bf16(s[4], s[5]), pack_bf16(s[6], s[7])};
                    const u32x4 p1 = {pack_bf16(s[8], s[9]), pack_bf16(s[10], s[11]), pack_bf16(s[12], s[13]), pack_bf16(s[14], s[15])};
                    f32x4 t4 = {0, 0, 0, 0};
                    o = MFMA32(f[2], __builtin_bit_cast(bf16x8, p0), o);
                    o = MFMA32(f[3], __builtin_bit_cast(bf16x8, p1), o);
                    t4 = ROWSUM(p0, p1, t4);
                    l = l * alpha + (t4[0] + t4[1]);
                };
                upd(qA0, qA1, oA, lA, mA);
                upd(qB0, qB1, oB, lB, mB);
            }
            store();
        }
    }
}

DI void ln_row(const u32x4 (&v)[2], __hip_bfloat16* __restrict__ Y, unsigned off) {
    float s1 = 0.f, s2 = 0.f;
#pragma unroll
    for (int e = 0; e < 8; ++e) s1 = dot2bf(v[e >> 2][e & 3], 0x3f803f80u, s1);
    const float mean = wave_sum(s1) * (1.f / 1024.f);
#pragma unroll
    for (int e = 0; e < 8; ++e) {
        const float d0 = bf_lo(v[e >> 2][e & 3]) - mean, d1 = bf_hi(v[e >> 2][e & 3]) - mean;
        s2 += d0 * d0 + d1 * d1;
    }
    const float mb = round_bf16(mean);
    const float rden = __builtin_amdgcn_rcpf(round_bf16(__builtin_amdgcn_sqrtf(round_bf16(round_bf16(wave_sum(s2) * (1.f / 1024.f)) + 1e-5f))));
#pragma unroll
    for (int hh = 0; hh < 2; ++hh) {
        u32x4 y;
#pragma unroll
        for (int e = 0; e < 4; ++e) {
            const unsigned d = pack_bf16(bf_lo(v[hh][e]) - mb, bf_hi(v[hh][e]) - mb);
            y[e] = pack_bf16(bf_lo(d) * rden, bf_hi(d) * rden);
        }
        st16((char*)Y + hh * 131072, off, y);
    }
}

extern "C" __global__ __launch_bounds__(512, 2) void mega_kernel(const __hip_bfloat16* x, const __hip_bfloat16* wqkvA,
                                                            const __hip_bfloat16* woA, const __hip_bfloat16* wqkvB,
                                                            const __hip_bfloat16* woB, __hip_bfloat16* out,
                                                            char* ws) {
    char* const smem = smem_base;
    typedef __hip_bfloat16 bf;
    const int w8 = __builtin_amdgcn_readfirstlane(threadIdx.x >> 6);
    if (w8 == 7 && (blockIdx.x >> 3) < 10) code_touch(blockIdx.x >> 3, threadIdx.x & 63);
    asm volatile("" :: "s"(x), "s"(wqkvA), "s"(woA), "s"(wqkvB), "s"(woB), "s"(out), "s"(ws));
    STAMP_DECL; STAMPR(14); STAMP(0);
    u64 tag;
    {
        const int lane = threadIdx.x & 63;
        const Ids I = ids();
        const unsigned off = (unsigned)((I.po * 128 + I.co * 8 + w8) * 2048 + lane * 16);
        u32x4 v[2];
        u32x2 ep;
        asm volatile("global_load_dwordx4 %0, %3, %4\n\tglobal_load_dwordx4 %1, %3, %4 offset:1024\n\tglobal_load_dwordx2 %2, %5, %6 sc1"
                     : "=&v"(v[0]), "=&v"(v[1]), "=&v"(ep) : "v"(off), "s"(x), "v"(0), "s"(ws + WS_EPOCH) : "memory");
        if (w8 == 7) {
            const int l = lane & 15, k = l % 5;
            const unsigned o = (l >= 10 || k == 3) ? flag_off(l, I.cq, I.pq, 0) : (k == 0 || k == 2) ? flag_off(l, I.pq, I.cq, 0) : flag_off(l, I.po, I.co, 0);
            touch_at(ws + WS_FLAG, o);
        } else if (w8 >= 4) touch(wqkvA, (w8 - 4) * 256 + blockIdx.x, lane);
        if (w8 >= 4) asm volatile("s_waitcnt vmcnt(2)" ::: "memory"); else asm volatile("s_waitcnt vmcnt(1)" ::: "memory");
        asm volatile("" : "+v"(v[0]), "+v"(v[1]));
        ln_row(v, (bf*)(ws + WS_Y0), (unsigned)(I.po * 262144 + (lane >> 3) * 16384 + (I.co * 8 + w8) * 128 + (lane & 7) * 16));
        if (w8 >= 4) asm volatile("s_waitcnt vmcnt(3)" ::: "memory"); else asm volatile("s_waitcnt vmcnt(2)" ::: "memory");
        asm volatile("" : "+v"(ep));
        tag = (((u64)(unsigned)__builtin_amdgcn_readfirstlane(ep[1]) << 32) | (unsigned)__builtin_amdgcn_readfirstlane(ep[0])) + 1;
    }
    if (w8 >= 4) {
        int w = w8 & 3;
#pragma unroll 1
        for (int it = 0; it < 2; ++it) {
            const int lane = lane_id();
            asm volatile("" : "+s"(x), "+s"(wqkvA), "+s"(woA), "+s"(wqkvB), "+s"(woB), "+s"(ws), "+s"(w));
            const unsigned lds0 = (unsigned)(uintptr_t)smem;
            u32x4 cb[8];
            const unsigned wv = wo_off(w, lane), rv = res_off(true, it, lane), cv = colsum_off(w, lane);
            qkv_loader<true>(smem, w, ws, it * 5, tag, (const bf*)(ws + (it ? WS_X2 : WS_Y0)), wqkvA, (const float*)(ws + WS_ST + 262144),
                             ws + WS_CSP, it == 0, woA, [&] {
                if (w < 3) {
                    colsum_load(cb, wqkvB, cv);
                    asm volatile("s_waitcnt vmcnt(8)" ::: "memory");
                } else {
                    touch(woA, blockIdx.x, lane);
                    asm volatile("s_waitcnt vmcnt(1)" ::: "memory");
                }
            }, [&](int part) {
                if (part == 0) wo_load(woA, wv, 29, 32, lds0, w); else wo_load(woA, wv, WA, 29, lds0, w);
            } SP_ARG);
            STAMP(3);
            u32x4 res[6];
            res_load(res, it ? (const void*)(ws + WS_X2) : x, true, it, rv, w);
            if (it) __builtin_amdgcn_s_sleep(WS1);
            wo_load(woA, wv, 0, 7, lds0, w);
            BAR();
            wo_load(woA, wv, 7, WA, lds0, w);
            STAMP(4);
            out_handoff(smem, 147456, res, w, ws, it * 5 + 1, tag, lane, cb, it ? -1 : 1);
            STAMP(5);
            qkv_loader<false>(smem, w, ws, it * 5 + 2, tag, (const bf*)(ws + (it ? WS_X3 : WS_X1)), wqkvB, (const float*)(ws + WS_ST + it * 524288), ws + WS_CSP + 196608,
                              false, nullptr, [] {}, [](int) {} SP_ARG);
            STAMP(8);
            attn_loader(smem, w, ws, it * 5 + 3, tag, (const bf*)(ws + WS_KVF + it * 8 * MBy), woB, ws + (it ? WS_X3 : WS_X1),
                        it ? nullptr : wqkvA SP_ARG);
            STAMP(13); STAMPR(15); STAMP_FLUSH(it);
        }
        if (blockIdx.x == 0 && w == 0 && lane_id() == 0) __hip_atomic_store((g_u64ptr)(ws + WS_EPOCH), tag, __ATOMIC_RELAXED, __HIP_MEMORY_SCOPE_AGENT);
    } else {
        __builtin_amdgcn_s_setprio(2);
#pragma unroll 1
        for (int it = 0; it < 2; ++it) {
            u32x4 q4[4];
            asm volatile("" : "+s"(ws), "+s"(wqkvA), "+s"(wqkvB));
            qkv_compute<true>(smem, w8, wqkvA, q4, nullptr, (bf*)(ws + WS_OF + it * 8 * MBy), it == 0,
                              ws + WS_ST + 262144, ws + WS_WN + blockIdx.x * 16 SP_ARG);
            STAMP(2);
            out_compute<true>(smem, w8, (const bf*)(ws + WS_OF + it * 8 * MBy),
                              (bf*)(ws + (it ? WS_X3 : WS_X1)), (float*)(ws + WS_ST + it * 524288) SP_ARG);
            STAMP(6);
            qkv_compute<false>(smem, w8, wqkvB, q4, (bf*)(ws + WS_KVF + it * 8 * MBy), nullptr, false,
                               ws + WS_ST + it * 524288, ws + WS_WN + 4096 + blockIdx.x * 16 SP_ARG);
            attn_compute(smem, w8, q4, (const bf*)(ws + WS_KVF + it * 8 * MBy), (bf*)(ws + WS_OF + (it * 8 + 4) * MBy) SP_ARG);
            out_compute<false>(smem, w8, (const bf*)(ws + WS_OF + (it * 8 + 4) * MBy),
                               it ? out : (bf*)(ws + WS_X2), it ? nullptr : (float*)(ws + WS_ST + 262144) SP_ARG);
            STAMP(13); STAMPR(15); STAMP_FLUSH(it);
        }
    }
}

extern "C" __global__ void zz_code_end_kernel() {}

extern "C" void kernel_launch(void* const* d_in, const int* in_sizes, int n_in, void* d_out, int out_size, void* d_ws,
                              size_t ws_size, hipStream_t stream) {
    typedef __hip_bfloat16 bf;
    (void)hipFuncSetAttribute((const void*)mega_kernel, hipFuncAttributeMaxDynamicSharedMemorySize, CH_LDS);
    mega_kernel<<<256, 512, CH_LDS, stream>>>((const bf*)d_in[0], (const bf*)d_in[1], (const bf*)d_in[2], (const bf*)d_in[4], (const bf*)d_in[5],
                                              (bf*)d_out, (char*)d_ws);
}
```

```cpp
#include <hip/hip_runtime.h>
#include <hip/hip_bf16.h>
#include <stdint.h>

typedef __attribute__((ext_vector_type(8))) short bf16x8;
typedef __attribute__((ext_vector_type(4))) short s16x4;
typedef __attribute__((ext_vector_type(4))) float f32x4;
typedef __attribute__((ext_vector_type(2))) float f32x2;
typedef __attribute__((ext_vector_type(16))) float f32x16;
typedef __attribute__((ext_vector_type(4))) unsigned u32x4;
typedef __attribute__((ext_vector_type(2))) unsigned u32x2;
typedef __attribute__((ext_vector_type(2))) __bf16 bf16x2_t;
typedef __attribute__((ext_vector_type(8))) __bf16 bf8_t;
typedef __attribute__((ext_vector_type(16))) __bf16 bf16v_t;
typedef __attribute__((address_space(3))) const char* lds_cptr;
typedef __attribute__((address_space(3))) char* lds_ptr;

#define DI __device__ __forceinline__
#define MFMA16(a, b, c) __builtin_amdgcn_mfma_f32_16x16x32_bf16((a), (b), (c), 0, 0, 0)
#define MFMA32(a, b, c) __builtin_amdgcn_mfma_f32_32x32x16_bf16((a), (b), (c), 0, 0, 0)
#define SBAR() __builtin_amdgcn_sched_barrier(0)
#define STAMP(i)
#define STAMPR(i)
#define STAMP_DECL
#define STAMP_FLUSH(it)
#define SP_PARAM
#define SP_ARG
typedef unsigned long long u64;
typedef __attribute__((ext_vector_type(32))) unsigned u32x32;

constexpr float QSCALE = 0.17677669529663687f * 1.4426950408889634f;
constexpr float ATT_SCALE = 0.17677669529663687f;
constexpr float LOG2E = 1.4426950408889634f;
constexpr float NEG_MASK = -1e9f / ATT_SCALE;

DI int lane_id() { int l; asm volatile("v_mbcnt_lo_u32_b32 %0, -1, 0\n\tv_mbcnt_hi_u32_b32 %0, -1, %0" : "=v"(l)); return l; }
DI unsigned pack_bf16(float lo, float hi) {
    f32x2 v = {lo, hi};
    bf16x2_t r = __builtin_convertvector(v, bf16x2_t);
    return __builtin_bit_cast(unsigned, r);
}
DI float bf_lo(unsigned u) { return __uint_as_float(u << 16); }
DI float bf_hi(unsigned u) { return __uint_as_float(u & 0xffff0000u); }
DI float round_bf16(float x) { return bf_lo(pack_bf16(x, 0.f)); }
DI void ln_stats(float sum, float sumsq, float& mean_out, float& rstd_out) {
    float mean;
    asm("v_mul_f32 %0, 0x3a800000, %1" : "=v"(mean) : "v"(sum));
    const float var = fmaxf(__builtin_fmaf(-mean, mean, sumsq * (1.f / 1024.f)), 0.f);
    mean_out = round_bf16(mean);
    rstd_out = __builtin_amdgcn_rcpf(round_bf16(__builtin_amdgcn_sqrtf(round_bf16(round_bf16(var) + 1e-5f))));
}

DI void glds16(const void* sbase, unsigned voff, unsigned ldsaddr) {
    asm volatile("s_mov_b32 m0, %0\n\ts_nop 0\n\tglobal_load_lds_dwordx4 %1, %2"
                 :: "s"(ldsaddr), "v"(voff), "s"(sbase) : "memory");
}
DI void glds16c(const void* sbase, unsigned voff, unsigned ldsaddr) {
    asm volatile("s_mov_b32 m0, %0\n\ts_nop 0\n\tglobal_load_lds_dwordx4 %1, %2 sc1"
                 :: "s"(ldsaddr), "v"(voff), "s"(sbase) : "memory");
}
DI void st16(void* sbase, unsigned voff, u32x4 v) {
    asm volatile("global_store_dwordx4 %0, %1, %2 sc1\n\ts_nop 1" :: "v"(voff), "v"(v), "s"(sbase) : "memory");
}
template <int OFF> DI void st16o(void* sbase, unsigned voff, u32x4 v) {
    asm volatile("global_store_dwordx4 %0, %1, %2 offset:%3 sc1\n\ts_nop 1" :: "v"(voff), "v"(v), "s"(sbase), "n"(OFF) : "memory");
}
DI bf16x8 lds_b128(lds_cptr p) { return *(const __attribute__((address_space(3))) bf16x8*)p; }
DI s16x4 lds_tr(lds_cptr p) {
    return __builtin_amdgcn_ds_read_tr16_b64_v4i16((__attribute__((address_space(3))) s16x4*)p);
}
DI bf16x8 cat8(s16x4 a, s16x4 b) { return __builtin_shufflevector(a, b, 0, 1, 2, 3, 4, 5, 6, 7); }
DI float dot2bf(unsigned a, unsigned b, float c) {
    return __builtin_amdgcn_fdot2_f32_bf16(__builtin_bit_cast(bf16x2_t, a), __builtin_bit_cast(bf16x2_t, b), c, false);
}
DI float sum_g(float v) {
    auto a = __builtin_amdgcn_permlane16_swap(__float_as_uint(v), __float_as_uint(v), false, false);
    v = __uint_as_float(a[0]) + __uint_as_float(a[1]);
    auto b = __builtin_amdgcn_permlane32_swap(__float_as_uint(v), __float_as_uint(v), false, false);
    return __uint_as_float(b[0]) + __uint_as_float(b[1]);
}
DI float wave_sum(float v) {
    v += __builtin_bit_cast(float, __builtin_amdgcn_update_dpp(0, __builtin_bit_cast(int, v), 0xB1, 0xF, 0xF, true));
    v += __builtin_bit_cast(float, __builtin_amdgcn_update_dpp(0, __builtin_bit_cast(int, v), 0x4E, 0xF, 0xF, true));
    v += __builtin_bit_cast(float, __builtin_amdgcn_update_dpp(0, __builtin_bit_cast(int, v), 0x141, 0xF, 0xF, true));
    v += __builtin_bit_cast(float, __builtin_amdgcn_update_dpp(0, __builtin_bit_cast(int, v), 0x140, 0xF, 0xF, true));
    return sum_g(v);
}
DI unsigned wave_min(unsigned v) {
    v = min(v, (unsigned)__builtin_amdgcn_update_dpp(0, (int)v, 0xB1, 0xF, 0xF, true));
    v = min(v, (unsigned)__builtin_amdgcn_update_dpp(0, (int)v, 0x4E, 0xF, 0xF, true));
    v = min(v, (unsigned)__builtin_amdgcn_update_dpp(0, (int)v, 0x141, 0xF, 0xF, true));
    v = min(v, (unsigned)__builtin_amdgcn_update_dpp(0, (int)v, 0x140, 0xF, 0xF, true));
    const auto a = __builtin_amdgcn_permlane16_swap(v, v, false, false);
    v = min(a[0], a[1]);
    const auto b = __builtin_amdgcn_permlane32_swap(v, v, false, false);
    return min(b[0], b[1]);
}
DI float amax3(float a, float b, float c) { float r; asm("v_max3_f32 %0, %1, %2, %3" : "=v"(r) : "v"(a), "v"(b), "v"(c)); return r; }
DI float max2f(float a, float b) { return __builtin_amdgcn_fmed3f(a, b, INFINITY); }
DI float max_h(float v) {
    auto b = __builtin_amdgcn_permlane32_swap(__float_as_uint(v), __float_as_uint(v), false, false);
    return max2f(__uint_as_float(b[0]), __uint_as_float(b[1]));
}

constexpr unsigned OFF_DUMP = 145408;
extern __shared__ __attribute__((aligned(16))) char smem_base[];
DI void touch_at(const void* p, unsigned off) {
    asm volatile("s_mov_b32 m0, %0\n\ts_nop 0\n\tglobal_load_lds_dword %1, %2"
                 :: "s"((unsigned)(uintptr_t)smem_base + OFF_DUMP), "v"(off), "s"(p) : "memory");
}
DI void touch(const void* p, int slice, int lane) { touch_at(p, (unsigned)(slice * 64 + lane) * 128u); }
DI void code_touch(int slice, int lane) {
    unsigned long long c0, c1;
    asm volatile("s_getpc_b64 s[96:97]\n\ts_add_u32 s96, s96, mega_kernel@rel32@lo+4\n\ts_addc_u32 s97, s97, mega_kernel@rel32@hi+12\n\ts_mov_b64 %0, s[96:97]\n\t"
                 "s_getpc_b64 s[96:97]\n\ts_add_u32 s96, s96, zz_code_end_kernel@rel32@lo+4\n\ts_addc_u32 s97, s97, zz_code_end_kernel@rel32@hi+12\n\ts_mov_b64 %1, s[96:97]"
                 : "=s"(c0), "=s"(c1) :: "s96", "s97", "scc");
    touch_at((const void*)c0, min((unsigned)(slice * 64 + lane) * 128u, (unsigned)(c1 - c0)));
}

#define BAR() asm volatile("s_barrier" ::: "memory")
#define WAIT_LGKM() do { SBAR(); __builtin_amdgcn_s_waitcnt(0xC07F); SBAR(); } while (0)
#define WAIT_LGKM_BAR() do { SBAR(); __builtin_amdgcn_s_waitcnt(0xC07F); asm volatile("s_barrier" ::: "memory"); SBAR(); } while (0)
#define VM_BAR(n) asm volatile("s_waitcnt vmcnt(" #n ")\n\ts_barrier" ::: "memory")
constexpr int CH_LDS = 163840;
typedef __attribute__((address_space(3))) unsigned* lds_uptr;
typedef __attribute__((address_space(1))) u64* g_u64ptr;

constexpr size_t MBy = 1 << 20;
constexpr size_t WS_X3 = 0, WS_X2 = 4 * MBy, WS_X1 = 8 * MBy, WS_OF = 12 * MBy  , WS_KVF = 36 * MBy  ,
                 WS_ST = 52 * MBy  , WS_CSP = 54 * MBy  , WS_FLAG = 55 * MBy  ,
                 WS_WN = WS_CSP + 458752  , WS_EPOCH = WS_CSP + 524288, WS_Y0 = 56 * MBy;
DI unsigned flag_off(int set, int group, int consumer, int producer) { return (unsigned)(set * 65536 + group * 4096 + consumer * 256 + producer * 8); }
DI void flag_store(char* ws, int set, int group, int producer, u64 tag, int lane) {
    asm volatile("" : "+s"(tag));
    if (lane < 16) __hip_atomic_store((g_u64ptr)(ws + WS_FLAG + flag_off(set, group, lane, producer)), tag, __ATOMIC_RELAXED, __HIP_MEMORY_SCOPE_AGENT);
}
DI void flag_poll(const char* ws, unsigned off, u64 tag) {
    asm volatile("" : "+s"(tag));
#pragma unroll 1
    for (int spin = 0; spin < (1 << 16); ++spin) {
        const u64 v = __hip_atomic_load((g_u64ptr)(ws + WS_FLAG + off), __ATOMIC_RELAXED, __HIP_MEMORY_SCOPE_AGENT);
        if (__all(v == tag)) break;
        __builtin_amdgcn_s_sleep(1);
    }
    asm volatile("" ::: "memory");
}
template <bool DRAIN, bool ALLPOLL>
DI void signal_poll(int follower, const char* ws, unsigned soff, unsigned poff, u64 pmask, u64 tag, u64& land) {
    u64 tagv, zero;
    asm volatile("s_not_b64 s[96:97], %[ts]\n\tv_mov_b64 %[tag], %[ts]\n\tv_mov_b64 %[zero], 0\n\tv_mov_b64 %[land], s[96:97]\n\t"
                 ".if %[dr]\n\ts_waitcnt vmcnt(0)\n\t.endif\n\ts_barrier\n\t"
                 "s_cmp_lg_u32 %[fl], 0\n\ts_cbranch_scc1 .Lsig_fol_%=\n\t"
                 "s_mov_b64 exec, 0xffff\n\t"
                 "global_store_dwordx2 %[so], %[tag], %[base] sc1\n"
                 ".if %[ap]\n.Lsig_fol_%=:\n.endif\n\t"
                 "s_mov_b64 exec, %[pm]\n\t"
                 "s_movk_i32 s96, 0x7fff\n"
                 ".Lsig_loop_%=:\n\t"
                 "global_atomic_or_x2 %[land], %[po], %[zero], %[base] sc0 sc1\n\t"
                 "s_sleep 1\n\t"
                 "s_waitcnt vmcnt(3)\n\t"
                 "v_cmp_ne_u64 vcc, %[land], %[tag]\n\t"
                 "s_sub_u32 s96, s96, 1\n\t"
                 "s_cbranch_scc1 .Lsig_out_%=\n\t"
                 "s_cbranch_vccnz .Lsig_loop_%=\n"
                 ".Lsig_out_%=:\n\t"
                 "s_mov_b64 exec, -1\n"
                 ".if !%[ap]\n.Lsig_fol_%=:\n.endif"
                 : [land] "=&v"(land), [tag] "=&v"(tagv), [zero] "=&v"(zero)
                 : [fl] "s"(follower), [so] "v"(soff), [po] "v"(poff), [ts] "s"(tag), [base] "s"(ws + WS_FLAG), [pm] "s"(pmask), [ap] "n"(ALLPOLL ? 1 : 0), [dr] "n"(DRAIN ? 1 : 0)
                 : "memory", "scc", "vcc", "s96", "s97");
}
struct Ids { int pq, cq, po, co; };
DI Ids ids() {
    int bid = blockIdx.x; asm volatile("" : "+s"(bid));
    const int xcd = bid & 7, idx = bid >> 3;
    int pq, cq, po, co;
#define IDS_BODY pq = (xcd & 1) * 8 + (idx & 7); cq = (xcd >> 1) * 4 + (idx >> 3); po = (xcd & 3) * 4 + (idx & 3); co = (xcd >> 2) * 8 + (idx >> 2);
    IDS_BODY
    return Ids{pq, cq, po, co};
}

DI void slow_warm(unsigned dump, const void* W, int w, int lane) {
    int bid = blockIdx.x; asm volatile("" : "+s"(bid));
    const int xcd = bid & 7, idx = bid >> 3;
    const int sc = (3 - (int)((uintptr_t)W >> 7)) & 7;
    if ((sc >> 2) != ((xcd >> 1) & 1)) return;
    const char* src = (const char*)W + (idx + 32 * w) * 49152 + (sc + (xcd >> 2) * 8) * 128;
#pragma unroll
    for (int G = 0; G < 3; ++G) glds16(src + G * 2048, (unsigned)((lane >> 3) * 6144 + (lane & 7) * 16), dump);
}

template <int V> struct IC { static constexpr int value = V; };
template <bool LOCAL> struct QC {
    static constexpr int AROWS = LOCAL ? 144 : 128;
    static constexpr int ABYTES = AROWS * 128;
    static constexpr int STAGE = ABYTES + 24576;
    static constexpr int OFF_MEAN = 143360;
    static constexpr int OFF_RSTD = OFF_MEAN + 576;
    static constexpr int OFF_CS = OFF_RSTD + 576;
};
constexpr int OFF_EX = 131072;

DI unsigned colsum_off(int G, int lane) {
    const Ids I = ids();
    unsigned off = (unsigned)(I.pq * 64 + (lane >> 3)) * 6144u + (unsigned)(G * 2048 + I.cq * 128 + (lane & 7) * 16);
    asm volatile("" : "+v"(off));
    return off;
}
DI void colsum_load(u32x4 (&c)[8], const void* W, unsigned off) {
#define CS_NEXT "s_add_u32 s96, s96, 0xc000\n\ts_addc_u32 s97, s97, 0\n\t"
    asm volatile("s_cmp_eq_u64 %9, 0\n\ts_cbranch_scc1 .Lcs_skip_%=\n\ts_mov_b64 s[96:97], %9\n\t"
                 "global_load_dwordx4 %0, %8, s[96:97]\n\t" CS_NEXT "global_load_dwordx4 %1, %8, s[96:97]\n\t" CS_NEXT
                 "global_load_dwordx4 %2, %8, s[96:97]\n\t" CS_NEXT "global_load_dwordx4 %3, %8, s[96:97]\n\t" CS_NEXT
                 "global_load_dwordx4 %4, %8, s[96:97]\n\t" CS_NEXT "global_load_dwordx4 %5, %8, s[96:97]\n\t" CS_NEXT
                 "global_load_dwordx4 %6, %8, s[96:97]\n\t" CS_NEXT "global_load_dwordx4 %7, %8, s[96:97]\n"
                 ".Lcs_skip_%=:"
                 : "=&v"(c[0]), "=&v"(c[1]), "=&v"(c[2]), "=&v"(c[3]), "=&v"(c[4]), "=&v"(c[5]), "=&v"(c[6]), "=&v"(c[7])
                 : "v"(off), "s"(W) : "memory", "scc", "s96", "s97");
}
DI void colsum_store(u32x4 (&c)[8], char* ws, int mat, int G, int lane) {
    asm volatile("" : "+v"(c[0]), "+v"(c[1]), "+v"(c[2]), "+v"(c[3]), "+v"(c[4]), "+v"(c[5]), "+v"(c[6]), "+v"(c[7]));
    const Ids I = ids();
    const int m = lane & 15;
    const unsigned one = (m & 1) ? 0xbf800000u : 0xbf80u;
    const u32x4 sl = {(m >> 1) == 0 ? one : 0u, (m >> 1) == 1 ? one : 0u, (m >> 1) == 2 ? one : 0u, (m >> 1) == 3 ? one : 0u};
    f32x4 d = {0, 0, 0, 0};
#pragma unroll
    for (int i = 0; i < 8; ++i) d = MFMA16(__builtin_bit_cast(bf16x8, sl), __builtin_bit_cast(bf16x8, c[i]), d);
#pragma unroll
    for (int r = 0; r < 4; ++r)
        d[r] += __int_as_float(__builtin_amdgcn_update_dpp(0, __float_as_int(d[r]), 0x128, 0xF, 0xF, true));
    if (lane < 32 && (lane & 8) == 0)
        st16(ws + WS_CSP + mat * 196608, (unsigned)((I.cq * 16 + I.pq) * 768 + G * 256 + (lane & 7) * 32 + (lane >> 4) * 16), __builtin_bit_cast(u32x4, d));
    typedef __attribute__((ext_vector_type(2))) unsigned short u16x2;
    u16x2 mn = {0x7fff, 0x7fff};
#pragma unroll
    for (int e = 0; e < 4; ++e) {
        u16x2 mx = {0, 0};
#pragma unroll
        for (int i = 0; i < 8; ++i) { const unsigned u = c[i][e] & 0x7fff7fffu; mx = __builtin_elementwise_max(mx, __builtin_bit_cast(u16x2, u)); }
        mn = __builtin_elementwise_min(mn, mx);
    }
    const unsigned wn = wave_min((unsigned)min(mn[0], mn[1])) << 16;
    if (lane == 0) __hip_atomic_store((__attribute__((address_space(1))) unsigned*)(ws + WS_WN + mat * 4096 + blockIdx.x * 16 + G * 4), wn,
                                      __ATOMIC_RELAXED, __HIP_MEMORY_SCOPE_AGENT);

}

template <bool LOCAL>
struct WFirst {
    unsigned v[2];
    DI WFirst(int w, int ct, int lane) {
#pragma unroll
        for (int i = 0; i < 2; ++i) {
            const int k = 8 * i + (lane >> 3), pos = lane & 7;
            v[i] = (unsigned)k * 6144u + (unsigned)(w * 1024 + ct * 64 + ((pos >> 1) ^ (((k >> 1) & 1) | (i << 1))) * 16 + (pos & 1) * 8) * 2u;
        }
        asm volatile("" : "+v"(v[0]), "+v"(v[1]));
    }
    DI void issue(const void* W, unsigned lds0, int w, int i0, int i1) const {
#pragma unroll
        for (int i = i0; i < i1; ++i)
            glds16((const char*)W + (size_t)(i >> 1) * (16 * 6144), v[i & 1], lds0 + (i >> 3) * QC<LOCAL>::STAGE + QC<LOCAL>::ABYTES + (w * 8 + (i & 7)) * 1024);
    }
};

template <bool LOCAL, class F, class F2>
DI void qkv_loader(char* smem, int w, char* ws, int set, u64 tag, const __hip_bfloat16* __restrict__ X, const __hip_bfloat16* __restrict__ W,
                   const float* __restrict__ statp, const char* __restrict__ csp, bool ident, const void* nextW, F tail, F2 mid SP_PARAM) {
    typedef QC<LOCAL> C;
    constexpr int NT = 16;
    const int lane = lane_id();
    asm volatile("" : "+s"(w));
    const int idx = blockIdx.x >> 3;
    const Ids I = ids();
    const int panel = I.pq, ct = I.cq;
    const int R0 = panel * 128;
    const unsigned lds0 = (unsigned)(uintptr_t)smem;
    auto grow = [&](int i) -> int {
        if (!LOCAL) return R0 + i;
        if (i < 128) { const int t = (i >> 4) & 3, a = i & 15; return R0 + (i & 64) + 2 * (16 * (t & 1) + a) + (t >> 1); }
        const int a = i & 15; const int r = R0 - 16 + 2 * (a & 7) + (a >> 3);
        return r < 0 ? 0 : r;
    };
    unsigned voffA[5], voffB[6];
#pragma unroll
    for (int i = 0; i < 6; ++i) {
        const int j = w + 4 * i, G = j >> 3, k = 8 * (j & 7) + (lane >> 3);
        const int pos = lane & 7, f = ((k >> 1) & 1) | (((k >> 3) & 1) << 1);
        const int seg = (pos >> 1) ^ f;
        voffB[i] = (unsigned)k * 6144u + (unsigned)(G * 1024 + ct * 64 + seg * 16 + (pos & 1) * 8) * 2u;
    }
    auto issueW = [&](int t, int buf) {
        const char* wb = (const char*)W + (size_t)t * (64 * 6144);
        const unsigned sb = lds0 + buf * C::STAGE;
#pragma unroll
        for (int i = 0; i < 6; ++i) glds16(wb, voffB[i], sb + C::ABYTES + (w + 4 * i) * 1024);
    };
    asm volatile("" : "+v"(voffB[0]), "+v"(voffB[1]), "+v"(voffB[2]), "+v"(voffB[3]), "+v"(voffB[4]), "+v"(voffB[5]));
    const WFirst<LOCAL> first(w, ct, lane);
    const int g = lane >> 4, r = lane & 15, pos = 32 * w + 2 * r + (g >> 1);
    const int lr = !LOCAL ? (pos & 64) + 2 * (pos & 31) + ((pos >> 5) & 1)
                          : (w == 0 && (g & 1)) ? 128 + (r & 7) + 8 * (g >> 1) : (pos & 64) + ((pos & 1) * 2 + ((pos >> 5) & 1)) * 16 + ((pos >> 1) & 15);
    unsigned so = (unsigned)(panel * 16384 + g * 1024 + w * 256 + r * 16), ho = (unsigned)(g * 1024 + (panel ? panel * 16384 - 15488 + (r & 7) * 16 : 0));
    unsigned co = (unsigned)((ct * 16 + g) * 768 + w * 192 + r * 12);
    lds_ptr mp = (lds_ptr)smem + C::OFF_MEAN + lr * 4, cp = (lds_ptr)smem + C::OFF_CS + w * 192 + r * 12 + ((g & 1) ? 8 : g * 2);
    unsigned fo = flag_off((LOCAL ? 13 : 10) + min(g, 2), ct, panel, r);
    asm volatile("" : "+v"(so), "+v"(ho), "+v"(co), "+v"(mp), "+v"(cp), "+v"(fo));
#pragma unroll
    for (int i = 0; i < 5; ++i) {
        const int j = (i < 4) ? (w + 4 * i) : (16 + (w & 1));
        const int row = 8 * j + (lane >> 3);
        const int c = (lane & 7) ^ ((row >> 1) & 7);
        const int gr = grow(row < C::AROWS ? row : 0);
        voffA[i] = (unsigned)((gr >> 7) * 262144 + (gr & 127) * 128 + c * 16);
        asm volatile("" : "+v"(voffA[i]));
    }
    auto issueA = [&](int t, int buf) {
        const char* xa = (const char*)X + t * 16384;
        const unsigned sb = lds0 + buf * C::STAGE;
        if (LOCAL && w < 2) glds16c(xa, voffA[4], sb + (16 + w) * 1024);
#pragma unroll
        for (int i = 0; i < 4; ++i) glds16c(xa, voffA[i], sb + (w + 4 * i) * 1024);
    };
    u64 land;
    if (w == 3) __builtin_amdgcn_s_setprio(3);
    signal_poll<LOCAL, false>(w - 3, ws, flag_off(set, I.po, lane & 15, I.co),
                              flag_off(set, (lane & 16) && panel ? panel - 1 : panel, ct, lane & 15),
                              !LOCAL ? 0xffffull : ident ? 0xc000ffffull : 0xffffffffull, tag, land);
    if (w < 3) first.issue(W, lds0, w, 0, 12);
    BAR();
    STAMP(LOCAL ? 1 : 6);
    issueA(0, 0);
    issueA(1, 1);
    if (w < 3) { first.issue(W, lds0, w, 12, 16); asm volatile("s_waitcnt vmcnt(8)" ::: "memory"); }
    else asm volatile("s_waitcnt vmcnt(4)" ::: "memory");
    BAR();
    asm volatile("" :: "v"(land));
    __builtin_amdgcn_s_setprio(0);
    STAMP(LOCAL ? 2 : 7);
    issueW(2, 2);
    issueA(2, 2);
    VM_BAR(10);
    asm volatile("global_load_dwordx2 %0, %1, %2 sc1" : "=&v"(land) : "v"(fo), "s"(ws + WS_FLAG) : "memory");
    issueA(3, 0); issueW(3, 0);
    int buf = 1;
#pragma unroll 1
    for (int t = 1; t < NT - 3; ++t) {
        VM_BAR(10);
        issueA(t + 3, buf); issueW(t + 3, buf);
        if (t == 10 && idx < 3) code_touch(idx * 4 + w, lane);
        if (LOCAL && t == 11)
            glds16(nextW, (unsigned)((idx + 32 * w) * 8 + (lane >> 3)) * 2048u +
                   (unsigned)(((I.co & 8) + ((3 - (int)((uintptr_t)nextW >> 7)) & 7)) * 128 + (lane & 7) * 16), lds0 + OFF_DUMP);
        buf = (buf == 2) ? 0 : buf + 1;
    }
    typedef __attribute__((ext_vector_type(3))) float f32x3;
    const bool halo = LOCAL && w == 0;
    asm volatile("" : "+v"(land));
    if (!ident) {
        if (!__all(land == tag)) flag_poll(ws, fo, tag);
        f32x3 cs[4];
        f32x4 sv[4], hv[4];
        typedef __attribute__((address_space(3))) float* lds_f;
        auto add = [](float a, float b) { float r; asm("v_add_f32 %0, %1, %2" : "=v"(r) : "v"(a), "v"(b)); return r; };
        auto fold32 = [](float a, float b) { asm("s_nop 1\n\tv_permlane32_swap_b32 %0, %1\n\tv_add_f32 %0, %0, %1" : "+v"(a), "+v"(b)); return a; };
        auto fold16 = [](float a, float b) { asm("s_nop 1\n\tv_permlane16_swap_b32 %0, %1\n\tv_add_f32 %0, %0, %1" : "+v"(a), "+v"(b)); return a; };
        auto sum4 = [&](auto (&v)[4], int e) { return add(add(v[0][e], v[1][e]), add(v[2][e], v[3][e])); };
#pragma unroll
        for (int i = 0; i < 4; ++i) asm volatile("global_load_dwordx3 %0, %1, %2 sc1" : "=&v"(cs[i]) : "v"(co), "s"(csp + i * 3072) : "memory");
        VM_BAR(14);
#pragma unroll
        for (int i = 0; i < 4; ++i) asm volatile("global_load_dwordx4 %0, %1, %2 sc1" : "=&v"(sv[i]) : "v"(so), "s"((const char*)statp + i * 4096) : "memory");
        if (LOCAL)
            asm volatile("s_cmp_lg_u32 %9, 0\n\ts_cbranch_scc1 .Lhv_skip_%=\n\t"
                         "global_load_dwordx4 %0, %4, %5 sc1\n\tglobal_load_dwordx4 %1, %4, %6 sc1\n\t"
                         "global_load_dwordx4 %2, %4, %7 sc1\n\tglobal_load_dwordx4 %3, %4, %8 sc1\n"
                         ".Lhv_skip_%=:" : "=&v"(hv[0]), "=&v"(hv[1]), "=&v"(hv[2]), "=&v"(hv[3])
                         : "v"(ho), "s"(statp), "s"(statp + 1024), "s"(statp + 2048), "s"(statp + 3072), "s"(w) : "memory", "scc");
        if (halo) asm volatile("s_waitcnt vmcnt(8)" ::: "memory"); else asm volatile("s_waitcnt vmcnt(4)" ::: "memory");
        asm volatile("" : "+v"(cs[0]), "+v"(cs[1]), "+v"(cs[2]), "+v"(cs[3]));
        const float c2 = sum4(cs, 2);
        *(lds_f)cp = fold16(fold32(sum4(cs, 0), sum4(cs, 1)), fold32(c2, c2));
        BAR();
        asm volatile("s_waitcnt vmcnt(0)" : "+v"(sv[0]), "+v"(sv[1]), "+v"(sv[2]), "+v"(sv[3]) :: "memory");
        mid(0);
        const float s1 = fold32(sum4(sv, 0), sum4(sv, 2)), s2 = fold32(sum4(sv, 1), sum4(sv, 3));
        float h1, h2;
        asm("v_mov_b32 %0, %2\n\tv_mov_b32 %1, %3" : "=&v"(h1), "=&v"(h2) : "v"(s1), "v"(s2));
        if (halo) {
            asm volatile("" : "+v"(hv[0]), "+v"(hv[1]), "+v"(hv[2]), "+v"(hv[3]));
            const float u0 = sum4(hv, 0), u1 = sum4(hv, 1), u2 = sum4(hv, 2), u3 = sum4(hv, 3);
            h1 = fold32(u0, panel ? u2 : u0); h2 = fold32(u1, panel ? u3 : u1);
        }
        float mean, rstd;
        ln_stats(fold16(s1, h1), fold16(s2, h2), mean, rstd);
        *(lds_f)mp = mean; *(lds_f)(mp + (C::OFF_RSTD - C::OFF_MEAN)) = rstd;
    } else { VM_BAR(10); tail(); BAR(); mid(0); }
    mid(1);
    WAIT_LGKM_BAR();
}

template <bool LOCAL>
DI void qkv_compute(char* smem, int w, const void* W, u32x4 (&qo4)[4], __hip_bfloat16* __restrict__ kvf, __hip_bfloat16* __restrict__ ofrag, bool ident,
                    const char* statp, const char* wnp SP_PARAM) {
    typedef QC<LOCAL> C;
    constexpr int NT = 16, ABYTES = C::ABYTES, STAGE = C::STAGE;
    const int lane = lane_id();
    asm volatile("" : "+s"(w));
    const int wm = w & 1, wn = w >> 1;
    const Ids I = ids();
    const int panel = I.pq, ct = I.cq;
    const int l15 = lane & 15, g = lane >> 4;
    const lds_cptr sm = (lds_cptr)smem;
    lds_ptr smw = (lds_ptr)smem;

    f32x4 acc[3][4][2];
    f32x4 acch[2];
    float cb;
    f32x4 bias;

    bf16x8 A[2][5][2];
    s16x4 B[3][2][2];
    lds_cptr pa[2], pb[2];
    auto rdA = [&](auto WM, int p, int i) { A[p][i >> 1][i & 1] = lds_b128(pa[i & 1] + ((i >> 1) < 4 ? (i >> 1) * 2048 : 16384 - WM.value * 8192)); };
    auto rdB = [&](int c, int i) { B[c % 3][i >> 1][i & 1] = lds_tr(pb[c & 1] + ((c >> 1) + 1) % 3 * 8192 + (i >> 1) * 4096 + (i & 1) * 512); };
    auto column = [&](auto WM, int p, int c, bool first, auto gap) {
        const int G = ((c >> 1) + 1) % 3, f = c & 1;
        const bool halo = LOCAL && G == 1 + WM.value;
#pragma unroll
        for (int n = 0; n < (halo ? 10 : 8); ++n) {
            const bf16x8 x = A[p][n >> 1][n & 1], y = cat8(B[c % 3][n & 1][0], B[c % 3][n & 1][1]);
            const bf16x8 u = G < 2 ? y : x, v = G < 2 ? x : y;
            f32x4& d = n < 8 ? acc[G][n >> 1][f] : acch[f];
            const f32x4 c0 = bias;
            if (!first || (n & 1)) asm volatile("v_mfma_f32_16x16x32_bf16 %0, %1, %2, %0" : "+v"(d) : "v"(u), "v"(v));
            else if (n == 8) asm volatile("v_mfma_f32_16x16x32_bf16 %0, %1, %2, 0" : "=&v"(d) : "v"(u), "v"(v));
            else asm volatile("v_mfma_f32_16x16x32_bf16 %0, %1, %2, %3" : "=&v"(d) : "v"(u), "v"(v), "v"(c0));
            SBAR();
            gap(n);
            SBAR();
        }
    };

    f32x4 cq[2], ck[2], mu4[5], rs4[5]; float cv[2], mu[5], rs[5];
    u32x4 qfr[4], kfr[4];
    u32x4 va[2][2];
    f32x4 mwin, mfirst;
    typedef __attribute__((address_space(3))) u32x4* lds_u4;
    const bool negpos = (wm == 0) && (panel == 0);
    VM_BAR(0);
    unsigned cm01 = 0, cm23 = 0;
    lds_cptr ka[2] = {sm, sm}, vo2[2] = {sm, sm};
    lds_u4 ex = (lds_u4)(smw + OFF_EX + wn * 6144) + lane;
    lds_cptr s1 = sm + C::OFF_MEAN + wm * 256 + l15 * 4, s4 = sm + C::OFF_MEAN + wm * 256 + g * 16;
    lds_cptr c1 = sm + C::OFF_CS + wn * 128 + l15 * 4, c4 = sm + C::OFF_CS + wn * 128 + g * 16;
    asm volatile("" : "+v"(s1), "+v"(s4), "+v"(c1), "+v"(c4));
    if (LOCAL) {
        unsigned keepv = negpos ? __float_as_uint(NEG_MASK) : 0u;
        asm volatile("" : "+s"(keepv));
        u32x4 mw, mf;
        unsigned prv[4];
#pragma unroll
        for (int r = 0; r < 4; ++r) {
            const int d = l15 - (4 * g + r);
            unsigned out = (unsigned)(((d << 28) >> 31));
            prv[r] = (unsigned)(d >> 31);
            asm volatile("" : "+v"(out), "+v"(prv[r]));
            mw[r] = out & 0xff800000u;
            mf[r] = (out & 0xff800000u) | (~out & keepv);
        }
        const lds_cptr e0 = sm + OFF_EX + wn * 6144;
        ka[0] = e0 + (wm ? lane : 256 + (lane ^ 8)) * 16;         ka[1] = e0 + (wm ? 64 + lane : 256 + lane) * 16;
        vo2[0] = e0 + (wm ? 128 + lane : 320 + (lane ^ 32)) * 16; vo2[1] = e0 + (wm ? 192 + lane : 320 + lane) * 16;
        cm01 = ~((prv[0] >> 16) | (prv[1] << 16)); cm23 = ~((prv[2] >> 16) | (prv[3] << 16));
        mwin = __builtin_bit_cast(f32x4, mw); mfirst = __builtin_bit_cast(f32x4, mf);
        asm volatile("" : "+v"(mwin), "+v"(mfirst), "+v"(cm01), "+v"(cm23), "+v"(ka[0]), "+v"(ka[1]), "+v"(vo2[0]), "+v"(vo2[1]), "+v"(ex));
    }
    typedef const __attribute__((address_space(3))) f32x4* lds_f4; typedef const __attribute__((address_space(3))) float* lds_f1;
    auto mainloop = [&](auto WM) {
        int ty = lane_id();
        const int l15 = ty & 15, g = ty >> 4;
        const int swA = (l15 >> 1) & 7;
        const int q4 = l15 >> 2, p4 = l15 & 3, fk = ((q4 >> 1) & 1) | ((g & 1) << 1);
        lds_cptr pA0 = sm + wm * 8192 + l15 * 128 + ((0 + g) ^ swA) * 16, pA1 = sm + wm * 8192 + l15 * 128 + ((4 + g) ^ swA) * 16;
        lds_cptr pB0 = sm + ABYTES + (8 * g + q4) * 128 + (((2 * wn + 0) ^ fk) * 32) + p4 * 8;
        lds_cptr pB1 = sm + ABYTES + (8 * g + q4) * 128 + (((2 * wn + 1) ^ fk) * 32) + p4 * 8;
        asm volatile("" : "+v"(pA0), "+v"(pA1), "+v"(pB0), "+v"(pB1));
        slow_warm((unsigned)(uintptr_t)smem + OFF_DUMP, W, w, ty);
        unsigned so = (unsigned)(panel * 16384 + wm * 512 + ty * 8);
        const char *p0 = ident ? (const char*)W : statp, *p1 = ident ? (const char*)W : wnp;
        u32x4 wn;
        asm volatile("global_load_dwordx4 %0, %2, %3 sc1" : "=&v"(wn), "+v"(so) : "v"(0), "s"(p1) : "memory");
        BAR();
        STAMP(LOCAL ? 1 : 7);
        f32x2 sq;
        asm volatile("global_load_dwordx2 %0, %1, %2 sc1" : "=&v"(sq) : "v"(so), "s"(p0) : "memory");
        BAR();
        pa[0] = pA0; pa[1] = pA1; pb[0] = pB0; pb[1] = pB1;
#pragma unroll
        for (int i = 0; i < 4; ++i) rdB(0, i);
#pragma unroll
        for (int i = 0; i < 8; ++i) rdA(WM, 0, i);
#pragma unroll
        for (int i = 0; i < 4; ++i) if (LOCAL && WM.value == 0 && i >= 2) rdA(WM, 0, 6 + i); else rdB(1, i);
        SBAR();
        asm volatile("s_waitcnt vmcnt(0)" : "+v"(sq), "+v"(wn) :: "memory");
        const float d = fmaxf(__builtin_fmaf(sq[0] * (-1.f / 64.f), sq[0], sq[1]), 0.f);
        cb = fminf(__builtin_amdgcn_sqrtf(__uint_as_float(wave_min(__float_as_uint(d)))) * 64.f * __uint_as_float(min(min(wn[0], wn[1]), wn[2])), 1.0e30f);
        if (ident) cb = 0.f;
        bias = f32x4{cb, cb, cb, cb};
        asm volatile("s_nop 1" : "+v"(bias));
        WAIT_LGKM();
        int off = 0;
        constexpr bool HK = LOCAL && WM.value == 0;
        auto front = [&](int p, bool first) {
#pragma unroll
            for (int c = 0; c < 4; ++c)
                column(WM, p, c, first, [&](int n) {
                    if (HK && c == 0) { if (n < 2) rdB(1, 2 + n); else if (n < 6) rdB(2, n - 2); }
                    else if (n < 4) rdB(c + 2, n);
                    if (LOCAL && !HK && c == 0 && (n == 4 || n == 5)) rdA(WM, p, 4 + n);
                    if (c == 1 && n == 5) { off = (off == 2 * STAGE) ? 0 : off + STAGE; asm volatile("" : "+s"(off)); }
                    if (c == 2 && n == 4) { pa[0] = pA0 + off; asm volatile("" : "+v"(pa[0])); }
                    if (c == 2 && n == 5) { pa[1] = pA1 + off; asm volatile("" : "+v"(pa[1])); }
                    if (c == 3 && n == 4) { pb[0] = pB0 + off; asm volatile("" : "+v"(pb[0])); }
                    if (c == 3 && n == 5) { pb[1] = pB1 + off; asm volatile("" : "+v"(pb[1])); }
                    if ((c == 0 && n == 3) || ((c == 1 || c == 2 || (HK && c == 0)) && n == 7)) __builtin_amdgcn_s_waitcnt(0xC47F);
                });
            WAIT_LGKM_BAR();
        };
        auto back = [&](int p, bool first) {
            column(WM, p, 4, first, [&](int n) { if (n & 1) rdB(0, n >> 1); else rdA(WM, p ^ 1, n >> 1); });
            column(WM, p, 5, first, [&](int n) {
                if (!(n & 1)) rdA(WM, p ^ 1, 4 + (n >> 1)); else if (HK && n > 4) rdA(WM, p ^ 1, 6 + (n >> 1)); else rdB(1, n >> 1);
                if (n == 7) __builtin_amdgcn_s_waitcnt(0xC87F);
            });
        };
        auto stage = [&](int p, bool first) { front(p, first); back(p, first); };
        stage(0, true);
        front(1, false);
#pragma unroll 1
        for (int t = 1; t < NT - 1; t += 2) { back(1, false); stage(0, false); front(1, false); }
        column(WM, 1, 4, false, [&](int n) {
            constexpr int RS = C::OFF_RSTD - C::OFF_MEAN, H = 512 - WM.value * 256;
            if (n == 0) { ck[0] = *(lds_f4)(c4 + 256); ck[1] = *(lds_f4)(c4 + 320); cv[0] = *(lds_f1)(c1 + 512); cv[1] = *(lds_f1)(c1 + 512 + 64); }
            else if (n < 5) {
                mu[n - 1] = *(lds_f1)(s1 + (n - 1) * 64); rs[n - 1] = *(lds_f1)(s1 + RS + (n - 1) * 64);
                mu4[n - 1] = *(lds_f4)(s4 + (n - 1) * 64); rs4[n - 1] = *(lds_f4)(s4 + RS + (n - 1) * 64);
            } else if (n == 5) {
                if (LOCAL && WM.value == 0) { mu[4] = *(lds_f1)(s1 + H); rs[4] = *(lds_f1)(s1 + RS + H); }
                else if (LOCAL) { mu4[4] = *(lds_f4)(s4 + H); rs4[4] = *(lds_f4)(s4 + RS + H); }
            } else if (n == 6) { cq[0] = *(lds_f4)c4; cq[1] = *(lds_f4)(c4 + 64); }
        });
        if (LOCAL) column(WM, 1, 5, false, [](int n) { if (n == 6) __builtin_amdgcn_s_waitcnt(0xC07F); });
        if (LOCAL) {
            auto fix = [&](int a, int t) {
#pragma unroll
                for (int f = 0; f < 2; ++f)
                    acc[a][t][f] = (a == 2) ? rs4[t] * (acc[2][t][f] + mu4[t] * cv[f]) + rs4[t] * -cb
                                            : rs[t] * (acc[a][t][f] + mu[t] * (a ? ck[f] : cq[f])) + rs[t] * -cb;
            };
            auto pk = [&](const f32x4 (&v)[2]) { return u32x4{pack_bf16(v[0][0], v[0][1]), pack_bf16(v[0][2], v[0][3]), pack_bf16(v[1][0], v[1][1]), pack_bf16(v[1][2], v[1][3])}; };
            if (!ident) {
                fix(1, 1); fix(1, 3); fix(2, 1); fix(2, 3);
#pragma unroll
                for (int f = 0; f < 2; ++f)
                    acch[f] = (WM.value == 0) ? rs[4] * (acch[f] + mu[4] * ck[f]) : rs4[4] * (acch[f] + mu4[4] * cv[f]);
            }
            auto pkv = [&](int t) {
#pragma unroll
                for (int f = 0; f < 2; ++f) {
                    va[t >> 1][f][(t & 1) * 2] = pack_bf16(acc[2][t][f][0], acc[2][t][f][1]);
                    va[t >> 1][f][(t & 1) * 2 + 1] = pack_bf16(acc[2][t][f][2], acc[2][t][f][3]);
                }
            };
            kfr[1] = pk(acc[1][1]); kfr[3] = pk(acc[1][3]); pkv(1); pkv(3);
            u32x4 hf = pk(acch);
            if (WM.value == 0) {
                ex[0] = kfr[1]; ex[64] = kfr[3];
#pragma unroll
                for (int u = 0; u < 2; ++u)
                    asm volatile("ds_write_b64 %0, %1 offset:%3\n\tds_write_b64 %0, %2 offset:%3+8" :: "v"(ex), "v"(u32x2{va[u][0][2], va[u][0][3]}),
                                 "v"(u32x2{va[u][1][2], va[u][1][3]}), "n"(2048 + u * 1024) : "memory");
                if (negpos) hf = u32x4{0, 0, 0, 0};
            }
            ex[256 + WM.value * 64] = hf;
            SBAR();
            if (!ident) { fix(0, 0); fix(0, 1); fix(0, 2); fix(0, 3); fix(1, 0); fix(1, 2); fix(2, 0); fix(2, 2); }
#pragma unroll
            for (int t = 0; t < 4; ++t) qfr[t] = pk(acc[0][t]);
            kfr[0] = pk(acc[1][0]); kfr[2] = pk(acc[1][2]); pkv(0); pkv(2);
            asm volatile("" : "+v"(qfr[0]), "+v"(qfr[1]), "+v"(qfr[2]), "+v"(qfr[3]), "+v"(kfr[0]), "+v"(kfr[2]),
                         "+v"(va[0][0]), "+v"(va[0][1]), "+v"(va[1][0]), "+v"(va[1][1]));
        }
    };
    const int head = ct * 2 + wn;
    unsigned qo = 0, ko = 0, vo = 0;
    if (LOCAL) {
        qo = (unsigned)((((panel * 8 + wm * 4) * 32 + head) * 64 + lane) * 16);
        asm volatile("" : "+v"(qo));
    } else {
        ko = (unsigned)((wm * 8 + (g & 1)) * 1024 + ((l15 & 7) + ((l15 & 8) << 1) + 32 * (g >> 1)) * 16);
        vo = (unsigned)((wm * 8 + 2 + (g >> 1)) * 1024 + (l15 + 32 * (g & 1)) * 16);
        asm volatile("" : "+v"(ko), "+v"(vo));
    }
    if (LOCAL && wm) mainloop(IC<1>{}); else mainloop(IC<0>{});

    if (!LOCAL) {
        auto pk = [&](f32x4 a, f32x4 b) { return u32x4{pack_bf16(a[0], a[1]), pack_bf16(a[2], a[3]), pack_bf16(b[0], b[1]), pack_bf16(b[2], b[3])}; };
        char* kvp = (char*)kvf + (size_t)(head * 16 + panel) * 16384;
        lds_ptr own = smw + wn * 16384;
        SBAR();
#pragma unroll
        for (int t = 0; t < 4; ++t) {
            const u32x4 kk = pk(rs[t] * (acc[1][t][0] + mu[t] * ck[0]) + rs[t] * -cb, rs[t] * (acc[1][t][1] + mu[t] * ck[1]) + rs[t] * -cb);
            if (t & 1) st16o<128>(kvp + (t >> 1) * 4096, ko, kk); else st16o<0>(kvp + (t >> 1) * 4096, ko, kk);
            kfr[t] = kk;
            SBAR();
            const int u = t >> 1, f = t & 1;
            const u32x4 vv = pk(rs4[2 * u] * (acc[2][2 * u][f] + mu4[2 * u] * cv[f]) + rs4[2 * u] * -cb,
                                rs4[2 * u + 1] * (acc[2][2 * u + 1][f] + mu4[2 * u + 1] * cv[f]) + rs4[2 * u + 1] * -cb);
            if (f) st16o<256>(kvp + u * 4096, vo, vv); else st16o<0>(kvp + u * 4096, vo, vv);
            qfr[t] = vv;
            SBAR();
        }
        column(IC<0>{}, 1, 5, false, [&](int n) {
            const int t = n >> 1;
            if (n & 1) *(lds_u4)(own + vo + (t >> 1) * 4096 + (t & 1) * 256) = qfr[t];
            else       *(lds_u4)(own + ko + (t >> 1) * 4096 + (t & 1) * 128) = kfr[t];
        });
        asm volatile("s_nop 7");
        SBAR();
#pragma unroll
        for (int u = 0; u < 2; ++u) {
            const float r0 = rs[2 * u] * QSCALE, r1 = rs[2 * u + 1] * QSCALE;
            const u32x4 qa = pk(r0 * (acc[0][2 * u][0] + mu[2 * u] * cq[0]) + r0 * -cb, r0 * (acc[0][2 * u][1] + mu[2 * u] * cq[1]) + r0 * -cb);
            const u32x4 qb = pk(r1 * (acc[0][2 * u + 1][0] + mu[2 * u + 1] * cq[0]) + r1 * -cb, r1 * (acc[0][2 * u + 1][1] + mu[2 * u + 1] * cq[1]) + r1 * -cb);
#pragma unroll
            for (int e = 0; e < 4; ++e) {
                const auto x = __builtin_amdgcn_permlane16_swap(qa[e], qb[e], false, false);
                qo4[2 * u][e] = x[0]; qo4[2 * u + 1][e] = x[1];
            }
        }
        return;
    }
    {
        const f32x4 zero4 = {0, 0, 0, 0};
        auto bf8 = [](u32x4 v) { return __builtin_bit_cast(bf16x8, v); };
        f32x4 sp[4], sc[4];
        sc[1] = MFMA16(bf8(kfr[1]), bf8(qfr[1]), mwin); sp[1] = MFMA16(bf8(kfr[0]), bf8(qfr[1]), mwin);
        sc[3] = MFMA16(bf8(kfr[3]), bf8(qfr[3]), mwin); sp[3] = MFMA16(bf8(kfr[2]), bf8(qfr[3]), mwin);
        asm volatile("" : "+v"(sc[1]), "+v"(sp[1]), "+v"(sc[3]), "+v"(sp[3]));
        WAIT_LGKM_BAR();
        const bf16x8 kp[2] = {lds_b128(ka[0]), lds_b128(ka[1])};
        u32x2 vp[2][2];
#pragma unroll
        for (int u = 0; u < 2; ++u)
            asm volatile("ds_read_b64 %0, %2\n\tds_read_b64 %1, %2 offset:8" : "=&v"(vp[u][0]), "=&v"(vp[u][1]) : "v"(vo2[u]) : "memory");
        sc[0] = MFMA16(bf8(kfr[0]), bf8(qfr[0]), mwin); sc[2] = MFMA16(bf8(kfr[2]), bf8(qfr[2]), mwin);
        SBAR();
        float x[4][4], m[4];
        unsigned a[4][2];
        auto logits = [&](int t0, int st) {
#pragma unroll
            for (int t = t0; t < 4; t += 2)
#pragma unroll
                for (int h = 0; h < 2; ++h) {
                    if (st == 0) {
                        const unsigned uc = pack_bf16(sc[t][2 * h], sc[t][2 * h + 1]), up = pack_bf16(sp[t][2 * h], sp[t][2 * h + 1]);
                        asm("v_bfi_b32 %0, %1, %2, %3" : "=v"(a[t][h]) : "v"(h ? cm23 : cm01), "v"(uc), "v"(up));
                    } else if (st == 1) { x[t][2 * h] = bf_lo(a[t][h]) * ATT_SCALE; x[t][2 * h + 1] = bf_hi(a[t][h]) * ATT_SCALE; }
                    else if (st == 2) a[t][h] = pack_bf16(x[t][2 * h], x[t][2 * h + 1]);
                    else { x[t][2 * h] = bf_lo(a[t][h]); x[t][2 * h + 1] = bf_hi(a[t][h]); }
                }
            SBAR();
        };
        logits(1, 0); logits(1, 1);
        sp[0] = MFMA16(kp[0], bf8(qfr[0]), mfirst); sp[2] = MFMA16(kp[1], bf8(qfr[2]), mfirst);
        SBAR();
        logits(1, 2); logits(1, 3);
        logits(0, 0); logits(0, 1); logits(0, 2); logits(0, 3);
        auto across = [&](float (&v)[4], auto op, auto fin) {
            const auto p = __builtin_amdgcn_permlane16_swap(__float_as_uint(v[0]), __float_as_uint(v[1]), false, false);
            const auto q = __builtin_amdgcn_permlane16_swap(__float_as_uint(v[2]), __float_as_uint(v[3]), false, false);
            const auto r = __builtin_amdgcn_permlane32_swap(__float_as_uint(op(__uint_as_float(p[0]), __uint_as_float(p[1]))),
                                                            __float_as_uint(op(__uint_as_float(q[0]), __uint_as_float(q[1]))), false, false);
            const unsigned z = __float_as_uint(fin(op(__uint_as_float(r[0]), __uint_as_float(r[1]))));
            const auto u = __builtin_amdgcn_permlane32_swap(z, z, false, false);
            const auto e = __builtin_amdgcn_permlane16_swap(u[0], u[0], false, false), f = __builtin_amdgcn_permlane16_swap(u[1], u[1], false, false);
            v[0] = __uint_as_float(e[0]); v[1] = __uint_as_float(e[1]); v[2] = __uint_as_float(f[0]); v[3] = __uint_as_float(f[1]);
        };
#pragma unroll
        for (int t = 0; t < 4; ++t) m[t] = amax3(x[t][0], x[t][1], x[t][2]);
        asm("v_max_f32 %0, %0, %4\n\tv_max_f32 %1, %1, %5\n\tv_max_f32 %2, %2, %6\n\tv_max_f32 %3, %3, %7\n\ts_nop 0"
            : "+v"(m[0]), "+v"(m[1]), "+v"(m[2]), "+v"(m[3]) : "v"(x[0][3]), "v"(x[1][3]), "v"(x[2][3]), "v"(x[3][3]));
        across(m, [](float p, float q) { float r; asm("v_max_f32 %0, %1, %2\n\ts_nop 0" : "=v"(r) : "v"(p), "v"(q)); return r; },
               [](float p) { return p * -LOG2E; });
        SBAR();
#pragma unroll
        for (int t = 0; t < 4; ++t)
#pragma unroll
            for (int r = 0; r < 4; ++r) x[t][r] = __builtin_fmaf(x[t][r], LOG2E, m[t]);
        SBAR();
#pragma unroll
        for (int t = 0; t < 4; ++t)
#pragma unroll
            for (int r = 0; r < 4; ++r) x[t][r] = __builtin_amdgcn_exp2f(x[t][r]);
        SBAR();
#pragma unroll
        for (int t = 0; t < 4; ++t) m[t] = (x[t][0] + x[t][1]) + (x[t][2] + x[t][3]);
        asm volatile("s_waitcnt lgkmcnt(0)" : "+v"(vp[0][0]), "+v"(vp[0][1]), "+v"(vp[1][0]), "+v"(vp[1][1]) :: "memory");
        u32x4 ve[2][2];
#pragma unroll
        for (int u = 0; u < 2; ++u)
#pragma unroll
            for (int f = 0; f < 2; ++f) ve[u][f] = u32x4{vp[u][f][0], vp[u][f][1], va[u][f][0], va[u][f][1]};
        across(m, [](float p, float q) { return p + q; }, [](float p) { return __builtin_amdgcn_rcpf(p); });
        asm volatile("" : "+v"(ve[0][0]), "+v"(ve[0][1]), "+v"(ve[1][0]), "+v"(ve[1][1]));
        SBAR();
#pragma unroll
        for (int t = 0; t < 4; ++t)
#pragma unroll
            for (int r = 0; r < 4; ++r) x[t][r] *= m[t];
        SBAR();
#pragma unroll
        for (int t = 0; t < 4; ++t)
#pragma unroll
            for (int h = 0; h < 2; ++h) a[t][h] = pack_bf16(x[t][2 * h], x[t][2 * h + 1]);
        SBAR();
        f32x4 o0[4], o1[4];
#pragma unroll
        for (int t = 0; t < 4; ++t) {
            const bf16x8 pb = bf8(u32x4{a[t][0] & ~cm01, a[t][1] & ~cm23, a[t][0] & cm01, a[t][1] & cm23});
            o0[t] = MFMA16(bf8((t & 1) ? va[t >> 1][0] : ve[t >> 1][0]), pb, zero4);
            o1[t] = MFMA16(bf8((t & 1) ? va[t >> 1][1] : ve[t >> 1][1]), pb, zero4);
        }
        SBAR();
#pragma unroll
        for (int t = 0; t < 4; ++t)
            st16(ofrag + t * 16384, qo, u32x4{pack_bf16(o0[t][0], o0[t][1]), pack_bf16(o0[t][2], o0[t][3]),
                                                 pack_bf16(o1[t][0], o1[t][1]), pack_bf16(o1[t][2], o1[t][3])});
    }
}

DI int out_row(int panel, bool localrows, int T, int a) {
    return localrows ? (panel * 128 + (T >> 2) * 64 + 2 * (16 * (T & 1) + a) + ((T >> 1) & 1)) : (panel * 128 + T * 16 + a);
}
DI int out_swz(int k) { return ((k >> 1) & 1) | (k & 4); }
DI unsigned wo_off(int w, int lane) {
    const int kl = lane >> 3;
    unsigned v = (unsigned)(w * 8 + kl) * 2048u + (unsigned)(ids().co * 128 + ((lane & 7) ^ out_swz(kl)) * 16);
    asm volatile("" : "+v"(v));
    return v;
}
DI void wo_load(const void* Wo, unsigned voff, int i0, int i1, unsigned lds0, int w) {
#pragma unroll
    for (int i = i0; i < i1; ++i) glds16((const char*)Wo + (size_t)i * 65536, voff, lds0 + (w + 4 * i) * 1024);
}
DI unsigned res_off(bool localrows, bool tiled, int lane) {
    const Ids I = ids();
    unsigned v = (unsigned)(I.po * 262144 + I.co * (tiled ? 16384 : 128) + out_row(0, localrows, 0, lane >> 3) * (tiled ? 128 : 2048) +
                            (((lane & 7) ^ (lane >> 3)) * 16));
    asm volatile("" : "+v"(v));
    return v;
}
DI void res_load(u32x4 (&res)[6], const void* Xin, bool localrows, bool tiled, unsigned off, int w) {
#pragma unroll
    for (int i = 0; i < 6; ++i) {
        const int j = min(w + 3 * i, 15);
        asm volatile("global_load_dwordx4 %0, %1, %2 sc1" : "=&v"(res[i]) : "v"(off), "s"((const char*)Xin + out_row(0, localrows, j >> 1, (j & 1) * 8) * (tiled ? 128 : 2048)) : "memory");
    }
}
DI void out_handoff(char* smem, int xoff, u32x4 (&res)[6], int w, char* ws, int set, u64 tag, int lane, u32x4 (&c)[8], int mat) {
    const Ids I = ids();
    u64 land;
    lds_ptr dst = (lds_ptr)smem + xoff + w * 1024 + lane * 16;
    asm volatile("" : "+v"(dst));
    if (w == 3) __builtin_amdgcn_s_setprio(3);
    signal_poll<false, false>(w - 3, ws, flag_off(set, I.pq, lane & 15, I.cq), flag_off(set, I.po, I.co, lane & 15), 0xffffull, tag, land);
    if (w < 3) {
        asm volatile("s_waitcnt vmcnt(0)" : "+v"(res[0]), "+v"(res[1]), "+v"(res[2]), "+v"(res[3]), "+v"(res[4]), "+v"(res[5]) :: "memory");
#pragma unroll
        for (int i = 0; i < 5; ++i) *(__attribute__((address_space(3))) u32x4*)(dst + i * 3072) = res[i];
        if (w == 0) *(__attribute__((address_space(3))) u32x4*)(dst + 15360) = res[5];
        if (mat >= 0) colsum_store(c, ws, mat, w, lane);
    }
    WAIT_LGKM_BAR();
    asm volatile("s_waitcnt vmcnt(0)" : "+v"(land) :: "memory");
    if (w < 3 && mat >= 0) flag_store(ws, (mat ? 10 : 13) + w, I.cq, I.pq, tag, lane);
    __builtin_amdgcn_s_setprio(0);
}

template <bool AFTER_LOCAL>
DI void out_compute(char* smem, int w, const __hip_bfloat16* __restrict__ ofrag,
                    __hip_bfloat16* __restrict__ Xout, float* __restrict__ statp SP_PARAM) {
    constexpr int XOFF = AFTER_LOCAL ? 147456 : 98304;
    STAMP(AFTER_LOCAL ? 3 : 10);
    VM_BAR(0);
    const int lane = lane_id();
    asm volatile("" : "+s"(w));
    const Ids I = ids();
    const int panel = I.po, ct = I.co;
    const int l15 = lane & 15, g = lane >> 4;
    const bool tiled = AFTER_LOCAL || statp != nullptr;
    const int rstr = tiled ? 128 : 2048;
    const int q4 = l15 >> 2, p4 = l15 & 3;
    const int krow = 4 * g + q4;
    const int sw = out_swz(krow);
    const lds_cptr sm = (lds_cptr)smem;
    lds_cptr bw[4], xo[4];
#pragma unroll
    for (int t = 0; t < 4; ++t) {
        bw[t] = sm + krow * 128 + ((((t >> 1) * 4 + (p4 >> 1) * 2 + (t & 1)) ^ sw) * 16) + (p4 & 1) * 8;
        xo[t] = sm + XOFF + (w * 32 + l15) * 128 + ((((t >> 1) * 4 + (g >> 1) * 2 + (t & 1)) ^ (l15 & 7)) * 16) + (g & 1) * 8;
    }
    unsigned so[2], sto, ao[2];
#pragma unroll
    for (int r2 = 0; r2 < 2; ++r2) {
        so[r2] = (unsigned)(panel * 262144 + ct * (tiled ? 16384 : 128) + out_row(0, AFTER_LOCAL, 2 * w + r2, l15 & 7) * rstr + (l15 >> 3) * 64 + g * 16);
        ao[r2] = (unsigned)((panel * 8 + 2 * w + r2) * 32768 + lane * 16);
    }
    sto = (unsigned)(((panel * 16 + ct) * 128 + 2 * w * 16 + l15) * 8);
    asm volatile("" : "+v"(bw[0]), "+v"(bw[1]), "+v"(bw[2]), "+v"(bw[3]), "+v"(xo[0]), "+v"(xo[1]), "+v"(xo[2]), "+v"(xo[3]),
                 "+v"(so[0]), "+v"(so[1]), "+v"(sto), "+v"(ao[0]), "+v"(ao[1]));
    bf16x8 ones;
    {
        unsigned o1; asm volatile("v_mov_b32 %0, 0x3f803f80" : "=v"(o1));
        ones = __builtin_bit_cast(bf16x8, (u32x4{o1, o1, o1, o1}));
        asm volatile("" : "+v"(ones));
    }
    const __amdgpu_buffer_rsrc_t ra = __builtin_amdgcn_make_buffer_rsrc((void*)ofrag, 0, 4 << 20, 0x00020000);
    asm volatile("" :: "s"(ra));
    u32x32 fr;
    BAR();
    STAMP(AFTER_LOCAL ? 4 : 11);
    u32x32 aw0, aw1;
    asm volatile(".irp j,0,1,2,3\n\tbuffer_load_dwordx4 v[64+8*\\j:67+8*\\j], %2, %4, 0 offen offset:1024*\\j sc1\n\t"
                 "buffer_load_dwordx4 v[68+8*\\j:71+8*\\j], %3, %4, 0 offen offset:1024*\\j sc1\n\t.endr\n\t"
                 ".irp j,0,1\n\tbuffer_load_dwordx4 v[96+8*\\j:99+8*\\j], %2, %4, %5 offen offset:1024*\\j sc1\n\t"
                 "buffer_load_dwordx4 v[100+8*\\j:103+8*\\j], %3, %4, %5 offen offset:1024*\\j sc1\n\t.endr"
                 : "=&{v[64:95]}"(aw0), "=&{v[96:127]}"(aw1) : "v"(ao[0]), "v"(ao[1]), "s"(ra), "s"(4096) : "memory");
    f32x16 acc[2];
#pragma unroll
    for (int r2 = 0; r2 < 2; ++r2)
#pragma unroll
        for (int c = 0; c < 4; ++c) {
            const u32x2 xr = *(const __attribute__((address_space(3))) u32x2*)(xo[c] + r2 * 2048);
            acc[r2][4 * c] = bf_lo(xr[0]); acc[r2][4 * c + 1] = bf_hi(xr[0]); acc[r2][4 * c + 2] = bf_lo(xr[1]); acc[r2][4 * c + 3] = bf_hi(xr[1]);
        }
    asm volatile(
        ".macro OUT_MM r, t, j\n\t"
        "v_mfma_f32_16x16x32_bf16 v[16*\\r+4*\\t:16*\\r+4*\\t+3], v[32+16*((\\j)&1)+4*\\t:35+16*((\\j)&1)+4*\\t], v[64+8*\\j+4*\\r:67+8*\\j+4*\\r], v[16*\\r+4*\\t:16*\\r+4*\\t+3]\n\t"
        ".endm\n\t"
        ".macro OUT_RD t, hi, j, on\n\t"
        ".if \\on\n\tds_read_b64_tr_b16 v[32+16*((\\j)&1)+4*\\t+2*\\hi:33+16*((\\j)&1)+4*\\t+2*\\hi], v[128+\\t] offset:((\\j)&7)*4096+2048*\\hi\n\t.endif\n\t"
        ".endm\n\t"
        ".macro OUT_UP t, on\n\t.if \\on\n\tv_add_u32 v[128+\\t], s97, v[128+\\t]\n\t.endif\n\t.endm\n\t"
        ".macro OUT_STEP j, ld, rd, up, vm, lg\n\t"
        "OUT_MM 0, 0, \\j\n\tOUT_RD 3, 1, \\j+1, 1\n\tOUT_UP 0, \\up\n\t"
        ".if \\ld && (((\\j+6)&3) == 0)\n\ts_addk_i32 s96, 0x1000\n\t.endif\n\t"
        "OUT_MM 1, 0, \\j\n\tOUT_RD 0, 0, \\j+2, \\rd\n\t"
        ".if \\ld\n\tbuffer_load_dwordx4 v[64+8*((\\j+6)&7):67+8*((\\j+6)&7)], %[a0], %[ra], s96 offen offset:1024*((\\j+6)&3) sc1\n\t.endif\n\t"
        "OUT_MM 0, 1, \\j\n\tOUT_RD 0, 1, \\j+2, \\rd\n\tOUT_UP 1, \\up\n\t"
        "OUT_MM 1, 1, \\j\n\tOUT_RD 1, 0, \\j+2, \\rd\n\t"
        "OUT_MM 0, 2, \\j\n\tOUT_RD 1, 1, \\j+2, \\rd\n\tOUT_UP 2, \\up\n\t"
        "OUT_MM 1, 2, \\j\n\tOUT_RD 2, 0, \\j+2, \\rd\n\t"
        ".if \\ld\n\tbuffer_load_dwordx4 v[68+8*((\\j+6)&7):71+8*((\\j+6)&7)], %[a1], %[ra], s96 offen offset:1024*((\\j+6)&3) sc1\n\t.endif\n\t"
        "OUT_MM 0, 3, \\j\n\tOUT_RD 2, 1, \\j+2, \\rd\n\tOUT_UP 3, \\up\n\t"
        "OUT_MM 1, 3, \\j\n\tOUT_RD 3, 0, \\j+2, \\rd\n\t"
        "s_waitcnt vmcnt(\\vm) lgkmcnt(\\lg)\n\t"
        ".endm\n\t"
        ".macro OUT_PACK y, c\n\t"
        ".irp i,0,1,2,3,4,5,6,7\n\tv_cvt_pk_bf16_f32 v[\\y+\\i], v[\\c+2*\\i], v[\\c+2*\\i+1]\n\t.endr\n\t"
        ".irp i,0,1,4,5\n\tv_permlane16_swap_b32 v[\\y+\\i], v[\\y+\\i+2]\n\t.endr\n\t"
        ".irp i,0,1,2,3\n\tv_mov_b32 v[\\y+8+\\i], v[\\y+\\i]\n\t.endr\n\t"
        "v_mfma_f32_16x16x32_bf16 v[\\y+12:\\y+15], v[\\y:\\y+3], v[\\y:\\y+3], 0\n\t"
        "v_mov_b32_dpp v[\\y+8], v[\\y+4] row_ror:8 row_mask:0xf bank_mask:0xc\n\tv_mov_b32_dpp v[\\y+9], v[\\y+5] row_ror:8 row_mask:0xf bank_mask:0xc\n\t"
        "v_mfma_f32_16x16x32_bf16 v[\\y+16:\\y+19], %[one], v[\\y:\\y+3], 0\n\t"
        "v_mov_b32_dpp v[\\y+10], v[\\y+6] row_ror:8 row_mask:0xf bank_mask:0xc\n\tv_mov_b32_dpp v[\\y+11], v[\\y+7] row_ror:8 row_mask:0xf bank_mask:0xc\n\t"
        "v_mfma_f32_16x16x32_bf16 v[\\y+12:\\y+15], v[\\y+4:\\y+7], v[\\y+4:\\y+7], v[\\y+12:\\y+15]\n\t"
        ".endm\n\t"
        ".macro OUT_REST y\n\t"
        "v_mfma_f32_16x16x32_bf16 v[\\y+16:\\y+19], %[one], v[\\y+4:\\y+7], v[\\y+16:\\y+19]\n\t"
        ".irp i,0,1,2,3\n\tv_mov_b32_dpp v[\\y+4+\\i], v[\\y+\\i] row_ror:8 row_mask:0xf bank_mask:0x3\n\t.endr\n\t"
        ".endm\n\t"
        ".macro OUT_DIAG y\n\t"
        "v_cndmask_b32 v[\\y+12], v[\\y+12], v[\\y+13], %[m0]\n\tv_cndmask_b32 v[\\y+14], v[\\y+14], v[\\y+15], %[m0]\n\t"
        "v_cndmask_b32 v[\\y+17], v[\\y+12], v[\\y+14], %[m1]\n\t"
        ".endm\n\t"
        ".irp t,0,1,2,3\n\tOUT_RD \\t, 0, 0, 1\n\tOUT_RD \\t, 1, 0, 1\n\t.endr\n\t"
        ".irp t,0,1,2\n\tOUT_RD \\t, 0, 1, 1\n\tOUT_RD \\t, 1, 1, 1\n\t.endr\n\tOUT_RD 3, 0, 1, 1\n\t"
        "s_movk_i32 s96, 0x1000\n\ts_mov_b32 s97, 0x8000\n\t"
        "s_waitcnt vmcnt(10) lgkmcnt(0)\n"
        ".Lout_loop_%=:\n\t"
        "OUT_STEP 0, 1, 1, 0, 10, 7\n\tOUT_STEP 1, 1, 1, 0, 10, 7\n\tOUT_STEP 2, 1, 1, 0, 10, 7\n\tOUT_STEP 3, 1, 1, 0, 10, 7\n\t"
        "OUT_STEP 4, 1, 1, 0, 10, 7\n\t"
        "s_cmpk_eq_u32 s96, 0x6000\n\ts_cselect_b32 s97, %[last], s97\n\t"
        "OUT_STEP 5, 1, 1, 0, 10, 7\n\tOUT_STEP 6, 1, 1, 1, 10, 7\n\t"
        "OUT_STEP 7, 1, 1, 0, 10, 7\n\t"
        "s_cmpk_lg_u32 s96, 0x7000\n\ts_cbranch_scc1 .Lout_loop_%=\n\t"
        "OUT_STEP 0, 1, 1, 0, 10, 7\n\tOUT_STEP 1, 1, 1, 0, 10, 7\n\tOUT_STEP 2, 0, 1, 0, 8, 7\n\tOUT_STEP 3, 0, 1, 0, 6, 7\n\t"
        "OUT_STEP 4, 0, 1, 0, 4, 7\n\tOUT_STEP 5, 0, 1, 0, 2, 7\n\tOUT_STEP 6, 0, 0, 0, 0, 0\n\t"
        ".irp r,0,1\n\t.irp t,0,1,2,3\n\tOUT_MM \\r, \\t, 7\n\t.endr\n\t.endr\n\t"
        "s_nop 0\n\t"
        "OUT_PACK 64, 0\n\tglobal_store_dwordx4 %[s0], v[72:75], %[xa] sc1\n\tOUT_REST 64\n\tglobal_store_dwordx4 %[s0], v[68:71], %[xb] sc1\n\t"
        "OUT_PACK 84, 16\n\tglobal_store_dwordx4 %[s1], v[92:95], %[xa] sc1\n\tOUT_REST 84\n\tglobal_store_dwordx4 %[s1], v[88:91], %[xb] sc1\n\t"
        "s_cmp_eq_u64 %[st], 0\n\ts_cbranch_scc1 .Lout_end_%=\n\t"
        "OUT_DIAG 64\n\tOUT_DIAG 84\n\t"
        "s_mov_b64 exec, %[md]\n\t"
        "global_store_dwordx2 %[so], v[80:81], %[st] sc1\n\tglobal_store_dwordx2 %[so], v[100:101], %[st] offset:128 sc1\n\t"
        "s_mov_b64 exec, -1\n"
        ".Lout_end_%=:\n\t"
        "s_nop 1\n\t"
        ".purgem OUT_MM\n\t.purgem OUT_RD\n\t.purgem OUT_UP\n\t.purgem OUT_STEP\n\t.purgem OUT_PACK\n\t.purgem OUT_REST\n\t.purgem OUT_DIAG"
        : "+{v[0:15]}"(acc[0]), "+{v[16:31]}"(acc[1]), "+{v[64:95]}"(aw0), "+{v[96:127]}"(aw1),
          "+{v128}"(bw[0]), "+{v129}"(bw[1]), "+{v130}"(bw[2]), "+{v131}"(bw[3]), "=&{v[32:63]}"(fr)
        : [a0] "v"(ao[0]), [a1] "v"(ao[1]), [ra] "s"(ra), [last] "s"(AFTER_LOCAL ? 32768 : 65536), [one] "v"(ones), [s0] "v"(so[0]), [s1] "v"(so[1]),
          [xa] "s"(Xout), [xb] "s"((char*)Xout + (AFTER_LOCAL ? 16 : 8) * rstr), [st] "s"(statp), [so] "v"(sto),
          [m0] "s"(0xaaaaaaaaaaaaaaaaull), [m1] "s"(0xccccccccccccccccull), [md] "s"(0xf0000f0000f0000full)
        : "memory", "scc", "s96", "s97");
    STAMP(AFTER_LOCAL ? 5 : 12);
}

#define ROWSUM(p0, p1, c) __builtin_amdgcn_smfmac_f32_16x16x64_bf16(sel, __builtin_bit_cast(bf16v_t, __builtin_shufflevector(p0, p1, 0, 1, 2, 3, 4, 5, 6, 7)), c, sidx, 0, 0)
constexpr int ATT_STAGE = 32768;
typedef __attribute__((ext_vector_type(8))) unsigned u32x8;

DI void attn_loader(char* smem, int w, char* ws, int set, u64 tag, const __hip_bfloat16* __restrict__ kvf, const void* Wo, const void* Xin,
                    const void* Wcs SP_PARAM) {
    const int lane = lane_id();
    asm volatile("" : "+s"(w));
    const Ids I = ids();
    const int panel = I.pq, ct = I.cq;
    const unsigned lds0 = (unsigned)(uintptr_t)smem;
    const char* kvh = (const char*)kvf + (size_t)(ct * 2 + (w >> 1)) * 262144 + (w & 1) * 4096;
    const unsigned voff = (unsigned)(lane * 16);
    auto order = [&](int st) { return ((panel & 8) + st - ((st - 1 < (panel & 7)) ? 1 : 0)) & 15; };
    auto load = [&](int st) {
        const char* src = kvh + (size_t)order(st) * 16384;
        const unsigned dst = lds0 + (w >> 1) * 16384 + (w & 1) * 4096 + (st & 3) * ATT_STAGE;
#pragma unroll
        for (int u = 0; u < 2; ++u)
            asm volatile("s_mov_b32 m0, %0\n\ts_nop 0\n\tglobal_load_lds_dwordx4 %1, %2 sc1\n\tglobal_load_lds_dwordx4 %1, %2 offset:1024 sc1\n\t"
                         "global_load_lds_dwordx4 %1, %2 offset:2048 sc1\n\tglobal_load_lds_dwordx4 %1, %2 offset:3072 sc1"
                         :: "s"(dst + u * 8192), "v"(voff), "s"(src + u * 8192) : "memory");
    };
    const unsigned wv = wo_off(w, lane), rv = res_off(false, true, lane), cv = colsum_off(w, lane);
    const unsigned poff = flag_off(set, ct, panel, lane & 15);
    u64 land;
    __builtin_amdgcn_s_setprio(3);
    BAR();
    signal_poll<false, true>(w, ws, flag_off(set, ct, lane & 15, panel), poff, 1ull << order(1), tag, land);
    STAMP(9);
    load(1);
    asm volatile("global_load_dwordx2 %0, %1, %2 sc1\n\ts_waitcnt vmcnt(0)" : "+v"(land) : "v"(poff), "s"(ws + WS_FLAG) : "memory");
    STAMP(10);
    BAR();
    if (!__all(land == tag)) flag_poll(ws, poff, tag);
    load(2); load(3);
    __builtin_amdgcn_s_setprio(0);
    STAMP(11);
    VM_BAR(8); load(4);
    VM_BAR(0); load(5); load(6);
    VM_BAR(0); load(7); load(8);
    u32x4 res[6], ca[8];
    colsum_load(ca, w < 3 ? Wcs : nullptr, cv);
#pragma unroll 1
    for (int st = 6; st < 12; st += 2) {
        VM_BAR(0);
        load(st + 3); load(st + 4);
        if (st == 8) wo_load(Wo, wv, 24, 32, lds0 + 32768, w);
    }
    VM_BAR(0); load(15); wo_load(Wo, wv, 0, 8, lds0, w);
    res_load(res, Xin, false, true, rv, w);
    VM_BAR(0); wo_load(Wo, wv, 8, 24, lds0, w);
    STAMP(12);
    out_handoff(smem, 98304, res, w, ws, set + 1, tag, lane, ca, Wcs ? 0 : -1);
}

DI void attn_compute(char* smem, int w, const u32x4 (&q4)[4], const __hip_bfloat16* __restrict__ kvf, __hip_bfloat16* __restrict__ ofrag SP_PARAM) {
    const int lane = lane_id();
    asm volatile("" : "+s"(w));
    const int wm = w & 1, wn = w >> 1;
    const Ids I = ids();
    const int panel = I.pq, head = I.cq * 2 + wn;
    const int l15 = lane & 15;
    const unsigned se = ((l15 & 2) == 0 && (l15 & 4) == 0) ? 0x3f803f80u : 0u, so = ((l15 & 2) == 0 && (l15 & 4) != 0) ? 0x3f803f80u : 0u;
    const bf8_t sel = __builtin_bit_cast(bf8_t, u32x4{se, se, so, so});
    const int sidx = (l15 & 1) ? (int)0xEEEEEEEEu : 0x44444444;
    const lds_cptr sm = (lds_cptr)smem + wn * 16384 + lane * 16;
    const f32x16 z16 = {0, 0, 0, 0, 0, 0, 0, 0, 0, 0, 0, 0, 0, 0, 0, 0};
    const bf16x8 qA0 = __builtin_bit_cast(bf16x8, q4[0]), qA1 = __builtin_bit_cast(bf16x8, q4[1]);
    const bf16x8 qB0 = __builtin_bit_cast(bf16x8, q4[2]), qB1 = __builtin_bit_cast(bf16x8, q4[3]);
    f32x16 oA, oB, nmA, nmB, sA, sB;
    f32x4 lsA, lsB;
    u32x8 pB;
    bf16x8 v11;
    asm volatile(".irp r,106,108,110,112,134,136,138,140,170,172\n\tv_mov_b64 v[\\r:\\r+1], 0\n\t.endr"
                 : "={v[106:113]}"(pB), "={v[134:137]}"(lsA), "={v[138:141]}"(lsB), "={v[170:173]}"(v11));
    WAIT_LGKM_BAR();
    STAMP(8);
    bf16x8 k00 = lds_b128(sm), k10 = lds_b128(sm + 1024), k01 = lds_b128(sm + 4096), k11 = lds_b128(sm + 4096 + 1024);
    bf16x8 v00 = lds_b128(sm + 2048), v10 = lds_b128(sm + 3072), v01 = lds_b128(sm + 4096 + 2048);
    float ob;
    {
        const u32x4 vu = __builtin_bit_cast(u32x4, v00);
        const unsigned vm = max(max(vu[0] & 0x7fff0000u, vu[1] & 0x7fff0000u), max(vu[2] & 0x7fff0000u, vu[3] & 0x7fff0000u));
        ob = __uint_as_float(wave_min(vm)) * 0.015625f;
        sA = MFMA32(k00, qA0, z16); sB = MFMA32(k00, qB0, z16);
        sA = MFMA32(k10, qA1, sA); sB = MFMA32(k10, qB1, sB);
        SBAR(); VM_BAR(0); SBAR();
        float mA = sA[0], mB = sB[0];
#pragma unroll
        for (int r = 1; r < 16; ++r) { mA = fmaxf(mA, sA[r]); mB = fmaxf(mB, sB[r]); }
        mA = max_h(mA) + 16.f; mB = max_h(mB) + 16.f;
#pragma unroll
        for (int r = 0; r < 16; ++r) { nmA[r] = -mA; nmB[r] = -mB; sA[r] -= mA; oA[r] = ob; oB[r] = ob; }
    }
    const unsigned a0 = (unsigned)(uintptr_t)sm;
    unsigned dst = (unsigned)((((panel * 8 + wm * 4 + ((lane & 31) >> 4)) * 32 + head) * 64 + (lane & 15) + (lane >> 5) * 16) * 16);
    asm volatile("" : "+v"(dst));
    asm volatile(
        ".macro ATT_RD dst, y\n\t"
        "ds_read_b128 v[\\dst:\\dst+3], v[175+(((\\y)&0x1ffff)>>16)] offset:(\\y)&0xffff\n\t"
        ".endm\n\t"
        ".macro ATT_E4 c\n\t"
        "v_exp_f32 v[\\c], v[\\c]\n\tv_exp_f32 v[\\c+1], v[\\c+1]\n\tv_exp_f32 v[\\c+2], v[\\c+2]\n\tv_exp_f32 v[\\c+3], v[\\c+3]\n\t"
        ".endm\n\t"
        ".macro ATT_C4 p, c\n\t"
        "v_cvt_pk_bf16_f32 v[\\p], v[\\c], v[\\c+1]\n\tv_cvt_pk_bf16_f32 v[\\p+1], v[\\c+2], v[\\c+3]\n\t"
        "v_cvt_pk_bf16_f32 v[\\p+2], v[\\c+4], v[\\c+5]\n\tv_cvt_pk_bf16_f32 v[\\p+3], v[\\c+6], v[\\c+7]\n\t"
        ".endm\n\t"
        ".macro ATT_STEP cur, nxt, ka, kb, q, nm, oc, od, lsd, pc, pd, vd1, vc0, r0, y0, r1, y1, bar, rd\n\t"
        "v_mfma_f32_32x32x16_bf16 v[\\nxt:\\nxt+15], v[\\ka:\\ka+3], v[\\q:\\q+3], v[\\nm:\\nm+15]\n\t"
        "ATT_E4 \\cur\n\t"
        "v_mfma_f32_32x32x16_bf16 v[\\nxt:\\nxt+15], v[\\kb:\\kb+3], v[\\q+4:\\q+7], v[\\nxt:\\nxt+15]\n\t"
        "ATT_E4 \\cur+4\n\t"
        "v_mfma_f32_32x32x16_bf16 v[\\od:\\od+15], v[\\vd1:\\vd1+3], v[\\pd+4:\\pd+7], v[\\od:\\od+15]\n\t"
        "s_waitcnt lgkmcnt(0)\n\t"
        "ATT_C4 \\pc, \\cur\n\t"
        "v_exp_f32 v[\\cur+8], v[\\cur+8]\n\tv_exp_f32 v[\\cur+9], v[\\cur+9]\n\t"
        "v_mfma_f32_32x32x16_bf16 v[\\oc:\\oc+15], v[\\vc0:\\vc0+3], v[\\pc:\\pc+3], v[\\oc:\\oc+15]\n\t"
        ".if \\bar\n\ts_barrier\n\t.endif\n\t"
        "ATT_E4 \\cur+10\n\t"
        "v_smfmac_f32_16x16x64_bf16 v[\\lsd:\\lsd+3], v[130:133], v[\\pd:\\pd+7], v174\n\t"
        ".if \\rd & 4\n\tATT_RD 142, 32768\n\tATT_RD 150, 33792\n\tATT_RD 158, 34816\n\t.endif\n\t"
        ".if \\rd & 8\n\tATT_RD \\r1-8, \\y1-1024\n\t.endif\n\t"
        ".if \\rd & 1\n\tATT_RD \\r0, \\y0\n\t.endif\n\t"
        ".if \\rd & 2\n\tATT_RD \\r1, \\y1\n\t.endif\n\t"
        "v_exp_f32 v[\\cur+14], v[\\cur+14]\n\tv_exp_f32 v[\\cur+15], v[\\cur+15]\n\t"
        "ATT_C4 \\pc+4, \\cur+8\n\t"
        ".if \\rd & 4\n\ts_waitcnt lgkmcnt(3)\n\t.endif\n\t"
        ".endm\n\t"
        ".macro ATT_TILE p, y1, y2, bar, ra=3, rb=3\n\t"
        "ATT_STEP 66, 82, 142+\\p, 150+\\p, 122, 50, 2, 18, 138, 98, 106, 170-\\p, 158+\\p, 170-\\p, \\y1+3072, 142+\\p, \\y2, \\bar&1, \\ra\n\t"
        "ATT_STEP 82, 66, 146-\\p, 154-\\p, 114, 34, 18, 2, 134, 106, 98, 166+\\p, 158+\\p, 158+\\p, \\y2+2048, 150+\\p, \\y2+1024, \\bar&2, \\rb\n\t"
        ".endm\n\t"
        ".macro ATT_STAGE b, bar\n\t"
        "ATT_TILE 0, \\b+4096, \\b+8192, 0\n\t"
        "ATT_TILE 4, \\b+8192, \\b+12288, 0\n\t"
        ".if \\bar\n\tATT_TILE 0, \\b+12288, \\b+32768, 2, 1, 11\n\t.else\n\tATT_TILE 0, \\b+12288, \\b+32768, 0\n\t.endif\n\t"
        "ATT_TILE 4, \\b+32768, \\b+36864, 0\n\t"
        ".endm\n\t"
        "v_add_u32 v176, 0x10000, v175\n\t"
        "s_mov_b32 s96, 4\n\t"
        "s_waitcnt lgkmcnt(0)\n\t"
        "ATT_TILE 0, 4096, 8192, 0\n\tATT_TILE 4, 8192, 12288, 0\n\tATT_TILE 0, 12288, 0, 0, 1, 0\n\tATT_TILE 4, 32768, 36864, 1, 7, 3\n\t"
        "ATT_STAGE 32768, 1\n\t"
        "s_branch .Latt_entry_%=\n\t"
        ".p2align 6\n"
        ".Latt_loop_%=:\n\t"
        "ATT_STAGE 0, 1\n\tATT_STAGE 32768, 0\n"
        ".Latt_entry_%=:\n\t"
        "ATT_STAGE 65536, 1\n\t"
        "s_sub_u32 s96, s96, 1\n\t"
        "ATT_STAGE 98304, 0\n\t"
        "s_cmp_lg_u32 s96, 0\n\t"
        "s_cbranch_scc1 .Latt_loop_%=\n\t"
        "s_nop 1\n\t"
        "v_mfma_f32_32x32x16_bf16 v[18:33], v[170:173], v[110:113], v[18:33]\n\t"
        "v_smfmac_f32_16x16x64_bf16 v[138:141], v[130:133], v[106:113], v174\n\t"
        "s_waitcnt lgkmcnt(0)\n\ts_nop 10\n\t"
        ".purgem ATT_RD\n\t.purgem ATT_E4\n\t.purgem ATT_C4\n\t.purgem ATT_STEP\n\t.purgem ATT_TILE\n\t.purgem ATT_STAGE"
        : "+{v[2:17]}"(oA), "+{v[18:33]}"(oB), "+{v[66:81]}"(sA), "+{v[106:113]}"(pB), "+{v[134:137]}"(lsA), "+{v[138:141]}"(lsB),
          "+{v[142:145]}"(k00), "+{v[146:149]}"(k01), "+{v[150:153]}"(k10), "+{v[154:157]}"(k11),
          "+{v[158:161]}"(v00), "+{v[162:165]}"(v01), "+{v[166:169]}"(v10), "+{v[170:173]}"(v11)
        : "{v[34:49]}"(nmA), "{v[50:65]}"(nmB), "{v[114:117]}"(qA0), "{v[118:121]}"(qA1), "{v[122:125]}"(qB0), "{v[126:129]}"(qB1),
          "{v[130:133]}"(sel), "{v174}"(sidx), "{v175}"(a0)
        : "memory", "scc", "s96", "v176", "v82", "v83", "v84", "v85", "v86", "v87", "v88", "v89", "v90", "v91", "v92", "v93", "v94", "v95",
          "v96", "v97", "v98", "v99", "v100", "v101", "v102", "v103", "v104", "v105");
    STAMP(9);
    float lA = lsA[0] + lsA[1], lB = lsB[0] + lsB[1];
    auto store = [&] {
#pragma unroll
    for (int u = 0; u < 2; ++u) {
        const float inv = __builtin_amdgcn_rcpf(u ? lB : lA), sub = -ob * inv;
        const f32x16 e = (u ? oB : oA) * inv + sub;
        st16o<0>((char*)ofrag + u * 65536, dst, u32x4{pack_bf16(e[0], e[1]), pack_bf16(e[2], e[3]), pack_bf16(e[8], e[9]), pack_bf16(e[10], e[11])});
        st16o<512>((char*)ofrag + u * 65536, dst, u32x4{pack_bf16(e[4], e[5]), pack_bf16(e[6], e[7]), pack_bf16(e[12], e[13]), pack_bf16(e[14], e[15])});
    }
    };
    store();
    {
        const f32x16 os = oA + oB;
        const float osum = (((os[0] + os[1]) + (os[2] + os[3])) + ((os[4] + os[5]) + (os[6] + os[7]))) +
                           (((os[8] + os[9]) + (os[10] + os[11])) + ((os[12] + os[13]) + (os[14] + os[15])));
        const bool bad = !(fmaxf(lA, lB) < 1.0e24f) || !(lA > 1.0e-24f) || !(lB > 1.0e-24f) || !(fabsf(osum) < 3.0e38f);
                if (__any(bad)) {
            const __amdgpu_buffer_rsrc_t rk = __builtin_amdgcn_make_buffer_rsrc((char*)kvf + (size_t)head * 262144, 0, 262144, 0x00020000);
            oA = z16; oB = z16; lA = 0.f; lB = 0.f; ob = 0.f;
            float mA = -INFINITY, mB = -INFINITY;
#pragma unroll 1
            for (int t = 0; t < 64; ++t) {
                bf16x8 f[4];
#pragma unroll
                for (int i = 0; i < 4; ++i) f[i] = __builtin_bit_cast(bf16x8, __builtin_amdgcn_raw_buffer_load_b128(rk, lane * 16 + i * 1024, t * 4096, 16));
                auto upd = [&](bf16x8 q0, bf16x8 q1, f32x16& o, float& l, float& m) {
                    f32x16 s = MFMA32(f[0], q0, z16); s = MFMA32(f[1], q1, s);
                    float tm = s[0];
#pragma unroll
                    for (int r = 1; r < 16; ++r) tm = fmaxf(tm, s[r]);
                    tm = max_h(tm);
                    const float mn = fmaxf(m, tm);
                    const float alpha = __builtin_amdgcn_exp2f(m - mn);
                    m = mn;
#pragma unroll
                    for (int r = 0; r < 16; ++r) { s[r] = __builtin_amdgcn_exp2f(s[r] - mn); o[r] *= alpha; }
                    const u32x4 p0 = {pack_bf16(s[0], s[1]), pack_bf16(s[2], s[3]), pack_bf16(s[4], s[5]), pack_bf16(s[6], s[7])};
                    const u32x4 p1 = {pack_bf16(s[8], s[9]), pack_bf16(s[10], s[11]), pack_bf16(s[12], s[13]), pack_bf16(s[14], s[15])};
                    f32x4 t4 = {0, 0, 0, 0};
                    o = MFMA32(f[2], __builtin_bit_cast(bf16x8, p0), o);
                    o = MFMA32(f[3], __builtin_bit_cast(bf16x8, p1), o);
                    t4 = ROWSUM(p0, p1, t4);
                    l = l * alpha + (t4[0] + t4[1]);
                };
                upd(qA0, qA1, oA, lA, mA);
                upd(qB0, qB1, oB, lB, mB);
            }
            store();
        }
    }
}

DI void ln_row(const u32x4 (&v)[2], __hip_bfloat16* __restrict__ Y, unsigned off) {
    float s1 = 0.f, s2 = 0.f;
#pragma unroll
    for (int e = 0; e < 8; ++e) s1 = dot2bf(v[e >> 2][e & 3], 0x3f803f80u, s1);
    const float mean = wave_sum(s1) * (1.f / 1024.f);
#pragma unroll
    for (int e = 0; e < 8; ++e) {
        const float d0 = bf_lo(v[e >> 2][e & 3]) - mean, d1 = bf_hi(v[e >> 2][e & 3]) - mean;
        s2 += d0 * d0 + d1 * d1;
    }
    const float mb = round_bf16(mean);
    const float rden = __builtin_amdgcn_rcpf(round_bf16(__builtin_amdgcn_sqrtf(round_bf16(round_bf16(wave_sum(s2) * (1.f / 1024.f)) + 1e-5f))));
#pragma unroll
    for (int hh = 0; hh < 2; ++hh) {
        u32x4 y;
#pragma unroll
        for (int e = 0; e < 4; ++e) {
            const unsigned d = pack_bf16(bf_lo(v[hh][e]) - mb, bf_hi(v[hh][e]) - mb);
            y[e] = pack_bf16(bf_lo(d) * rden, bf_hi(d) * rden);
        }
        st16((char*)Y + hh * 131072, off, y);
    }
}

extern "C" __global__ __launch_bounds__(512, 2) void mega_kernel(const __hip_bfloat16* x, const __hip_bfloat16* wqkvA,
                                                            const __hip_bfloat16* woA, const __hip_bfloat16* wqkvB,
                                                            const __hip_bfloat16* woB, __hip_bfloat16* out,
                                                            char* ws) {
    char* const smem = smem_base;
    typedef __hip_bfloat16 bf;
    const int w8 = __builtin_amdgcn_readfirstlane(threadIdx.x >> 6);
    if (w8 == 7 && (blockIdx.x >> 3) < 10) code_touch(blockIdx.x >> 3, threadIdx.x & 63);
    asm volatile("" :: "s"(x), "s"(wqkvA), "s"(woA), "s"(wqkvB), "s"(woB), "s"(out), "s"(ws));
    STAMP_DECL; STAMPR(14); STAMP(0);
    u64 tag;
    {
        const int lane = threadIdx.x & 63;
        const Ids I = ids();
        const unsigned off = (unsigned)((I.po * 128 + I.co * 8 + w8) * 2048 + lane * 16);
        u32x4 v[2];
        u32x2 ep;
        asm volatile("global_load_dwordx4 %0, %3, %4\n\tglobal_load_dwordx4 %1, %3, %4 offset:1024\n\tglobal_load_dwordx2 %2, %5, %6 sc1"
                     : "=&v"(v[0]), "=&v"(v[1]), "=&v"(ep) : "v"(off), "s"(x), "v"(0), "s"(ws + WS_EPOCH) : "memory");
        if (w8 == 7) {
            const int l = lane & 15, k = l % 5;
            const unsigned o = (l >= 10 || k == 3) ? flag_off(l, I.cq, I.pq, 0) : (k == 0 || k == 2) ? flag_off(l, I.pq, I.cq, 0) : flag_off(l, I.po, I.co, 0);
            touch_at(ws + WS_FLAG, o);
        } else if (w8 >= 4) touch(wqkvA, (w8 - 4) * 256 + blockIdx.x, lane);
        if (w8 >= 4) asm volatile("s_waitcnt vmcnt(2)" ::: "memory"); else asm volatile("s_waitcnt vmcnt(1)" ::: "memory");
        asm volatile("" : "+v"(v[0]), "+v"(v[1]));
        ln_row(v, (bf*)(ws + WS_Y0), (unsigned)(I.po * 262144 + (lane >> 3) * 16384 + (I.co * 8 + w8) * 128 + (lane & 7) * 16));
        if (w8 >= 4) asm volatile("s_waitcnt vmcnt(3)" ::: "memory"); else asm volatile("s_waitcnt vmcnt(2)" ::: "memory");
        asm volatile("" : "+v"(ep));
        tag = (((u64)(unsigned)__builtin_amdgcn_readfirstlane(ep[1]) << 32) | (unsigned)__builtin_amdgcn_readfirstlane(ep[0])) + 1;
    }
    if (w8 >= 4) {
        int w = w8 & 3;
#pragma unroll 1
        for (int it = 0; it < 2; ++it) {
            const int lane = lane_id();
            asm volatile("" : "+s"(x), "+s"(wqkvA), "+s"(woA), "+s"(wqkvB), "+s"(woB), "+s"(ws), "+s"(w));
            const unsigned lds0 = (unsigned)(uintptr_t)smem;
            u32x4 cb[8];
            const unsigned wv = wo_off(w, lane), rv = res_off(true, it, lane), cv = colsum_off(w, lane);
            qkv_loader<true>(smem, w, ws, it * 5, tag, (const bf*)(ws + (it ? WS_X2 : WS_Y0)), wqkvA, (const float*)(ws + WS_ST + 262144),
                             ws + WS_CSP, it == 0, woA, [&] {
                if (w < 3) {
                    colsum_load(cb, wqkvB, cv);
                    asm volatile("s_waitcnt vmcnt(8)" ::: "memory");
                } else {
                    touch(woA, blockIdx.x, lane);
                    asm volatile("s_waitcnt vmcnt(1)" ::: "memory");
                }
            }, [&](int part) {
                if (part == 0) wo_load(woA, wv, 29, 32, lds0, w); else wo_load(woA, wv, 25, 29, lds0, w);
            } SP_ARG);
            STAMP(3);
            u32x4 res[6];
            res_load(res, it ? (const void*)(ws + WS_X2) : x, true, it, rv, w);
            if (it) __builtin_amdgcn_s_sleep(8);
            wo_load(woA, wv, 0, 7, lds0, w);
            BAR();
            wo_load(woA, wv, 7, 25, lds0, w);
            STAMP(4);
            out_handoff(smem, 147456, res, w, ws, it * 5 + 1, tag, lane, cb, it ? -1 : 1);
            STAMP(5);
            qkv_loader<false>(smem, w, ws, it * 5 + 2, tag, (const bf*)(ws + (it ? WS_X3 : WS_X1)), wqkvB, (const float*)(ws + WS_ST + it * 524288), ws + WS_CSP + 196608,
                              false, nullptr, [] {}, [](int) {} SP_ARG);
            STAMP(8);
            attn_loader(smem, w, ws, it * 5 + 3, tag, (const bf*)(ws + WS_KVF + it * 8 * MBy), woB, ws + (it ? WS_X3 : WS_X1),
                        it ? nullptr : wqkvA SP_ARG);
            STAMP(13); STAMPR(15); STAMP_FLUSH(it);
        }
        if (blockIdx.x == 0 && w == 0 && lane_id() == 0) __hip_atomic_store((g_u64ptr)(ws + WS_EPOCH), tag, __ATOMIC_RELAXED, __HIP_MEMORY_SCOPE_AGENT);
    } else {
        __builtin_amdgcn_s_setprio(2);
#pragma unroll 1
        for (int it = 0; it < 2; ++it) {
            u32x4 q4[4];
            asm volatile("" : "+s"(ws), "+s"(wqkvA), "+s"(wqkvB));
            qkv_compute<true>(smem, w8, wqkvA, q4, nullptr, (bf*)(ws + WS_OF + it * 8 * MBy), it == 0,
                              ws + WS_ST + 262144, ws + WS_WN + blockIdx.x * 16 SP_ARG);
            STAMP(2);
            out_compute<true>(smem, w8, (const bf*)(ws + WS_OF + it * 8 * MBy),
                              (bf*)(ws + (it ? WS_X3 : WS_X1)), (float*)(ws + WS_ST + it * 524288) SP_ARG);
            STAMP(6);
            qkv_compute<false>(smem, w8, wqkvB, q4, (bf*)(ws + WS_KVF + it * 8 * MBy), nullptr, false,
                               ws + WS_ST + it * 524288, ws + WS_WN + 4096 + blockIdx.x * 16 SP_ARG);
            attn_compute(smem, w8, q4, (const bf*)(ws + WS_KVF + it * 8 * MBy), (bf*)(ws + WS_OF + (it * 8 + 4) * MBy) SP_ARG);
            out_compute<false>(smem, w8, (const bf*)(ws + WS_OF + (it * 8 + 4) * MBy),
                               it ? out : (bf*)(ws + WS_X2), it ? nullptr : (float*)(ws + WS_ST + 262144) SP_ARG);
            STAMP(13); STAMPR(15); STAMP_FLUSH(it);
        }
    }
}

extern "C" __global__ void zz_code_end_kernel() {}

extern "C" void kernel_launch(void* const* d_in, const int* in_sizes, int n_in, void* d_out, int out_size, void* d_ws,
                              size_t ws_size, hipStream_t stream) {
    typedef __hip_bfloat16 bf;
    (void)hipFuncSetAttribute((const void*)mega_kernel, hipFuncAttributeMaxDynamicSharedMemorySize, CH_LDS);
    mega_kernel<<<256, 512, CH_LDS, stream>>>((const bf*)d_in[0], (const bf*)d_in[1], (const bf*)d_in[2], (const bf*)d_in[4], (const bf*)d_in[5],
                                              (bf*)d_out, (char*)d_ws);
}
```

```cpp
#define EPI 1
#include <hip/hip_runtime.h>
#include <hip/hip_bf16.h>
#include <stdint.h>

typedef __attribute__((ext_vector_type(8))) short bf16x8;
typedef __attribute__((ext_vector_type(4))) short s16x4;
typedef __attribute__((ext_vector_type(4))) float f32x4;
typedef __attribute__((ext_vector_type(2))) float f32x2;
typedef __attribute__((ext_vector_type(16))) float f32x16;
typedef __attribute__((ext_vector_type(4))) unsigned u32x4;
typedef __attribute__((ext_vector_type(2))) unsigned u32x2;
typedef __attribute__((ext_vector_type(2))) __bf16 bf16x2_t;
typedef __attribute__((ext_vector_type(8))) __bf16 bf8_t;
typedef __attribute__((ext_vector_type(16))) __bf16 bf16v_t;
typedef __attribute__((address_space(3))) const char* lds_cptr;
typedef __attribute__((address_space(3))) char* lds_ptr;

#define DI __device__ __forceinline__
#define MFMA16(a, b, c) __builtin_amdgcn_mfma_f32_16x16x32_bf16((a), (b), (c), 0, 0, 0)
#define MFMA32(a, b, c) __builtin_amdgcn_mfma_f32_32x32x16_bf16((a), (b), (c), 0, 0, 0)
#define SBAR() __builtin_amdgcn_sched_barrier(0)
#define STAMP(i)
#define STAMPR(i)
#define STAMP_DECL
#define STAMP_FLUSH(it)
#define SP_PARAM
#define SP_ARG
typedef unsigned long long u64;
typedef __attribute__((ext_vector_type(32))) unsigned u32x32;

constexpr float QSCALE = 0.17677669529663687f * 1.4426950408889634f;
constexpr float ATT_SCALE = 0.17677669529663687f;
constexpr float LOG2E = 1.4426950408889634f;
constexpr float NEG_MASK = -1e9f / ATT_SCALE;

DI int lane_id() { int l; asm volatile("v_mbcnt_lo_u32_b32 %0, -1, 0\n\tv_mbcnt_hi_u32_b32 %0, -1, %0" : "=v"(l)); return l; }
DI unsigned pack_bf16(float lo, float hi) {
    f32x2 v = {lo, hi};
    bf16x2_t r = __builtin_convertvector(v, bf16x2_t);
    return __builtin_bit_cast(unsigned, r);
}
DI float bf_lo(unsigned u) { return __uint_as_float(u << 16); }
DI float bf_hi(unsigned u) { return __uint_as_float(u & 0xffff0000u); }
DI float round_bf16(float x) { return bf_lo(pack_bf16(x, 0.f)); }
DI void ln_stats(float sum, float sumsq, float& mean_out, float& rstd_out) {
    float mean;
    asm("v_mul_f32 %0, 0x3a800000, %1" : "=v"(mean) : "v"(sum));
    const float var = fmaxf(__builtin_fmaf(-mean, mean, sumsq * (1.f / 1024.f)), 0.f);
    mean_out = round_bf16(mean);
    rstd_out = __builtin_amdgcn_rcpf(round_bf16(__builtin_amdgcn_sqrtf(round_bf16(round_bf16(var) + 1e-5f))));
}

DI void glds16(const void* sbase, unsigned voff, unsigned ldsaddr) {
    asm volatile("s_mov_b32 m0, %0\n\ts_nop 0\n\tglobal_load_lds_dwordx4 %1, %2"
                 :: "s"(ldsaddr), "v"(voff), "s"(sbase) : "memory");
}
DI void glds16c(const void* sbase, unsigned voff, unsigned ldsaddr) {
    asm volatile("s_mov_b32 m0, %0\n\ts_nop 0\n\tglobal_load_lds_dwordx4 %1, %2 sc1"
                 :: "s"(ldsaddr), "v"(voff), "s"(sbase) : "memory");
}
DI void st16(void* sbase, unsigned voff, u32x4 v) {
    asm volatile("global_store_dwordx4 %0, %1, %2 sc1\n\ts_nop 1" :: "v"(voff), "v"(v), "s"(sbase) : "memory");
}
template <int OFF> DI void st16o(void* sbase, unsigned voff, u32x4 v) {
    asm volatile("global_store_dwordx4 %0, %1, %2 offset:%3 sc1\n\ts_nop 1" :: "v"(voff), "v"(v), "s"(sbase), "n"(OFF) : "memory");
}
DI bf16x8 lds_b128(lds_cptr p) { return *(const __attribute__((address_space(3))) bf16x8*)p; }
DI s16x4 lds_tr(lds_cptr p) {
    return __builtin_amdgcn_ds_read_tr16_b64_v4i16((__attribute__((address_space(3))) s16x4*)p);
}
DI bf16x8 cat8(s16x4 a, s16x4 b) { return __builtin_shufflevector(a, b, 0, 1, 2, 3, 4, 5, 6, 7); }
DI float dot2bf(unsigned a, unsigned b, float c) {
    return __builtin_amdgcn_fdot2_f32_bf16(__builtin_bit_cast(bf16x2_t, a), __builtin_bit_cast(bf16x2_t, b), c, false);
}
DI float sum_g(float v) {
    auto a = __builtin_amdgcn_permlane16_swap(__float_as_uint(v), __float_as_uint(v), false, false);
    v = __uint_as_float(a[0]) + __uint_as_float(a[1]);
    auto b = __builtin_amdgcn_permlane32_swap(__float_as_uint(v), __float_as_uint(v), false, false);
    return __uint_as_float(b[0]) + __uint_as_float(b[1]);
}
DI float wave_sum(float v) {
    v += __builtin_bit_cast(float, __builtin_amdgcn_update_dpp(0, __builtin_bit_cast(int, v), 0xB1, 0xF, 0xF, true));
    v += __builtin_bit_cast(float, __builtin_amdgcn_update_dpp(0, __builtin_bit_cast(int, v), 0x4E, 0xF, 0xF, true));
    v += __builtin_bit_cast(float, __builtin_amdgcn_update_dpp(0, __builtin_bit_cast(int, v), 0x141, 0xF, 0xF, true));
    v += __builtin_bit_cast(float, __builtin_amdgcn_update_dpp(0, __builtin_bit_cast(int, v), 0x140, 0xF, 0xF, true));
    return sum_g(v);
}
DI unsigned wave_min(unsigned v) {
    v = min(v, (unsigned)__builtin_amdgcn_update_dpp(0, (int)v, 0xB1, 0xF, 0xF, true));
    v = min(v, (unsigned)__builtin_amdgcn_update_dpp(0, (int)v, 0x4E, 0xF, 0xF, true));
    v = min(v, (unsigned)__builtin_amdgcn_update_dpp(0, (int)v, 0x141, 0xF, 0xF, true));
    v = min(v, (unsigned)__builtin_amdgcn_update_dpp(0, (int)v, 0x140, 0xF, 0xF, true));
    const auto a = __builtin_amdgcn_permlane16_swap(v, v, false, false);
    v = min(a[0], a[1]);
    const auto b = __builtin_amdgcn_permlane32_swap(v, v, false, false);
    return min(b[0], b[1]);
}
DI float amax3(float a, float b, float c) { float r; asm("v_max3_f32 %0, %1, %2, %3" : "=v"(r) : "v"(a), "v"(b), "v"(c)); return r; }
DI float max2f(float a, float b) { return __builtin_amdgcn_fmed3f(a, b, INFINITY); }
DI float max_h(float v) {
    auto b = __builtin_amdgcn_permlane32_swap(__float_as_uint(v), __float_as_uint(v), false, false);
    return max2f(__uint_as_float(b[0]), __uint_as_float(b[1]));
}

constexpr unsigned OFF_DUMP = 145408;
extern __shared__ __attribute__((aligned(16))) char smem_base[];
DI void touch_at(const void* p, unsigned off) {
    asm volatile("s_mov_b32 m0, %0\n\ts_nop 0\n\tglobal_load_lds_dword %1, %2"
                 :: "s"((unsigned)(uintptr_t)smem_base + OFF_DUMP), "v"(off), "s"(p) : "memory");
}
DI void touch(const void* p, int slice, int lane) { touch_at(p, (unsigned)(slice * 64 + lane) * 128u); }
DI void code_touch(int slice, int lane) {
    unsigned long long c0, c1;
    asm volatile("s_getpc_b64 s[96:97]\n\ts_add_u32 s96, s96, mega_kernel@rel32@lo+4\n\ts_addc_u32 s97, s97, mega_kernel@rel32@hi+12\n\ts_mov_b64 %0, s[96:97]\n\t"
                 "s_getpc_b64 s[96:97]\n\ts_add_u32 s96, s96, zz_code_end_kernel@rel32@lo+4\n\ts_addc_u32 s97, s97, zz_code_end_kernel@rel32@hi+12\n\ts_mov_b64 %1, s[96:97]"
                 : "=s"(c0), "=s"(c1) :: "s96", "s97", "scc");
    touch_at((const void*)c0, min((unsigned)(slice * 64 + lane) * 128u, (unsigned)(c1 - c0)));
}

#define BAR() asm volatile("s_barrier" ::: "memory")
#define WAIT_LGKM() do { SBAR(); __builtin_amdgcn_s_waitcnt(0xC07F); SBAR(); } while (0)
#define WAIT_LGKM_BAR() do { SBAR(); __builtin_amdgcn_s_waitcnt(0xC07F); asm volatile("s_barrier" ::: "memory"); SBAR(); } while (0)
#define VM_BAR(n) asm volatile("s_waitcnt vmcnt(" #n ")\n\ts_barrier" ::: "memory")
constexpr int CH_LDS = 163840;
typedef __attribute__((address_space(3))) unsigned* lds_uptr;
typedef __attribute__((address_space(1))) u64* g_u64ptr;

constexpr size_t MBy = 1 << 20;
constexpr size_t WS_X3 = 0, WS_X2 = 4 * MBy, WS_X1 = 8 * MBy, WS_OF = 12 * MBy  , WS_KVF = 36 * MBy  ,
                 WS_ST = 52 * MBy  , WS_CSP = 54 * MBy  , WS_FLAG = 55 * MBy  ,
                 WS_WN = WS_CSP + 458752  , WS_EPOCH = WS_CSP + 524288, WS_Y0 = 56 * MBy;
DI unsigned flag_off(int set, int group, int consumer, int producer) { return (unsigned)(set * 65536 + group * 4096 + consumer * 256 + producer * 8); }
DI void flag_store(char* ws, int set, int group, int producer, u64 tag, int lane) {
    asm volatile("" : "+s"(tag));
    if (lane < 16) __hip_atomic_store((g_u64ptr)(ws + WS_FLAG + flag_off(set, group, lane, producer)), tag, __ATOMIC_RELAXED, __HIP_MEMORY_SCOPE_AGENT);
}
DI void flag_poll(const char* ws, unsigned off, u64 tag) {
    asm volatile("" : "+s"(tag));
#pragma unroll 1
    for (int spin = 0; spin < (1 << 16); ++spin) {
        const u64 v = __hip_atomic_load((g_u64ptr)(ws + WS_FLAG + off), __ATOMIC_RELAXED, __HIP_MEMORY_SCOPE_AGENT);
        if (__all(v == tag)) break;
        __builtin_amdgcn_s_sleep(1);
    }
    asm volatile("" ::: "memory");
}
template <bool DRAIN, bool ALLPOLL>
DI void signal_poll(int follower, const char* ws, unsigned soff, unsigned poff, u64 pmask, u64 tag, u64& land) {
    u64 tagv, zero;
    asm volatile("s_not_b64 s[96:97], %[ts]\n\tv_mov_b64 %[tag], %[ts]\n\tv_mov_b64 %[zero], 0\n\tv_mov_b64 %[land], s[96:97]\n\t"
                 ".if %[dr]\n\ts_waitcnt vmcnt(0)\n\t.endif\n\ts_barrier\n\t"
                 "s_cmp_lg_u32 %[fl], 0\n\ts_cbranch_scc1 .Lsig_fol_%=\n\t"
                 "s_mov_b64 exec, 0xffff\n\t"
                 "global_store_dwordx2 %[so], %[tag], %[base] sc1\n"
                 ".if %[ap]\n.Lsig_fol_%=:\n.endif\n\t"
                 "s_mov_b64 exec, %[pm]\n\t"
                 "s_movk_i32 s96, 0x7fff\n"
                 ".Lsig_loop_%=:\n\t"
                 "global_atomic_or_x2 %[land], %[po], %[zero], %[base] sc0 sc1\n\t"
                 "s_sleep 1\n\t"
                 "s_waitcnt vmcnt(3)\n\t"
                 "v_cmp_ne_u64 vcc, %[land], %[tag]\n\t"
                 "s_sub_u32 s96, s96, 1\n\t"
                 "s_cbranch_scc1 .Lsig_out_%=\n\t"
                 "s_cbranch_vccnz .Lsig_loop_%=\n"
                 ".Lsig_out_%=:\n\t"
                 "s_mov_b64 exec, -1\n"
                 ".if !%[ap]\n.Lsig_fol_%=:\n.endif"
                 : [land] "=&v"(land), [tag] "=&v"(tagv), [zero] "=&v"(zero)
                 : [fl] "s"(follower), [so] "v"(soff), [po] "v"(poff), [ts] "s"(tag), [base] "s"(ws + WS_FLAG), [pm] "s"(pmask), [ap] "n"(ALLPOLL ? 1 : 0), [dr] "n"(DRAIN ? 1 : 0)
                 : "memory", "scc", "vcc", "s96", "s97");
}
struct Ids { int pq, cq, po, co; };
DI Ids ids() {
    int bid = blockIdx.x; asm volatile("" : "+s"(bid));
    const int xcd = bid & 7, idx = bid >> 3;
    int pq, cq, po, co;
#define IDS_BODY pq = (xcd & 1) * 8 + (idx & 7); cq = (xcd >> 1) * 4 + (idx >> 3); po = (xcd & 3) * 4 + (idx & 3); co = (xcd >> 2) * 8 + (idx >> 2);
    IDS_BODY
    return Ids{pq, cq, po, co};
}

DI void slow_warm(unsigned dump, const void* W, int w, int lane) {
    int bid = blockIdx.x; asm volatile("" : "+s"(bid));
    const int xcd = bid & 7, idx = bid >> 3;
    const int sc = (3 - (int)((uintptr_t)W >> 7)) & 7;
    if ((sc >> 2) != ((xcd >> 1) & 1)) return;
    const char* src = (const char*)W + (idx + 32 * w) * 49152 + (sc + (xcd >> 2) * 8) * 128;
#pragma unroll
    for (int G = 0; G < 3; ++G) glds16(src + G * 2048, (unsigned)((lane >> 3) * 6144 + (lane & 7) * 16), dump);
}

template <int V> struct IC { static constexpr int value = V; };
template <bool LOCAL> struct QC {
    static constexpr int AROWS = LOCAL ? 144 : 128;
    static constexpr int ABYTES = AROWS * 128;
    static constexpr int STAGE = ABYTES + 24576;
    static constexpr int OFF_MEAN = 143360;
    static constexpr int OFF_RSTD = OFF_MEAN + 576;
    static constexpr int OFF_CS = OFF_RSTD + 576;
};
constexpr int OFF_EX = 131072;

DI unsigned colsum_off(int G, int lane) {
    const Ids I = ids();
    unsigned off = (unsigned)(I.pq * 64 + (lane >> 3)) * 6144u + (unsigned)(G * 2048 + I.cq * 128 + (lane & 7) * 16);
    asm volatile("" : "+v"(off));
    return off;
}
DI void colsum_load(u32x4 (&c)[8], const void* W, unsigned off) {
#define CS_NEXT "s_add_u32 s96, s96, 0xc000\n\ts_addc_u32 s97, s97, 0\n\t"
    asm volatile("s_cmp_eq_u64 %9, 0\n\ts_cbranch_scc1 .Lcs_skip_%=\n\ts_mov_b64 s[96:97], %9\n\t"
                 "global_load_dwordx4 %0, %8, s[96:97]\n\t" CS_NEXT "global_load_dwordx4 %1, %8, s[96:97]\n\t" CS_NEXT
                 "global_load_dwordx4 %2, %8, s[96:97]\n\t" CS_NEXT "global_load_dwordx4 %3, %8, s[96:97]\n\t" CS_NEXT
                 "global_load_dwordx4 %4, %8, s[96:97]\n\t" CS_NEXT "global_load_dwordx4 %5, %8, s[96:97]\n\t" CS_NEXT
                 "global_load_dwordx4 %6, %8, s[96:97]\n\t" CS_NEXT "global_load_dwordx4 %7, %8, s[96:97]\n"
                 ".Lcs_skip_%=:"
                 : "=&v"(c[0]), "=&v"(c[1]), "=&v"(c[2]), "=&v"(c[3]), "=&v"(c[4]), "=&v"(c[5]), "=&v"(c[6]), "=&v"(c[7])
                 : "v"(off), "s"(W) : "memory", "scc", "s96", "s97");
}
DI void colsum_store(u32x4 (&c)[8], char* ws, int mat, int G, int lane) {
    asm volatile("" : "+v"(c[0]), "+v"(c[1]), "+v"(c[2]), "+v"(c[3]), "+v"(c[4]), "+v"(c[5]), "+v"(c[6]), "+v"(c[7]));
    const Ids I = ids();
    const int m = lane & 15;
    const unsigned one = (m & 1) ? 0xbf800000u : 0xbf80u;
    const u32x4 sl = {(m >> 1) == 0 ? one : 0u, (m >> 1) == 1 ? one : 0u, (m >> 1) == 2 ? one : 0u, (m >> 1) == 3 ? one : 0u};
    f32x4 d = {0, 0, 0, 0};
#pragma unroll
    for (int i = 0; i < 8; ++i) d = MFMA16(__builtin_bit_cast(bf16x8, sl), __builtin_bit_cast(bf16x8, c[i]), d);
#pragma unroll
    for (int r = 0; r < 4; ++r)
        d[r] += __int_as_float(__builtin_amdgcn_update_dpp(0, __float_as_int(d[r]), 0x128, 0xF, 0xF, true));
    if (lane < 32 && (lane & 8) == 0)
        st16(ws + WS_CSP + mat * 196608, (unsigned)((I.cq * 16 + I.pq) * 768 + G * 256 + (lane & 7) * 32 + (lane >> 4) * 16), __builtin_bit_cast(u32x4, d));
    typedef __attribute__((ext_vector_type(2))) unsigned short u16x2;
    u16x2 mn = {0x7fff, 0x7fff};
#pragma unroll
    for (int e = 0; e < 4; ++e) {
        u16x2 mx = {0, 0};
#pragma unroll
        for (int i = 0; i < 8; ++i) { const unsigned u = c[i][e] & 0x7fff7fffu; mx = __builtin_elementwise_max(mx, __builtin_bit_cast(u16x2, u)); }
        mn = __builtin_elementwise_min(mn, mx);
    }
    const unsigned wn = wave_min((unsigned)min(mn[0], mn[1])) << 16;
    if (lane == 0) __hip_atomic_store((__attribute__((address_space(1))) unsigned*)(ws + WS_WN + mat * 4096 + blockIdx.x * 16 + G * 4), wn,
                                      __ATOMIC_RELAXED, __HIP_MEMORY_SCOPE_AGENT);

}

template <bool LOCAL>
struct WFirst {
    unsigned v[2];
    DI WFirst(int w, int ct, int lane) {
#pragma unroll
        for (int i = 0; i < 2; ++i) {
            const int k = 8 * i + (lane >> 3), pos = lane & 7;
            v[i] = (unsigned)k * 6144u + (unsigned)(w * 1024 + ct * 64 + ((pos >> 1) ^ (((k >> 1) & 1) | (i << 1))) * 16 + (pos & 1) * 8) * 2u;
        }
        asm volatile("" : "+v"(v[0]), "+v"(v[1]));
    }
    DI void issue(const void* W, unsigned lds0, int w, int i0, int i1) const {
#pragma unroll
        for (int i = i0; i < i1; ++i)
            glds16((const char*)W + (size_t)(i >> 1) * (16 * 6144), v[i & 1], lds0 + (i >> 3) * QC<LOCAL>::STAGE + QC<LOCAL>::ABYTES + (w * 8 + (i & 7)) * 1024);
    }
};

template <bool LOCAL, class F, class F2>
DI void qkv_loader(char* smem, int w, char* ws, int set, u64 tag, const __hip_bfloat16* __restrict__ X, const __hip_bfloat16* __restrict__ W,
                   const float* __restrict__ statp, const char* __restrict__ csp, bool ident, const void* nextW, F tail, F2 mid SP_PARAM) {
    typedef QC<LOCAL> C;
    constexpr int NT = 16;
    const int lane = lane_id();
    asm volatile("" : "+s"(w));
    const int idx = blockIdx.x >> 3;
    const Ids I = ids();
    const int panel = I.pq, ct = I.cq;
    const int R0 = panel * 128;
    const unsigned lds0 = (unsigned)(uintptr_t)smem;
    auto grow = [&](int i) -> int {
        if (!LOCAL) return R0 + i;
        if (i < 128) { const int t = (i >> 4) & 3, a = i & 15; return R0 + (i & 64) + 2 * (16 * (t & 1) + a) + (t >> 1); }
        const int a = i & 15; const int r = R0 - 16 + 2 * (a & 7) + (a >> 3);
        return r < 0 ? 0 : r;
    };
    unsigned voffA[5], voffB[6];
#pragma unroll
    for (int i = 0; i < 6; ++i) {
        const int j = w + 4 * i, G = j >> 3, k = 8 * (j & 7) + (lane >> 3);
        const int pos = lane & 7, f = ((k >> 1) & 1) | (((k >> 3) & 1) << 1);
        const int seg = (pos >> 1) ^ f;
        voffB[i] = (unsigned)k * 6144u + (unsigned)(G * 1024 + ct * 64 + seg * 16 + (pos & 1) * 8) * 2u;
    }
    auto issueW = [&](int t, int buf) {
        const char* wb = (const char*)W + (size_t)t * (64 * 6144);
        const unsigned sb = lds0 + buf * C::STAGE;
#pragma unroll
        for (int i = 0; i < 6; ++i) glds16(wb, voffB[i], sb + C::ABYTES + (w + 4 * i) * 1024);
    };
    asm volatile("" : "+v"(voffB[0]), "+v"(voffB[1]), "+v"(voffB[2]), "+v"(voffB[3]), "+v"(voffB[4]), "+v"(voffB[5]));
    const WFirst<LOCAL> first(w, ct, lane);
    const int g = lane >> 4, r = lane & 15, pos = 32 * w + 2 * r + (g >> 1);
    const int lr = !LOCAL ? (pos & 64) + 2 * (pos & 31) + ((pos >> 5) & 1)
                          : (w == 0 && (g & 1)) ? 128 + (r & 7) + 8 * (g >> 1) : (pos & 64) + ((pos & 1) * 2 + ((pos >> 5) & 1)) * 16 + ((pos >> 1) & 15);
    unsigned so = (unsigned)(panel * 16384 + g * 1024 + w * 256 + r * 16), ho = (unsigned)(g * 1024 + (panel ? panel * 16384 - 15488 + (r & 7) * 16 : 0));
    unsigned co = (unsigned)((ct * 16 + g) * 768 + w * 192 + r * 12);
    lds_ptr mp = (lds_ptr)smem + C::OFF_MEAN + lr * 4, cp = (lds_ptr)smem + C::OFF_CS + w * 192 + r * 12 + ((g & 1) ? 8 : g * 2);
    unsigned fo = flag_off((LOCAL ? 13 : 10) + min(g, 2), ct, panel, r);
    asm volatile("" : "+v"(so), "+v"(ho), "+v"(co), "+v"(mp), "+v"(cp), "+v"(fo));
#pragma unroll
    for (int i = 0; i < 5; ++i) {
        const int j = (i < 4) ? (w + 4 * i) : (16 + (w & 1));
        const int row = 8 * j + (lane >> 3);
        const int c = (lane & 7) ^ ((row >> 1) & 7);
        const int gr = grow(row < C::AROWS ? row : 0);
        voffA[i] = (unsigned)((gr >> 7) * 262144 + (gr & 127) * 128 + c * 16);
        asm volatile("" : "+v"(voffA[i]));
    }
    auto issueA = [&](int t, int buf) {
        const char* xa = (const char*)X + t * 16384;
        const unsigned sb = lds0 + buf * C::STAGE;
        if (LOCAL && w < 2) glds16c(xa, voffA[4], sb + (16 + w) * 1024);
#pragma unroll
        for (int i = 0; i < 4; ++i) glds16c(xa, voffA[i], sb + (w + 4 * i) * 1024);
    };
    u64 land;
    if (w == 3) __builtin_amdgcn_s_setprio(3);
    signal_poll<LOCAL, false>(w - 3, ws, flag_off(set, I.po, lane & 15, I.co),
                              flag_off(set, (lane & 16) && panel ? panel - 1 : panel, ct, lane & 15),
                              !LOCAL ? 0xffffull : ident ? 0xc000ffffull : 0xffffffffull, tag, land);
    if (w < 3) first.issue(W, lds0, w, 0, 12);
    BAR();
    STAMP(LOCAL ? 1 : 6);
    issueA(0, 0);
    issueA(1, 1);
    if (w < 3) { first.issue(W, lds0, w, 12, 16); asm volatile("s_waitcnt vmcnt(8)" ::: "memory"); }
    else asm volatile("s_waitcnt vmcnt(4)" ::: "memory");
    BAR();
    asm volatile("" :: "v"(land));
    __builtin_amdgcn_s_setprio(0);
    STAMP(LOCAL ? 2 : 7);
    issueW(2, 2);
    issueA(2, 2);
    VM_BAR(10);
    asm volatile("global_load_dwordx2 %0, %1, %2 sc1" : "=&v"(land) : "v"(fo), "s"(ws + WS_FLAG) : "memory");
    issueA(3, 0); issueW(3, 0);
    int buf = 1;
#pragma unroll 1
    for (int t = 1; t < NT - 3; ++t) {
        VM_BAR(10);
        issueA(t + 3, buf); issueW(t + 3, buf);
        if (t == 10 && idx < 3) code_touch(idx * 4 + w, lane);
        if (LOCAL && t == 11)
            glds16(nextW, (unsigned)((idx + 32 * w) * 8 + (lane >> 3)) * 2048u +
                   (unsigned)(((I.co & 8) + ((3 - (int)((uintptr_t)nextW >> 7)) & 7)) * 128 + (lane & 7) * 16), lds0 + OFF_DUMP);
        buf = (buf == 2) ? 0 : buf + 1;
    }
    typedef __attribute__((ext_vector_type(3))) float f32x3;
    const bool halo = LOCAL && w == 0;
    asm volatile("" : "+v"(land));
    if (!ident) {
        if (!__all(land == tag)) flag_poll(ws, fo, tag);
        f32x3 cs[4];
        f32x4 sv[4], hv[4];
        typedef __attribute__((address_space(3))) float* lds_f;
        auto add = [](float a, float b) { float r; asm("v_add_f32 %0, %1, %2" : "=v"(r) : "v"(a), "v"(b)); return r; };
        auto fold32 = [](float a, float b) { asm("s_nop 1\n\tv_permlane32_swap_b32 %0, %1\n\tv_add_f32 %0, %0, %1" : "+v"(a), "+v"(b)); return a; };
        auto fold16 = [](float a, float b) { asm("s_nop 1\n\tv_permlane16_swap_b32 %0, %1\n\tv_add_f32 %0, %0, %1" : "+v"(a), "+v"(b)); return a; };
        auto sum4 = [&](auto (&v)[4], int e) { return add(add(v[0][e], v[1][e]), add(v[2][e], v[3][e])); };
#pragma unroll
        for (int i = 0; i < 4; ++i) asm volatile("global_load_dwordx3 %0, %1, %2 sc1" : "=&v"(cs[i]) : "v"(co), "s"(csp + i * 3072) : "memory");
        VM_BAR(14);
#pragma unroll
        for (int i = 0; i < 4; ++i) asm volatile("global_load_dwordx4 %0, %1, %2 sc1" : "=&v"(sv[i]) : "v"(so), "s"((const char*)statp + i * 4096) : "memory");
        if (LOCAL)
            asm volatile("s_cmp_lg_u32 %9, 0\n\ts_cbranch_scc1 .Lhv_skip_%=\n\t"
                         "global_load_dwordx4 %0, %4, %5 sc1\n\tglobal_load_dwordx4 %1, %4, %6 sc1\n\t"
                         "global_load_dwordx4 %2, %4, %7 sc1\n\tglobal_load_dwordx4 %3, %4, %8 sc1\n"
                         ".Lhv_skip_%=:" : "=&v"(hv[0]), "=&v"(hv[1]), "=&v"(hv[2]), "=&v"(hv[3])
                         : "v"(ho), "s"(statp), "s"(statp + 1024), "s"(statp + 2048), "s"(statp + 3072), "s"(w) : "memory", "scc");
        if (halo) asm volatile("s_waitcnt vmcnt(8)" ::: "memory"); else asm volatile("s_waitcnt vmcnt(4)" ::: "memory");
        asm volatile("" : "+v"(cs[0]), "+v"(cs[1]), "+v"(cs[2]), "+v"(cs[3]));
        const float c2 = sum4(cs, 2);
        *(lds_f)cp = fold16(fold32(sum4(cs, 0), sum4(cs, 1)), fold32(c2, c2));
        BAR();
        asm volatile("s_waitcnt vmcnt(0)" : "+v"(sv[0]), "+v"(sv[1]), "+v"(sv[2]), "+v"(sv[3]) :: "memory");
        mid(0);
        const float s1 = fold32(sum4(sv, 0), sum4(sv, 2)), s2 = fold32(sum4(sv, 1), sum4(sv, 3));
        float h1, h2;
        asm("v_mov_b32 %0, %2\n\tv_mov_b32 %1, %3" : "=&v"(h1), "=&v"(h2) : "v"(s1), "v"(s2));
        if (halo) {
            asm volatile("" : "+v"(hv[0]), "+v"(hv[1]), "+v"(hv[2]), "+v"(hv[3]));
            const float u0 = sum4(hv, 0), u1 = sum4(hv, 1), u2 = sum4(hv, 2), u3 = sum4(hv, 3);
            h1 = fold32(u0, panel ? u2 : u0); h2 = fold32(u1, panel ? u3 : u1);
        }
        float mean, rstd;
        ln_stats(fold16(s1, h1), fold16(s2, h2), mean, rstd);
        *(lds_f)mp = mean; *(lds_f)(mp + (C::OFF_RSTD - C::OFF_MEAN)) = rstd;
    } else { VM_BAR(10); tail(); BAR(); mid(0); }
    mid(1);
    WAIT_LGKM_BAR();
}

template <bool LOCAL>
DI void qkv_compute(char* smem, int w, const void* W, u32x4 (&qo4)[4], __hip_bfloat16* __restrict__ kvf, __hip_bfloat16* __restrict__ ofrag, bool ident,
                    const char* statp, const char* wnp SP_PARAM) {
    typedef QC<LOCAL> C;
    constexpr int NT = 16, ABYTES = C::ABYTES, STAGE = C::STAGE;
    const int lane = lane_id();
    asm volatile("" : "+s"(w));
    const int wm = w & 1, wn = w >> 1;
    const Ids I = ids();
    const int panel = I.pq, ct = I.cq;
    const int l15 = lane & 15, g = lane >> 4;
    const lds_cptr sm = (lds_cptr)smem;
    lds_ptr smw = (lds_ptr)smem;

    f32x4 acc[3][4][2];
    f32x4 acch[2];
    float cb;
    f32x4 bias;

    bf16x8 A[2][5][2];
    s16x4 B[3][2][2];
    lds_cptr pa[2], pb[2];
    auto rdA = [&](auto WM, int p, int i) { A[p][i >> 1][i & 1] = lds_b128(pa[i & 1] + ((i >> 1) < 4 ? (i >> 1) * 2048 : 16384 - WM.value * 8192)); };
    auto rdB = [&](int c, int i) { B[c % 3][i >> 1][i & 1] = lds_tr(pb[c & 1] + ((c >> 1) + 1) % 3 * 8192 + (i >> 1) * 4096 + (i & 1) * 512); };
    auto column = [&](auto WM, int p, int c, bool first, auto gap) {
        const int G = ((c >> 1) + 1) % 3, f = c & 1;
        const bool halo = LOCAL && G == 1 + WM.value;
#pragma unroll
        for (int n = 0; n < (halo ? 10 : 8); ++n) {
            const bf16x8 x = A[p][n >> 1][n & 1], y = cat8(B[c % 3][n & 1][0], B[c % 3][n & 1][1]);
            const bf16x8 u = G < 2 ? y : x, v = G < 2 ? x : y;
            f32x4& d = n < 8 ? acc[G][n >> 1][f] : acch[f];
            const f32x4 c0 = bias;
            if (!first || (n & 1)) asm volatile("v_mfma_f32_16x16x32_bf16 %0, %1, %2, %0" : "+v"(d) : "v"(u), "v"(v));
            else if (n == 8) asm volatile("v_mfma_f32_16x16x32_bf16 %0, %1, %2, 0" : "=&v"(d) : "v"(u), "v"(v));
            else asm volatile("v_mfma_f32_16x16x32_bf16 %0, %1, %2, %3" : "=&v"(d) : "v"(u), "v"(v), "v"(c0));
            SBAR();
            gap(n);
            SBAR();
        }
    };

    f32x4 cq[2], ck[2], mu4[5], rs4[5]; float cv[2], mu[5], rs[5];
    u32x4 qfr[4], kfr[4];
    u32x4 va[2][2];
    f32x4 mwin, mfirst;
    typedef __attribute__((address_space(3))) u32x4* lds_u4;
    const bool negpos = (wm == 0) && (panel == 0);
    VM_BAR(0);
    unsigned cm01 = 0, cm23 = 0;
    lds_cptr ka[2] = {sm, sm}, vo2[2] = {sm, sm};
    lds_u4 ex = (lds_u4)(smw + OFF_EX + wn * 6144) + lane;
    lds_cptr s1 = sm + C::OFF_MEAN + wm * 256 + l15 * 4, s4 = sm + C::OFF_MEAN + wm * 256 + g * 16;
    lds_cptr c1 = sm + C::OFF_CS + wn * 128 + l15 * 4, c4 = sm + C::OFF_CS + wn * 128 + g * 16;
    asm volatile("" : "+v"(s1), "+v"(s4), "+v"(c1), "+v"(c4));
    if (LOCAL) {
        unsigned keepv = negpos ? __float_as_uint(NEG_MASK) : 0u;
        asm volatile("" : "+s"(keepv));
        u32x4 mw, mf;
        unsigned prv[4];
#pragma unroll
        for (int r = 0; r < 4; ++r) {
            const int d = l15 - (4 * g + r);
            unsigned out = (unsigned)(((d << 28) >> 31));
            prv[r] = (unsigned)(d >> 31);
            asm volatile("" : "+v"(out), "+v"(prv[r]));
            mw[r] = out & 0xff800000u;
            mf[r] = (out & 0xff800000u) | (~out & keepv);
        }
        const lds_cptr e0 = sm + OFF_EX + wn * 6144;
        ka[0] = e0 + (wm ? lane : 256 + (lane ^ 8)) * 16;         ka[1] = e0 + (wm ? 64 + lane : 256 + lane) * 16;
        vo2[0] = e0 + (wm ? 128 + lane : 320 + (lane ^ 32)) * 16; vo2[1] = e0 + (wm ? 192 + lane : 320 + lane) * 16;
        cm01 = ~((prv[0] >> 16) | (prv[1] << 16)); cm23 = ~((prv[2] >> 16) | (prv[3] << 16));
        mwin = __builtin_bit_cast(f32x4, mw); mfirst = __builtin_bit_cast(f32x4, mf);
        asm volatile("" : "+v"(mwin), "+v"(mfirst), "+v"(cm01), "+v"(cm23), "+v"(ka[0]), "+v"(ka[1]), "+v"(vo2[0]), "+v"(vo2[1]), "+v"(ex));
    }
    typedef const __attribute__((address_space(3))) f32x4* lds_f4; typedef const __attribute__((address_space(3))) float* lds_f1;
    auto mainloop = [&](auto WM) {
        int ty = lane_id();
        const int l15 = ty & 15, g = ty >> 4;
        const int swA = (l15 >> 1) & 7;
        const int q4 = l15 >> 2, p4 = l15 & 3, fk = ((q4 >> 1) & 1) | ((g & 1) << 1);
        lds_cptr pA0 = sm + wm * 8192 + l15 * 128 + ((0 + g) ^ swA) * 16, pA1 = sm + wm * 8192 + l15 * 128 + ((4 + g) ^ swA) * 16;
        lds_cptr pB0 = sm + ABYTES + (8 * g + q4) * 128 + (((2 * wn + 0) ^ fk) * 32) + p4 * 8;
        lds_cptr pB1 = sm + ABYTES + (8 * g + q4) * 128 + (((2 * wn + 1) ^ fk) * 32) + p4 * 8;
        asm volatile("" : "+v"(pA0), "+v"(pA1), "+v"(pB0), "+v"(pB1));
        slow_warm((unsigned)(uintptr_t)smem + OFF_DUMP, W, w, ty);
        unsigned so = (unsigned)(panel * 16384 + wm * 512 + ty * 8);
        const char *p0 = ident ? (const char*)W : statp, *p1 = ident ? (const char*)W : wnp;
        u32x4 wn;
        asm volatile("global_load_dwordx4 %0, %2, %3 sc1" : "=&v"(wn), "+v"(so) : "v"(0), "s"(p1) : "memory");
        BAR();
        STAMP(LOCAL ? 1 : 7);
        f32x2 sq;
        asm volatile("global_load_dwordx2 %0, %1, %2 sc1" : "=&v"(sq) : "v"(so), "s"(p0) : "memory");
        BAR();
        pa[0] = pA0; pa[1] = pA1; pb[0] = pB0; pb[1] = pB1;
#pragma unroll
        for (int i = 0; i < 4; ++i) rdB(0, i);
#pragma unroll
        for (int i = 0; i < 8; ++i) rdA(WM, 0, i);
#pragma unroll
        for (int i = 0; i < 4; ++i) if (LOCAL && WM.value == 0 && i >= 2) rdA(WM, 0, 6 + i); else rdB(1, i);
        SBAR();
        asm volatile("s_waitcnt vmcnt(0)" : "+v"(sq), "+v"(wn) :: "memory");
        const float d = fmaxf(__builtin_fmaf(sq[0] * (-1.f / 64.f), sq[0], sq[1]), 0.f);
        cb = fminf(__builtin_amdgcn_sqrtf(__uint_as_float(wave_min(__float_as_uint(d)))) * 64.f * __uint_as_float(min(min(wn[0], wn[1]), wn[2])), 1.0e30f);
        if (ident) cb = 0.f;
        bias = f32x4{cb, cb, cb, cb};
        asm volatile("s_nop 1" : "+v"(bias));
        WAIT_LGKM();
        int off = 0;
        constexpr bool HK = LOCAL && WM.value == 0;
        auto front = [&](int p, bool first) {
#pragma unroll
            for (int c = 0; c < 4; ++c)
                column(WM, p, c, first, [&](int n) {
                    if (HK && c == 0) { if (n < 2) rdB(1, 2 + n); else if (n < 6) rdB(2, n - 2); }
                    else if (n < 4) rdB(c + 2, n);
                    if (LOCAL && !HK && c == 0 && (n == 4 || n == 5)) rdA(WM, p, 4 + n);
                    if (c == 1 && n == 5) { off = (off == 2 * STAGE) ? 0 : off + STAGE; asm volatile("" : "+s"(off)); }
                    if (c == 2 && n == 4) { pa[0] = pA0 + off; asm volatile("" : "+v"(pa[0])); }
                    if (c == 2 && n == 5) { pa[1] = pA1 + off; asm volatile("" : "+v"(pa[1])); }
                    if (c == 3 && n == 4) { pb[0] = pB0 + off; asm volatile("" : "+v"(pb[0])); }
                    if (c == 3 && n == 5) { pb[1] = pB1 + off; asm volatile("" : "+v"(pb[1])); }
                    if ((c == 0 && n == 3) || ((c == 1 || c == 2 || (HK && c == 0)) && n == 7)) __builtin_amdgcn_s_waitcnt(0xC47F);
                });
            WAIT_LGKM_BAR();
        };
        auto back = [&](int p, bool first) {
            column(WM, p, 4, first, [&](int n) { if (n & 1) rdB(0, n >> 1); else rdA(WM, p ^ 1, n >> 1); });
            column(WM, p, 5, first, [&](int n) {
                if (!(n & 1)) rdA(WM, p ^ 1, 4 + (n >> 1)); else if (HK && n > 4) rdA(WM, p ^ 1, 6 + (n >> 1)); else rdB(1, n >> 1);
                if (n == 7) __builtin_amdgcn_s_waitcnt(0xC87F);
            });
        };
        auto stage = [&](int p, bool first) { front(p, first); back(p, first); };
        stage(0, true);
        front(1, false);
#pragma unroll 1
        for (int t = 1; t < NT - 1; t += 2) { back(1, false); stage(0, false); front(1, false); }
        column(WM, 1, 4, false, [&](int n) {
            constexpr int RS = C::OFF_RSTD - C::OFF_MEAN, H = 512 - WM.value * 256;
            if (n == 0) { ck[0] = *(lds_f4)(c4 + 256); ck[1] = *(lds_f4)(c4 + 320); cv[0] = *(lds_f1)(c1 + 512); cv[1] = *(lds_f1)(c1 + 512 + 64); }
            else if (n < 5) {
                mu[n - 1] = *(lds_f1)(s1 + (n - 1) * 64); rs[n - 1] = *(lds_f1)(s1 + RS + (n - 1) * 64);
                mu4[n - 1] = *(lds_f4)(s4 + (n - 1) * 64); rs4[n - 1] = *(lds_f4)(s4 + RS + (n - 1) * 64);
            } else if (n == 5) {
                if (LOCAL && WM.value == 0) { mu[4] = *(lds_f1)(s1 + H); rs[4] = *(lds_f1)(s1 + RS + H); }
                else if (LOCAL) { mu4[4] = *(lds_f4)(s4 + H); rs4[4] = *(lds_f4)(s4 + RS + H); }
            } else if (n == 6) { cq[0] = *(lds_f4)c4; cq[1] = *(lds_f4)(c4 + 64); }
        });
        if (LOCAL) column(WM, 1, 5, false, [](int n) { if (n == 6) __builtin_amdgcn_s_waitcnt(0xC07F); });
        if (LOCAL) {
            auto fix = [&](int a, int t) {
#pragma unroll
                for (int f = 0; f < 2; ++f)
                    acc[a][t][f] = (a == 2) ? rs4[t] * (acc[2][t][f] + mu4[t] * cv[f]) + rs4[t] * -cb
                                            : rs[t] * (acc[a][t][f] + mu[t] * (a ? ck[f] : cq[f])) + rs[t] * -cb;
            };
            auto pk = [&](const f32x4 (&v)[2]) { return u32x4{pack_bf16(v[0][0], v[0][1]), pack_bf16(v[0][2], v[0][3]), pack_bf16(v[1][0], v[1][1]), pack_bf16(v[1][2], v[1][3])}; };
            if (!ident) {
                fix(1, 1); fix(1, 3); fix(2, 1); fix(2, 3);
#pragma unroll
                for (int f = 0; f < 2; ++f)
                    acch[f] = (WM.value == 0) ? rs[4] * (acch[f] + mu[4] * ck[f]) : rs4[4] * (acch[f] + mu4[4] * cv[f]);
            }
            auto pkv = [&](int t) {
#pragma unroll
                for (int f = 0; f < 2; ++f) {
                    va[t >> 1][f][(t & 1) * 2] = pack_bf16(acc[2][t][f][0], acc[2][t][f][1]);
                    va[t >> 1][f][(t & 1) * 2 + 1] = pack_bf16(acc[2][t][f][2], acc[2][t][f][3]);
                }
            };
            kfr[1] = pk(acc[1][1]); kfr[3] = pk(acc[1][3]); pkv(1); pkv(3);
            u32x4 hf = pk(acch);
            if (WM.value == 0) {
                ex[0] = kfr[1]; ex[64] = kfr[3];
#pragma unroll
                for (int u = 0; u < 2; ++u)
                    asm volatile("ds_write_b64 %0, %1 offset:%3\n\tds_write_b64 %0, %2 offset:%3+8" :: "v"(ex), "v"(u32x2{va[u][0][2], va[u][0][3]}),
                                 "v"(u32x2{va[u][1][2], va[u][1][3]}), "n"(2048 + u * 1024) : "memory");
                if (negpos) hf = u32x4{0, 0, 0, 0};
            }
            ex[256 + WM.value * 64] = hf;
            SBAR();
            if (!ident) { fix(0, 0); fix(0, 1); fix(0, 2); fix(0, 3); fix(1, 0); fix(1, 2); fix(2, 0); fix(2, 2); }
#pragma unroll
            for (int t = 0; t < 4; ++t) qfr[t] = pk(acc[0][t]);
            kfr[0] = pk(acc[1][0]); kfr[2] = pk(acc[1][2]); pkv(0); pkv(2);
            asm volatile("" : "+v"(qfr[0]), "+v"(qfr[1]), "+v"(qfr[2]), "+v"(qfr[3]), "+v"(kfr[0]), "+v"(kfr[2]),
                         "+v"(va[0][0]), "+v"(va[0][1]), "+v"(va[1][0]), "+v"(va[1][1]));
        }
    };
    const int head = ct * 2 + wn;
    unsigned qo = 0, ko = 0, vo = 0;
    if (LOCAL) {
        qo = (unsigned)((((panel * 8 + wm * 4) * 32 + head) * 64 + lane) * 16);
        asm volatile("" : "+v"(qo));
    } else {
        ko = (unsigned)((wm * 8 + (g & 1)) * 1024 + ((l15 & 7) + ((l15 & 8) << 1) + 32 * (g >> 1)) * 16);
        vo = (unsigned)((wm * 8 + 2 + (g >> 1)) * 1024 + (l15 + 32 * (g & 1)) * 16);
        asm volatile("" : "+v"(ko), "+v"(vo));
    }
    if (LOCAL && wm) mainloop(IC<1>{}); else mainloop(IC<0>{});

    if (!LOCAL) {
        auto pk = [&](f32x4 a, f32x4 b) { return u32x4{pack_bf16(a[0], a[1]), pack_bf16(a[2], a[3]), pack_bf16(b[0], b[1]), pack_bf16(b[2], b[3])}; };
        char* kvp = (char*)kvf + (size_t)(head * 16 + panel) * 16384;
        lds_ptr own = smw + wn * 16384;
        SBAR();
#pragma unroll
        for (int t = 0; t < 4; ++t) {
            const u32x4 kk = pk(rs[t] * (acc[1][t][0] + mu[t] * ck[0]) + rs[t] * -cb, rs[t] * (acc[1][t][1] + mu[t] * ck[1]) + rs[t] * -cb);
            if (t & 1) st16o<128>(kvp + (t >> 1) * 4096, ko, kk); else st16o<0>(kvp + (t >> 1) * 4096, ko, kk);
            kfr[t] = kk;
            SBAR();
            const int u = t >> 1, f = t & 1;
            const u32x4 vv = pk(rs4[2 * u] * (acc[2][2 * u][f] + mu4[2 * u] * cv[f]) + rs4[2 * u] * -cb,
                                rs4[2 * u + 1] * (acc[2][2 * u + 1][f] + mu4[2 * u + 1] * cv[f]) + rs4[2 * u + 1] * -cb);
            if (f) st16o<256>(kvp + u * 4096, vo, vv); else st16o<0>(kvp + u * 4096, vo, vv);
            qfr[t] = vv;
            SBAR();
        }
        column(IC<0>{}, 1, 5, false, [&](int n) {
            const int t = n >> 1;
            if (n & 1) *(lds_u4)(own + vo + (t >> 1) * 4096 + (t & 1) * 256) = qfr[t];
            else       *(lds_u4)(own + ko + (t >> 1) * 4096 + (t & 1) * 128) = kfr[t];
        });
        asm volatile("s_nop 7");
        SBAR();
#pragma unroll
        for (int u = 0; u < 2; ++u) {
            const float r0 = rs[2 * u] * QSCALE, r1 = rs[2 * u + 1] * QSCALE;
            const u32x4 qa = pk(r0 * (acc[0][2 * u][0] + mu[2 * u] * cq[0]) + r0 * -cb, r0 * (acc[0][2 * u][1] + mu[2 * u] * cq[1]) + r0 * -cb);
            const u32x4 qb = pk(r1 * (acc[0][2 * u + 1][0] + mu[2 * u + 1] * cq[0]) + r1 * -cb, r1 * (acc[0][2 * u + 1][1] + mu[2 * u + 1] * cq[1]) + r1 * -cb);
#pragma unroll
            for (int e = 0; e < 4; ++e) {
                const auto x = __builtin_amdgcn_permlane16_swap(qa[e], qb[e], false, false);
                qo4[2 * u][e] = x[0]; qo4[2 * u + 1][e] = x[1];
            }
        }
        return;
    }
    {
        const f32x4 zero4 = {0, 0, 0, 0};
        auto bf8 = [](u32x4 v) { return __builtin_bit_cast(bf16x8, v); };
        f32x4 sp[4], sc[4];
        sc[1] = MFMA16(bf8(kfr[1]), bf8(qfr[1]), mwin); sp[1] = MFMA16(bf8(kfr[0]), bf8(qfr[1]), mwin);
        sc[3] = MFMA16(bf8(kfr[3]), bf8(qfr[3]), mwin); sp[3] = MFMA16(bf8(kfr[2]), bf8(qfr[3]), mwin);
        asm volatile("" : "+v"(sc[1]), "+v"(sp[1]), "+v"(sc[3]), "+v"(sp[3]));
        WAIT_LGKM_BAR();
        const bf16x8 kp[2] = {lds_b128(ka[0]), lds_b128(ka[1])};
        u32x2 vp[2][2];
#pragma unroll
        for (int u = 0; u < 2; ++u)
            asm volatile("ds_read_b64 %0, %2\n\tds_read_b64 %1, %2 offset:8" : "=&v"(vp[u][0]), "=&v"(vp[u][1]) : "v"(vo2[u]) : "memory");
        sc[0] = MFMA16(bf8(kfr[0]), bf8(qfr[0]), mwin); sc[2] = MFMA16(bf8(kfr[2]), bf8(qfr[2]), mwin);
        SBAR();
        float x[4][4], m[4];
        unsigned a[4][2];
        auto logits = [&](int t0, int st) {
#pragma unroll
            for (int t = t0; t < 4; t += 2)
#pragma unroll
                for (int h = 0; h < 2; ++h) {
                    if (st == 0) {
                        const unsigned uc = pack_bf16(sc[t][2 * h], sc[t][2 * h + 1]), up = pack_bf16(sp[t][2 * h], sp[t][2 * h + 1]);
                        asm("v_bfi_b32 %0, %1, %2, %3" : "=v"(a[t][h]) : "v"(h ? cm23 : cm01), "v"(uc), "v"(up));
                    } else if (st == 1) { x[t][2 * h] = bf_lo(a[t][h]) * ATT_SCALE; x[t][2 * h + 1] = bf_hi(a[t][h]) * ATT_SCALE; }
                    else if (st == 2) a[t][h] = pack_bf16(x[t][2 * h], x[t][2 * h + 1]);
                    else { x[t][2 * h] = bf_lo(a[t][h]); x[t][2 * h + 1] = bf_hi(a[t][h]); }
                }
            SBAR();
        };
        logits(1, 0); logits(1, 1);
        sp[0] = MFMA16(kp[0], bf8(qfr[0]), mfirst); sp[2] = MFMA16(kp[1], bf8(qfr[2]), mfirst);
        SBAR();
        logits(1, 2); logits(1, 3);
        logits(0, 0); logits(0, 1); logits(0, 2); logits(0, 3);
        auto across = [&](float (&v)[4], auto op, auto fin) {
            const auto p = __builtin_amdgcn_permlane16_swap(__float_as_uint(v[0]), __float_as_uint(v[1]), false, false);
            const auto q = __builtin_amdgcn_permlane16_swap(__float_as_uint(v[2]), __float_as_uint(v[3]), false, false);
            const auto r = __builtin_amdgcn_permlane32_swap(__float_as_uint(op(__uint_as_float(p[0]), __uint_as_float(p[1]))),
                                                            __float_as_uint(op(__uint_as_float(q[0]), __uint_as_float(q[1]))), false, false);
            const unsigned z = __float_as_uint(fin(op(__uint_as_float(r[0]), __uint_as_float(r[1]))));
            const auto u = __builtin_amdgcn_permlane32_swap(z, z, false, false);
            const auto e = __builtin_amdgcn_permlane16_swap(u[0], u[0], false, false), f = __builtin_amdgcn_permlane16_swap(u[1], u[1], false, false);
            v[0] = __uint_as_float(e[0]); v[1] = __uint_as_float(e[1]); v[2] = __uint_as_float(f[0]); v[3] = __uint_as_float(f[1]);
        };
#pragma unroll
        for (int t = 0; t < 4; ++t) m[t] = amax3(x[t][0], x[t][1], x[t][2]);
        asm("v_max_f32 %0, %0, %4\n\tv_max_f32 %1, %1, %5\n\tv_max_f32 %2, %2, %6\n\tv_max_f32 %3, %3, %7\n\ts_nop 0"
            : "+v"(m[0]), "+v"(m[1]), "+v"(m[2]), "+v"(m[3]) : "v"(x[0][3]), "v"(x[1][3]), "v"(x[2][3]), "v"(x[3][3]));
        across(m, [](float p, float q) { float r; asm("v_max_f32 %0, %1, %2\n\ts_nop 0" : "=v"(r) : "v"(p), "v"(q)); return r; },
               [](float p) { return p * -LOG2E; });
        SBAR();
#pragma unroll
        for (int t = 0; t < 4; ++t)
#pragma unroll
            for (int r = 0; r < 4; ++r) x[t][r] = __builtin_fmaf(x[t][r], LOG2E, m[t]);
        SBAR();
#pragma unroll
        for (int t = 0; t < 4; ++t)
#pragma unroll
            for (int r = 0; r < 4; ++r) x[t][r] = __builtin_amdgcn_exp2f(x[t][r]);
        SBAR();
#pragma unroll
        for (int t = 0; t < 4; ++t) m[t] = (x[t][0] + x[t][1]) + (x[t][2] + x[t][3]);
        asm volatile("s_waitcnt lgkmcnt(0)" : "+v"(vp[0][0]), "+v"(vp[0][1]), "+v"(vp[1][0]), "+v"(vp[1][1]) :: "memory");
        u32x4 ve[2][2];
#pragma unroll
        for (int u = 0; u < 2; ++u)
#pragma unroll
            for (int f = 0; f < 2; ++f) ve[u][f] = u32x4{vp[u][f][0], vp[u][f][1], va[u][f][0], va[u][f][1]};
        across(m, [](float p, float q) { return p + q; }, [](float p) { return __builtin_amdgcn_rcpf(p); });
        asm volatile("" : "+v"(ve[0][0]), "+v"(ve[0][1]), "+v"(ve[1][0]), "+v"(ve[1][1]));
        SBAR();
#pragma unroll
        for (int t = 0; t < 4; ++t)
#pragma unroll
            for (int r = 0; r < 4; ++r) x[t][r] *= m[t];
        SBAR();
#pragma unroll
        for (int t = 0; t < 4; ++t)
#pragma unroll
            for (int h = 0; h < 2; ++h) a[t][h] = pack_bf16(x[t][2 * h], x[t][2 * h + 1]);
        SBAR();
        f32x4 o0[4], o1[4];
#pragma unroll
        for (int t = 0; t < 4; ++t) {
            const bf16x8 pb = bf8(u32x4{a[t][0] & ~cm01, a[t][1] & ~cm23, a[t][0] & cm01, a[t][1] & cm23});
            o0[t] = MFMA16(bf8((t & 1) ? va[t >> 1][0] : ve[t >> 1][0]), pb, zero4);
            o1[t] = MFMA16(bf8((t & 1) ? va[t >> 1][1] : ve[t >> 1][1]), pb, zero4);
        }
        SBAR();
#pragma unroll
        for (int t = 0; t < 4; ++t)
            st16(ofrag + t * 16384, qo, u32x4{pack_bf16(o0[t][0], o0[t][1]), pack_bf16(o0[t][2], o0[t][3]),
                                                 pack_bf16(o1[t][0], o1[t][1]), pack_bf16(o1[t][2], o1[t][3])});
    }
}

DI int out_row(int panel, bool localrows, int T, int a) {
    return localrows ? (panel * 128 + (T >> 2) * 64 + 2 * (16 * (T & 1) + a) + ((T >> 1) & 1)) : (panel * 128 + T * 16 + a);
}
DI int out_swz(int k) { return ((k >> 1) & 1) | (k & 4); }
DI unsigned wo_off(int w, int lane) {
    const int kl = lane >> 3;
    unsigned v = (unsigned)(w * 8 + kl) * 2048u + (unsigned)(ids().co * 128 + ((lane & 7) ^ out_swz(kl)) * 16);
    asm volatile("" : "+v"(v));
    return v;
}
DI void wo_load(const void* Wo, unsigned voff, int i0, int i1, unsigned lds0, int w) {
#pragma unroll
    for (int i = i0; i < i1; ++i) glds16((const char*)Wo + (size_t)i * 65536, voff, lds0 + (w + 4 * i) * 1024);
}
DI unsigned res_off(bool localrows, bool tiled, int lane) {
    const Ids I = ids();
    unsigned v = (unsigned)(I.po * 262144 + I.co * (tiled ? 16384 : 128) + out_row(0, localrows, 0, lane >> 3) * (tiled ? 128 : 2048) +
                            (((lane & 7) ^ (lane >> 3)) * 16));
    asm volatile("" : "+v"(v));
    return v;
}
DI void res_load(u32x4 (&res)[6], const void* Xin, bool localrows, bool tiled, unsigned off, int w) {
#pragma unroll
    for (int i = 0; i < 6; ++i) {
        const int j = min(w + 3 * i, 15);
        asm volatile("global_load_dwordx4 %0, %1, %2 sc1" : "=&v"(res[i]) : "v"(off), "s"((const char*)Xin + out_row(0, localrows, j >> 1, (j & 1) * 8) * (tiled ? 128 : 2048)) : "memory");
    }
}
DI void out_handoff(char* smem, int xoff, u32x4 (&res)[6], int w, char* ws, int set, u64 tag, int lane, u32x4 (&c)[8], int mat) {
    const Ids I = ids();
    u64 land;
    lds_ptr dst = (lds_ptr)smem + xoff + w * 1024 + lane * 16;
    asm volatile("" : "+v"(dst));
    if (w == 3) __builtin_amdgcn_s_setprio(3);
    signal_poll<false, false>(w - 3, ws, flag_off(set, I.pq, lane & 15, I.cq), flag_off(set, I.po, I.co, lane & 15), 0xffffull, tag, land);
    if (w < 3) {
        asm volatile("s_waitcnt vmcnt(0)" : "+v"(res[0]), "+v"(res[1]), "+v"(res[2]), "+v"(res[3]), "+v"(res[4]), "+v"(res[5]) :: "memory");
#pragma unroll
        for (int i = 0; i < 5; ++i) *(__attribute__((address_space(3))) u32x4*)(dst + i * 3072) = res[i];
        if (w == 0) *(__attribute__((address_space(3))) u32x4*)(dst + 15360) = res[5];
        if (mat >= 0) colsum_store(c, ws, mat, w, lane);
    }
    WAIT_LGKM_BAR();
    asm volatile("s_waitcnt vmcnt(0)" : "+v"(land) :: "memory");
    if (w < 3 && mat >= 0) flag_store(ws, (mat ? 10 : 13) + w, I.cq, I.pq, tag, lane);
    __builtin_amdgcn_s_setprio(0);
}

template <bool AFTER_LOCAL>
DI void out_compute(char* smem, int w, const __hip_bfloat16* __restrict__ ofrag,
                    __hip_bfloat16* __restrict__ Xout, float* __restrict__ statp SP_PARAM) {
    constexpr int XOFF = AFTER_LOCAL ? 147456 : 98304;
    STAMP(AFTER_LOCAL ? 3 : 10);
    VM_BAR(0);
    const int lane = lane_id();
    asm volatile("" : "+s"(w));
    const Ids I = ids();
    const int panel = I.po, ct = I.co;
    const int l15 = lane & 15, g = lane >> 4;
    const bool tiled = AFTER_LOCAL || statp != nullptr;
    const int rstr = tiled ? 128 : 2048;
    const int q4 = l15 >> 2, p4 = l15 & 3;
    const int krow = 4 * g + q4;
    const int sw = out_swz(krow);
    const lds_cptr sm = (lds_cptr)smem;
    lds_cptr bw[4], xo[4];
#pragma unroll
    for (int t = 0; t < 4; ++t) {
        bw[t] = sm + krow * 128 + ((((t >> 1) * 4 + (p4 >> 1) * 2 + (t & 1)) ^ sw) * 16) + (p4 & 1) * 8;
        xo[t] = sm + XOFF + (w * 32 + l15) * 128 + ((((t >> 1) * 4 + (g >> 1) * 2 + (t & 1)) ^ (l15 & 7)) * 16) + (g & 1) * 8;
    }
    unsigned so[2], sto, ao[2];
#pragma unroll
    for (int r2 = 0; r2 < 2; ++r2) {
        so[r2] = (unsigned)(panel * 262144 + ct * (tiled ? 16384 : 128) + out_row(0, AFTER_LOCAL, 2 * w + r2, l15 & 7) * rstr + (l15 >> 3) * 64 + g * 16);
        ao[r2] = (unsigned)((panel * 8 + 2 * w + r2) * 32768 + lane * 16);
    }
    sto = (unsigned)(((panel * 16 + ct) * 128 + 2 * w * 16 + l15) * 8);
    asm volatile("" : "+v"(bw[0]), "+v"(bw[1]), "+v"(bw[2]), "+v"(bw[3]), "+v"(xo[0]), "+v"(xo[1]), "+v"(xo[2]), "+v"(xo[3]),
                 "+v"(so[0]), "+v"(so[1]), "+v"(sto), "+v"(ao[0]), "+v"(ao[1]));
    bf16x8 ones;
    {
        unsigned o1; asm volatile("v_mov_b32 %0, 0x3f803f80" : "=v"(o1));
        ones = __builtin_bit_cast(bf16x8, (u32x4{o1, o1, o1, o1}));
        asm volatile("" : "+v"(ones));
    }
    const __amdgpu_buffer_rsrc_t ra = __builtin_amdgcn_make_buffer_rsrc((void*)ofrag, 0, 4 << 20, 0x00020000);
    asm volatile("" :: "s"(ra));
    u32x32 fr;
    BAR();
    STAMP(AFTER_LOCAL ? 4 : 11);
    u32x32 aw0, aw1, aw2, fr1;
    asm volatile(".irp j,0,1,2,3\n\tbuffer_load_dwordx4 v[96+4*\\j:99+4*\\j], %2, %4, 0 offen offset:1024*\\j sc1\n\t.endr\n\t"
                 ".irp j,0,1,2,3\n\tbuffer_load_dwordx4 v[112+4*\\j:115+4*\\j], %3, %4, 0 offen offset:1024*\\j sc1\n\t.endr\n\t"
                 ".irp j,0,1,2,3\n\tbuffer_load_dwordx4 v[128+4*\\j:131+4*\\j], %2, %4, %5 offen offset:1024*\\j sc1\n\t.endr"
                 : "=&{v[96:127]}"(aw0), "=&{v[128:159]}"(aw1) : "v"(ao[0]), "v"(ao[1]), "s"(ra), "s"(4096) : "memory");
    f32x16 acc[2];
#pragma unroll
    for (int r2 = 0; r2 < 2; ++r2)
#pragma unroll
        for (int c = 0; c < 4; ++c) {
            const u32x2 xr = *(const __attribute__((address_space(3))) u32x2*)(xo[c] + r2 * 2048);
            acc[r2][4 * c] = bf_lo(xr[0]); acc[r2][4 * c + 1] = bf_hi(xr[0]); acc[r2][4 * c + 2] = bf_lo(xr[1]); acc[r2][4 * c + 3] = bf_hi(xr[1]);
        }
    asm volatile(
        ".macro OUT_MM r, t, j, s\n\t"
        "v_mfma_f32_16x16x32_bf16 v[16*\\r+4*\\t:16*\\r+4*\\t+3], v[32+16*\\t+4*\\j:35+16*\\t+4*\\j], v[96+32*\\s+16*\\r+4*\\j:99+32*\\s+16*\\r+4*\\j], v[16*\\r+4*\\t:16*\\r+4*\\t+3]\n\t"
        ".endm\n\t"
        ".macro OUT_RD t, i, g\n\t"
        "ds_read_b64_tr_b16 v[32+16*\\t+2*(\\i):33+16*\\t+2*(\\i)], v[192+\\t] offset:(\\g)*16384+(\\i)*2048\n\t"
        ".endm\n\t"
        ".macro OUT_LD i, s\n\t"
        ".if (\\i) < 4\n\tbuffer_load_dwordx4 v[96+32*(\\s)+4*(\\i):99+32*(\\s)+4*(\\i)], %[a0], %[ra], s96 offen offset:1024*(\\i) sc1\n\t"
        ".else\n\tbuffer_load_dwordx4 v[96+32*(\\s)+4*(\\i):99+32*(\\s)+4*(\\i)], %[a1], %[ra], s96 offen offset:1024*((\\i)-4) sc1\n\t.endif\n\t"
        ".endm\n\t"
        ".macro OUT_CH r, t, s, rt, g\n\t"
        ".irp j,0,1,2,3\n\tOUT_MM \\r, \\t, \\j, \\s\n\tOUT_RD \\rt, 4*\\r+\\j, \\g\n\t.endr\n\t"
        ".endm\n\t"
        ".macro OUT_GRP s, g, gn, ld, up, v0, v1\n\t"
        "OUT_CH 0, 0, \\s, 3, \\g\n\ts_waitcnt vmcnt(\\v0)\n\t.if \\ld\n\tOUT_LD 0, (\\s+2)%%3\n\t.endif\n\t"
        "OUT_CH 1, 0, \\s, 3, \\g\n\t.if \\up\n\t.irp t,0,1,2,3\n\tv_add_u32 v[192+\\t], s97, v[192+\\t]\n\t.endr\n\t.endif\n\t.if \\ld\n\tOUT_LD 1, (\\s+2)%%3\n\t.endif\n\t"
        "OUT_CH 0, 1, \\s, 0, \\gn\n\t.if \\ld\n\tOUT_LD 2, (\\s+2)%%3\n\t.endif\n\t"
        "OUT_CH 1, 1, \\s, 0, \\gn\n\t.if \\ld\n\tOUT_LD 3, (\\s+2)%%3\n\t.endif\n\ts_waitcnt lgkmcnt(8)\n\t"
        "OUT_CH 0, 2, \\s, 1, \\gn\n\t.if \\ld\n\tOUT_LD 4, (\\s+2)%%3\n\t.endif\n\t"
        "OUT_CH 1, 2, \\s, 1, \\gn\n\t.if \\ld\n\tOUT_LD 5, (\\s+2)%%3\n\t.endif\n\t"
        "OUT_CH 0, 3, \\s, 2, \\gn\n\t.if \\ld\n\tOUT_LD 6, (\\s+2)%%3\n\t.endif\n\t"
        "OUT_CH 1, 3, \\s, 2, \\gn\n\t.if \\ld\n\tOUT_LD 7, (\\s+2)%%3\n\ts_addk_i32 s96, 0x1000\n\t.endif\n\t"
        "s_waitcnt vmcnt(\\v1) lgkmcnt(8)\n\t"
        ".endm\n\t"
        ".macro OUT_PACK y, c\n\t"
        ".irp i,0,1,2,3,4,5,6,7\n\tv_cvt_pk_bf16_f32 v[\\y+\\i], v[\\c+2*\\i], v[\\c+2*\\i+1]\n\t.endr\n\t"
        ".irp i,0,1,4,5\n\tv_permlane16_swap_b32 v[\\y+\\i], v[\\y+\\i+2]\n\t.endr\n\t"
        ".irp i,0,1,2,3\n\tv_mov_b32 v[\\y+8+\\i], v[\\y+\\i]\n\t.endr\n\t"
        "v_mfma_f32_16x16x32_bf16 v[\\y+12:\\y+15], v[\\y:\\y+3], v[\\y:\\y+3], 0\n\t"
        "v_mov_b32_dpp v[\\y+8], v[\\y+4] row_ror:8 row_mask:0xf bank_mask:0xc\n\tv_mov_b32_dpp v[\\y+9], v[\\y+5] row_ror:8 row_mask:0xf bank_mask:0xc\n\t"
        "v_mfma_f32_16x16x32_bf16 v[\\y+16:\\y+19], %[one], v[\\y:\\y+3], 0\n\t"
        "v_mov_b32_dpp v[\\y+10], v[\\y+6] row_ror:8 row_mask:0xf bank_mask:0xc\n\tv_mov_b32_dpp v[\\y+11], v[\\y+7] row_ror:8 row_mask:0xf bank_mask:0xc\n\t"
        "v_mfma_f32_16x16x32_bf16 v[\\y+12:\\y+15], v[\\y+4:\\y+7], v[\\y+4:\\y+7], v[\\y+12:\\y+15]\n\t"
        ".endm\n\t"
        ".macro OUT_CV y, c, i\n\tv_cvt_pk_bf16_f32 v[\\y+\\i], v[\\c+2*\\i], v[\\c+2*\\i+1]\n\tv_cvt_pk_bf16_f32 v[\\y+\\i+1], v[\\c+2*\\i+2], v[\\c+2*\\i+3]\n\t.endm\n\t"
        ".macro OUT_DP d, s, m\n\tv_mov_b32_dpp v[\\d], v[\\s] row_ror:8 row_mask:0xf bank_mask:\\m\n\tv_mov_b32_dpp v[\\d+1], v[\\s+1] row_ror:8 row_mask:0xf bank_mask:\\m\n\t.endm\n\t"
        ".macro OUT_REST y\n\t"
        "v_mfma_f32_16x16x32_bf16 v[\\y+16:\\y+19], %[one], v[\\y+4:\\y+7], v[\\y+16:\\y+19]\n\t"
        ".irp i,0,1,2,3\n\tv_mov_b32_dpp v[\\y+4+\\i], v[\\y+\\i] row_ror:8 row_mask:0xf bank_mask:0x3\n\t.endr\n\t"
        ".endm\n\t"
        ".macro OUT_DIAG y\n\t"
        "v_cndmask_b32 v[\\y+12], v[\\y+12], v[\\y+13], %[m0]\n\tv_cndmask_b32 v[\\y+14], v[\\y+14], v[\\y+15], %[m0]\n\t"
        "v_cndmask_b32 v[\\y+17], v[\\y+12], v[\\y+14], %[m1]\n\t"
        ".endm\n\t"
        "s_movk_i32 s96, 0x1000\n\t.irp i,4,5,6,7\n\tOUT_LD \\i, 1\n\t.endr\n\t"
        ".irp t,0,1,2\n\t.irp i,0,1,2,3,4,5,6,7\n\tOUT_RD \\t, \\i, 0\n\t.endr\n\ts_waitcnt lgkmcnt(4)\n\t.endr\n\t"
        "s_movk_i32 s96, 0x2000\n\ts_mov_b32 s97, 0xc000\n\t"
        "s_waitcnt vmcnt(12) lgkmcnt(0)\n"
        ".Lout_loop_%=:\n\t"
        "OUT_GRP 0, 0, 1, 1, 0, 8, 12\n\tOUT_GRP 1, 1, 2, 1, 0, 8, 12\n\tOUT_GRP 2, 2, 0, 1, 1, 8, 12\n\t"
        "s_mov_b32 s97, %[last]\n\t"
        "s_cmpk_lg_u32 s96, 0x8000\n\ts_cbranch_scc1 .Lout_loop_%=\n\t"
        "OUT_GRP 0, 0, 1, 0, 0, 8, 4\n\t"
        ".irp j,0,1,2,3\n\tOUT_MM 0, 0, \\j, 1\n\tOUT_RD 3, \\j, 1\n\t.endr\n\t.irp j,0,1,2,3\n\tOUT_MM 0, 1, \\j, 1\n\tOUT_RD 3, 4+\\j, 1\n\t.endr\n\t"
        "s_waitcnt lgkmcnt(8)\n\t.irp j,0,1,2,3\n\tOUT_MM 0, 2, \\j, 1\n\t.endr\n\t"
        "s_waitcnt lgkmcnt(0)\n\t.irp j,0,1,2,3\n\tOUT_MM 0, 3, \\j, 1\n\t.endr\n\t"
        "s_nop 1\n\t"
        "OUT_PACK 96, 0\n\tglobal_store_dwordx4 %[s0], v[104:107], %[xa] sc1\n\tOUT_REST 96\n\tglobal_store_dwordx4 %[s0], v[100:103], %[xb] sc1\n\t"
        "s_waitcnt vmcnt(2)\n\t.irp t,0,1,2,3\n\t.irp j,0,1,2,3\n\tOUT_MM 1, \\t, \\j, 1\n\t.endr\n\t.endr\n\t"
        "s_nop 1\n\t"
        "OUT_PACK 160, 16\n\tglobal_store_dwordx4 %[s1], v[168:171], %[xa] sc1\n\tOUT_REST 160\n\tglobal_store_dwordx4 %[s1], v[164:167], %[xb] sc1\n\t"
        "s_cmp_eq_u64 %[st], 0\n\ts_cbranch_scc1 .Lout_end_%=\n\t"
        "OUT_DIAG 96\n\tOUT_DIAG 160\n\t"
        "s_mov_b64 exec, %[md]\n\t"
        "global_store_dwordx2 %[so], v[112:113], %[st] sc1\n\tglobal_store_dwordx2 %[so], v[176:177], %[st] offset:128 sc1\n\t"
        "s_mov_b64 exec, -1\n"
        ".Lout_end_%=:\n\t"
        "s_nop 1\n\t"
        ".purgem OUT_MM\n\t.purgem OUT_RD\n\t.purgem OUT_LD\n\t.purgem OUT_CV\n\t.purgem OUT_DP\n\t.purgem OUT_CH\n\t.purgem OUT_GRP\n\t.purgem OUT_PACK\n\t.purgem OUT_REST\n\t.purgem OUT_DIAG"
        : "+{v[0:15]}"(acc[0]), "+{v[16:31]}"(acc[1]), "+{v[96:127]}"(aw0), "+{v[128:159]}"(aw1), "=&{v[160:191]}"(aw2),
          "+{v192}"(bw[0]), "+{v193}"(bw[1]), "+{v194}"(bw[2]), "+{v195}"(bw[3]), "=&{v[32:63]}"(fr), "=&{v[64:95]}"(fr1)
        : [a0] "v"(ao[0]), [a1] "v"(ao[1]), [ra] "s"(ra), [last] "s"(AFTER_LOCAL ? 49152 : 81920), [one] "v"(ones), [s0] "v"(so[0]), [s1] "v"(so[1]),
          [xa] "s"(Xout), [xb] "s"((char*)Xout + (AFTER_LOCAL ? 16 : 8) * rstr), [st] "s"(statp), [so] "v"(sto),
          [m0] "s"(0xaaaaaaaaaaaaaaaaull), [m1] "s"(0xccccccccccccccccull), [md] "s"(0xf0000f0000f0000full)
        : "memory", "scc", "s96", "s97");
    STAMP(AFTER_LOCAL ? 5 : 12);
}

#define ROWSUM(p0, p1, c) __builtin_amdgcn_smfmac_f32_16x16x64_bf16(sel, __builtin_bit_cast(bf16v_t, __builtin_shufflevector(p0, p1, 0, 1, 2, 3, 4, 5, 6, 7)), c, sidx, 0, 0)
constexpr int ATT_STAGE = 32768;
typedef __attribute__((ext_vector_type(8))) unsigned u32x8;

DI void attn_loader(char* smem, int w, char* ws, int set, u64 tag, const __hip_bfloat16* __restrict__ kvf, const void* Wo, const void* Xin,
                    const void* Wcs SP_PARAM) {
    const int lane = lane_id();
    asm volatile("" : "+s"(w));
    const Ids I = ids();
    const int panel = I.pq, ct = I.cq;
    const unsigned lds0 = (unsigned)(uintptr_t)smem;
    const char* kvh = (const char*)kvf + (size_t)(ct * 2 + (w >> 1)) * 262144 + (w & 1) * 4096;
    const unsigned voff = (unsigned)(lane * 16);
    auto order = [&](int st) { return ((panel & 8) + st - ((st - 1 < (panel & 7)) ? 1 : 0)) & 15; };
    auto load = [&](int st) {
        const char* src = kvh + (size_t)order(st) * 16384;
        const unsigned dst = lds0 + (w >> 1) * 16384 + (w & 1) * 4096 + (st & 3) * ATT_STAGE;
#pragma unroll
        for (int u = 0; u < 2; ++u)
            asm volatile("s_mov_b32 m0, %0\n\ts_nop 0\n\tglobal_load_lds_dwordx4 %1, %2 sc1\n\tglobal_load_lds_dwordx4 %1, %2 offset:1024 sc1\n\t"
                         "global_load_lds_dwordx4 %1, %2 offset:2048 sc1\n\tglobal_load_lds_dwordx4 %1, %2 offset:3072 sc1"
                         :: "s"(dst + u * 8192), "v"(voff), "s"(src + u * 8192) : "memory");
    };
    const unsigned wv = wo_off(w, lane), rv = res_off(false, true, lane), cv = colsum_off(w, lane);
    const unsigned poff = flag_off(set, ct, panel, lane & 15);
    u64 land;
    __builtin_amdgcn_s_setprio(3);
    BAR();
    signal_poll<false, true>(w, ws, flag_off(set, ct, lane & 15, panel), poff, 1ull << order(1), tag, land);
    STAMP(9);
    load(1);
    asm volatile("global_load_dwordx2 %0, %1, %2 sc1\n\ts_waitcnt vmcnt(0)" : "+v"(land) : "v"(poff), "s"(ws + WS_FLAG) : "memory");
    STAMP(10);
    BAR();
    if (!__all(land == tag)) flag_poll(ws, poff, tag);
    load(2); load(3);
    __builtin_amdgcn_s_setprio(0);
    STAMP(11);
    VM_BAR(8); load(4);
    VM_BAR(0); load(5); load(6);
    VM_BAR(0); load(7); load(8);
    u32x4 res[6], ca[8];
    colsum_load(ca, w < 3 ? Wcs : nullptr, cv);
#pragma unroll 1
    for (int st = 6; st < 12; st += 2) {
        VM_BAR(0);
        load(st + 3); load(st + 4);
        if (st == 8) wo_load(Wo, wv, 24, 32, lds0 + 32768, w);
    }
    VM_BAR(0); load(15); wo_load(Wo, wv, 0, 8, lds0, w);
    res_load(res, Xin, false, true, rv, w);
    VM_BAR(0); wo_load(Wo, wv, 8, 24, lds0, w);
    STAMP(12);
    out_handoff(smem, 98304, res, w, ws, set + 1, tag, lane, ca, Wcs ? 0 : -1);
}

DI void attn_compute(char* smem, int w, const u32x4 (&q4)[4], const __hip_bfloat16* __restrict__ kvf, __hip_bfloat16* __restrict__ ofrag SP_PARAM) {
    const int lane = lane_id();
    asm volatile("" : "+s"(w));
    const int wm = w & 1, wn = w >> 1;
    const Ids I = ids();
    const int panel = I.pq, head = I.cq * 2 + wn;
    const int l15 = lane & 15;
    const unsigned se = ((l15 & 2) == 0 && (l15 & 4) == 0) ? 0x3f803f80u : 0u, so = ((l15 & 2) == 0 && (l15 & 4) != 0) ? 0x3f803f80u : 0u;
    const bf8_t sel = __builtin_bit_cast(bf8_t, u32x4{se, se, so, so});
    const int sidx = (l15 & 1) ? (int)0xEEEEEEEEu : 0x44444444;
    const lds_cptr sm = (lds_cptr)smem + wn * 16384 + lane * 16;
    const f32x16 z16 = {0, 0, 0, 0, 0, 0, 0, 0, 0, 0, 0, 0, 0, 0, 0, 0};
    const bf16x8 qA0 = __builtin_bit_cast(bf16x8, q4[0]), qA1 = __builtin_bit_cast(bf16x8, q4[1]);
    const bf16x8 qB0 = __builtin_bit_cast(bf16x8, q4[2]), qB1 = __builtin_bit_cast(bf16x8, q4[3]);
    f32x16 oA, oB, nmA, nmB, sA, sB;
    f32x4 lsA, lsB;
    u32x8 pB;
    bf16x8 v11;
    asm volatile(".irp r,106,108,110,112,134,136,138,140,170,172\n\tv_mov_b64 v[\\r:\\r+1], 0\n\t.endr"
                 : "={v[106:113]}"(pB), "={v[134:137]}"(lsA), "={v[138:141]}"(lsB), "={v[170:173]}"(v11));
    WAIT_LGKM_BAR();
    STAMP(8);
    bf16x8 k00 = lds_b128(sm), k10 = lds_b128(sm + 1024), k01 = lds_b128(sm + 4096), k11 = lds_b128(sm + 4096 + 1024);
    bf16x8 v00 = lds_b128(sm + 2048), v10 = lds_b128(sm + 3072), v01 = lds_b128(sm + 4096 + 2048);
    float ob;
    {
        const u32x4 vu = __builtin_bit_cast(u32x4, v00);
        const unsigned vm = max(max(vu[0] & 0x7fff0000u, vu[1] & 0x7fff0000u), max(vu[2] & 0x7fff0000u, vu[3] & 0x7fff0000u));
        ob = __uint_as_float(wave_min(vm)) * 0.015625f;
        sA = MFMA32(k00, qA0, z16); sB = MFMA32(k00, qB0, z16);
        sA = MFMA32(k10, qA1, sA); sB = MFMA32(k10, qB1, sB);
        SBAR(); VM_BAR(0); SBAR();
        float mA = sA[0], mB = sB[0];
#pragma unroll
        for (int r = 1; r < 16; ++r) { mA = fmaxf(mA, sA[r]); mB = fmaxf(mB, sB[r]); }
        mA = max_h(mA) + 16.f; mB = max_h(mB) + 16.f;
#pragma unroll
        for (int r = 0; r < 16; ++r) { nmA[r] = -mA; nmB[r] = -mB; sA[r] -= mA; oA[r] = ob; oB[r] = ob; }
    }
    const unsigned a0 = (unsigned)(uintptr_t)sm;
    unsigned dst = (unsigned)((((panel * 8 + wm * 4 + ((lane & 31) >> 4)) * 32 + head) * 64 + (lane & 15) + (lane >> 5) * 16) * 16);
    asm volatile("" : "+v"(dst));
    asm volatile(
        ".macro ATT_RD dst, y\n\t"
        "ds_read_b128 v[\\dst:\\dst+3], v[175+(((\\y)&0x1ffff)>>16)] offset:(\\y)&0xffff\n\t"
        ".endm\n\t"
        ".macro ATT_E4 c\n\t"
        "v_exp_f32 v[\\c], v[\\c]\n\tv_exp_f32 v[\\c+1], v[\\c+1]\n\tv_exp_f32 v[\\c+2], v[\\c+2]\n\tv_exp_f32 v[\\c+3], v[\\c+3]\n\t"
        ".endm\n\t"
        ".macro ATT_C4 p, c\n\t"
        "v_cvt_pk_bf16_f32 v[\\p], v[\\c], v[\\c+1]\n\tv_cvt_pk_bf16_f32 v[\\p+1], v[\\c+2], v[\\c+3]\n\t"
        "v_cvt_pk_bf16_f32 v[\\p+2], v[\\c+4], v[\\c+5]\n\tv_cvt_pk_bf16_f32 v[\\p+3], v[\\c+6], v[\\c+7]\n\t"
        ".endm\n\t"
        ".macro ATT_STEP cur, nxt, ka, kb, q, nm, oc, od, lsd, pc, pd, vd1, vc0, r0, y0, r1, y1, bar, rd\n\t"
        "v_mfma_f32_32x32x16_bf16 v[\\nxt:\\nxt+15], v[\\ka:\\ka+3], v[\\q:\\q+3], v[\\nm:\\nm+15]\n\t"
        "ATT_E4 \\cur\n\t"
        "v_mfma_f32_32x32x16_bf16 v[\\nxt:\\nxt+15], v[\\kb:\\kb+3], v[\\q+4:\\q+7], v[\\nxt:\\nxt+15]\n\t"
        "ATT_E4 \\cur+4\n\t"
        "v_mfma_f32_32x32x16_bf16 v[\\od:\\od+15], v[\\vd1:\\vd1+3], v[\\pd+4:\\pd+7], v[\\od:\\od+15]\n\t"
        "s_waitcnt lgkmcnt(0)\n\t"
        "ATT_C4 \\pc, \\cur\n\t"
        "v_exp_f32 v[\\cur+8], v[\\cur+8]\n\tv_exp_f32 v[\\cur+9], v[\\cur+9]\n\t"
        "v_mfma_f32_32x32x16_bf16 v[\\oc:\\oc+15], v[\\vc0:\\vc0+3], v[\\pc:\\pc+3], v[\\oc:\\oc+15]\n\t"
        ".if \\bar\n\ts_barrier\n\t.endif\n\t"
        "ATT_E4 \\cur+10\n\t"
        "v_smfmac_f32_16x16x64_bf16 v[\\lsd:\\lsd+3], v[130:133], v[\\pd:\\pd+7], v174\n\t"
        ".if \\rd & 4\n\tATT_RD 142, 32768\n\tATT_RD 150, 33792\n\tATT_RD 158, 34816\n\t.endif\n\t"
        ".if \\rd & 8\n\tATT_RD \\r1-8, \\y1-1024\n\t.endif\n\t"
        ".if \\rd & 1\n\tATT_RD \\r0, \\y0\n\t.endif\n\t"
        ".if \\rd & 2\n\tATT_RD \\r1, \\y1\n\t.endif\n\t"
        "v_exp_f32 v[\\cur+14], v[\\cur+14]\n\tv_exp_f32 v[\\cur+15], v[\\cur+15]\n\t"
        "ATT_C4 \\pc+4, \\cur+8\n\t"
        ".if \\rd & 4\n\ts_waitcnt lgkmcnt(3)\n\t.endif\n\t"
        ".endm\n\t"
        ".macro ATT_TILE p, y1, y2, bar, ra=3, rb=3\n\t"
        "ATT_STEP 66, 82, 142+\\p, 150+\\p, 122, 50, 2, 18, 138, 98, 106, 170-\\p, 158+\\p, 170-\\p, \\y1+3072, 142+\\p, \\y2, \\bar&1, \\ra\n\t"
        "ATT_STEP 82, 66, 146-\\p, 154-\\p, 114, 34, 18, 2, 134, 106, 98, 166+\\p, 158+\\p, 158+\\p, \\y2+2048, 150+\\p, \\y2+1024, \\bar&2, \\rb\n\t"
        ".endm\n\t"
        ".macro ATT_STAGE b, bar\n\t"
        "ATT_TILE 0, \\b+4096, \\b+8192, 0\n\t"
        "ATT_TILE 4, \\b+8192, \\b+12288, 0\n\t"
        ".if \\bar\n\tATT_TILE 0, \\b+12288, \\b+32768, 2, 1, 11\n\t.else\n\tATT_TILE 0, \\b+12288, \\b+32768, 0\n\t.endif\n\t"
        "ATT_TILE 4, \\b+32768, \\b+36864, 0\n\t"
        ".endm\n\t"
        "v_add_u32 v176, 0x10000, v175\n\t"
        "s_mov_b32 s96, 4\n\t"
        "s_waitcnt lgkmcnt(0)\n\t"
        "ATT_TILE 0, 4096, 8192, 0\n\tATT_TILE 4, 8192, 12288, 0\n\tATT_TILE 0, 12288, 0, 0, 1, 0\n\tATT_TILE 4, 32768, 36864, 1, 7, 3\n\t"
        "ATT_STAGE 32768, 1\n\t"
        "s_branch .Latt_entry_%=\n\t"
        ".p2align 6\n"
        ".Latt_loop_%=:\n\t"
        "ATT_STAGE 0, 1\n\tATT_STAGE 32768, 0\n"
        ".Latt_entry_%=:\n\t"
        "ATT_STAGE 65536, 1\n\t"
        "s_sub_u32 s96, s96, 1\n\t"
        "ATT_STAGE 98304, 0\n\t"
        "s_cmp_lg_u32 s96, 0\n\t"
        "s_cbranch_scc1 .Latt_loop_%=\n\t"
        "s_nop 1\n\t"
        "v_mfma_f32_32x32x16_bf16 v[18:33], v[170:173], v[110:113], v[18:33]\n\t"
        "v_smfmac_f32_16x16x64_bf16 v[138:141], v[130:133], v[106:113], v174\n\t"
        "s_waitcnt lgkmcnt(0)\n\ts_nop 10\n\t"
        ".purgem ATT_RD\n\t.purgem ATT_E4\n\t.purgem ATT_C4\n\t.purgem ATT_STEP\n\t.purgem ATT_TILE\n\t.purgem ATT_STAGE"
        : "+{v[2:17]}"(oA), "+{v[18:33]}"(oB), "+{v[66:81]}"(sA), "+{v[106:113]}"(pB), "+{v[134:137]}"(lsA), "+{v[138:141]}"(lsB),
          "+{v[142:145]}"(k00), "+{v[146:149]}"(k01), "+{v[150:153]}"(k10), "+{v[154:157]}"(k11),
          "+{v[158:161]}"(v00), "+{v[162:165]}"(v01), "+{v[166:169]}"(v10), "+{v[170:173]}"(v11)
        : "{v[34:49]}"(nmA), "{v[50:65]}"(nmB), "{v[114:117]}"(qA0), "{v[118:121]}"(qA1), "{v[122:125]}"(qB0), "{v[126:129]}"(qB1),
          "{v[130:133]}"(sel), "{v174}"(sidx), "{v175}"(a0)
        : "memory", "scc", "s96", "v176", "v82", "v83", "v84", "v85", "v86", "v87", "v88", "v89", "v90", "v91", "v92", "v93", "v94", "v95",
          "v96", "v97", "v98", "v99", "v100", "v101", "v102", "v103", "v104", "v105");
    STAMP(9);
    float lA = lsA[0] + lsA[1], lB = lsB[0] + lsB[1];
    auto store = [&] {
#pragma unroll
    for (int u = 0; u < 2; ++u) {
        const float inv = __builtin_amdgcn_rcpf(u ? lB : lA), sub = -ob * inv;
        const f32x16 e = (u ? oB : oA) * inv + sub;
        st16o<0>((char*)ofrag + u * 65536, dst, u32x4{pack_bf16(e[0], e[1]), pack_bf16(e[2], e[3]), pack_bf16(e[8], e[9]), pack_bf16(e[10], e[11])});
        st16o<512>((char*)ofrag + u * 65536, dst, u32x4{pack_bf16(e[4], e[5]), pack_bf16(e[6], e[7]), pack_bf16(e[12], e[13]), pack_bf16(e[14], e[15])});
    }
    };
    store();
    {
        const f32x16 os = oA + oB;
        const float osum = (((os[0] + os[1]) + (os[2] + os[3])) + ((os[4] + os[5]) + (os[6] + os[7]))) +
                           (((os[8] + os[9]) + (os[10] + os[11])) + ((os[12] + os[13]) + (os[14] + os[15])));
        const bool bad = !(fmaxf(lA, lB) < 1.0e24f) || !(lA > 1.0e-24f) || !(lB > 1.0e-24f) || !(fabsf(osum) < 3.0e38f);
                if (__any(bad)) {
            const __amdgpu_buffer_rsrc_t rk = __builtin_amdgcn_make_buffer_rsrc((char*)kvf + (size_t)head * 262144, 0, 262144, 0x00020000);
            oA = z16; oB = z16; lA = 0.f; lB = 0.f; ob = 0.f;
            float mA = -INFINITY, mB = -INFINITY;
#pragma unroll 1
            for (int t = 0; t < 64; ++t) {
                bf16x8 f[4];
#pragma unroll
                for (int i = 0; i < 4; ++i) f[i] = __builtin_bit_cast(bf16x8, __builtin_amdgcn_raw_buffer_load_b128(rk, lane * 16 + i * 1024, t * 4096, 16));
                auto upd = [&](bf16x8 q0, bf16x8 q1, f32x16& o, float& l, float& m) {
                    f32x16 s = MFMA32(f[0], q0, z16); s = MFMA32(f[1], q1, s);
                    float tm = s[0];
#pragma unroll
                    for (int r = 1; r < 16; ++r) tm = fmaxf(tm, s[r]);
                    tm = max_h(tm);
                    const float mn = fmaxf(m, tm);
                    const float alpha = __builtin_amdgcn_exp2f(m - mn);
                    m = mn;
#pragma unroll
                    for (int r = 0; r < 16; ++r) { s[r] = __builtin_amdgcn_exp2f(s[r] - mn); o[r] *= alpha; }
                    const u32x4 p0 = {pack_bf16(s[0], s[1]), pack_bf16(s[2], s[3]), pack_bf16(s[4], s[5]), pack_bf16(s[6], s[7])};
                    const u32x4 p1 = {pack_bf16(s[8], s[9]), pack_bf16(s[10], s[11]), pack_bf16(s[12], s[13]), pack_bf16(s[14], s[15])};
                    f32x4 t4 = {0, 0, 0, 0};
                    o = MFMA32(f[2], __builtin_bit_cast(bf16x8, p0), o);
                    o = MFMA32(f[3], __builtin_bit_cast(bf16x8, p1), o);
                    t4 = ROWSUM(p0, p1, t4);
                    l = l * alpha + (t4[0] + t4[1]);
                };
                upd(qA0, qA1, oA, lA, mA);
                upd(qB0, qB1, oB, lB, mB);
            }
            store();
        }
    }
}

DI void ln_row(const u32x4 (&v)[2], __hip_bfloat16* __restrict__ Y, unsigned off) {
    float s1 = 0.f, s2 = 0.f;
#pragma unroll
    for (int e = 0; e < 8; ++e) s1 = dot2bf(v[e >> 2][e & 3], 0x3f803f80u, s1);
    const float mean = wave_sum(s1) * (1.f / 1024.f);
#pragma unroll
    for (int e = 0; e < 8; ++e) {
        const float d0 = bf_lo(v[e >> 2][e & 3]) - mean, d1 = bf_hi(v[e >> 2][e & 3]) - mean;
        s2 += d0 * d0 + d1 * d1;
    }
    const float mb = round_bf16(mean);
    const float rden = __builtin_amdgcn_rcpf(round_bf16(__builtin_amdgcn_sqrtf(round_bf16(round_bf16(wave_sum(s2) * (1.f / 1024.f)) + 1e-5f))));
#pragma unroll
    for (int hh = 0; hh < 2; ++hh) {
        u32x4 y;
#pragma unroll
        for (int e = 0; e < 4; ++e) {
            const unsigned d = pack_bf16(bf_lo(v[hh][e]) - mb, bf_hi(v[hh][e]) - mb);
            y[e] = pack_bf16(bf_lo(d) * rden, bf_hi(d) * rden);
        }
        st16((char*)Y + hh * 131072, off, y);
    }
}

extern "C" __global__ __launch_bounds__(512, 2) void mega_kernel(const __hip_bfloat16* x, const __hip_bfloat16* wqkvA,
                                                            const __hip_bfloat16* woA, const __hip_bfloat16* wqkvB,
                                                            const __hip_bfloat16* woB, __hip_bfloat16* out,
                                                            char* ws) {
    char* const smem = smem_base;
    typedef __hip_bfloat16 bf;
    const int w8 = __builtin_amdgcn_readfirstlane(threadIdx.x >> 6);
    if (w8 == 7 && (blockIdx.x >> 3) < 10) code_touch(blockIdx.x >> 3, threadIdx.x & 63);
    asm volatile("" :: "s"(x), "s"(wqkvA), "s"(woA), "s"(wqkvB), "s"(woB), "s"(out), "s"(ws));
    STAMP_DECL; STAMPR(14); STAMP(0);
    u64 tag;
    {
        const int lane = threadIdx.x & 63;
        const Ids I = ids();
        const unsigned off = (unsigned)((I.po * 128 + I.co * 8 + w8) * 2048 + lane * 16);
        u32x4 v[2];
        u32x2 ep;
        asm volatile("global_load_dwordx4 %0, %3, %4\n\tglobal_load_dwordx4 %1, %3, %4 offset:1024\n\tglobal_load_dwordx2 %2, %5, %6 sc1"
                     : "=&v"(v[0]), "=&v"(v[1]), "=&v"(ep) : "v"(off), "s"(x), "v"(0), "s"(ws + WS_EPOCH) : "memory");
        if (w8 == 7) {
            const int l = lane & 15, k = l % 5;
            const unsigned o = (l >= 10 || k == 3) ? flag_off(l, I.cq, I.pq, 0) : (k == 0 || k == 2) ? flag_off(l, I.pq, I.cq, 0) : flag_off(l, I.po, I.co, 0);
            touch_at(ws + WS_FLAG, o);
        } else if (w8 >= 4) touch(wqkvA, (w8 - 4) * 256 + blockIdx.x, lane);
        if (w8 >= 4) asm volatile("s_waitcnt vmcnt(2)" ::: "memory"); else asm volatile("s_waitcnt vmcnt(1)" ::: "memory");
        asm volatile("" : "+v"(v[0]), "+v"(v[1]));
        ln_row(v, (bf*)(ws + WS_Y0), (unsigned)(I.po * 262144 + (lane >> 3) * 16384 + (I.co * 8 + w8) * 128 + (lane & 7) * 16));
        if (w8 >= 4) asm volatile("s_waitcnt vmcnt(3)" ::: "memory"); else asm volatile("s_waitcnt vmcnt(2)" ::: "memory");
        asm volatile("" : "+v"(ep));
        tag = (((u64)(unsigned)__builtin_amdgcn_readfirstlane(ep[1]) << 32) | (unsigned)__builtin_amdgcn_readfirstlane(ep[0])) + 1;
    }
    if (w8 >= 4) {
        int w = w8 & 3;
#pragma unroll 1
        for (int it = 0; it < 2; ++it) {
            const int lane = lane_id();
            asm volatile("" : "+s"(x), "+s"(wqkvA), "+s"(woA), "+s"(wqkvB), "+s"(woB), "+s"(ws), "+s"(w));
            const unsigned lds0 = (unsigned)(uintptr_t)smem;
            u32x4 cb[8];
            const unsigned wv = wo_off(w, lane), rv = res_off(true, it, lane), cv = colsum_off(w, lane);
            qkv_loader<true>(smem, w, ws, it * 5, tag, (const bf*)(ws + (it ? WS_X2 : WS_Y0)), wqkvA, (const float*)(ws + WS_ST + 262144),
                             ws + WS_CSP, it == 0, woA, [&] {
                if (w < 3) {
                    colsum_load(cb, wqkvB, cv);
                    asm volatile("s_waitcnt vmcnt(8)" ::: "memory");
                } else {
                    touch(woA, blockIdx.x, lane);
                    asm volatile("s_waitcnt vmcnt(1)" ::: "memory");
                }
            }, [&](int part) {
                if (part == 0) wo_load(woA, wv, 29, 32, lds0, w); else wo_load(woA, wv, 25, 29, lds0, w);
            } SP_ARG);
            STAMP(3);
            u32x4 res[6];
            res_load(res, it ? (const void*)(ws + WS_X2) : x, true, it, rv, w);
            if (it) __builtin_amdgcn_s_sleep(8);
            wo_load(woA, wv, 0, 7, lds0, w);
            BAR();
            wo_load(woA, wv, 7, 25, lds0, w);
            STAMP(4);
            out_handoff(smem, 147456, res, w, ws, it * 5 + 1, tag, lane, cb, it ? -1 : 1);
            STAMP(5);
            qkv_loader<false>(smem, w, ws, it * 5 + 2, tag, (const bf*)(ws + (it ? WS_X3 : WS_X1)), wqkvB, (const float*)(ws + WS_ST + it * 524288), ws + WS_CSP + 196608,
                              false, nullptr, [] {}, [](int) {} SP_ARG);
            STAMP(8);
            attn_loader(smem, w, ws, it * 5 + 3, tag, (const bf*)(ws + WS_KVF + it * 8 * MBy), woB, ws + (it ? WS_X3 : WS_X1),
                        it ? nullptr : wqkvA SP_ARG);
            STAMP(13); STAMPR(15); STAMP_FLUSH(it);
        }
        if (blockIdx.x == 0 && w == 0 && lane_id() == 0) __hip_atomic_store((g_u64ptr)(ws + WS_EPOCH), tag, __ATOMIC_RELAXED, __HIP_MEMORY_SCOPE_AGENT);
    } else {
        __builtin_amdgcn_s_setprio(2);
#pragma unroll 1
        for (int it = 0; it < 2; ++it) {
            u32x4 q4[4];
            asm volatile("" : "+s"(ws), "+s"(wqkvA), "+s"(wqkvB));
            qkv_compute<true>(smem, w8, wqkvA, q4, nullptr, (bf*)(ws + WS_OF + it * 8 * MBy), it == 0,
                              ws + WS_ST + 262144, ws + WS_WN + blockIdx.x * 16 SP_ARG);
            STAMP(2);
            out_compute<true>(smem, w8, (const bf*)(ws + WS_OF + it * 8 * MBy),
                              (bf*)(ws + (it ? WS_X3 : WS_X1)), (float*)(ws + WS_ST + it * 524288) SP_ARG);
            STAMP(6);
            qkv_compute<false>(smem, w8, wqkvB, q4, (bf*)(ws + WS_KVF + it * 8 * MBy), nullptr, false,
                               ws + WS_ST + it * 524288, ws + WS_WN + 4096 + blockIdx.x * 16 SP_ARG);
            attn_compute(smem, w8, q4, (const bf*)(ws + WS_KVF + it * 8 * MBy), (bf*)(ws + WS_OF + (it * 8 + 4) * MBy) SP_ARG);
            out_compute<false>(smem, w8, (const bf*)(ws + WS_OF + (it * 8 + 4) * MBy),
                               it ? out : (bf*)(ws + WS_X2), it ? nullptr : (float*)(ws + WS_ST + 262144) SP_ARG);
            STAMP(13); STAMPR(15); STAMP_FLUSH(it);
        }
    }
}

extern "C" __global__ void zz_code_end_kernel() {}

extern "C" void kernel_launch(void* const* d_in, const int* in_sizes, int n_in, void* d_out, int out_size, void* d_ws,
                              size_t ws_size, hipStream_t stream) {
    typedef __hip_bfloat16 bf;
    (void)hipFuncSetAttribute((const void*)mega_kernel, hipFuncAttributeMaxDynamicSharedMemorySize, CH_LDS);
    mega_kernel<<<256, 512, CH_LDS, stream>>>((const bf*)d_in[0], (const bf*)d_in[1], (const bf*)d_in[2], (const bf*)d_in[4], (const bf*)d_in[5],
                                              (bf*)d_out, (char*)d_ws);
}
```

```cpp
#include <hip/hip_runtime.h>
#include <hip/hip_bf16.h>
#include <stdint.h>

typedef __attribute__((ext_vector_type(8))) short bf16x8;
typedef __attribute__((ext_vector_type(4))) short s16x4;
typedef __attribute__((ext_vector_type(4))) float f32x4;
typedef __attribute__((ext_vector_type(2))) float f32x2;
typedef __attribute__((ext_vector_type(16))) float f32x16;
typedef __attribute__((ext_vector_type(4))) unsigned u32x4;
typedef __attribute__((ext_vector_type(2))) unsigned u32x2;
typedef __attribute__((ext_vector_type(2))) __bf16 bf16x2_t;
typedef __attribute__((ext_vector_type(8))) __bf16 bf8_t;
typedef __attribute__((ext_vector_type(16))) __bf16 bf16v_t;
typedef __attribute__((address_space(3))) const char* lds_cptr;
typedef __attribute__((address_space(3))) char* lds_ptr;

#define DI __device__ __forceinline__
#define MFMA16(a, b, c) __builtin_amdgcn_mfma_f32_16x16x32_bf16((a), (b), (c), 0, 0, 0)
#define MFMA32(a, b, c) __builtin_amdgcn_mfma_f32_32x32x16_bf16((a), (b), (c), 0, 0, 0)
#define SBAR() __builtin_amdgcn_sched_barrier(0)
#define STAMP(i)
#define STAMPR(i)
#define STAMP_DECL
#define STAMP_FLUSH(it)
#define SP_PARAM
#define SP_ARG
typedef unsigned long long u64;
typedef __attribute__((ext_vector_type(32))) unsigned u32x32;

constexpr float QSCALE = 0.17677669529663687f * 1.4426950408889634f;
constexpr float ATT_SCALE = 0.17677669529663687f;
constexpr float LOG2E = 1.4426950408889634f;
constexpr float NEG_MASK = -1e9f / ATT_SCALE;

DI int lane_id() { int l; asm volatile("v_mbcnt_lo_u32_b32 %0, -1, 0\n\tv_mbcnt_hi_u32_b32 %0, -1, %0" : "=v"(l)); return l; }
DI unsigned pack_bf16(float lo, float hi) {
    f32x2 v = {lo, hi};
    bf16x2_t r = __builtin_convertvector(v, bf16x2_t);
    return __builtin_bit_cast(unsigned, r);
}
DI float bf_lo(unsigned u) { return __uint_as_float(u << 16); }
DI float bf_hi(unsigned u) { return __uint_as_float(u & 0xffff0000u); }
DI float round_bf16(float x) { return bf_lo(pack_bf16(x, 0.f)); }
DI void ln_stats(float sum, float sumsq, float& mean_out, float& rstd_out) {
    float mean;
    asm("v_mul_f32 %0, 0x3a800000, %1" : "=v"(mean) : "v"(sum));
    const float var = fmaxf(__builtin_fmaf(-mean, mean, sumsq * (1.f / 1024.f)), 0.f);
    mean_out = round_bf16(mean);
    rstd_out = __builtin_amdgcn_rcpf(round_bf16(__builtin_amdgcn_sqrtf(round_bf16(round_bf16(var) + 1e-5f))));
}

DI void glds16(const void* sbase, unsigned voff, unsigned ldsaddr) {
    asm volatile("s_mov_b32 m0, %0\n\ts_nop 0\n\tglobal_load_lds_dwordx4 %1, %2"
                 :: "s"(ldsaddr), "v"(voff), "s"(sbase) : "memory");
}
DI void glds16c(const void* sbase, unsigned voff, unsigned ldsaddr) {
    asm volatile("s_mov_b32 m0, %0\n\ts_nop 0\n\tglobal_load_lds_dwordx4 %1, %2 sc1"
                 :: "s"(ldsaddr), "v"(voff), "s"(sbase) : "memory");
}
DI void st16(void* sbase, unsigned voff, u32x4 v) {
    asm volatile("global_store_dwordx4 %0, %1, %2 sc1\n\ts_nop 1" :: "v"(voff), "v"(v), "s"(sbase) : "memory");
}
template <int OFF> DI void st16o(void* sbase, unsigned voff, u32x4 v) {
    asm volatile("global_store_dwordx4 %0, %1, %2 offset:%3 sc1\n\ts_nop 1" :: "v"(voff), "v"(v), "s"(sbase), "n"(OFF) : "memory");
}
DI bf16x8 lds_b128(lds_cptr p) { return *(const __attribute__((address_space(3))) bf16x8*)p; }
DI s16x4 lds_tr(lds_cptr p) {
    return __builtin_amdgcn_ds_read_tr16_b64_v4i16((__attribute__((address_space(3))) s16x4*)p);
}
DI bf16x8 cat8(s16x4 a, s16x4 b) { return __builtin_shufflevector(a, b, 0, 1, 2, 3, 4, 5, 6, 7); }
DI float dot2bf(unsigned a, unsigned b, float c) {
    return __builtin_amdgcn_fdot2_f32_bf16(__builtin_bit_cast(bf16x2_t, a), __builtin_bit_cast(bf16x2_t, b), c, false);
}
DI float sum_g(float v) {
    auto a = __builtin_amdgcn_permlane16_swap(__float_as_uint(v), __float_as_uint(v), false, false);
    v = __uint_as_float(a[0]) + __uint_as_float(a[1]);
    auto b = __builtin_amdgcn_permlane32_swap(__float_as_uint(v), __float_as_uint(v), false, false);
    return __uint_as_float(b[0]) + __uint_as_float(b[1]);
}
DI float wave_sum(float v) {
    v += __builtin_bit_cast(float, __builtin_amdgcn_update_dpp(0, __builtin_bit_cast(int, v), 0xB1, 0xF, 0xF, true));
    v += __builtin_bit_cast(float, __builtin_amdgcn_update_dpp(0, __builtin_bit_cast(int, v), 0x4E, 0xF, 0xF, true));
    v += __builtin_bit_cast(float, __builtin_amdgcn_update_dpp(0, __builtin_bit_cast(int, v), 0x141, 0xF, 0xF, true));
    v += __builtin_bit_cast(float, __builtin_amdgcn_update_dpp(0, __builtin_bit_cast(int, v), 0x140, 0xF, 0xF, true));
    return sum_g(v);
}
DI unsigned wave_min(unsigned v) {
    v = min(v, (unsigned)__builtin_amdgcn_update_dpp(0, (int)v, 0xB1, 0xF, 0xF, true));
    v = min(v, (unsigned)__builtin_amdgcn_update_dpp(0, (int)v, 0x4E, 0xF, 0xF, true));
    v = min(v, (unsigned)__builtin_amdgcn_update_dpp(0, (int)v, 0x141, 0xF, 0xF, true));
    v = min(v, (unsigned)__builtin_amdgcn_update_dpp(0, (int)v, 0x140, 0xF, 0xF, true));
    const auto a = __builtin_amdgcn_permlane16_swap(v, v, false, false);
    v = min(a[0], a[1]);
    const auto b = __builtin_amdgcn_permlane32_swap(v, v, false, false);
    return min(b[0], b[1]);
}
DI float amax3(float a, float b, float c) { float r; asm("v_max3_f32 %0, %1, %2, %3" : "=v"(r) : "v"(a), "v"(b), "v"(c)); return r; }
DI float max2f(float a, float b) { return __builtin_amdgcn_fmed3f(a, b, INFINITY); }
DI float max_h(float v) {
    auto b = __builtin_amdgcn_permlane32_swap(__float_as_uint(v), __float_as_uint(v), false, false);
    return max2f(__uint_as_float(b[0]), __uint_as_float(b[1]));
}

constexpr unsigned OFF_DUMP = 145408;
extern __shared__ __attribute__((aligned(16))) char smem_base[];
DI void touch_at(const void* p, unsigned off) {
    asm volatile("s_mov_b32 m0, %0\n\ts_nop 0\n\tglobal_load_lds_dword %1, %2"
                 :: "s"((unsigned)(uintptr_t)smem_base + OFF_DUMP), "v"(off), "s"(p) : "memory");
}
DI void touch(const void* p, int slice, int lane) { touch_at(p, (unsigned)(slice * 64 + lane) * 128u); }
DI void code_touch(int slice, int lane) {
    unsigned long long c0, c1;
    asm volatile("s_getpc_b64 s[96:97]\n\ts_add_u32 s96, s96, mega_kernel@rel32@lo+4\n\ts_addc_u32 s97, s97, mega_kernel@rel32@hi+12\n\ts_mov_b64 %0, s[96:97]\n\t"
                 "s_getpc_b64 s[96:97]\n\ts_add_u32 s96, s96, zz_code_end_kernel@rel32@lo+4\n\ts_addc_u32 s97, s97, zz_code_end_kernel@rel32@hi+12\n\ts_mov_b64 %1, s[96:97]"
                 : "=s"(c0), "=s"(c1) :: "s96", "s97", "scc");
    touch_at((const void*)c0, min((unsigned)(slice * 64 + lane) * 128u, (unsigned)(c1 - c0)));
}

#define BAR() asm volatile("s_barrier" ::: "memory")
#define WAIT_LGKM() do { SBAR(); __builtin_amdgcn_s_waitcnt(0xC07F); SBAR(); } while (0)
#define WAIT_LGKM_BAR() do { SBAR(); __builtin_amdgcn_s_waitcnt(0xC07F); asm volatile("s_barrier" ::: "memory"); SBAR(); } while (0)
#define VM_BAR(n) asm volatile("s_waitcnt vmcnt(" #n ")\n\ts_barrier" ::: "memory")
constexpr int CH_LDS = 163840;
typedef __attribute__((address_space(3))) unsigned* lds_uptr;
typedef __attribute__((address_space(1))) u64* g_u64ptr;

constexpr size_t MBy = 1 << 20;
constexpr size_t WS_X3 = 0, WS_X2 = 4 * MBy, WS_X1 = 8 * MBy, WS_OF = 12 * MBy  , WS_KVF = 36 * MBy  ,
                 WS_ST = 52 * MBy  , WS_CSP = 54 * MBy  , WS_FLAG = 55 * MBy  ,
                 WS_WN = WS_CSP + 458752  , WS_EPOCH = WS_CSP + 524288, WS_Y0 = 56 * MBy;
DI unsigned flag_off(int set, int group, int consumer, int producer) { return (unsigned)(set * 65536 + group * 4096 + consumer * 256 + producer * 8); }
DI void flag_store(char* ws, int set, int group, int producer, u64 tag, int lane) {
    asm volatile("" : "+s"(tag));
    if (lane < 16) __hip_atomic_store((g_u64ptr)(ws + WS_FLAG + flag_off(set, group, lane, producer)), tag, __ATOMIC_RELAXED, __HIP_MEMORY_SCOPE_AGENT);
}
DI void flag_poll(const char* ws, unsigned off, u64 tag) {
    asm volatile("" : "+s"(tag));
#pragma unroll 1
    for (int spin = 0; spin < (1 << 16); ++spin) {
        const u64 v = __hip_atomic_load((g_u64ptr)(ws + WS_FLAG + off), __ATOMIC_RELAXED, __HIP_MEMORY_SCOPE_AGENT);
        if (__all(v == tag)) break;
        __builtin_amdgcn_s_sleep(1);
    }
    asm volatile("" ::: "memory");
}
template <bool DRAIN, bool ALLPOLL>
DI void signal_poll(int follower, const char* ws, unsigned soff, unsigned poff, u64 pmask, u64 tag, u64& land) {
    u64 tagv, zero;
    asm volatile("s_not_b64 s[96:97], %[ts]\n\tv_mov_b64 %[tag], %[ts]\n\tv_mov_b64 %[zero], 0\n\tv_mov_b64 %[land], s[96:97]\n\t"
                 ".if %[dr]\n\ts_waitcnt vmcnt(0)\n\t.endif\n\ts_barrier\n\t"
                 "s_cmp_lg_u32 %[fl], 0\n\ts_cbranch_scc1 .Lsig_fol_%=\n\t"
                 "s_mov_b64 exec, 0xffff\n\t"
                 "global_store_dwordx2 %[so], %[tag], %[base] sc1\n"
                 ".if %[ap]\n.Lsig_fol_%=:\n.endif\n\t"
                 "s_mov_b64 exec, %[pm]\n\t"
                 "s_movk_i32 s96, 0x7fff\n"
                 ".Lsig_loop_%=:\n\t"
                 "global_atomic_or_x2 %[land], %[po], %[zero], %[base] sc0 sc1\n\t"
                 "s_sleep 1\n\t"
                 "s_waitcnt vmcnt(3)\n\t"
                 "v_cmp_ne_u64 vcc, %[land], %[tag]\n\t"
                 "s_sub_u32 s96, s96, 1\n\t"
                 "s_cbranch_scc1 .Lsig_out_%=\n\t"
                 "s_cbranch_vccnz .Lsig_loop_%=\n"
                 ".Lsig_out_%=:\n\t"
                 "s_mov_b64 exec, -1\n"
                 ".if !%[ap]\n.Lsig_fol_%=:\n.endif"
                 : [land] "=&v"(land), [tag] "=&v"(tagv), [zero] "=&v"(zero)
                 : [fl] "s"(follower), [so] "v"(soff), [po] "v"(poff), [ts] "s"(tag), [base] "s"(ws + WS_FLAG), [pm] "s"(pmask), [ap] "n"(ALLPOLL ? 1 : 0), [dr] "n"(DRAIN ? 1 : 0)
                 : "memory", "scc", "vcc", "s96", "s97");
}
struct Ids { int pq, cq, po, co; };
DI Ids ids() {
    int bid = blockIdx.x; asm volatile("" : "+s"(bid));
    const int xcd = bid & 7, idx = bid >> 3;
    int pq, cq, po, co;
#define IDS_BODY pq = (xcd & 1) * 8 + (idx & 7); cq = (xcd >> 1) * 4 + (idx >> 3); po = (xcd & 3) * 4 + (idx & 3); co = (xcd >> 2) * 8 + (idx >> 2);
    IDS_BODY
    return Ids{pq, cq, po, co};
}

DI void slow_warm(unsigned dump, const void* W, int w, int lane) {
    int bid = blockIdx.x; asm volatile("" : "+s"(bid));
    const int xcd = bid & 7, idx = bid >> 3;
    const int sc = (3 - (int)((uintptr_t)W >> 7)) & 7;
    if ((sc >> 2) != ((xcd >> 1) & 1)) return;
    const char* src = (const char*)W + (idx + 32 * w) * 49152 + (sc + (xcd >> 2) * 8) * 128;
#pragma unroll
    for (int G = 0; G < 3; ++G) glds16(src + G * 2048, (unsigned)((lane >> 3) * 6144 + (lane & 7) * 16), dump);
}

template <int V> struct IC { static constexpr int value = V; };
template <bool LOCAL> struct QC {
    static constexpr int AROWS = LOCAL ? 144 : 128;
    static constexpr int ABYTES = AROWS * 128;
    static constexpr int STAGE = ABYTES + 24576;
    static constexpr int OFF_MEAN = 143360;
    static constexpr int OFF_RSTD = OFF_MEAN + 576;
    static constexpr int OFF_CS = OFF_RSTD + 576;
};
constexpr int OFF_EX = 131072;

DI unsigned colsum_off(int G, int lane) {
    const Ids I = ids();
    unsigned off = (unsigned)(I.pq * 64 + (lane >> 3)) * 6144u + (unsigned)(G * 2048 + I.cq * 128 + (lane & 7) * 16);
    asm volatile("" : "+v"(off));
    return off;
}
DI void colsum_load(u32x4 (&c)[8], const void* W, unsigned off) {
#define CS_NEXT "s_add_u32 s96, s96, 0xc000\n\ts_addc_u32 s97, s97, 0\n\t"
    asm volatile("s_cmp_eq_u64 %9, 0\n\ts_cbranch_scc1 .Lcs_skip_%=\n\ts_mov_b64 s[96:97], %9\n\t"
                 "global_load_dwordx4 %0, %8, s[96:97]\n\t" CS_NEXT "global_load_dwordx4 %1, %8, s[96:97]\n\t" CS_NEXT
                 "global_load_dwordx4 %2, %8, s[96:97]\n\t" CS_NEXT "global_load_dwordx4 %3, %8, s[96:97]\n\t" CS_NEXT
                 "global_load_dwordx4 %4, %8, s[96:97]\n\t" CS_NEXT "global_load_dwordx4 %5, %8, s[96:97]\n\t" CS_NEXT
                 "global_load_dwordx4 %6, %8, s[96:97]\n\t" CS_NEXT "global_load_dwordx4 %7, %8, s[96:97]\n"
                 ".Lcs_skip_%=:"
                 : "=&v"(c[0]), "=&v"(c[1]), "=&v"(c[2]), "=&v"(c[3]), "=&v"(c[4]), "=&v"(c[5]), "=&v"(c[6]), "=&v"(c[7])
                 : "v"(off), "s"(W) : "memory", "scc", "s96", "s97");
}
DI void colsum_store(u32x4 (&c)[8], char* ws, int mat, int G, int lane) {
    asm volatile("" : "+v"(c[0]), "+v"(c[1]), "+v"(c[2]), "+v"(c[3]), "+v"(c[4]), "+v"(c[5]), "+v"(c[6]), "+v"(c[7]));
    const Ids I = ids();
    const int m = lane & 15;
    const unsigned one = (m & 1) ? 0xbf800000u : 0xbf80u;
    const u32x4 sl = {(m >> 1) == 0 ? one : 0u, (m >> 1) == 1 ? one : 0u, (m >> 1) == 2 ? one : 0u, (m >> 1) == 3 ? one : 0u};
    f32x4 d = {0, 0, 0, 0};
#pragma unroll
    for (int i = 0; i < 8; ++i) d = MFMA16(__builtin_bit_cast(bf16x8, sl), __builtin_bit_cast(bf16x8, c[i]), d);
#pragma unroll
    for (int r = 0; r < 4; ++r)
        d[r] += __int_as_float(__builtin_amdgcn_update_dpp(0, __float_as_int(d[r]), 0x128, 0xF, 0xF, true));
    if (lane < 32 && (lane & 8) == 0)
        st16(ws + WS_CSP + mat * 196608, (unsigned)((I.cq * 16 + I.pq) * 768 + G * 256 + (lane & 7) * 32 + (lane >> 4) * 16), __builtin_bit_cast(u32x4, d));
    typedef __attribute__((ext_vector_type(2))) unsigned short u16x2;
    u16x2 mn = {0x7fff, 0x7fff};
#pragma unroll
    for (int e = 0; e < 4; ++e) {
        u16x2 mx = {0, 0};
#pragma unroll
        for (int i = 0; i < 8; ++i) { const unsigned u = c[i][e] & 0x7fff7fffu; mx = __builtin_elementwise_max(mx, __builtin_bit_cast(u16x2, u)); }
        mn = __builtin_elementwise_min(mn, mx);
    }
    const unsigned wn = wave_min((unsigned)min(mn[0], mn[1])) << 16;
    if (lane == 0) __hip_atomic_store((__attribute__((address_space(1))) unsigned*)(ws + WS_WN + mat * 4096 + blockIdx.x * 16 + G * 4), wn,
                                      __ATOMIC_RELAXED, __HIP_MEMORY_SCOPE_AGENT);

}

template <bool LOCAL>
struct WFirst {
    unsigned v[2];
    DI WFirst(int w, int ct, int lane) {
#pragma unroll
        for (int i = 0; i < 2; ++i) {
            const int k = 8 * i + (lane >> 3), pos = lane & 7;
            v[i] = (unsigned)k * 6144u + (unsigned)(w * 1024 + ct * 64 + ((pos >> 1) ^ (((k >> 1) & 1) | (i << 1))) * 16 + (pos & 1) * 8) * 2u;
        }
        asm volatile("" : "+v"(v[0]), "+v"(v[1]));
    }
    DI void issue(const void* W, unsigned lds0, int w, int i0, int i1) const {
#pragma unroll
        for (int i = i0; i < i1; ++i)
            glds16((const char*)W + (size_t)(i >> 1) * (16 * 6144), v[i & 1], lds0 + (i >> 3) * QC<LOCAL>::STAGE + QC<LOCAL>::ABYTES + (w * 8 + (i & 7)) * 1024);
    }
};

template <bool LOCAL, class F, class F2>
DI void qkv_loader(char* smem, int w, char* ws, int set, u64 tag, const __hip_bfloat16* __restrict__ X, const __hip_bfloat16* __restrict__ W,
                   const float* __restrict__ statp, const char* __restrict__ csp, bool ident, const void* nextW, F tail, F2 mid SP_PARAM) {
    typedef QC<LOCAL> C;
    constexpr int NT = 16;
    const int lane = lane_id();
    asm volatile("" : "+s"(w));
    const int idx = blockIdx.x >> 3;
    const Ids I = ids();
    const int panel = I.pq, ct = I.cq;
    const int R0 = panel * 128;
    const unsigned lds0 = (unsigned)(uintptr_t)smem;
    auto grow = [&](int i) -> int {
        if (!LOCAL) return R0 + i;
        if (i < 128) { const int t = (i >> 4) & 3, a = i & 15; return R0 + (i & 64) + 2 * (16 * (t & 1) + a) + (t >> 1); }
        const int a = i & 15; const int r = R0 - 16 + 2 * (a & 7) + (a >> 3);
        return r < 0 ? 0 : r;
    };
    unsigned voffA[5], voffB[6];
#pragma unroll
    for (int i = 0; i < 6; ++i) {
        const int j = w + 4 * i, G = j >> 3, k = 8 * (j & 7) + (lane >> 3);
        const int pos = lane & 7, f = ((k >> 1) & 1) | (((k >> 3) & 1) << 1);
        const int seg = (pos >> 1) ^ f;
        voffB[i] = (unsigned)k * 6144u + (unsigned)(G * 1024 + ct * 64 + seg * 16 + (pos & 1) * 8) * 2u;
    }
    auto issueW = [&](int t, int buf) {
        const char* wb = (const char*)W + (size_t)t * (64 * 6144);
        const unsigned sb = lds0 + buf * C::STAGE;
#pragma unroll
        for (int i = 0; i < 6; ++i) glds16(wb, voffB[i], sb + C::ABYTES + (w + 4 * i) * 1024);
    };
    asm volatile("" : "+v"(voffB[0]), "+v"(voffB[1]), "+v"(voffB[2]), "+v"(voffB[3]), "+v"(voffB[4]), "+v"(voffB[5]));
    const WFirst<LOCAL> first(w, ct, lane);
    const int g = lane >> 4, r = lane & 15, pos = 32 * w + 2 * r + (g >> 1);
    const int lr = !LOCAL ? (pos & 64) + 2 * (pos & 31) + ((pos >> 5) & 1)
                          : (w == 0 && (g & 1)) ? 128 + (r & 7) + 8 * (g >> 1) : (pos & 64) + ((pos & 1) * 2 + ((pos >> 5) & 1)) * 16 + ((pos >> 1) & 15);
    unsigned so = (unsigned)(panel * 16384 + g * 1024 + w * 256 + r * 16), ho = (unsigned)(g * 1024 + (panel ? panel * 16384 - 15488 + (r & 7) * 16 : 0));
    unsigned co = (unsigned)((ct * 16 + g) * 768 + w * 192 + r * 12);
    lds_ptr mp = (lds_ptr)smem + C::OFF_MEAN + lr * 4, cp = (lds_ptr)smem + C::OFF_CS + w * 192 + r * 12 + ((g & 1) ? 8 : g * 2);
    unsigned fo = flag_off((LOCAL ? 13 : 10) + min(g, 2), ct, panel, r);
    asm volatile("" : "+v"(so), "+v"(ho), "+v"(co), "+v"(mp), "+v"(cp), "+v"(fo));
#pragma unroll
    for (int i = 0; i < 5; ++i) {
        const int j = (i < 4) ? (w + 4 * i) : (16 + (w & 1));
        const int row = 8 * j + (lane >> 3);
        const int c = (lane & 7) ^ ((row >> 1) & 7);
        const int gr = grow(row < C::AROWS ? row : 0);
        voffA[i] = (unsigned)((gr >> 7) * 262144 + (gr & 127) * 128 + c * 16);
        asm volatile("" : "+v"(voffA[i]));
    }
    auto issueA = [&](int t, int buf) {
        const char* xa = (const char*)X + t * 16384;
        const unsigned sb = lds0 + buf * C::STAGE;
        if (LOCAL && w < 2) glds16c(xa, voffA[4], sb + (16 + w) * 1024);
#pragma unroll
        for (int i = 0; i < 4; ++i) glds16c(xa, voffA[i], sb + (w + 4 * i) * 1024);
    };
    u64 land;
    if (w == 3) __builtin_amdgcn_s_setprio(3);
    signal_poll<LOCAL, false>(w - 3, ws, flag_off(set, I.po, lane & 15, I.co),
                              flag_off(set, (lane & 16) && panel ? panel - 1 : panel, ct, lane & 15),
                              !LOCAL ? 0xffffull : ident ? 0xc000ffffull : 0xffffffffull, tag, land);
    if (w < 3) first.issue(W, lds0, w, 0, 12);
    BAR();
    STAMP(LOCAL ? 1 : 6);
    issueA(0, 0);
    issueA(1, 1);
    if (w < 3) { first.issue(W, lds0, w, 12, 16); asm volatile("s_waitcnt vmcnt(8)" ::: "memory"); }
    else asm volatile("s_waitcnt vmcnt(4)" ::: "memory");
    BAR();
    asm volatile("" :: "v"(land));
    __builtin_amdgcn_s_setprio(0);
    STAMP(LOCAL ? 2 : 7);
    issueW(2, 2);
    issueA(2, 2);
    VM_BAR(10);
    asm volatile("global_load_dwordx2 %0, %1, %2 sc1" : "=&v"(land) : "v"(fo), "s"(ws + WS_FLAG) : "memory");
    issueA(3, 0); issueW(3, 0);
    int buf = 1;
#pragma unroll 1
    for (int t = 1; t < NT - 3; ++t) {
        VM_BAR(10);
        issueA(t + 3, buf); issueW(t + 3, buf);
        if (t == 10 && idx < 3) code_touch(idx * 4 + w, lane);
        if (LOCAL && t == 11)
            glds16(nextW, (unsigned)((idx + 32 * w) * 8 + (lane >> 3)) * 2048u +
                   (unsigned)(((I.co & 8) + ((3 - (int)((uintptr_t)nextW >> 7)) & 7)) * 128 + (lane & 7) * 16), lds0 + OFF_DUMP);
        buf = (buf == 2) ? 0 : buf + 1;
    }
    typedef __attribute__((ext_vector_type(3))) float f32x3;
    const bool halo = LOCAL && w == 0;
    asm volatile("" : "+v"(land));
    if (!ident) {
        if (!__all(land == tag)) flag_poll(ws, fo, tag);
        f32x3 cs[4];
        f32x4 sv[4], hv[4];
        typedef __attribute__((address_space(3))) float* lds_f;
        auto add = [](float a, float b) { float r; asm("v_add_f32 %0, %1, %2" : "=v"(r) : "v"(a), "v"(b)); return r; };
        auto fold32 = [](float a, float b) { asm("s_nop 1\n\tv_permlane32_swap_b32 %0, %1\n\tv_add_f32 %0, %0, %1" : "+v"(a), "+v"(b)); return a; };
        auto fold16 = [](float a, float b) { asm("s_nop 1\n\tv_permlane16_swap_b32 %0, %1\n\tv_add_f32 %0, %0, %1" : "+v"(a), "+v"(b)); return a; };
        auto sum4 = [&](auto (&v)[4], int e) { return add(add(v[0][e], v[1][e]), add(v[2][e], v[3][e])); };
#pragma unroll
        for (int i = 0; i < 4; ++i) asm volatile("global_load_dwordx3 %0, %1, %2 sc1" : "=&v"(cs[i]) : "v"(co), "s"(csp + i * 3072) : "memory");
        VM_BAR(14);
#pragma unroll
        for (int i = 0; i < 4; ++i) asm volatile("global_load_dwordx4 %0, %1, %2 sc1" : "=&v"(sv[i]) : "v"(so), "s"((const char*)statp + i * 4096) : "memory");
        if (LOCAL)
            asm volatile("s_cmp_lg_u32 %9, 0\n\ts_cbranch_scc1 .Lhv_skip_%=\n\t"
                         "global_load_dwordx4 %0, %4, %5 sc1\n\tglobal_load_dwordx4 %1, %4, %6 sc1\n\t"
                         "global_load_dwordx4 %2, %4, %7 sc1\n\tglobal_load_dwordx4 %3, %4, %8 sc1\n"
                         ".Lhv_skip_%=:" : "=&v"(hv[0]), "=&v"(hv[1]), "=&v"(hv[2]), "=&v"(hv[3])
                         : "v"(ho), "s"(statp), "s"(statp + 1024), "s"(statp + 2048), "s"(statp + 3072), "s"(w) : "memory", "scc");
        if (halo) asm volatile("s_waitcnt vmcnt(8)" ::: "memory"); else asm volatile("s_waitcnt vmcnt(4)" ::: "memory");
        asm volatile("" : "+v"(cs[0]), "+v"(cs[1]), "+v"(cs[2]), "+v"(cs[3]));
        const float c2 = sum4(cs, 2);
        *(lds_f)cp = fold16(fold32(sum4(cs, 0), sum4(cs, 1)), fold32(c2, c2));
        BAR();
        asm volatile("s_waitcnt vmcnt(0)" : "+v"(sv[0]), "+v"(sv[1]), "+v"(sv[2]), "+v"(sv[3]) :: "memory");
        mid(0);
        const float s1 = fold32(sum4(sv, 0), sum4(sv, 2)), s2 = fold32(sum4(sv, 1), sum4(sv, 3));
        float h1, h2;
        asm("v_mov_b32 %0, %2\n\tv_mov_b32 %1, %3" : "=&v"(h1), "=&v"(h2) : "v"(s1), "v"(s2));
        if (halo) {
            asm volatile("" : "+v"(hv[0]), "+v"(hv[1]), "+v"(hv[2]), "+v"(hv[3]));
            const float u0 = sum4(hv, 0), u1 = sum4(hv, 1), u2 = sum4(hv, 2), u3 = sum4(hv, 3);
            h1 = fold32(u0, panel ? u2 : u0); h2 = fold32(u1, panel ? u3 : u1);
        }
        float mean, rstd;
        ln_stats(fold16(s1, h1), fold16(s2, h2), mean, rstd);
        *(lds_f)mp = mean; *(lds_f)(mp + (C::OFF_RSTD - C::OFF_MEAN)) = rstd;
    } else { VM_BAR(10); tail(); BAR(); mid(0); }
    mid(1);
    WAIT_LGKM_BAR();
}

template <bool LOCAL>
DI void qkv_compute(char* smem, int w, const void* W, u32x4 (&qo4)[4], __hip_bfloat16* __restrict__ kvf, __hip_bfloat16* __restrict__ ofrag, bool ident,
                    const char* statp, const char* wnp SP_PARAM) {
    typedef QC<LOCAL> C;
    constexpr int NT = 16, ABYTES = C::ABYTES, STAGE = C::STAGE;
    const int lane = lane_id();
    asm volatile("" : "+s"(w));
    const int wm = w & 1, wn = w >> 1;
    const Ids I = ids();
    const int panel = I.pq, ct = I.cq;
    const int l15 = lane & 15, g = lane >> 4;
    const lds_cptr sm = (lds_cptr)smem;
    lds_ptr smw = (lds_ptr)smem;

    f32x4 acc[3][4][2];
    f32x4 acch[2];
    float cb;
    f32x4 bias;

    bf16x8 A[2][5][2];
    s16x4 B[3][2][2];
    lds_cptr pa[2], pb[2];
    auto rdA = [&](auto WM, int p, int i) { A[p][i >> 1][i & 1] = lds_b128(pa[i & 1] + ((i >> 1) < 4 ? (i >> 1) * 2048 : 16384 - WM.value * 8192)); };
    auto rdB = [&](int c, int i) { B[c % 3][i >> 1][i & 1] = lds_tr(pb[c & 1] + ((c >> 1) + 1) % 3 * 8192 + (i >> 1) * 4096 + (i & 1) * 512); };
    auto column = [&](auto WM, int p, int c, bool first, auto gap) {
        const int G = ((c >> 1) + 1) % 3, f = c & 1;
        const bool halo = LOCAL && G == 1 + WM.value;
#pragma unroll
        for (int n = 0; n < (halo ? 10 : 8); ++n) {
            const bf16x8 x = A[p][n >> 1][n & 1], y = cat8(B[c % 3][n & 1][0], B[c % 3][n & 1][1]);
            const bf16x8 u = G < 2 ? y : x, v = G < 2 ? x : y;
            f32x4& d = n < 8 ? acc[G][n >> 1][f] : acch[f];
            const f32x4 c0 = bias;
            if (!first || (n & 1)) asm volatile("v_mfma_f32_16x16x32_bf16 %0, %1, %2, %0" : "+v"(d) : "v"(u), "v"(v));
            else if (n == 8) asm volatile("v_mfma_f32_16x16x32_bf16 %0, %1, %2, 0" : "=&v"(d) : "v"(u), "v"(v));
            else asm volatile("v_mfma_f32_16x16x32_bf16 %0, %1, %2, %3" : "=&v"(d) : "v"(u), "v"(v), "v"(c0));
            SBAR();
            gap(n);
            SBAR();
        }
    };

    f32x4 cq[2], ck[2], mu4[5], rs4[5]; float cv[2], mu[5], rs[5];
    u32x4 qfr[4], kfr[4];
    u32x4 va[2][2];
    f32x4 mwin, mfirst;
    typedef __attribute__((address_space(3))) u32x4* lds_u4;
    const bool negpos = (wm == 0) && (panel == 0);
    VM_BAR(0);
    unsigned cm01 = 0, cm23 = 0;
    lds_cptr ka[2] = {sm, sm}, vo2[2] = {sm, sm};
    lds_u4 ex = (lds_u4)(smw + OFF_EX + wn * 6144) + lane;
    lds_cptr s1 = sm + C::OFF_MEAN + wm * 256 + l15 * 4, s4 = sm + C::OFF_MEAN + wm * 256 + g * 16;
    lds_cptr c1 = sm + C::OFF_CS + wn * 128 + l15 * 4, c4 = sm + C::OFF_CS + wn * 128 + g * 16;
    asm volatile("" : "+v"(s1), "+v"(s4), "+v"(c1), "+v"(c4));
    if (LOCAL) {
        unsigned keepv = negpos ? __float_as_uint(NEG_MASK) : 0u;
        asm volatile("" : "+s"(keepv));
        u32x4 mw, mf;
        unsigned prv[4];
#pragma unroll
        for (int r = 0; r < 4; ++r) {
            const int d = l15 - (4 * g + r);
            unsigned out = (unsigned)(((d << 28) >> 31));
            prv[r] = (unsigned)(d >> 31);
            asm volatile("" : "+v"(out), "+v"(prv[r]));
            mw[r] = out & 0xff800000u;
            mf[r] = (out & 0xff800000u) | (~out & keepv);
        }
        const lds_cptr e0 = sm + OFF_EX + wn * 6144;
        ka[0] = e0 + (wm ? lane : 256 + (lane ^ 8)) * 16;         ka[1] = e0 + (wm ? 64 + lane : 256 + lane) * 16;
        vo2[0] = e0 + (wm ? 128 + lane : 320 + (lane ^ 32)) * 16; vo2[1] = e0 + (wm ? 192 + lane : 320 + lane) * 16;
        cm01 = ~((prv[0] >> 16) | (prv[1] << 16)); cm23 = ~((prv[2] >> 16) | (prv[3] << 16));
        mwin = __builtin_bit_cast(f32x4, mw); mfirst = __builtin_bit_cast(f32x4, mf);
        asm volatile("" : "+v"(mwin), "+v"(mfirst), "+v"(cm01), "+v"(cm23), "+v"(ka[0]), "+v"(ka[1]), "+v"(vo2[0]), "+v"(vo2[1]), "+v"(ex));
    }
    typedef const __attribute__((address_space(3))) f32x4* lds_f4; typedef const __attribute__((address_space(3))) float* lds_f1;
    auto mainloop = [&](auto WM) {
        int ty = lane_id();
        const int l15 = ty & 15, g = ty >> 4;
        const int swA = (l15 >> 1) & 7;
        const int q4 = l15 >> 2, p4 = l15 & 3, fk = ((q4 >> 1) & 1) | ((g & 1) << 1);
        lds_cptr pA0 = sm + wm * 8192 + l15 * 128 + ((0 + g) ^ swA) * 16, pA1 = sm + wm * 8192 + l15 * 128 + ((4 + g) ^ swA) * 16;
        lds_cptr pB0 = sm + ABYTES + (8 * g + q4) * 128 + (((2 * wn + 0) ^ fk) * 32) + p4 * 8;
        lds_cptr pB1 = sm + ABYTES + (8 * g + q4) * 128 + (((2 * wn + 1) ^ fk) * 32) + p4 * 8;
        asm volatile("" : "+v"(pA0), "+v"(pA1), "+v"(pB0), "+v"(pB1));
        slow_warm((unsigned)(uintptr_t)smem + OFF_DUMP, W, w, ty);
        unsigned so = (unsigned)(panel * 16384 + wm * 512 + ty * 8);
        const char *p0 = ident ? (const char*)W : statp, *p1 = ident ? (const char*)W : wnp;
        u32x4 wn;
        asm volatile("global_load_dwordx4 %0, %2, %3 sc1" : "=&v"(wn), "+v"(so) : "v"(0), "s"(p1) : "memory");
        BAR();
        STAMP(LOCAL ? 1 : 7);
        f32x2 sq;
        asm volatile("global_load_dwordx2 %0, %1, %2 sc1" : "=&v"(sq) : "v"(so), "s"(p0) : "memory");
        BAR();
        pa[0] = pA0; pa[1] = pA1; pb[0] = pB0; pb[1] = pB1;
#pragma unroll
        for (int i = 0; i < 4; ++i) rdB(0, i);
#pragma unroll
        for (int i = 0; i < 8; ++i) rdA(WM, 0, i);
#pragma unroll
        for (int i = 0; i < 4; ++i) if (LOCAL && WM.value == 0 && i >= 2) rdA(WM, 0, 6 + i); else rdB(1, i);
        SBAR();
        asm volatile("s_waitcnt vmcnt(0)" : "+v"(sq), "+v"(wn) :: "memory");
        const float d = fmaxf(__builtin_fmaf(sq[0] * (-1.f / 64.f), sq[0], sq[1]), 0.f);
        cb = fminf(__builtin_amdgcn_sqrtf(__uint_as_float(wave_min(__float_as_uint(d)))) * 64.f * __uint_as_float(min(min(wn[0], wn[1]), wn[2])), 1.0e30f);
        if (ident) cb = 0.f;
        bias = f32x4{cb, cb, cb, cb};
        asm volatile("s_nop 1" : "+v"(bias));
        WAIT_LGKM();
        int off = 0;
        constexpr bool HK = LOCAL && WM.value == 0;
        auto front = [&](int p, bool first) {
#pragma unroll
            for (int c = 0; c < 4; ++c)
                column(WM, p, c, first, [&](int n) {
                    if (HK && c == 0) { if (n < 2) rdB(1, 2 + n); else if (n < 6) rdB(2, n - 2); }
                    else if (n < 4) rdB(c + 2, n);
                    if (LOCAL && !HK && c == 0 && (n == 4 || n == 5)) rdA(WM, p, 4 + n);
                    if (c == 1 && n == 5) { off = (off == 2 * STAGE) ? 0 : off + STAGE; asm volatile("" : "+s"(off)); }
                    if (c == 2 && n == 4) { pa[0] = pA0 + off; asm volatile("" : "+v"(pa[0])); }
                    if (c == 2 && n == 5) { pa[1] = pA1 + off; asm volatile("" : "+v"(pa[1])); }
                    if (c == 3 && n == 4) { pb[0] = pB0 + off; asm volatile("" : "+v"(pb[0])); }
                    if (c == 3 && n == 5) { pb[1] = pB1 + off; asm volatile("" : "+v"(pb[1])); }
                    if ((c == 0 && n == 3) || ((c == 1 || c == 2 || (HK && c == 0)) && n == 7)) __builtin_amdgcn_s_waitcnt(0xC47F);
                });
            WAIT_LGKM_BAR();
        };
        auto back = [&](int p, bool first) {
            column(WM, p, 4, first, [&](int n) { if (n & 1) rdB(0, n >> 1); else rdA(WM, p ^ 1, n >> 1); });
            column(WM, p, 5, first, [&](int n) {
                if (!(n & 1)) rdA(WM, p ^ 1, 4 + (n >> 1)); else if (HK && n > 4) rdA(WM, p ^ 1, 6 + (n >> 1)); else rdB(1, n >> 1);
                if (n == 7) __builtin_amdgcn_s_waitcnt(0xC87F);
            });
        };
        auto stage = [&](int p, bool first) { front(p, first); back(p, first); };
        stage(0, true);
        front(1, false);
#pragma unroll 1
        for (int t = 1; t < NT - 1; t += 2) { back(1, false); stage(0, false); front(1, false); }
        column(WM, 1, 4, false, [&](int n) {
            constexpr int RS = C::OFF_RSTD - C::OFF_MEAN, H = 512 - WM.value * 256;
            if (n == 0) { ck[0] = *(lds_f4)(c4 + 256); ck[1] = *(lds_f4)(c4 + 320); cv[0] = *(lds_f1)(c1 + 512); cv[1] = *(lds_f1)(c1 + 512 + 64); }
            else if (n < 5) {
                mu[n - 1] = *(lds_f1)(s1 + (n - 1) * 64); rs[n - 1] = *(lds_f1)(s1 + RS + (n - 1) * 64);
                mu4[n - 1] = *(lds_f4)(s4 + (n - 1) * 64); rs4[n - 1] = *(lds_f4)(s4 + RS + (n - 1) * 64);
            } else if (n == 5) {
                if (LOCAL && WM.value == 0) { mu[4] = *(lds_f1)(s1 + H); rs[4] = *(lds_f1)(s1 + RS + H); }
                else if (LOCAL) { mu4[4] = *(lds_f4)(s4 + H); rs4[4] = *(lds_f4)(s4 + RS + H); }
            } else if (n == 6) { cq[0] = *(lds_f4)c4; cq[1] = *(lds_f4)(c4 + 64); }
        });
        if (LOCAL) column(WM, 1, 5, false, [](int n) { if (n == 6) __builtin_amdgcn_s_waitcnt(0xC07F); });
        if (LOCAL) {
            auto fix = [&](int a, int t) {
#pragma unroll
                for (int f = 0; f < 2; ++f)
                    acc[a][t][f] = (a == 2) ? rs4[t] * (acc[2][t][f] + mu4[t] * cv[f]) + rs4[t] * -cb
                                            : rs[t] * (acc[a][t][f] + mu[t] * (a ? ck[f] : cq[f])) + rs[t] * -cb;
            };
            auto pk = [&](const f32x4 (&v)[2]) { return u32x4{pack_bf16(v[0][0], v[0][1]), pack_bf16(v[0][2], v[0][3]), pack_bf16(v[1][0], v[1][1]), pack_bf16(v[1][2], v[1][3])}; };
            if (!ident) {
                fix(1, 1); fix(1, 3); fix(2, 1); fix(2, 3);
#pragma unroll
                for (int f = 0; f < 2; ++f)
                    acch[f] = (WM.value == 0) ? rs[4] * (acch[f] + mu[4] * ck[f]) : rs4[4] * (acch[f] + mu4[4] * cv[f]);
            }
            auto pkv = [&](int t) {
#pragma unroll
                for (int f = 0; f < 2; ++f) {
                    va[t >> 1][f][(t & 1) * 2] = pack_bf16(acc[2][t][f][0], acc[2][t][f][1]);
                    va[t >> 1][f][(t & 1) * 2 + 1] = pack_bf16(acc[2][t][f][2], acc[2][t][f][3]);
                }
            };
            kfr[1] = pk(acc[1][1]); kfr[3] = pk(acc[1][3]); pkv(1); pkv(3);
            u32x4 hf = pk(acch);
            if (WM.value == 0) {
                ex[0] = kfr[1]; ex[64] = kfr[3];
#pragma unroll
                for (int u = 0; u < 2; ++u)
                    asm volatile("ds_write_b64 %0, %1 offset:%3\n\tds_write_b64 %0, %2 offset:%3+8" :: "v"(ex), "v"(u32x2{va[u][0][2], va[u][0][3]}),
                                 "v"(u32x2{va[u][1][2], va[u][1][3]}), "n"(2048 + u * 1024) : "memory");
                if (negpos) hf = u32x4{0, 0, 0, 0};
            }
            ex[256 + WM.value * 64] = hf;
            SBAR();
            if (!ident) { fix(0, 0); fix(0, 1); fix(0, 2); fix(0, 3); fix(1, 0); fix(1, 2); fix(2, 0); fix(2, 2); }
#pragma unroll
            for (int t = 0; t < 4; ++t) qfr[t] = pk(acc[0][t]);
            kfr[0] = pk(acc[1][0]); kfr[2] = pk(acc[1][2]); pkv(0); pkv(2);
            asm volatile("" : "+v"(qfr[0]), "+v"(qfr[1]), "+v"(qfr[2]), "+v"(qfr[3]), "+v"(kfr[0]), "+v"(kfr[2]),
                         "+v"(va[0][0]), "+v"(va[0][1]), "+v"(va[1][0]), "+v"(va[1][1]));
        }
    };
    const int head = ct * 2 + wn;
    unsigned qo = 0, ko = 0, vo = 0;
    if (LOCAL) {
        qo = (unsigned)((((panel * 8 + wm * 4) * 32 + head) * 64 + lane) * 16);
        asm volatile("" : "+v"(qo));
    } else {
        ko = (unsigned)((wm * 8 + (g & 1)) * 1024 + ((l15 & 7) + ((l15 & 8) << 1) + 32 * (g >> 1)) * 16);
        vo = (unsigned)((wm * 8 + 2 + (g >> 1)) * 1024 + (l15 + 32 * (g & 1)) * 16);
        asm volatile("" : "+v"(ko), "+v"(vo));
    }
    if (LOCAL && wm) mainloop(IC<1>{}); else mainloop(IC<0>{});

    if (!LOCAL) {
        auto pk = [&](f32x4 a, f32x4 b) { return u32x4{pack_bf16(a[0], a[1]), pack_bf16(a[2], a[3]), pack_bf16(b[0], b[1]), pack_bf16(b[2], b[3])}; };
        char* kvp = (char*)kvf + (size_t)(head * 16 + panel) * 16384;
        lds_ptr own = smw + wn * 16384;
        SBAR();
#pragma unroll
        for (int t = 0; t < 4; ++t) {
            const u32x4 kk = pk(rs[t] * (acc[1][t][0] + mu[t] * ck[0]) + rs[t] * -cb, rs[t] * (acc[1][t][1] + mu[t] * ck[1]) + rs[t] * -cb);
            if (t & 1) st16o<128>(kvp + (t >> 1) * 4096, ko, kk); else st16o<0>(kvp + (t >> 1) * 4096, ko, kk);
            kfr[t] = kk;
            SBAR();
            const int u = t >> 1, f = t & 1;
            const u32x4 vv = pk(rs4[2 * u] * (acc[2][2 * u][f] + mu4[2 * u] * cv[f]) + rs4[2 * u] * -cb,
                                rs4[2 * u + 1] * (acc[2][2 * u + 1][f] + mu4[2 * u + 1] * cv[f]) + rs4[2 * u + 1] * -cb);
            if (f) st16o<256>(kvp + u * 4096, vo, vv); else st16o<0>(kvp + u * 4096, vo, vv);
            qfr[t] = vv;
            SBAR();
        }
        column(IC<0>{}, 1, 5, false, [&](int n) {
            const int t = n >> 1;
            if (n & 1) *(lds_u4)(own + vo + (t >> 1) * 4096 + (t & 1) * 256) = qfr[t];
            else       *(lds_u4)(own + ko + (t >> 1) * 4096 + (t & 1) * 128) = kfr[t];
        });
        asm volatile("s_nop 7");
        SBAR();
#pragma unroll
        for (int u = 0; u < 2; ++u) {
            const float r0 = rs[2 * u] * QSCALE, r1 = rs[2 * u + 1] * QSCALE;
            const u32x4 qa = pk(r0 * (acc[0][2 * u][0] + mu[2 * u] * cq[0]) + r0 * -cb, r0 * (acc[0][2 * u][1] + mu[2 * u] * cq[1]) + r0 * -cb);
            const u32x4 qb = pk(r1 * (acc[0][2 * u + 1][0] + mu[2 * u + 1] * cq[0]) + r1 * -cb, r1 * (acc[0][2 * u + 1][1] + mu[2 * u + 1] * cq[1]) + r1 * -cb);
#pragma unroll
            for (int e = 0; e < 4; ++e) {
                const auto x = __builtin_amdgcn_permlane16_swap(qa[e], qb[e], false, false);
                qo4[2 * u][e] = x[0]; qo4[2 * u + 1][e] = x[1];
            }
        }
        return;
    }
    {
        const f32x4 zero4 = {0, 0, 0, 0};
        auto bf8 = [](u32x4 v) { return __builtin_bit_cast(bf16x8, v); };
        f32x4 sp[4], sc[4];
        sc[1] = MFMA16(bf8(kfr[1]), bf8(qfr[1]), mwin); sp[1] = MFMA16(bf8(kfr[0]), bf8(qfr[1]), mwin);
        sc[3] = MFMA16(bf8(kfr[3]), bf8(qfr[3]), mwin); sp[3] = MFMA16(bf8(kfr[2]), bf8(qfr[3]), mwin);
        asm volatile("" : "+v"(sc[1]), "+v"(sp[1]), "+v"(sc[3]), "+v"(sp[3]));
        WAIT_LGKM_BAR();
        const bf16x8 kp[2] = {lds_b128(ka[0]), lds_b128(ka[1])};
        u32x2 vp[2][2];
#pragma unroll
        for (int u = 0; u < 2; ++u)
            asm volatile("ds_read_b64 %0, %2\n\tds_read_b64 %1, %2 offset:8" : "=&v"(vp[u][0]), "=&v"(vp[u][1]) : "v"(vo2[u]) : "memory");
        sc[0] = MFMA16(bf8(kfr[0]), bf8(qfr[0]), mwin); sc[2] = MFMA16(bf8(kfr[2]), bf8(qfr[2]), mwin);
        SBAR();
        float x[4][4], m[4];
        unsigned a[4][2];
        auto logits = [&](int t0, int st) {
#pragma unroll
            for (int t = t0; t < 4; t += 2)
#pragma unroll
                for (int h = 0; h < 2; ++h) {
                    if (st == 0) {
                        const unsigned uc = pack_bf16(sc[t][2 * h], sc[t][2 * h + 1]), up = pack_bf16(sp[t][2 * h], sp[t][2 * h + 1]);
                        asm("v_bfi_b32 %0, %1, %2, %3" : "=v"(a[t][h]) : "v"(h ? cm23 : cm01), "v"(uc), "v"(up));
                    } else if (st == 1) { x[t][2 * h] = bf_lo(a[t][h]) * ATT_SCALE; x[t][2 * h + 1] = bf_hi(a[t][h]) * ATT_SCALE; }
                    else if (st == 2) a[t][h] = pack_bf16(x[t][2 * h], x[t][2 * h + 1]);
                    else { x[t][2 * h] = bf_lo(a[t][h]); x[t][2 * h + 1] = bf_hi(a[t][h]); }
                }
            SBAR();
        };
        logits(1, 0); logits(1, 1);
        sp[0] = MFMA16(kp[0], bf8(qfr[0]), mfirst); sp[2] = MFMA16(kp[1], bf8(qfr[2]), mfirst);
        SBAR();
        logits(1, 2); logits(1, 3);
        logits(0, 0); logits(0, 1); logits(0, 2); logits(0, 3);
        auto across = [&](float (&v)[4], auto op, auto fin) {
            const auto p = __builtin_amdgcn_permlane16_swap(__float_as_uint(v[0]), __float_as_uint(v[1]), false, false);
            const auto q = __builtin_amdgcn_permlane16_swap(__float_as_uint(v[2]), __float_as_uint(v[3]), false, false);
            const auto r = __builtin_amdgcn_permlane32_swap(__float_as_uint(op(__uint_as_float(p[0]), __uint_as_float(p[1]))),
                                                            __float_as_uint(op(__uint_as_float(q[0]), __uint_as_float(q[1]))), false, false);
            const unsigned z = __float_as_uint(fin(op(__uint_as_float(r[0]), __uint_as_float(r[1]))));
            const auto u = __builtin_amdgcn_permlane32_swap(z, z, false, false);
            const auto e = __builtin_amdgcn_permlane16_swap(u[0], u[0], false, false), f = __builtin_amdgcn_permlane16_swap(u[1], u[1], false, false);
            v[0] = __uint_as_float(e[0]); v[1] = __uint_as_float(e[1]); v[2] = __uint_as_float(f[0]); v[3] = __uint_as_float(f[1]);
        };
#pragma unroll
        for (int t = 0; t < 4; ++t) m[t] = amax3(x[t][0], x[t][1], x[t][2]);
        asm("v_max_f32 %0, %0, %4\n\tv_max_f32 %1, %1, %5\n\tv_max_f32 %2, %2, %6\n\tv_max_f32 %3, %3, %7\n\ts_nop 0"
            : "+v"(m[0]), "+v"(m[1]), "+v"(m[2]), "+v"(m[3]) : "v"(x[0][3]), "v"(x[1][3]), "v"(x[2][3]), "v"(x[3][3]));
        across(m, [](float p, float q) { float r; asm("v_max_f32 %0, %1, %2\n\ts_nop 0" : "=v"(r) : "v"(p), "v"(q)); return r; },
               [](float p) { return p * -LOG2E; });
        SBAR();
#pragma unroll
        for (int t = 0; t < 4; ++t)
#pragma unroll
            for (int r = 0; r < 4; ++r) x[t][r] = __builtin_fmaf(x[t][r], LOG2E, m[t]);
        SBAR();
#pragma unroll
        for (int t = 0; t < 4; ++t)
#pragma unroll
            for (int r = 0; r < 4; ++r) x[t][r] = __builtin_amdgcn_exp2f(x[t][r]);
        SBAR();
#pragma unroll
        for (int t = 0; t < 4; ++t) m[t] = (x[t][0] + x[t][1]) + (x[t][2] + x[t][3]);
        asm volatile("s_waitcnt lgkmcnt(0)" : "+v"(vp[0][0]), "+v"(vp[0][1]), "+v"(vp[1][0]), "+v"(vp[1][1]) :: "memory");
        u32x4 ve[2][2];
#pragma unroll
        for (int u = 0; u < 2; ++u)
#pragma unroll
            for (int f = 0; f < 2; ++f) ve[u][f] = u32x4{vp[u][f][0], vp[u][f][1], va[u][f][0], va[u][f][1]};
        across(m, [](float p, float q) { return p + q; }, [](float p) { return __builtin_amdgcn_rcpf(p); });
        asm volatile("" : "+v"(ve[0][0]), "+v"(ve[0][1]), "+v"(ve[1][0]), "+v"(ve[1][1]));
        SBAR();
#pragma unroll
        for (int t = 0; t < 4; ++t)
#pragma unroll
            for (int r = 0; r < 4; ++r) x[t][r] *= m[t];
        SBAR();
#pragma unroll
        for (int t = 0; t < 4; ++t)
#pragma unroll
            for (int h = 0; h < 2; ++h) a[t][h] = pack_bf16(x[t][2 * h], x[t][2 * h + 1]);
        SBAR();
        f32x4 o0[4], o1[4];
#pragma unroll
        for (int t = 0; t < 4; ++t) {
            const bf16x8 pb = bf8(u32x4{a[t][0] & ~cm01, a[t][1] & ~cm23, a[t][0] & cm01, a[t][1] & cm23});
            o0[t] = MFMA16(bf8((t & 1) ? va[t >> 1][0] : ve[t >> 1][0]), pb, zero4);
            o1[t] = MFMA16(bf8((t & 1) ? va[t >> 1][1] : ve[t >> 1][1]), pb, zero4);
        }
        SBAR();
#pragma unroll
        for (int t = 0; t < 4; ++t)
            st16(ofrag + t * 16384, qo, u32x4{pack_bf16(o0[t][0], o0[t][1]), pack_bf16(o0[t][2], o0[t][3]),
                                                 pack_bf16(o1[t][0], o1[t][1]), pack_bf16(o1[t][2], o1[t][3])});
    }
}

DI int out_row(int panel, bool localrows, int T, int a) {
    return localrows ? (panel * 128 + (T >> 2) * 64 + 2 * (16 * (T & 1) + a) + ((T >> 1) & 1)) : (panel * 128 + T * 16 + a);
}
DI int out_swz(int k) { return ((k >> 1) & 1) | (k & 4); }
DI unsigned wo_off(int w, int lane) {
    const int kl = lane >> 3;
    unsigned v = (unsigned)(w * 8 + kl) * 2048u + (unsigned)(ids().co * 128 + ((lane & 7) ^ out_swz(kl)) * 16);
    asm volatile("" : "+v"(v));
    return v;
}
DI void wo_load(const void* Wo, unsigned voff, int i0, int i1, unsigned lds0, int w) {
#pragma unroll
    for (int i = i0; i < i1; ++i) glds16((const char*)Wo + (size_t)i * 65536, voff, lds0 + (w + 4 * i) * 1024);
}
DI unsigned res_off(bool localrows, bool tiled, int lane) {
    const Ids I = ids();
    unsigned v = (unsigned)(I.po * 262144 + I.co * (tiled ? 16384 : 128) + out_row(0, localrows, 0, lane >> 3) * (tiled ? 128 : 2048) +
                            (((lane & 7) ^ (lane >> 3)) * 16));
    asm volatile("" : "+v"(v));
    return v;
}
DI void res_load(u32x4 (&res)[6], const void* Xin, bool localrows, bool tiled, unsigned off, int w) {
#pragma unroll
    for (int i = 0; i < 6; ++i) {
        const int j = min(w + 3 * i, 15);
        asm volatile("global_load_dwordx4 %0, %1, %2 sc1" : "=&v"(res[i]) : "v"(off), "s"((const char*)Xin + out_row(0, localrows, j >> 1, (j & 1) * 8) * (tiled ? 128 : 2048)) : "memory");
    }
}
DI void out_handoff(char* smem, int xoff, u32x4 (&res)[6], int w, char* ws, int set, u64 tag, int lane, u32x4 (&c)[8], int mat) {
    const Ids I = ids();
    u64 land;
    lds_ptr dst = (lds_ptr)smem + xoff + w * 1024 + lane * 16;
    asm volatile("" : "+v"(dst));
    if (w == 3) __builtin_amdgcn_s_setprio(3);
    signal_poll<false, false>(w - 3, ws, flag_off(set, I.pq, lane & 15, I.cq), flag_off(set, I.po, I.co, lane & 15), 0xffffull, tag, land);
    if (w < 3) {
        asm volatile("s_waitcnt vmcnt(0)" : "+v"(res[0]), "+v"(res[1]), "+v"(res[2]), "+v"(res[3]), "+v"(res[4]), "+v"(res[5]) :: "memory");
#pragma unroll
        for (int i = 0; i < 5; ++i) *(__attribute__((address_space(3))) u32x4*)(dst + i * 3072) = res[i];
        if (w == 0) *(__attribute__((address_space(3))) u32x4*)(dst + 15360) = res[5];
        if (mat >= 0) colsum_store(c, ws, mat, w, lane);
    }
    WAIT_LGKM_BAR();
    asm volatile("s_waitcnt vmcnt(0)" : "+v"(land) :: "memory");
    if (w < 3 && mat >= 0) flag_store(ws, (mat ? 10 : 13) + w, I.cq, I.pq, tag, lane);
    __builtin_amdgcn_s_setprio(0);
}

template <bool AFTER_LOCAL>
DI void out_compute(char* smem, int w, const __hip_bfloat16* __restrict__ ofrag,
                    __hip_bfloat16* __restrict__ Xout, float* __restrict__ statp SP_PARAM) {
    constexpr int XOFF = AFTER_LOCAL ? 147456 : 98304;
    STAMP(AFTER_LOCAL ? 3 : 10);
    VM_BAR(0);
    const int lane = lane_id();
    asm volatile("" : "+s"(w));
    const Ids I = ids();
    const int panel = I.po, ct = I.co;
    const int l15 = lane & 15, g = lane >> 4;
    const bool tiled = AFTER_LOCAL || statp != nullptr;
    const int rstr = tiled ? 128 : 2048;
    const int q4 = l15 >> 2, p4 = l15 & 3;
    const int krow = 4 * g + q4;
    const int sw = out_swz(krow);
    const lds_cptr sm = (lds_cptr)smem;
    lds_cptr bw[4], xo[4];
#pragma unroll
    for (int t = 0; t < 4; ++t) {
        bw[t] = sm + krow * 128 + ((((t >> 1) * 4 + (p4 >> 1) * 2 + (t & 1)) ^ sw) * 16) + (p4 & 1) * 8;
        xo[t] = sm + XOFF + (w * 32 + l15) * 128 + ((((t >> 1) * 4 + (g >> 1) * 2 + (t & 1)) ^ (l15 & 7)) * 16) + (g & 1) * 8;
    }
    unsigned so[2], sto, ao[2];
#pragma unroll
    for (int r2 = 0; r2 < 2; ++r2) {
        so[r2] = (unsigned)(panel * 262144 + ct * (tiled ? 16384 : 128) + out_row(0, AFTER_LOCAL, 2 * w + r2, l15 & 7) * rstr + (l15 >> 3) * 64 + g * 16);
        ao[r2] = (unsigned)((panel * 8 + 2 * w + r2) * 32768 + lane * 16);
    }
    sto = (unsigned)(((panel * 16 + ct) * 128 + 2 * w * 16 + l15) * 8);
    asm volatile("" : "+v"(bw[0]), "+v"(bw[1]), "+v"(bw[2]), "+v"(bw[3]), "+v"(xo[0]), "+v"(xo[1]), "+v"(xo[2]), "+v"(xo[3]),
                 "+v"(so[0]), "+v"(so[1]), "+v"(sto), "+v"(ao[0]), "+v"(ao[1]));
    bf16x8 ones;
    {
        unsigned o1; asm volatile("v_mov_b32 %0, 0x3f803f80" : "=v"(o1));
        ones = __builtin_bit_cast(bf16x8, (u32x4{o1, o1, o1, o1}));
        asm volatile("" : "+v"(ones));
    }
    const __amdgpu_buffer_rsrc_t ra = __builtin_amdgcn_make_buffer_rsrc((void*)ofrag, 0, 4 << 20, 0x00020000);
    asm volatile("" :: "s"(ra));
    u32x32 fr;
    BAR();
    STAMP(AFTER_LOCAL ? 4 : 11);
    u32x32 aw0, aw1, aw2, fr1;
    asm volatile(".irp j,0,1,2,3\n\tbuffer_load_dwordx4 v[96+4*\\j:99+4*\\j], %2, %4, 0 offen offset:1024*\\j sc1\n\t.endr\n\t"
                 ".irp j,0,1,2,3\n\tbuffer_load_dwordx4 v[112+4*\\j:115+4*\\j], %3, %4, 0 offen offset:1024*\\j sc1\n\t.endr\n\t"
                 ".irp j,0,1,2,3\n\tbuffer_load_dwordx4 v[128+4*\\j:131+4*\\j], %2, %4, %5 offen offset:1024*\\j sc1\n\t.endr"
                 : "=&{v[96:127]}"(aw0), "=&{v[128:159]}"(aw1) : "v"(ao[0]), "v"(ao[1]), "s"(ra), "s"(4096) : "memory");
    f32x16 acc[2];
#pragma unroll
    for (int r2 = 0; r2 < 2; ++r2)
#pragma unroll
        for (int c = 0; c < 4; ++c) {
            const u32x2 xr = *(const __attribute__((address_space(3))) u32x2*)(xo[c] + r2 * 2048);
            acc[r2][4 * c] = bf_lo(xr[0]); acc[r2][4 * c + 1] = bf_hi(xr[0]); acc[r2][4 * c + 2] = bf_lo(xr[1]); acc[r2][4 * c + 3] = bf_hi(xr[1]);
        }
    asm volatile(
        ".macro OUT_MM r, t, j, s\n\t"
        "v_mfma_f32_16x16x32_bf16 v[16*\\r+4*\\t:16*\\r+4*\\t+3], v[32+16*\\t+4*\\j:35+16*\\t+4*\\j], v[96+32*\\s+16*\\r+4*\\j:99+32*\\s+16*\\r+4*\\j], v[16*\\r+4*\\t:16*\\r+4*\\t+3]\n\t"
        ".endm\n\t"
        ".macro OUT_RD t, i, g\n\t"
        "ds_read_b64_tr_b16 v[32+16*\\t+2*(\\i):33+16*\\t+2*(\\i)], v[192+\\t] offset:(\\g)*16384+(\\i)*2048\n\t"
        ".endm\n\t"
        ".macro OUT_LD i, s\n\t"
        ".if (\\i) < 4\n\tbuffer_load_dwordx4 v[96+32*(\\s)+4*(\\i):99+32*(\\s)+4*(\\i)], %[a0], %[ra], s96 offen offset:1024*(\\i) sc1\n\t"
        ".else\n\tbuffer_load_dwordx4 v[96+32*(\\s)+4*(\\i):99+32*(\\s)+4*(\\i)], %[a1], %[ra], s96 offen offset:1024*((\\i)-4) sc1\n\t.endif\n\t"
        ".endm\n\t"
        ".macro OUT_CH r, t, s, rt, g\n\t"
        ".irp j,0,1,2,3\n\tOUT_MM \\r, \\t, \\j, \\s\n\tOUT_RD \\rt, 4*\\r+\\j, \\g\n\t.endr\n\t"
        ".endm\n\t"
        ".macro OUT_GRP s, g, gn, ld, up, v0, v1\n\t"
        "OUT_CH 0, 0, \\s, 3, \\g\n\ts_waitcnt vmcnt(\\v0)\n\t.if \\ld\n\tOUT_LD 0, (\\s+2)%%3\n\t.endif\n\t"
        "OUT_CH 1, 0, \\s, 3, \\g\n\t.if \\up\n\t.irp t,0,1,2,3\n\tv_add_u32 v[192+\\t], s97, v[192+\\t]\n\t.endr\n\t.endif\n\t.if \\ld\n\tOUT_LD 1, (\\s+2)%%3\n\t.endif\n\t"
        "OUT_CH 0, 1, \\s, 0, \\gn\n\t.if \\ld\n\tOUT_LD 2, (\\s+2)%%3\n\t.endif\n\t"
        "OUT_CH 1, 1, \\s, 0, \\gn\n\t.if \\ld\n\tOUT_LD 3, (\\s+2)%%3\n\t.endif\n\ts_waitcnt lgkmcnt(8)\n\t"
        "OUT_CH 0, 2, \\s, 1, \\gn\n\t.if \\ld\n\tOUT_LD 4, (\\s+2)%%3\n\t.endif\n\t"
        "OUT_CH 1, 2, \\s, 1, \\gn\n\t.if \\ld\n\tOUT_LD 5, (\\s+2)%%3\n\t.endif\n\t"
        "OUT_CH 0, 3, \\s, 2, \\gn\n\t.if \\ld\n\tOUT_LD 6, (\\s+2)%%3\n\t.endif\n\t"
        "OUT_CH 1, 3, \\s, 2, \\gn\n\t.if \\ld\n\tOUT_LD 7, (\\s+2)%%3\n\ts_addk_i32 s96, 0x1000\n\t.endif\n\t"
        "s_waitcnt vmcnt(\\v1) lgkmcnt(8)\n\t"
        ".endm\n\t"
        ".macro OUT_PACK y, c\n\t"
        ".irp i,0,1,2,3,4,5,6,7\n\tv_cvt_pk_bf16_f32 v[\\y+\\i], v[\\c+2*\\i], v[\\c+2*\\i+1]\n\t.endr\n\t"
        ".irp i,0,1,4,5\n\tv_permlane16_swap_b32 v[\\y+\\i], v[\\y+\\i+2]\n\t.endr\n\t"
        ".irp i,0,1,2,3\n\tv_mov_b32 v[\\y+8+\\i], v[\\y+\\i]\n\t.endr\n\t"
        "v_mfma_f32_16x16x32_bf16 v[\\y+12:\\y+15], v[\\y:\\y+3], v[\\y:\\y+3], 0\n\t"
        "v_mov_b32_dpp v[\\y+8], v[\\y+4] row_ror:8 row_mask:0xf bank_mask:0xc\n\tv_mov_b32_dpp v[\\y+9], v[\\y+5] row_ror:8 row_mask:0xf bank_mask:0xc\n\t"
        "v_mfma_f32_16x16x32_bf16 v[\\y+16:\\y+19], %[one], v[\\y:\\y+3], 0\n\t"
        "v_mov_b32_dpp v[\\y+10], v[\\y+6] row_ror:8 row_mask:0xf bank_mask:0xc\n\tv_mov_b32_dpp v[\\y+11], v[\\y+7] row_ror:8 row_mask:0xf bank_mask:0xc\n\t"
        "v_mfma_f32_16x16x32_bf16 v[\\y+12:\\y+15], v[\\y+4:\\y+7], v[\\y+4:\\y+7], v[\\y+12:\\y+15]\n\t"
        ".endm\n\t"
        ".macro OUT_REST y\n\t"
        "v_mfma_f32_16x16x32_bf16 v[\\y+16:\\y+19], %[one], v[\\y+4:\\y+7], v[\\y+16:\\y+19]\n\t"
        ".irp i,0,1,2,3\n\tv_mov_b32_dpp v[\\y+4+\\i], v[\\y+\\i] row_ror:8 row_mask:0xf bank_mask:0x3\n\t.endr\n\t"
        ".endm\n\t"
        ".macro OUT_DIAG y\n\t"
        "v_cndmask_b32 v[\\y+12], v[\\y+12], v[\\y+13], %[m0]\n\tv_cndmask_b32 v[\\y+14], v[\\y+14], v[\\y+15], %[m0]\n\t"
        "v_cndmask_b32 v[\\y+17], v[\\y+12], v[\\y+14], %[m1]\n\t"
        ".endm\n\t"
        "s_movk_i32 s96, 0x1000\n\t.irp i,4,5,6,7\n\tOUT_LD \\i, 1\n\t.endr\n\t"
        ".irp t,0,1,2\n\t.irp i,0,1,2,3,4,5,6,7\n\tOUT_RD \\t, \\i, 0\n\t.endr\n\ts_waitcnt lgkmcnt(4)\n\t.endr\n\t"
        "s_movk_i32 s96, 0x2000\n\ts_mov_b32 s97, 0xc000\n\t"
        "s_waitcnt vmcnt(12) lgkmcnt(0)\n"
        ".Lout_loop_%=:\n\t"
        "OUT_GRP 0, 0, 1, 1, 0, 8, 12\n\tOUT_GRP 1, 1, 2, 1, 0, 8, 12\n\tOUT_GRP 2, 2, 0, 1, 1, 8, 12\n\t"
        "s_mov_b32 s97, %[last]\n\t"
        "s_cmpk_lg_u32 s96, 0x8000\n\ts_cbranch_scc1 .Lout_loop_%=\n\t"
        "OUT_GRP 0, 0, 1, 0, 0, 8, 4\n\t"
        ".irp j,0,1,2,3\n\tOUT_MM 0, 0, \\j, 1\n\tOUT_RD 3, \\j, 1\n\t.endr\n\t.irp j,0,1,2,3\n\tOUT_MM 0, 1, \\j, 1\n\tOUT_RD 3, 4+\\j, 1\n\t.endr\n\t"
        "s_waitcnt lgkmcnt(8)\n\t.irp j,0,1,2,3\n\tOUT_MM 0, 2, \\j, 1\n\t.endr\n\t"
        "s_waitcnt lgkmcnt(0)\n\t.irp j,0,1,2,3\n\tOUT_MM 0, 3, \\j, 1\n\t.endr\n\t"
        "s_nop 1\n\t"
        "OUT_PACK 96, 0\n\tglobal_store_dwordx4 %[s0], v[104:107], %[xa] sc1\n\tOUT_REST 96\n\tglobal_store_dwordx4 %[s0], v[100:103], %[xb] sc1\n\t"
        "s_waitcnt vmcnt(2)\n\t.irp t,0,1,2,3\n\t.irp j,0,1,2,3\n\tOUT_MM 1, \\t, \\j, 1\n\t.endr\n\t.endr\n\t"
        "s_nop 1\n\t"
        "OUT_PACK 160, 16\n\tglobal_store_dwordx4 %[s1], v[168:171], %[xa] sc1\n\tOUT_REST 160\n\tglobal_store_dwordx4 %[s1], v[164:167], %[xb] sc1\n\t"
        "s_cmp_eq_u64 %[st], 0\n\ts_cbranch_scc1 .Lout_end_%=\n\t"
        "OUT_DIAG 96\n\tOUT_DIAG 160\n\t"
        "s_mov_b64 exec, %[md]\n\t"
        "global_store_dwordx2 %[so], v[112:113], %[st] sc1\n\tglobal_store_dwordx2 %[so], v[176:177], %[st] offset:128 sc1\n\t"
        "s_mov_b64 exec, -1\n"
        ".Lout_end_%=:\n\t"
        "s_nop 1\n\t"
        ".purgem OUT_MM\n\t.purgem OUT_RD\n\t.purgem OUT_LD\n\t.purgem OUT_CH\n\t.purgem OUT_GRP\n\t.purgem OUT_PACK\n\t.purgem OUT_REST\n\t.purgem OUT_DIAG"
        : "+{v[0:15]}"(acc[0]), "+{v[16:31]}"(acc[1]), "+{v[96:127]}"(aw0), "+{v[128:159]}"(aw1), "=&{v[160:191]}"(aw2),
          "+{v192}"(bw[0]), "+{v193}"(bw[1]), "+{v194}"(bw[2]), "+{v195}"(bw[3]), "=&{v[32:63]}"(fr), "=&{v[64:95]}"(fr1)
        : [a0] "v"(ao[0]), [a1] "v"(ao[1]), [ra] "s"(ra), [last] "s"(AFTER_LOCAL ? 49152 : 81920), [one] "v"(ones), [s0] "v"(so[0]), [s1] "v"(so[1]),
          [xa] "s"(Xout), [xb] "s"((char*)Xout + (AFTER_LOCAL ? 16 : 8) * rstr), [st] "s"(statp), [so] "v"(sto),
          [m0] "s"(0xaaaaaaaaaaaaaaaaull), [m1] "s"(0xccccccccccccccccull), [md] "s"(0xf0000f0000f0000full)
        : "memory", "scc", "s96", "s97");
    STAMP(AFTER_LOCAL ? 5 : 12);
}

#define ROWSUM(p0, p1, c) __builtin_amdgcn_smfmac_f32_16x16x64_bf16(sel, __builtin_bit_cast(bf16v_t, __builtin_shufflevector(p0, p1, 0, 1, 2, 3, 4, 5, 6, 7)), c, sidx, 0, 0)
constexpr int ATT_STAGE = 32768;
typedef __attribute__((ext_vector_type(8))) unsigned u32x8;

DI void attn_loader(char* smem, int w, char* ws, int set, u64 tag, const __hip_bfloat16* __restrict__ kvf, const void* Wo, const void* Xin,
                    const void* Wcs SP_PARAM) {
    const int lane = lane_id();
    asm volatile("" : "+s"(w));
    const Ids I = ids();
    const int panel = I.pq, ct = I.cq;
    const unsigned lds0 = (unsigned)(uintptr_t)smem;
    const char* kvh = (const char*)kvf + (size_t)(ct * 2 + (w >> 1)) * 262144 + (w & 1) * 4096;
    const unsigned voff = (unsigned)(lane * 16);
    auto order = [&](int st) { return ((panel & 8) + st - ((st - 1 < (panel & 7)) ? 1 : 0)) & 15; };
    auto load = [&](int st) {
        const char* src = kvh + (size_t)order(st) * 16384;
        const unsigned dst = lds0 + (w >> 1) * 16384 + (w & 1) * 4096 + (st & 3) * ATT_STAGE;
#pragma unroll
        for (int u = 0; u < 2; ++u)
            asm volatile("s_mov_b32 m0, %0\n\ts_nop 0\n\tglobal_load_lds_dwordx4 %1, %2 sc1\n\tglobal_load_lds_dwordx4 %1, %2 offset:1024 sc1\n\t"
                         "global_load_lds_dwordx4 %1, %2 offset:2048 sc1\n\tglobal_load_lds_dwordx4 %1, %2 offset:3072 sc1"
                         :: "s"(dst + u * 8192), "v"(voff), "s"(src + u * 8192) : "memory");
    };
    const unsigned wv = wo_off(w, lane), rv = res_off(false, true, lane), cv = colsum_off(w, lane);
    const unsigned poff = flag_off(set, ct, panel, lane & 15);
    u64 land;
    __builtin_amdgcn_s_setprio(3);
    BAR();
    signal_poll<false, true>(w, ws, flag_off(set, ct, lane & 15, panel), poff, 1ull << order(1), tag, land);
    STAMP(9);
    load(1);
    asm volatile("global_load_dwordx2 %0, %1, %2 sc1\n\ts_waitcnt vmcnt(0)" : "+v"(land) : "v"(poff), "s"(ws + WS_FLAG) : "memory");
    STAMP(10);
    BAR();
    if (!__all(land == tag)) flag_poll(ws, poff, tag);
    load(2); load(3);
    __builtin_amdgcn_s_setprio(0);
    STAMP(11);
    VM_BAR(8); load(4);
    VM_BAR(0); load(5); load(6);
    VM_BAR(0); load(7); load(8);
    u32x4 res[6], ca[8];
    colsum_load(ca, w < 3 ? Wcs : nullptr, cv);
#pragma unroll 1
    for (int st = 6; st < 12; st += 2) {
        VM_BAR(0);
        load(st + 3); load(st + 4);
        if (st == 8) wo_load(Wo, wv, 24, 32, lds0 + 32768, w);
    }
    VM_BAR(0); load(15); wo_load(Wo, wv, 0, 8, lds0, w);
    res_load(res, Xin, false, true, rv, w);
    VM_BAR(0); wo_load(Wo, wv, 8, 24, lds0, w);
    STAMP(12);
    out_handoff(smem, 98304, res, w, ws, set + 1, tag, lane, ca, Wcs ? 0 : -1);
}

DI void attn_compute(char* smem, int w, const u32x4 (&q4)[4], const __hip_bfloat16* __restrict__ kvf, __hip_bfloat16* __restrict__ ofrag SP_PARAM) {
    const int lane = lane_id();
    asm volatile("" : "+s"(w));
    const int wm = w & 1, wn = w >> 1;
    const Ids I = ids();
    const int panel = I.pq, head = I.cq * 2 + wn;
    const int l15 = lane & 15;
    const unsigned se = ((l15 & 2) == 0 && (l15 & 4) == 0) ? 0x3f803f80u : 0u, so = ((l15 & 2) == 0 && (l15 & 4) != 0) ? 0x3f803f80u : 0u;
    const bf8_t sel = __builtin_bit_cast(bf8_t, u32x4{se, se, so, so});
    const int sidx = (l15 & 1) ? (int)0xEEEEEEEEu : 0x44444444;
    const lds_cptr sm = (lds_cptr)smem + wn * 16384 + lane * 16;
    const f32x16 z16 = {0, 0, 0, 0, 0, 0, 0, 0, 0, 0, 0, 0, 0, 0, 0, 0};
    const bf16x8 qA0 = __builtin_bit_cast(bf16x8, q4[0]), qA1 = __builtin_bit_cast(bf16x8, q4[1]);
    const bf16x8 qB0 = __builtin_bit_cast(bf16x8, q4[2]), qB1 = __builtin_bit_cast(bf16x8, q4[3]);
    f32x16 oA, oB, nmA, nmB, sA, sB;
    f32x4 lsA, lsB;
    u32x8 pB;
    bf16x8 v11;
    asm volatile(".irp r,106,108,110,112,134,136,138,140,170,172\n\tv_mov_b64 v[\\r:\\r+1], 0\n\t.endr"
                 : "={v[106:113]}"(pB), "={v[134:137]}"(lsA), "={v[138:141]}"(lsB), "={v[170:173]}"(v11));
    WAIT_LGKM_BAR();
    STAMP(8);
    bf16x8 k00 = lds_b128(sm), k10 = lds_b128(sm + 1024), k01 = lds_b128(sm + 4096), k11 = lds_b128(sm + 4096 + 1024);
    bf16x8 v00 = lds_b128(sm + 2048), v10 = lds_b128(sm + 3072), v01 = lds_b128(sm + 4096 + 2048);
    float ob;
    {
        const u32x4 vu = __builtin_bit_cast(u32x4, v00);
        const unsigned vm = max(max(vu[0] & 0x7fff0000u, vu[1] & 0x7fff0000u), max(vu[2] & 0x7fff0000u, vu[3] & 0x7fff0000u));
        ob = __uint_as_float(wave_min(vm)) * 0.015625f;
        sA = MFMA32(k00, qA0, z16); sB = MFMA32(k00, qB0, z16);
        sA = MFMA32(k10, qA1, sA); sB = MFMA32(k10, qB1, sB);
        SBAR(); VM_BAR(0); SBAR();
        float mA = sA[0], mB = sB[0];
#pragma unroll
        for (int r = 1; r < 16; ++r) { mA = fmaxf(mA, sA[r]); mB = fmaxf(mB, sB[r]); }
        mA = max_h(mA) + 16.f; mB = max_h(mB) + 16.f;
#pragma unroll
        for (int r = 0; r < 16; ++r) { nmA[r] = -mA; nmB[r] = -mB; sA[r] -= mA; oA[r] = ob; oB[r] = ob; }
    }
    const unsigned a0 = (unsigned)(uintptr_t)sm;
    unsigned dst = (unsigned)((((panel * 8 + wm * 4 + ((lane & 31) >> 4)) * 32 + head) * 64 + (lane & 15) + (lane >> 5) * 16) * 16);
    asm volatile("" : "+v"(dst));
    asm volatile(
        ".macro ATT_RD dst, y\n\t"
        "ds_read_b128 v[\\dst:\\dst+3], v[175+(((\\y)&0x1ffff)>>16)] offset:(\\y)&0xffff\n\t"
        ".endm\n\t"
        ".macro ATT_E4 c\n\t"
        "v_exp_f32 v[\\c], v[\\c]\n\tv_exp_f32 v[\\c+1], v[\\c+1]\n\tv_exp_f32 v[\\c+2], v[\\c+2]\n\tv_exp_f32 v[\\c+3], v[\\c+3]\n\t"
        ".endm\n\t"
        ".macro ATT_C4 p, c\n\t"
        "v_cvt_pk_bf16_f32 v[\\p], v[\\c], v[\\c+1]\n\tv_cvt_pk_bf16_f32 v[\\p+1], v[\\c+2], v[\\c+3]\n\t"
        "v_cvt_pk_bf16_f32 v[\\p+2], v[\\c+4], v[\\c+5]\n\tv_cvt_pk_bf16_f32 v[\\p+3], v[\\c+6], v[\\c+7]\n\t"
        ".endm\n\t"
        ".macro ATT_STEP cur, nxt, ka, kb, q, nm, oc, od, lsd, pc, pd, vd1, vc0, r0, y0, r1, y1, bar, rd\n\t"
        "v_mfma_f32_32x32x16_bf16 v[\\nxt:\\nxt+15], v[\\ka:\\ka+3], v[\\q:\\q+3], v[\\nm:\\nm+15]\n\t"
        "ATT_E4 \\cur\n\t"
        "v_mfma_f32_32x32x16_bf16 v[\\nxt:\\nxt+15], v[\\kb:\\kb+3], v[\\q+4:\\q+7], v[\\nxt:\\nxt+15]\n\t"
        "ATT_E4 \\cur+4\n\t"
        "v_mfma_f32_32x32x16_bf16 v[\\od:\\od+15], v[\\vd1:\\vd1+3], v[\\pd+4:\\pd+7], v[\\od:\\od+15]\n\t"
        "s_waitcnt lgkmcnt(0)\n\t"
        "ATT_C4 \\pc, \\cur\n\t"
        "v_exp_f32 v[\\cur+8], v[\\cur+8]\n\tv_exp_f32 v[\\cur+9], v[\\cur+9]\n\t"
        "v_mfma_f32_32x32x16_bf16 v[\\oc:\\oc+15], v[\\vc0:\\vc0+3], v[\\pc:\\pc+3], v[\\oc:\\oc+15]\n\t"
        ".if \\bar\n\ts_barrier\n\t.endif\n\t"
        "ATT_E4 \\cur+10\n\t"
        "v_smfmac_f32_16x16x64_bf16 v[\\lsd:\\lsd+3], v[130:133], v[\\pd:\\pd+7], v174\n\t"
        ".if \\rd & 4\n\tATT_RD 142, 32768\n\tATT_RD 150, 33792\n\tATT_RD 158, 34816\n\t.endif\n\t"
        ".if \\rd & 8\n\tATT_RD \\r1-8, \\y1-1024\n\t.endif\n\t"
        ".if \\rd & 1\n\tATT_RD \\r0, \\y0\n\t.endif\n\t"
        ".if \\rd & 2\n\tATT_RD \\r1, \\y1\n\t.endif\n\t"
        "v_exp_f32 v[\\cur+14], v[\\cur+14]\n\tv_exp_f32 v[\\cur+15], v[\\cur+15]\n\t"
        "ATT_C4 \\pc+4, \\cur+8\n\t"
        ".if \\rd & 4\n\ts_waitcnt lgkmcnt(3)\n\t.endif\n\t"
        ".endm\n\t"
        ".macro ATT_TILE p, y1, y2, bar, ra=3, rb=3\n\t"
        "ATT_STEP 66, 82, 142+\\p, 150+\\p, 122, 50, 2, 18, 138, 98, 106, 170-\\p, 158+\\p, 170-\\p, \\y1+3072, 142+\\p, \\y2, \\bar&1, \\ra\n\t"
        "ATT_STEP 82, 66, 146-\\p, 154-\\p, 114, 34, 18, 2, 134, 106, 98, 166+\\p, 158+\\p, 158+\\p, \\y2+2048, 150+\\p, \\y2+1024, \\bar&2, \\rb\n\t"
        ".endm\n\t"
        ".macro ATT_STAGE b, bar\n\t"
        "ATT_TILE 0, \\b+4096, \\b+8192, 0\n\t"
        "ATT_TILE 4, \\b+8192, \\b+12288, 0\n\t"
        ".if \\bar\n\tATT_TILE 0, \\b+12288, \\b+32768, 2, 1, 11\n\t.else\n\tATT_TILE 0, \\b+12288, \\b+32768, 0\n\t.endif\n\t"
        "ATT_TILE 4, \\b+32768, \\b+36864, 0\n\t"
        ".endm\n\t"
        "v_add_u32 v176, 0x10000, v175\n\t"
        "s_mov_b32 s96, 4\n\t"
        "s_waitcnt lgkmcnt(0)\n\t"
        "ATT_TILE 0, 4096, 8192, 0\n\tATT_TILE 4, 8192, 12288, 0\n\tATT_TILE 0, 12288, 0, 0, 1, 0\n\tATT_TILE 4, 32768, 36864, 1, 7, 3\n\t"
        "ATT_STAGE 32768, 1\n\t"
        "s_branch .Latt_entry_%=\n\t"
        ".p2align 6\n"
        ".Latt_loop_%=:\n\t"
        "ATT_STAGE 0, 1\n\tATT_STAGE 32768, 0\n"
        ".Latt_entry_%=:\n\t"
        "ATT_STAGE 65536, 1\n\t"
        "s_sub_u32 s96, s96, 1\n\t"
        "ATT_STAGE 98304, 0\n\t"
        "s_cmp_lg_u32 s96, 0\n\t"
        "s_cbranch_scc1 .Latt_loop_%=\n\t"
        "s_nop 1\n\t"
        "v_mfma_f32_32x32x16_bf16 v[18:33], v[170:173], v[110:113], v[18:33]\n\t"
        "v_smfmac_f32_16x16x64_bf16 v[138:141], v[130:133], v[106:113], v174\n\t"
        "s_waitcnt lgkmcnt(0)\n\ts_nop 10\n\t"
        ".purgem ATT_RD\n\t.purgem ATT_E4\n\t.purgem ATT_C4\n\t.purgem ATT_STEP\n\t.purgem ATT_TILE\n\t.purgem ATT_STAGE"
        : "+{v[2:17]}"(oA), "+{v[18:33]}"(oB), "+{v[66:81]}"(sA), "+{v[106:113]}"(pB), "+{v[134:137]}"(lsA), "+{v[138:141]}"(lsB),
          "+{v[142:145]}"(k00), "+{v[146:149]}"(k01), "+{v[150:153]}"(k10), "+{v[154:157]}"(k11),
          "+{v[158:161]}"(v00), "+{v[162:165]}"(v01), "+{v[166:169]}"(v10), "+{v[170:173]}"(v11)
        : "{v[34:49]}"(nmA), "{v[50:65]}"(nmB), "{v[114:117]}"(qA0), "{v[118:121]}"(qA1), "{v[122:125]}"(qB0), "{v[126:129]}"(qB1),
          "{v[130:133]}"(sel), "{v174}"(sidx), "{v175}"(a0)
        : "memory", "scc", "s96", "v176", "v82", "v83", "v84", "v85", "v86", "v87", "v88", "v89", "v90", "v91", "v92", "v93", "v94", "v95",
          "v96", "v97", "v98", "v99", "v100", "v101", "v102", "v103", "v104", "v105");
    STAMP(9);
    float lA = lsA[0] + lsA[1], lB = lsB[0] + lsB[1];
    auto store = [&] {
#pragma unroll
    for (int u = 0; u < 2; ++u) {
        const float inv = __builtin_amdgcn_rcpf(u ? lB : lA), sub = -ob * inv;
        const f32x16 e = (u ? oB : oA) * inv + sub;
        st16o<0>((char*)ofrag + u * 65536, dst, u32x4{pack_bf16(e[0], e[1]), pack_bf16(e[2], e[3]), pack_bf16(e[8], e[9]), pack_bf16(e[10], e[11])});
        st16o<512>((char*)ofrag + u * 65536, dst, u32x4{pack_bf16(e[4], e[5]), pack_bf16(e[6], e[7]), pack_bf16(e[12], e[13]), pack_bf16(e[14], e[15])});
    }
    };
    store();
    {
        const f32x16 os = oA + oB;
        const float osum = (((os[0] + os[1]) + (os[2] + os[3])) + ((os[4] + os[5]) + (os[6] + os[7]))) +
                           (((os[8] + os[9]) + (os[10] + os[11])) + ((os[12] + os[13]) + (os[14] + os[15])));
        const bool bad = !(fmaxf(lA, lB) < 1.0e24f) || !(lA > 1.0e-24f) || !(lB > 1.0e-24f) || !(fabsf(osum) < 3.0e38f);
                if (__any(bad)) {
            const __amdgpu_buffer_rsrc_t rk = __builtin_amdgcn_make_buffer_rsrc((char*)kvf + (size_t)head * 262144, 0, 262144, 0x00020000);
            oA = z16; oB = z16; lA = 0.f; lB = 0.f; ob = 0.f;
            float mA = -INFINITY, mB = -INFINITY;
#pragma unroll 1
            for (int t = 0; t < 64; ++t) {
                bf16x8 f[4];
#pragma unroll
                for (int i = 0; i < 4; ++i) f[i] = __builtin_bit_cast(bf16x8, __builtin_amdgcn_raw_buffer_load_b128(rk, lane * 16 + i * 1024, t * 4096, 16));
                auto upd = [&](bf16x8 q0, bf16x8 q1, f32x16& o, float& l, float& m) {
                    f32x16 s = MFMA32(f[0], q0, z16); s = MFMA32(f[1], q1, s);
                    float tm = s[0];
#pragma unroll
                    for (int r = 1; r < 16; ++r) tm = fmaxf(tm, s[r]);
                    tm = max_h(tm);
                    const float mn = fmaxf(m, tm);
                    const float alpha = __builtin_amdgcn_exp2f(m - mn);
                    m = mn;
#pragma unroll
                    for (int r = 0; r < 16; ++r) { s[r] = __builtin_amdgcn_exp2f(s[r] - mn); o[r] *= alpha; }
                    const u32x4 p0 = {pack_bf16(s[0], s[1]), pack_bf16(s[2], s[3]), pack_bf16(s[4], s[5]), pack_bf16(s[6], s[7])};
                    const u32x4 p1 = {pack_bf16(s[8], s[9]), pack_bf16(s[10], s[11]), pack_bf16(s[12], s[13]), pack_bf16(s[14], s[15])};
                    f32x4 t4 = {0, 0, 0, 0};
                    o = MFMA32(f[2], __builtin_bit_cast(bf16x8, p0), o);
                    o = MFMA32(f[3], __builtin_bit_cast(bf16x8, p1), o);
                    t4 = ROWSUM(p0, p1, t4);
                    l = l * alpha + (t4[0] + t4[1]);
                };
                upd(qA0, qA1, oA, lA, mA);
                upd(qB0, qB1, oB, lB, mB);
            }
            store();
        }
    }
}

DI void ln_row(const u32x4 (&v)[2], __hip_bfloat16* __restrict__ Y, unsigned off) {
    float s1 = 0.f, s2 = 0.f;
#pragma unroll
    for (int e = 0; e < 8; ++e) s1 = dot2bf(v[e >> 2][e & 3], 0x3f803f80u, s1);
    const float mean = wave_sum(s1) * (1.f / 1024.f);
#pragma unroll
    for (int e = 0; e < 8; ++e) {
        const float d0 = bf_lo(v[e >> 2][e & 3]) - mean, d1 = bf_hi(v[e >> 2][e & 3]) - mean;
        s2 += d0 * d0 + d1 * d1;
    }
    const float mb = round_bf16(mean);
    const float rden = __builtin_amdgcn_rcpf(round_bf16(__builtin_amdgcn_sqrtf(round_bf16(round_bf16(wave_sum(s2) * (1.f / 1024.f)) + 1e-5f))));
#pragma unroll
    for (int hh = 0; hh < 2; ++hh) {
        u32x4 y;
#pragma unroll
        for (int e = 0; e < 4; ++e) {
            const unsigned d = pack_bf16(bf_lo(v[hh][e]) - mb, bf_hi(v[hh][e]) - mb);
            y[e] = pack_bf16(bf_lo(d) * rden, bf_hi(d) * rden);
        }
        st16((char*)Y + hh * 131072, off, y);
    }
}

extern "C" __global__ __launch_bounds__(512, 2) void mega_kernel(const __hip_bfloat16* x, const __hip_bfloat16* wqkvA,
                                                            const __hip_bfloat16* woA, const __hip_bfloat16* wqkvB,
                                                            const __hip_bfloat16* woB, __hip_bfloat16* out,
                                                            char* ws) {
    char* const smem = smem_base;
    typedef __hip_bfloat16 bf;
    const int w8 = __builtin_amdgcn_readfirstlane(threadIdx.x >> 6);
    if (w8 == 7 && (blockIdx.x >> 3) < 10) code_touch(blockIdx.x >> 3, threadIdx.x & 63);
    asm volatile("" :: "s"(x), "s"(wqkvA), "s"(woA), "s"(wqkvB), "s"(woB), "s"(out), "s"(ws));
    STAMP_DECL; STAMPR(14); STAMP(0);
    u64 tag;
    {
        const int lane = threadIdx.x & 63;
        const Ids I = ids();
        const unsigned off = (unsigned)((I.po * 128 + I.co * 8 + w8) * 2048 + lane * 16);
        u32x4 v[2];
        u32x2 ep;
        asm volatile("global_load_dwordx4 %0, %3, %4\n\tglobal_load_dwordx4 %1, %3, %4 offset:1024\n\tglobal_load_dwordx2 %2, %5, %6 sc1"
                     : "=&v"(v[0]), "=&v"(v[1]), "=&v"(ep) : "v"(off), "s"(x), "v"(0), "s"(ws + WS_EPOCH) : "memory");
        if (w8 == 7) {
            const int l = lane & 15, k = l % 5;
            const unsigned o = (l >= 10 || k == 3) ? flag_off(l, I.cq, I.pq, 0) : (k == 0 || k == 2) ? flag_off(l, I.pq, I.cq, 0) : flag_off(l, I.po, I.co, 0);
            touch_at(ws + WS_FLAG, o);
        } else if (w8 >= 4) touch(wqkvA, (w8 - 4) * 256 + blockIdx.x, lane);
        if (w8 >= 4) asm volatile("s_waitcnt vmcnt(2)" ::: "memory"); else asm volatile("s_waitcnt vmcnt(1)" ::: "memory");
        asm volatile("" : "+v"(v[0]), "+v"(v[1]));
        ln_row(v, (bf*)(ws + WS_Y0), (unsigned)(I.po * 262144 + (lane >> 3) * 16384 + (I.co * 8 + w8) * 128 + (lane & 7) * 16));
        if (w8 >= 4) asm volatile("s_waitcnt vmcnt(3)" ::: "memory"); else asm volatile("s_waitcnt vmcnt(2)" ::: "memory");
        asm volatile("" : "+v"(ep));
        tag = (((u64)(unsigned)__builtin_amdgcn_readfirstlane(ep[1]) << 32) | (unsigned)__builtin_amdgcn_readfirstlane(ep[0])) + 1;
    }
    if (w8 >= 4) {
        int w = w8 & 3;
#pragma unroll 1
        for (int it = 0; it < 2; ++it) {
            const int lane = lane_id();
            asm volatile("" : "+s"(x), "+s"(wqkvA), "+s"(woA), "+s"(wqkvB), "+s"(woB), "+s"(ws), "+s"(w));
            const unsigned lds0 = (unsigned)(uintptr_t)smem;
            u32x4 cb[8];
            const unsigned wv = wo_off(w, lane), rv = res_off(true, it, lane), cv = colsum_off(w, lane);
            qkv_loader<true>(smem, w, ws, it * 5, tag, (const bf*)(ws + (it ? WS_X2 : WS_Y0)), wqkvA, (const float*)(ws + WS_ST + 262144),
                             ws + WS_CSP, it == 0, woA, [&] {
                if (w < 3) {
                    colsum_load(cb, wqkvB, cv);
                    asm volatile("s_waitcnt vmcnt(8)" ::: "memory");
                } else {
                    touch(woA, blockIdx.x, lane);
                    asm volatile("s_waitcnt vmcnt(1)" ::: "memory");
                }
            }, [&](int part) {
                if (part == 0) wo_load(woA, wv, 29, 32, lds0, w); else wo_load(woA, wv, 25, 29, lds0, w);
            } SP_ARG);
            STAMP(3);
            u32x4 res[6];
            res_load(res, it ? (const void*)(ws + WS_X2) : x, true, it, rv, w);
            if (it) __builtin_amdgcn_s_sleep(8);
            wo_load(woA, wv, 0, 7, lds0, w);
            BAR();
            wo_load(woA, wv, 7, 25, lds0, w);
            STAMP(4);
            out_handoff(smem, 147456, res, w, ws, it * 5 + 1, tag, lane, cb, it ? -1 : 1);
            STAMP(5);
            qkv_loader<false>(smem, w, ws, it * 5 + 2, tag, (const bf*)(ws + (it ? WS_X3 : WS_X1)), wqkvB, (const float*)(ws + WS_ST + it * 524288), ws + WS_CSP + 196608,
                              false, nullptr, [] {}, [](int) {} SP_ARG);
            STAMP(8);
            attn_loader(smem, w, ws, it * 5 + 3, tag, (const bf*)(ws + WS_KVF + it * 8 * MBy), woB, ws + (it ? WS_X3 : WS_X1),
                        it ? nullptr : wqkvA SP_ARG);
            STAMP(13); STAMPR(15); STAMP_FLUSH(it);
        }
        if (blockIdx.x == 0 && w == 0 && lane_id() == 0) __hip_atomic_store((g_u64ptr)(ws + WS_EPOCH), tag, __ATOMIC_RELAXED, __HIP_MEMORY_SCOPE_AGENT);
    } else {
        __builtin_amdgcn_s_setprio(2);
#pragma unroll 1
        for (int it = 0; it < 2; ++it) {
            u32x4 q4[4];
            asm volatile("" : "+s"(ws), "+s"(wqkvA), "+s"(wqkvB));
            qkv_compute<true>(smem, w8, wqkvA, q4, nullptr, (bf*)(ws + WS_OF + it * 8 * MBy), it == 0,
                              ws + WS_ST + 262144, ws + WS_WN + blockIdx.x * 16 SP_ARG);
            STAMP(2);
            out_compute<true>(smem, w8, (const bf*)(ws + WS_OF + it * 8 * MBy),
                              (bf*)(ws + (it ? WS_X3 : WS_X1)), (float*)(ws + WS_ST + it * 524288) SP_ARG);
            STAMP(6);
            qkv_compute<false>(smem, w8, wqkvB, q4, (bf*)(ws + WS_KVF + it * 8 * MBy), nullptr, false,
                               ws + WS_ST + it * 524288, ws + WS_WN + 4096 + blockIdx.x * 16 SP_ARG);
            attn_compute(smem, w8, q4, (const bf*)(ws + WS_KVF + it * 8 * MBy), (bf*)(ws + WS_OF + (it * 8 + 4) * MBy) SP_ARG);
            out_compute<false>(smem, w8, (const bf*)(ws + WS_OF + (it * 8 + 4) * MBy),
                               it ? out : (bf*)(ws + WS_X2), it ? nullptr : (float*)(ws + WS_ST + 262144) SP_ARG);
            STAMP(13); STAMPR(15); STAMP_FLUSH(it);
        }
    }
}

extern "C" __global__ void zz_code_end_kernel() {}

extern "C" void kernel_launch(void* const* d_in, const int* in_sizes, int n_in, void* d_out, int out_size, void* d_ws,
                              size_t ws_size, hipStream_t stream) {
    typedef __hip_bfloat16 bf;
    (void)hipFuncSetAttribute((const void*)mega_kernel, hipFuncAttributeMaxDynamicSharedMemorySize, CH_LDS);
    mega_kernel<<<256, 512, CH_LDS, stream>>>((const bf*)d_in[0], (const bf*)d_in[1], (const bf*)d_in[2], (const bf*)d_in[4], (const bf*)d_in[5],
                                              (bf*)d_out, (char*)d_ws);
}
```

```cpp
#include <hip/hip_runtime.h>
#include <hip/hip_bf16.h>
#include <stdint.h>

typedef __attribute__((ext_vector_type(8))) short bf16x8;
typedef __attribute__((ext_vector_type(4))) short s16x4;
typedef __attribute__((ext_vector_type(4))) float f32x4;
typedef __attribute__((ext_vector_type(2))) float f32x2;
typedef __attribute__((ext_vector_type(16))) float f32x16;
typedef __attribute__((ext_vector_type(4))) unsigned u32x4;
typedef __attribute__((ext_vector_type(2))) unsigned u32x2;
typedef __attribute__((ext_vector_type(2))) __bf16 bf16x2_t;
typedef __attribute__((ext_vector_type(8))) __bf16 bf8_t;
typedef __attribute__((ext_vector_type(16))) __bf16 bf16v_t;
typedef __attribute__((address_space(3))) const char* lds_cptr;
typedef __attribute__((address_space(3))) char* lds_ptr;

#define DI __device__ __forceinline__
#define MFMA16(a, b, c) __builtin_amdgcn_mfma_f32_16x16x32_bf16((a), (b), (c), 0, 0, 0)
#define MFMA32(a, b, c) __builtin_amdgcn_mfma_f32_32x32x16_bf16((a), (b), (c), 0, 0, 0)
#define SBAR() __builtin_amdgcn_sched_barrier(0)
#define STAMP(i)
#define STAMPR(i)
#define STAMP_DECL
#define STAMP_FLUSH(it)
#define SP_PARAM
#define SP_ARG
typedef unsigned long long u64;
typedef __attribute__((ext_vector_type(32))) unsigned u32x32;

constexpr float QSCALE = 0.17677669529663687f * 1.4426950408889634f;
constexpr float ATT_SCALE = 0.17677669529663687f;
constexpr float LOG2E = 1.4426950408889634f;
constexpr float NEG_MASK = -1e9f / ATT_SCALE;

DI int lane_id() { int l; asm volatile("v_mbcnt_lo_u32_b32 %0, -1, 0\n\tv_mbcnt_hi_u32_b32 %0, -1, %0" : "=v"(l)); return l; }
DI unsigned pack_bf16(float lo, float hi) {
    f32x2 v = {lo, hi};
    bf16x2_t r = __builtin_convertvector(v, bf16x2_t);
    return __builtin_bit_cast(unsigned, r);
}
DI float bf_lo(unsigned u) { return __uint_as_float(u << 16); }
DI float bf_hi(unsigned u) { return __uint_as_float(u & 0xffff0000u); }
DI float round_bf16(float x) { return bf_lo(pack_bf16(x, 0.f)); }
DI void ln_stats(float sum, float sumsq, float& mean_out, float& rstd_out) {
    float mean;
    asm("v_mul_f32 %0, 0x3a800000, %1" : "=v"(mean) : "v"(sum));
    const float var = fmaxf(__builtin_fmaf(-mean, mean, sumsq * (1.f / 1024.f)), 0.f);
    mean_out = round_bf16(mean);
    rstd_out = __builtin_amdgcn_rcpf(round_bf16(__builtin_amdgcn_sqrtf(round_bf16(round_bf16(var) + 1e-5f))));
}

DI void glds16(const void* sbase, unsigned voff, unsigned ldsaddr) {
    asm volatile("s_mov_b32 m0, %0\n\ts_nop 0\n\tglobal_load_lds_dwordx4 %1, %2"
                 :: "s"(ldsaddr), "v"(voff), "s"(sbase) : "memory");
}
DI void glds16c(const void* sbase, unsigned voff, unsigned ldsaddr) {
    asm volatile("s_mov_b32 m0, %0\n\ts_nop 0\n\tglobal_load_lds_dwordx4 %1, %2 sc1"
                 :: "s"(ldsaddr), "v"(voff), "s"(sbase) : "memory");
}
DI void st16(void* sbase, unsigned voff, u32x4 v) {
    asm volatile("global_store_dwordx4 %0, %1, %2 sc1\n\ts_nop 1" :: "v"(voff), "v"(v), "s"(sbase) : "memory");
}
template <int OFF> DI void st16o(void* sbase, unsigned voff, u32x4 v) {
    asm volatile("global_store_dwordx4 %0, %1, %2 offset:%3 sc1\n\ts_nop 1" :: "v"(voff), "v"(v), "s"(sbase), "n"(OFF) : "memory");
}
DI bf16x8 lds_b128(lds_cptr p) { return *(const __attribute__((address_space(3))) bf16x8*)p; }
DI s16x4 lds_tr(lds_cptr p) {
    return __builtin_amdgcn_ds_read_tr16_b64_v4i16((__attribute__((address_space(3))) s16x4*)p);
}
DI bf16x8 cat8(s16x4 a, s16x4 b) { return __builtin_shufflevector(a, b, 0, 1, 2, 3, 4, 5, 6, 7); }
DI float dot2bf(unsigned a, unsigned b, float c) {
    return __builtin_amdgcn_fdot2_f32_bf16(__builtin_bit_cast(bf16x2_t, a), __builtin_bit_cast(bf16x2_t, b), c, false);
}
DI float sum_g(float v) {
    auto a = __builtin_amdgcn_permlane16_swap(__float_as_uint(v), __float_as_uint(v), false, false);
    v = __uint_as_float(a[0]) + __uint_as_float(a[1]);
    auto b = __builtin_amdgcn_permlane32_swap(__float_as_uint(v), __float_as_uint(v), false, false);
    return __uint_as_float(b[0]) + __uint_as_float(b[1]);
}
DI float wave_sum(float v) {
    v += __builtin_bit_cast(float, __builtin_amdgcn_update_dpp(0, __builtin_bit_cast(int, v), 0xB1, 0xF, 0xF, true));
    v += __builtin_bit_cast(float, __builtin_amdgcn_update_dpp(0, __builtin_bit_cast(int, v), 0x4E, 0xF, 0xF, true));
    v += __builtin_bit_cast(float, __builtin_amdgcn_update_dpp(0, __builtin_bit_cast(int, v), 0x141, 0xF, 0xF, true));
    v += __builtin_bit_cast(float, __builtin_amdgcn_update_dpp(0, __builtin_bit_cast(int, v), 0x140, 0xF, 0xF, true));
    return sum_g(v);
}
DI unsigned wave_min(unsigned v) {
    v = min(v, (unsigned)__builtin_amdgcn_update_dpp(0, (int)v, 0xB1, 0xF, 0xF, true));
    v = min(v, (unsigned)__builtin_amdgcn_update_dpp(0, (int)v, 0x4E, 0xF, 0xF, true));
    v = min(v, (unsigned)__builtin_amdgcn_update_dpp(0, (int)v, 0x141, 0xF, 0xF, true));
    v = min(v, (unsigned)__builtin_amdgcn_update_dpp(0, (int)v, 0x140, 0xF, 0xF, true));
    const auto a = __builtin_amdgcn_permlane16_swap(v, v, false, false);
    v = min(a[0], a[1]);
    const auto b = __builtin_amdgcn_permlane32_swap(v, v, false, false);
    return min(b[0], b[1]);
}
DI float amax3(float a, float b, float c) { float r; asm("v_max3_f32 %0, %1, %2, %3" : "=v"(r) : "v"(a), "v"(b), "v"(c)); return r; }
DI float max2f(float a, float b) { return __builtin_amdgcn_fmed3f(a, b, INFINITY); }
DI float max_h(float v) {
    auto b = __builtin_amdgcn_permlane32_swap(__float_as_uint(v), __float_as_uint(v), false, false);
    return max2f(__uint_as_float(b[0]), __uint_as_float(b[1]));
}

constexpr unsigned OFF_DUMP = 145408;
extern __shared__ __attribute__((aligned(16))) char smem_base[];
DI void touch_at(const void* p, unsigned off) {
    asm volatile("s_mov_b32 m0, %0\n\ts_nop 0\n\tglobal_load_lds_dword %1, %2"
                 :: "s"((unsigned)(uintptr_t)smem_base + OFF_DUMP), "v"(off), "s"(p) : "memory");
}
DI void touch(const void* p, int slice, int lane) { touch_at(p, (unsigned)(slice * 64 + lane) * 128u); }
DI void code_touch(int slice, int lane) {
    unsigned long long c0, c1;
    asm volatile("s_getpc_b64 s[96:97]\n\ts_add_u32 s96, s96, mega_kernel@rel32@lo+4\n\ts_addc_u32 s97, s97, mega_kernel@rel32@hi+12\n\ts_mov_b64 %0, s[96:97]\n\t"
                 "s_getpc_b64 s[96:97]\n\ts_add_u32 s96, s96, zz_code_end_kernel@rel32@lo+4\n\ts_addc_u32 s97, s97, zz_code_end_kernel@rel32@hi+12\n\ts_mov_b64 %1, s[96:97]"
                 : "=s"(c0), "=s"(c1) :: "s96", "s97", "scc");
    touch_at((const void*)c0, min((unsigned)(slice * 64 + lane) * 128u, (unsigned)(c1 - c0)));
}

#define BAR() asm volatile("s_barrier" ::: "memory")
#define WAIT_LGKM() do { SBAR(); __builtin_amdgcn_s_waitcnt(0xC07F); SBAR(); } while (0)
#define WAIT_LGKM_BAR() do { SBAR(); __builtin_amdgcn_s_waitcnt(0xC07F); asm volatile("s_barrier" ::: "memory"); SBAR(); } while (0)
#define VM_BAR(n) asm volatile("s_waitcnt vmcnt(" #n ")\n\ts_barrier" ::: "memory")
constexpr int CH_LDS = 163840;
typedef __attribute__((address_space(3))) unsigned* lds_uptr;
typedef __attribute__((address_space(1))) u64* g_u64ptr;

constexpr size_t MBy = 1 << 20;
constexpr size_t WS_X3 = 0, WS_X2 = 4 * MBy, WS_X1 = 8 * MBy, WS_OF = 12 * MBy  , WS_KVF = 36 * MBy  ,
                 WS_ST = 52 * MBy  , WS_CSP = 54 * MBy  , WS_FLAG = 55 * MBy  ,
                 WS_WN = WS_CSP + 458752  , WS_EPOCH = WS_CSP + 524288, WS_Y0 = 56 * MBy;
DI unsigned flag_off(int set, int group, int consumer, int producer) { return (unsigned)(set * 65536 + group * 4096 + consumer * 256 + producer * 8); }
DI void flag_store(char* ws, int set, int group, int producer, u64 tag, int lane) {
    asm volatile("" : "+s"(tag));
    if (lane < 16) __hip_atomic_store((g_u64ptr)(ws + WS_FLAG + flag_off(set, group, lane, producer)), tag, __ATOMIC_RELAXED, __HIP_MEMORY_SCOPE_AGENT);
}
DI void flag_poll(const char* ws, unsigned off, u64 tag) {
    asm volatile("" : "+s"(tag));
#pragma unroll 1
    for (int spin = 0; spin < (1 << 16); ++spin) {
        const u64 v = __hip_atomic_load((g_u64ptr)(ws + WS_FLAG + off), __ATOMIC_RELAXED, __HIP_MEMORY_SCOPE_AGENT);
        if (__all(v == tag)) break;
        __builtin_amdgcn_s_sleep(1);
    }
    asm volatile("" ::: "memory");
}
template <bool DRAIN, bool ALLPOLL>
DI void signal_poll(int follower, const char* ws, unsigned soff, unsigned poff, u64 pmask, u64 tag, u64& land) {
    u64 tagv, zero;
    asm volatile("s_not_b64 s[96:97], %[ts]\n\tv_mov_b64 %[tag], %[ts]\n\tv_mov_b64 %[zero], 0\n\tv_mov_b64 %[land], s[96:97]\n\t"
                 ".if %[dr]\n\ts_waitcnt vmcnt(0)\n\t.endif\n\ts_barrier\n\t"
                 "s_cmp_lg_u32 %[fl], 0\n\ts_cbranch_scc1 .Lsig_fol_%=\n\t"
                 "s_mov_b64 exec, 0xffff\n\t"
                 "global_store_dwordx2 %[so], %[tag], %[base] sc1\n"
                 ".if %[ap]\n.Lsig_fol_%=:\n.endif\n\t"
                 "s_mov_b64 exec, %[pm]\n\t"
                 "s_movk_i32 s96, 0x7fff\n"
                 ".Lsig_loop_%=:\n\t"
                 "global_atomic_or_x2 %[land], %[po], %[zero], %[base] sc0 sc1\n\t"
                 "s_sleep 1\n\t"
                 "s_waitcnt vmcnt(3)\n\t"
                 "v_cmp_ne_u64 vcc, %[land], %[tag]\n\t"
                 "s_sub_u32 s96, s96, 1\n\t"
                 "s_cbranch_scc1 .Lsig_out_%=\n\t"
                 "s_cbranch_vccnz .Lsig_loop_%=\n"
                 ".Lsig_out_%=:\n\t"
                 "s_mov_b64 exec, -1\n"
                 ".if !%[ap]\n.Lsig_fol_%=:\n.endif"
                 : [land] "=&v"(land), [tag] "=&v"(tagv), [zero] "=&v"(zero)
                 : [fl] "s"(follower), [so] "v"(soff), [po] "v"(poff), [ts] "s"(tag), [base] "s"(ws + WS_FLAG), [pm] "s"(pmask), [ap] "n"(ALLPOLL ? 1 : 0), [dr] "n"(DRAIN ? 1 : 0)
                 : "memory", "scc", "vcc", "s96", "s97");
}
struct Ids { int pq, cq, po, co; };
DI Ids ids() {
    int bid = blockIdx.x; asm volatile("" : "+s"(bid));
    const int xcd = bid & 7, idx = bid >> 3;
    int pq, cq, po, co;
#define IDS_BODY pq = (xcd & 1) * 8 + (idx & 7); cq = (xcd >> 1) * 4 + (idx >> 3); po = (xcd & 3) * 4 + (idx & 3); co = (xcd >> 2) * 8 + (idx >> 2);
    IDS_BODY
    return Ids{pq, cq, po, co};
}

DI void slow_warm(unsigned dump, const void* W, int w, int lane) {
    int bid = blockIdx.x; asm volatile("" : "+s"(bid));
    const int xcd = bid & 7, idx = bid >> 3;
    const int sc = (3 - (int)((uintptr_t)W >> 7)) & 7;
    if ((sc >> 2) != ((xcd >> 1) & 1)) return;
    const char* src = (const char*)W + (idx + 32 * w) * 49152 + (sc + (xcd >> 2) * 8) * 128;
#pragma unroll
    for (int G = 0; G < 3; ++G) glds16(src + G * 2048, (unsigned)((lane >> 3) * 6144 + (lane & 7) * 16), dump);
}

template <int V> struct IC { static constexpr int value = V; };
template <bool LOCAL> struct QC {
    static constexpr int AROWS = LOCAL ? 144 : 128;
    static constexpr int ABYTES = AROWS * 128;
    static constexpr int STAGE = ABYTES + 24576;
    static constexpr int OFF_MEAN = 143360;
    static constexpr int OFF_RSTD = OFF_MEAN + 576;
    static constexpr int OFF_CS = OFF_RSTD + 576;
};
constexpr int OFF_EX = 131072;

DI unsigned colsum_off(int G, int lane) {
    const Ids I = ids();
    unsigned off = (unsigned)(I.pq * 64 + (lane >> 3)) * 6144u + (unsigned)(G * 2048 + I.cq * 128 + (lane & 7) * 16);
    asm volatile("" : "+v"(off));
    return off;
}
DI void colsum_load(u32x4 (&c)[8], const void* W, unsigned off) {
#define CS_NEXT "s_add_u32 s96, s96, 0xc000\n\ts_addc_u32 s97, s97, 0\n\t"
    asm volatile("s_cmp_eq_u64 %9, 0\n\ts_cbranch_scc1 .Lcs_skip_%=\n\ts_mov_b64 s[96:97], %9\n\t"
                 "global_load_dwordx4 %0, %8, s[96:97]\n\t" CS_NEXT "global_load_dwordx4 %1, %8, s[96:97]\n\t" CS_NEXT
                 "global_load_dwordx4 %2, %8, s[96:97]\n\t" CS_NEXT "global_load_dwordx4 %3, %8, s[96:97]\n\t" CS_NEXT
                 "global_load_dwordx4 %4, %8, s[96:97]\n\t" CS_NEXT "global_load_dwordx4 %5, %8, s[96:97]\n\t" CS_NEXT
                 "global_load_dwordx4 %6, %8, s[96:97]\n\t" CS_NEXT "global_load_dwordx4 %7, %8, s[96:97]\n"
                 ".Lcs_skip_%=:"
                 : "=&v"(c[0]), "=&v"(c[1]), "=&v"(c[2]), "=&v"(c[3]), "=&v"(c[4]), "=&v"(c[5]), "=&v"(c[6]), "=&v"(c[7])
                 : "v"(off), "s"(W) : "memory", "scc", "s96", "s97");
}
DI void colsum_store(u32x4 (&c)[8], char* ws, int mat, int G, int lane) {
    asm volatile("" : "+v"(c[0]), "+v"(c[1]), "+v"(c[2]), "+v"(c[3]), "+v"(c[4]), "+v"(c[5]), "+v"(c[6]), "+v"(c[7]));
    const Ids I = ids();
    const int m = lane & 15;
    const unsigned one = (m & 1) ? 0xbf800000u : 0xbf80u;
    const u32x4 sl = {(m >> 1) == 0 ? one : 0u, (m >> 1) == 1 ? one : 0u, (m >> 1) == 2 ? one : 0u, (m >> 1) == 3 ? one : 0u};
    f32x4 d = {0, 0, 0, 0};
#pragma unroll
    for (int i = 0; i < 8; ++i) d = MFMA16(__builtin_bit_cast(bf16x8, sl), __builtin_bit_cast(bf16x8, c[i]), d);
#pragma unroll
    for (int r = 0; r < 4; ++r)
        d[r] += __int_as_float(__builtin_amdgcn_update_dpp(0, __float_as_int(d[r]), 0x128, 0xF, 0xF, true));
    if (lane < 32 && (lane & 8) == 0)
        st16(ws + WS_CSP + mat * 196608, (unsigned)((I.cq * 16 + I.pq) * 768 + G * 256 + (lane & 7) * 32 + (lane >> 4) * 16), __builtin_bit_cast(u32x4, d));
    typedef __attribute__((ext_vector_type(2))) unsigned short u16x2;
    u16x2 mn = {0x7fff, 0x7fff};
#pragma unroll
    for (int e = 0; e < 4; ++e) {
        u16x2 mx = {0, 0};
#pragma unroll
        for (int i = 0; i < 8; ++i) { const unsigned u = c[i][e] & 0x7fff7fffu; mx = __builtin_elementwise_max(mx, __builtin_bit_cast(u16x2, u)); }
        mn = __builtin_elementwise_min(mn, mx);
    }
    const unsigned wn = wave_min((unsigned)min(mn[0], mn[1])) << 16;
    if (lane == 0) __hip_atomic_store((__attribute__((address_space(1))) unsigned*)(ws + WS_WN + mat * 4096 + blockIdx.x * 16 + G * 4), wn,
                                      __ATOMIC_RELAXED, __HIP_MEMORY_SCOPE_AGENT);

}

template <bool LOCAL>
struct WFirst {
    unsigned v[2];
    DI WFirst(int w, int ct, int lane) {
#pragma unroll
        for (int i = 0; i < 2; ++i) {
            const int k = 8 * i + (lane >> 3), pos = lane & 7;
            v[i] = (unsigned)k * 6144u + (unsigned)(w * 1024 + ct * 64 + ((pos >> 1) ^ (((k >> 1) & 1) | (i << 1))) * 16 + (pos & 1) * 8) * 2u;
        }
        asm volatile("" : "+v"(v[0]), "+v"(v[1]));
    }
    DI void issue(const void* W, unsigned lds0, int w, int i0, int i1) const {
#pragma unroll
        for (int i = i0; i < i1; ++i)
            glds16((const char*)W + (size_t)(i >> 1) * (16 * 6144), v[i & 1], lds0 + (i >> 3) * QC<LOCAL>::STAGE + QC<LOCAL>::ABYTES + (w * 8 + (i & 7)) * 1024);
    }
};

template <bool LOCAL, class F, class F2>
DI void qkv_loader(char* smem, int w, char* ws, int set, u64 tag, const __hip_bfloat16* __restrict__ X, const __hip_bfloat16* __restrict__ W,
                   const float* __restrict__ statp, const char* __restrict__ csp, bool ident, const void* nextW, F tail, F2 mid SP_PARAM) {
    typedef QC<LOCAL> C;
    constexpr int NT = 16;
    const int lane = lane_id();
    asm volatile("" : "+s"(w));
    const int idx = blockIdx.x >> 3;
    const Ids I = ids();
    const int panel = I.pq, ct = I.cq;
    const int R0 = panel * 128;
    const unsigned lds0 = (unsigned)(uintptr_t)smem;
    auto grow = [&](int i) -> int {
        if (!LOCAL) return R0 + i;
        if (i < 128) { const int t = (i >> 4) & 3, a = i & 15; return R0 + (i & 64) + 2 * (16 * (t & 1) + a) + (t >> 1); }
        const int a = i & 15; const int r = R0 - 16 + 2 * (a & 7) + (a >> 3);
        return r < 0 ? 0 : r;
    };
    unsigned voffA[5], voffB[6];
#pragma unroll
    for (int i = 0; i < 6; ++i) {
        const int j = w + 4 * i, G = j >> 3, k = 8 * (j & 7) + (lane >> 3);
        const int pos = lane & 7, f = ((k >> 1) & 1) | (((k >> 3) & 1) << 1);
        const int seg = (pos >> 1) ^ f;
        voffB[i] = (unsigned)k * 6144u + (unsigned)(G * 1024 + ct * 64 + seg * 16 + (pos & 1) * 8) * 2u;
    }
    auto issueW = [&](int t, int buf) {
        const char* wb = (const char*)W + (size_t)t * (64 * 6144);
        const unsigned sb = lds0 + buf * C::STAGE;
#pragma unroll
        for (int i = 0; i < 6; ++i) glds16(wb, voffB[i], sb + C::ABYTES + (w + 4 * i) * 1024);
    };
    asm volatile("" : "+v"(voffB[0]), "+v"(voffB[1]), "+v"(voffB[2]), "+v"(voffB[3]), "+v"(voffB[4]), "+v"(voffB[5]));
    const WFirst<LOCAL> first(w, ct, lane);
    const int g = lane >> 4, r = lane & 15, pos = 32 * w + 2 * r + (g >> 1);
    const int lr = !LOCAL ? (pos & 64) + 2 * (pos & 31) + ((pos >> 5) & 1)
                          : (w == 0 && (g & 1)) ? 128 + (r & 7) + 8 * (g >> 1) : (pos & 64) + ((pos & 1) * 2 + ((pos >> 5) & 1)) * 16 + ((pos >> 1) & 15);
    unsigned so = (unsigned)(panel * 16384 + g * 1024 + w * 256 + r * 16), ho = (unsigned)(g * 1024 + (panel ? panel * 16384 - 15488 + (r & 7) * 16 : 0));
    unsigned co = (unsigned)((ct * 16 + g) * 768 + w * 192 + r * 12);
    lds_ptr mp = (lds_ptr)smem + C::OFF_MEAN + lr * 4, cp = (lds_ptr)smem + C::OFF_CS + w * 192 + r * 12 + ((g & 1) ? 8 : g * 2);
    unsigned fo = flag_off((LOCAL ? 13 : 10) + min(g, 2), ct, panel, r);
    asm volatile("" : "+v"(so), "+v"(ho), "+v"(co), "+v"(mp), "+v"(cp), "+v"(fo));
#pragma unroll
    for (int i = 0; i < 5; ++i) {
        const int j = (i < 4) ? (w + 4 * i) : (16 + (w & 1));
        const int row = 8 * j + (lane >> 3);
        const int c = (lane & 7) ^ ((row >> 1) & 7);
        const int gr = grow(row < C::AROWS ? row : 0);
        voffA[i] = (unsigned)((gr >> 7) * 262144 + (gr & 127) * 128 + c * 16);
        asm volatile("" : "+v"(voffA[i]));
    }
    auto issueA = [&](int t, int buf) {
        const char* xa = (const char*)X + t * 16384;
        const unsigned sb = lds0 + buf * C::STAGE;
        if (LOCAL && w < 2) glds16c(xa, voffA[4], sb + (16 + w) * 1024);
#pragma unroll
        for (int i = 0; i < 4; ++i) glds16c(xa, voffA[i], sb + (w + 4 * i) * 1024);
    };
    u64 land;
    if (w == 3) __builtin_amdgcn_s_setprio(3);
    signal_poll<LOCAL, false>(w - 3, ws, flag_off(set, I.po, lane & 15, I.co),
                              flag_off(set, (lane & 16) && panel ? panel - 1 : panel, ct, lane & 15),
                              !LOCAL ? 0xffffull : ident ? 0xc000ffffull : 0xffffffffull, tag, land);
    if (w < 3) first.issue(W, lds0, w, 0, 12);
    BAR();
    STAMP(LOCAL ? 1 : 6);
    issueA(0, 0);
    issueA(1, 1);
    if (w < 3) { first.issue(W, lds0, w, 12, 16); asm volatile("s_waitcnt vmcnt(8)" ::: "memory"); }
    else asm volatile("s_waitcnt vmcnt(4)" ::: "memory");
    BAR();
    asm volatile("" :: "v"(land));
    __builtin_amdgcn_s_setprio(0);
    STAMP(LOCAL ? 2 : 7);
    issueW(2, 2);
    issueA(2, 2);
    VM_BAR(10);
    asm volatile("global_load_dwordx2 %0, %1, %2 sc1" : "=&v"(land) : "v"(fo), "s"(ws + WS_FLAG) : "memory");
    issueA(3, 0); issueW(3, 0);
    int buf = 1;
#pragma unroll 1
    for (int t = 1; t < NT - 3; ++t) {
        VM_BAR(10);
        issueA(t + 3, buf); issueW(t + 3, buf);
        if (t == 10 && idx < 3) code_touch(idx * 4 + w, lane);
        if (LOCAL && t == 11)
            glds16(nextW, (unsigned)((idx + 32 * w) * 8 + (lane >> 3)) * 2048u +
                   (unsigned)(((I.co & 8) + ((3 - (int)((uintptr_t)nextW >> 7)) & 7)) * 128 + (lane & 7) * 16), lds0 + OFF_DUMP);
        buf = (buf == 2) ? 0 : buf + 1;
    }
    typedef __attribute__((ext_vector_type(3))) float f32x3;
    const bool halo = LOCAL && w == 0;
    asm volatile("" : "+v"(land));
    if (!ident) {
        if (!__all(land == tag)) flag_poll(ws, fo, tag);
        f32x3 cs[4];
        f32x4 sv[4], hv[4];
        typedef __attribute__((address_space(3))) float* lds_f;
        auto add = [](float a, float b) { float r; asm("v_add_f32 %0, %1, %2" : "=v"(r) : "v"(a), "v"(b)); return r; };
        auto fold32 = [](float a, float b) { asm("s_nop 1\n\tv_permlane32_swap_b32 %0, %1\n\tv_add_f32 %0, %0, %1" : "+v"(a), "+v"(b)); return a; };
        auto fold16 = [](float a, float b) { asm("s_nop 1\n\tv_permlane16_swap_b32 %0, %1\n\tv_add_f32 %0, %0, %1" : "+v"(a), "+v"(b)); return a; };
        auto sum4 = [&](auto (&v)[4], int e) { return add(add(v[0][e], v[1][e]), add(v[2][e], v[3][e])); };
#pragma unroll
        for (int i = 0; i < 4; ++i) asm volatile("global_load_dwordx3 %0, %1, %2 sc1" : "=&v"(cs[i]) : "v"(co), "s"(csp + i * 3072) : "memory");
        VM_BAR(14);
#pragma unroll
        for (int i = 0; i < 4; ++i) asm volatile("global_load_dwordx4 %0, %1, %2 sc1" : "=&v"(sv[i]) : "v"(so), "s"((const char*)statp + i * 4096) : "memory");
        if (LOCAL)
            asm volatile("s_cmp_lg_u32 %9, 0\n\ts_cbranch_scc1 .Lhv_skip_%=\n\t"
                         "global_load_dwordx4 %0, %4, %5 sc1\n\tglobal_load_dwordx4 %1, %4, %6 sc1\n\t"
                         "global_load_dwordx4 %2, %4, %7 sc1\n\tglobal_load_dwordx4 %3, %4, %8 sc1\n"
                         ".Lhv_skip_%=:" : "=&v"(hv[0]), "=&v"(hv[1]), "=&v"(hv[2]), "=&v"(hv[3])
                         : "v"(ho), "s"(statp), "s"(statp + 1024), "s"(statp + 2048), "s"(statp + 3072), "s"(w) : "memory", "scc");
        if (halo) asm volatile("s_waitcnt vmcnt(8)" ::: "memory"); else asm volatile("s_waitcnt vmcnt(4)" ::: "memory");
        asm volatile("" : "+v"(cs[0]), "+v"(cs[1]), "+v"(cs[2]), "+v"(cs[3]));
        const float c2 = sum4(cs, 2);
        *(lds_f)cp = fold16(fold32(sum4(cs, 0), sum4(cs, 1)), fold32(c2, c2));
        BAR();
        asm volatile("s_waitcnt vmcnt(0)" : "+v"(sv[0]), "+v"(sv[1]), "+v"(sv[2]), "+v"(sv[3]) :: "memory");
        mid(0);
        const float s1 = fold32(sum4(sv, 0), sum4(sv, 2)), s2 = fold32(sum4(sv, 1), sum4(sv, 3));
        float h1, h2;
        asm("v_mov_b32 %0, %2\n\tv_mov_b32 %1, %3" : "=&v"(h1), "=&v"(h2) : "v"(s1), "v"(s2));
        if (halo) {
            asm volatile("" : "+v"(hv[0]), "+v"(hv[1]), "+v"(hv[2]), "+v"(hv[3]));
            const float u0 = sum4(hv, 0), u1 = sum4(hv, 1), u2 = sum4(hv, 2), u3 = sum4(hv, 3);
            h1 = fold32(u0, panel ? u2 : u0); h2 = fold32(u1, panel ? u3 : u1);
        }
        float mean, rstd;
        ln_stats(fold16(s1, h1), fold16(s2, h2), mean, rstd);
        *(lds_f)mp = mean; *(lds_f)(mp + (C::OFF_RSTD - C::OFF_MEAN)) = rstd;
    } else { VM_BAR(10); tail(); BAR(); mid(0); }
    mid(1);
    WAIT_LGKM_BAR();
}

template <bool LOCAL>
DI void qkv_compute(char* smem, int w, const void* W, u32x4 (&qo4)[4], __hip_bfloat16* __restrict__ kvf, __hip_bfloat16* __restrict__ ofrag, bool ident,
                    const char* statp, const char* wnp SP_PARAM) {
    typedef QC<LOCAL> C;
    constexpr int NT = 16, ABYTES = C::ABYTES, STAGE = C::STAGE;
    const int lane = lane_id();
    asm volatile("" : "+s"(w));
    const int wm = w & 1, wn = w >> 1;
    const Ids I = ids();
    const int panel = I.pq, ct = I.cq;
    const int l15 = lane & 15, g = lane >> 4;
    const lds_cptr sm = (lds_cptr)smem;
    lds_ptr smw = (lds_ptr)smem;

    f32x4 acc[3][4][2];
    f32x4 acch[2];
    float cb;
    f32x4 bias;

    bf16x8 A[2][5][2];
    s16x4 B[3][2][2];
    lds_cptr pa[2], pb[2];
    auto rdA = [&](auto WM, int p, int i) { A[p][i >> 1][i & 1] = lds_b128(pa[i & 1] + ((i >> 1) < 4 ? (i >> 1) * 2048 : 16384 - WM.value * 8192)); };
    auto rdB = [&](int c, int i) { B[c % 3][i >> 1][i & 1] = lds_tr(pb[c & 1] + ((c >> 1) + 1) % 3 * 8192 + (i >> 1) * 4096 + (i & 1) * 512); };
    auto column = [&](auto WM, int p, int c, bool first, auto gap) {
        const int G = ((c >> 1) + 1) % 3, f = c & 1;
        const bool halo = LOCAL && G == 1 + WM.value;
#pragma unroll
        for (int n = 0; n < (halo ? 10 : 8); ++n) {
            const bf16x8 x = A[p][n >> 1][n & 1], y = cat8(B[c % 3][n & 1][0], B[c % 3][n & 1][1]);
            const bf16x8 u = G < 2 ? y : x, v = G < 2 ? x : y;
            f32x4& d = n < 8 ? acc[G][n >> 1][f] : acch[f];
            const f32x4 c0 = bias;
            if (!first || (n & 1)) asm volatile("v_mfma_f32_16x16x32_bf16 %0, %1, %2, %0" : "+v"(d) : "v"(u), "v"(v));
            else if (n == 8) asm volatile("v_mfma_f32_16x16x32_bf16 %0, %1, %2, 0" : "=&v"(d) : "v"(u), "v"(v));
            else asm volatile("v_mfma_f32_16x16x32_bf16 %0, %1, %2, %3" : "=&v"(d) : "v"(u), "v"(v), "v"(c0));
            SBAR();
            gap(n);
            SBAR();
        }
    };

    f32x4 cq[2], ck[2], mu4[5], rs4[5]; float cv[2], mu[5], rs[5];
    u32x4 qfr[4], kfr[4];
    u32x4 va[2][2];
    f32x4 mwin, mfirst;
    typedef __attribute__((address_space(3))) u32x4* lds_u4;
    const bool negpos = (wm == 0) && (panel == 0);
    VM_BAR(0);
    unsigned cm01 = 0, cm23 = 0;
    lds_cptr ka[2] = {sm, sm}, vo2[2] = {sm, sm};
    lds_u4 ex = (lds_u4)(smw + OFF_EX + wn * 6144) + lane;
    lds_cptr s1 = sm + C::OFF_MEAN + wm * 256 + l15 * 4, s4 = sm + C::OFF_MEAN + wm * 256 + g * 16;
    lds_cptr c1 = sm + C::OFF_CS + wn * 128 + l15 * 4, c4 = sm + C::OFF_CS + wn * 128 + g * 16;
    asm volatile("" : "+v"(s1), "+v"(s4), "+v"(c1), "+v"(c4));
    if (LOCAL) {
        unsigned keepv = negpos ? __float_as_uint(NEG_MASK) : 0u;
        asm volatile("" : "+s"(keepv));
        u32x4 mw, mf;
        unsigned prv[4];
#pragma unroll
        for (int r = 0; r < 4; ++r) {
            const int d = l15 - (4 * g + r);
            unsigned out = (unsigned)(((d << 28) >> 31));
            prv[r] = (unsigned)(d >> 31);
            asm volatile("" : "+v"(out), "+v"(prv[r]));
            mw[r] = out & 0xff800000u;
            mf[r] = (out & 0xff800000u) | (~out & keepv);
        }
        const lds_cptr e0 = sm + OFF_EX + wn * 6144;
        ka[0] = e0 + (wm ? lane : 256 + (lane ^ 8)) * 16;         ka[1] = e0 + (wm ? 64 + lane : 256 + lane) * 16;
        vo2[0] = e0 + (wm ? 128 + lane : 320 + (lane ^ 32)) * 16; vo2[1] = e0 + (wm ? 192 + lane : 320 + lane) * 16;
        cm01 = ~((prv[0] >> 16) | (prv[1] << 16)); cm23 = ~((prv[2] >> 16) | (prv[3] << 16));
        mwin = __builtin_bit_cast(f32x4, mw); mfirst = __builtin_bit_cast(f32x4, mf);
        asm volatile("" : "+v"(mwin), "+v"(mfirst), "+v"(cm01), "+v"(cm23), "+v"(ka[0]), "+v"(ka[1]), "+v"(vo2[0]), "+v"(vo2[1]), "+v"(ex));
    }
    typedef const __attribute__((address_space(3))) f32x4* lds_f4; typedef const __attribute__((address_space(3))) float* lds_f1;
    auto mainloop = [&](auto WM) {
        int ty = lane_id();
        const int l15 = ty & 15, g = ty >> 4;
        const int swA = (l15 >> 1) & 7;
        const int q4 = l15 >> 2, p4 = l15 & 3, fk = ((q4 >> 1) & 1) | ((g & 1) << 1);
        lds_cptr pA0 = sm + wm * 8192 + l15 * 128 + ((0 + g) ^ swA) * 16, pA1 = sm + wm * 8192 + l15 * 128 + ((4 + g) ^ swA) * 16;
        lds_cptr pB0 = sm + ABYTES + (8 * g + q4) * 128 + (((2 * wn + 0) ^ fk) * 32) + p4 * 8;
        lds_cptr pB1 = sm + ABYTES + (8 * g + q4) * 128 + (((2 * wn + 1) ^ fk) * 32) + p4 * 8;
        asm volatile("" : "+v"(pA0), "+v"(pA1), "+v"(pB0), "+v"(pB1));
        slow_warm((unsigned)(uintptr_t)smem + OFF_DUMP, W, w, ty);
        unsigned so = (unsigned)(panel * 16384 + wm * 512 + ty * 8);
        const char *p0 = ident ? (const char*)W : statp, *p1 = ident ? (const char*)W : wnp;
        u32x4 wn;
        asm volatile("global_load_dwordx4 %0, %2, %3 sc1" : "=&v"(wn), "+v"(so) : "v"(0), "s"(p1) : "memory");
        BAR();
        STAMP(LOCAL ? 1 : 7);
        f32x2 sq;
        asm volatile("global_load_dwordx2 %0, %1, %2 sc1" : "=&v"(sq) : "v"(so), "s"(p0) : "memory");
        BAR();
        pa[0] = pA0; pa[1] = pA1; pb[0] = pB0; pb[1] = pB1;
#pragma unroll
        for (int i = 0; i < 4; ++i) rdB(0, i);
#pragma unroll
        for (int i = 0; i < 8; ++i) rdA(WM, 0, i);
#pragma unroll
        for (int i = 0; i < 4; ++i) if (LOCAL && WM.value == 0 && i >= 2) rdA(WM, 0, 6 + i); else rdB(1, i);
        SBAR();
        asm volatile("s_waitcnt vmcnt(0)" : "+v"(sq), "+v"(wn) :: "memory");
        const float d = fmaxf(__builtin_fmaf(sq[0] * (-1.f / 64.f), sq[0], sq[1]), 0.f);
        cb = fminf(__builtin_amdgcn_sqrtf(__uint_as_float(wave_min(__float_as_uint(d)))) * 64.f * __uint_as_float(min(min(wn[0], wn[1]), wn[2])), 1.0e30f);
        if (ident) cb = 0.f;
        bias = f32x4{cb, cb, cb, cb};
        asm volatile("s_nop 1" : "+v"(bias));
        WAIT_LGKM();
        int off = 0;
        constexpr bool HK = LOCAL && WM.value == 0;
        auto front = [&](int p, bool first) {
#pragma unroll
            for (int c = 0; c < 4; ++c)
                column(WM, p, c, first, [&](int n) {
                    if (HK && c == 0) { if (n < 2) rdB(1, 2 + n); else if (n < 6) rdB(2, n - 2); }
                    else if (n < 4) rdB(c + 2, n);
                    if (LOCAL && !HK && c == 0 && (n == 4 || n == 5)) rdA(WM, p, 4 + n);
                    if (c == 1 && n == 5) { off = (off == 2 * STAGE) ? 0 : off + STAGE; asm volatile("" : "+s"(off)); }
                    if (c == 2 && n == 4) { pa[0] = pA0 + off; asm volatile("" : "+v"(pa[0])); }
                    if (c == 2 && n == 5) { pa[1] = pA1 + off; asm volatile("" : "+v"(pa[1])); }
                    if (c == 3 && n == 4) { pb[0] = pB0 + off; asm volatile("" : "+v"(pb[0])); }
                    if (c == 3 && n == 5) { pb[1] = pB1 + off; asm volatile("" : "+v"(pb[1])); }
                    if ((c == 0 && n == 3) || ((c == 1 || c == 2 || (HK && c == 0)) && n == 7)) __builtin_amdgcn_s_waitcnt(0xC47F);
                });
            WAIT_LGKM_BAR();
        };
        auto back = [&](int p, bool first) {
            column(WM, p, 4, first, [&](int n) { if (n & 1) rdB(0, n >> 1); else rdA(WM, p ^ 1, n >> 1); });
            column(WM, p, 5, first, [&](int n) {
                if (!(n & 1)) rdA(WM, p ^ 1, 4 + (n >> 1)); else if (HK && n > 4) rdA(WM, p ^ 1, 6 + (n >> 1)); else rdB(1, n >> 1);
                if (n == 7) __builtin_amdgcn_s_waitcnt(0xC87F);
            });
        };
        auto stage = [&](int p, bool first) { front(p, first); back(p, first); };
        stage(0, true);
        front(1, false);
#pragma unroll 1
        for (int t = 1; t < NT - 1; t += 2) { back(1, false); stage(0, false); front(1, false); }
        column(WM, 1, 4, false, [&](int n) {
            constexpr int RS = C::OFF_RSTD - C::OFF_MEAN, H = 512 - WM.value * 256;
            if (n == 0) { ck[0] = *(lds_f4)(c4 + 256); ck[1] = *(lds_f4)(c4 + 320); cv[0] = *(lds_f1)(c1 + 512); cv[1] = *(lds_f1)(c1 + 512 + 64); }
            else if (n < 5) {
                mu[n - 1] = *(lds_f1)(s1 + (n - 1) * 64); rs[n - 1] = *(lds_f1)(s1 + RS + (n - 1) * 64);
                mu4[n - 1] = *(lds_f4)(s4 + (n - 1) * 64); rs4[n - 1] = *(lds_f4)(s4 + RS + (n - 1) * 64);
            } else if (n == 5) {
                if (LOCAL && WM.value == 0) { mu[4] = *(lds_f1)(s1 + H); rs[4] = *(lds_f1)(s1 + RS + H); }
                else if (LOCAL) { mu4[4] = *(lds_f4)(s4 + H); rs4[4] = *(lds_f4)(s4 + RS + H); }
            } else if (n == 6) { cq[0] = *(lds_f4)c4; cq[1] = *(lds_f4)(c4 + 64); }
        });
        if (LOCAL) column(WM, 1, 5, false, [](int n) { if (n == 6) __builtin_amdgcn_s_waitcnt(0xC07F); });
        if (LOCAL) {
            auto fix = [&](int a, int t) {
#pragma unroll
                for (int f = 0; f < 2; ++f)
                    acc[a][t][f] = (a == 2) ? rs4[t] * (acc[2][t][f] + mu4[t] * cv[f]) + rs4[t] * -cb
                                            : rs[t] * (acc[a][t][f] + mu[t] * (a ? ck[f] : cq[f])) + rs[t] * -cb;
            };
            auto pk = [&](const f32x4 (&v)[2]) { return u32x4{pack_bf16(v[0][0], v[0][1]), pack_bf16(v[0][2], v[0][3]), pack_bf16(v[1][0], v[1][1]), pack_bf16(v[1][2], v[1][3])}; };
            if (!ident) {
                fix(1, 1); fix(1, 3); fix(2, 1); fix(2, 3);
#pragma unroll
                for (int f = 0; f < 2; ++f)
                    acch[f] = (WM.value == 0) ? rs[4] * (acch[f] + mu[4] * ck[f]) : rs4[4] * (acch[f] + mu4[4] * cv[f]);
            }
            auto pkv = [&](int t) {
#pragma unroll
                for (int f = 0; f < 2; ++f) {
                    va[t >> 1][f][(t & 1) * 2] = pack_bf16(acc[2][t][f][0], acc[2][t][f][1]);
                    va[t >> 1][f][(t & 1) * 2 + 1] = pack_bf16(acc[2][t][f][2], acc[2][t][f][3]);
                }
            };
            kfr[1] = pk(acc[1][1]); kfr[3] = pk(acc[1][3]); pkv(1); pkv(3);
            u32x4 hf = pk(acch);
            if (WM.value == 0) {
                ex[0] = kfr[1]; ex[64] = kfr[3];
#pragma unroll
                for (int u = 0; u < 2; ++u)
                    asm volatile("ds_write_b64 %0, %1 offset:%3\n\tds_write_b64 %0, %2 offset:%3+8" :: "v"(ex), "v"(u32x2{va[u][0][2], va[u][0][3]}),
                                 "v"(u32x2{va[u][1][2], va[u][1][3]}), "n"(2048 + u * 1024) : "memory");
                if (negpos) hf = u32x4{0, 0, 0, 0};
            }
            ex[256 + WM.value * 64] = hf;
            SBAR();
            if (!ident) { fix(0, 0); fix(0, 1); fix(0, 2); fix(0, 3); fix(1, 0); fix(1, 2); fix(2, 0); fix(2, 2); }
#pragma unroll
            for (int t = 0; t < 4; ++t) qfr[t] = pk(acc[0][t]);
            kfr[0] = pk(acc[1][0]); kfr[2] = pk(acc[1][2]); pkv(0); pkv(2);
            asm volatile("" : "+v"(qfr[0]), "+v"(qfr[1]), "+v"(qfr[2]), "+v"(qfr[3]), "+v"(kfr[0]), "+v"(kfr[2]),
                         "+v"(va[0][0]), "+v"(va[0][1]), "+v"(va[1][0]), "+v"(va[1][1]));
        }
    };
    const int head = ct * 2 + wn;
    unsigned qo = 0, ko = 0, vo = 0;
    if (LOCAL) {
        qo = (unsigned)((((panel * 8 + wm * 4) * 32 + head) * 64 + lane) * 16);
        asm volatile("" : "+v"(qo));
    } else {
        ko = (unsigned)((wm * 8 + (g & 1)) * 1024 + ((l15 & 7) + ((l15 & 8) << 1) + 32 * (g >> 1)) * 16);
        vo = (unsigned)((wm * 8 + 2 + (g >> 1)) * 1024 + (l15 + 32 * (g & 1)) * 16);
        asm volatile("" : "+v"(ko), "+v"(vo));
    }
    if (LOCAL && wm) mainloop(IC<1>{}); else mainloop(IC<0>{});

    if (!LOCAL) {
        auto pk = [&](f32x4 a, f32x4 b) { return u32x4{pack_bf16(a[0], a[1]), pack_bf16(a[2], a[3]), pack_bf16(b[0], b[1]), pack_bf16(b[2], b[3])}; };
        char* kvp = (char*)kvf + (size_t)(head * 16 + panel) * 16384;
        lds_ptr own = smw + wn * 16384;
        SBAR();
#pragma unroll
        for (int t = 0; t < 4; ++t) {
            const u32x4 kk = pk(rs[t] * (acc[1][t][0] + mu[t] * ck[0]) + rs[t] * -cb, rs[t] * (acc[1][t][1] + mu[t] * ck[1]) + rs[t] * -cb);
            if (t & 1) st16o<128>(kvp + (t >> 1) * 4096, ko, kk); else st16o<0>(kvp + (t >> 1) * 4096, ko, kk);
            kfr[t] = kk;
            SBAR();
            const int u = t >> 1, f = t & 1;
            const u32x4 vv = pk(rs4[2 * u] * (acc[2][2 * u][f] + mu4[2 * u] * cv[f]) + rs4[2 * u] * -cb,
                                rs4[2 * u + 1] * (acc[2][2 * u + 1][f] + mu4[2 * u + 1] * cv[f]) + rs4[2 * u + 1] * -cb);
            if (f) st16o<256>(kvp + u * 4096, vo, vv); else st16o<0>(kvp + u * 4096, vo, vv);
            qfr[t] = vv;
            SBAR();
        }
        column(IC<0>{}, 1, 5, false, [&](int n) {
            const int t = n >> 1;
            if (n & 1) *(lds_u4)(own + vo + (t >> 1) * 4096 + (t & 1) * 256) = qfr[t];
            else       *(lds_u4)(own + ko + (t >> 1) * 4096 + (t & 1) * 128) = kfr[t];
        });
        asm volatile("s_nop 7");
        SBAR();
#pragma unroll
        for (int u = 0; u < 2; ++u) {
            const float r0 = rs[2 * u] * QSCALE, r1 = rs[2 * u + 1] * QSCALE;
            const u32x4 qa = pk(r0 * (acc[0][2 * u][0] + mu[2 * u] * cq[0]) + r0 * -cb, r0 * (acc[0][2 * u][1] + mu[2 * u] * cq[1]) + r0 * -cb);
            const u32x4 qb = pk(r1 * (acc[0][2 * u + 1][0] + mu[2 * u + 1] * cq[0]) + r1 * -cb, r1 * (acc[0][2 * u + 1][1] + mu[2 * u + 1] * cq[1]) + r1 * -cb);
#pragma unroll
            for (int e = 0; e < 4; ++e) {
                const auto x = __builtin_amdgcn_permlane16_swap(qa[e], qb[e], false, false);
                qo4[2 * u][e] = x[0]; qo4[2 * u + 1][e] = x[1];
            }
        }
        return;
    }
    {
        const f32x4 zero4 = {0, 0, 0, 0};
        auto bf8 = [](u32x4 v) { return __builtin_bit_cast(bf16x8, v); };
        f32x4 sp[4], sc[4];
        sc[1] = MFMA16(bf8(kfr[1]), bf8(qfr[1]), mwin); sp[1] = MFMA16(bf8(kfr[0]), bf8(qfr[1]), mwin);
        sc[3] = MFMA16(bf8(kfr[3]), bf8(qfr[3]), mwin); sp[3] = MFMA16(bf8(kfr[2]), bf8(qfr[3]), mwin);
        asm volatile("" : "+v"(sc[1]), "+v"(sp[1]), "+v"(sc[3]), "+v"(sp[3]));
        WAIT_LGKM_BAR();
        const bf16x8 kp[2] = {lds_b128(ka[0]), lds_b128(ka[1])};
        u32x2 vp[2][2];
#pragma unroll
        for (int u = 0; u < 2; ++u)
            asm volatile("ds_read_b64 %0, %2\n\tds_read_b64 %1, %2 offset:8" : "=&v"(vp[u][0]), "=&v"(vp[u][1]) : "v"(vo2[u]) : "memory");
        sc[0] = MFMA16(bf8(kfr[0]), bf8(qfr[0]), mwin); sc[2] = MFMA16(bf8(kfr[2]), bf8(qfr[2]), mwin);
        SBAR();
        float x[4][4], m[4];
        unsigned a[4][2];
        auto logits = [&](int t0, int st) {
#pragma unroll
            for (int t = t0; t < 4; t += 2)
#pragma unroll
                for (int h = 0; h < 2; ++h) {
                    if (st == 0) {
                        const unsigned uc = pack_bf16(sc[t][2 * h], sc[t][2 * h + 1]), up = pack_bf16(sp[t][2 * h], sp[t][2 * h + 1]);
                        asm("v_bfi_b32 %0, %1, %2, %3" : "=v"(a[t][h]) : "v"(h ? cm23 : cm01), "v"(uc), "v"(up));
                    } else if (st == 1) { x[t][2 * h] = bf_lo(a[t][h]) * ATT_SCALE; x[t][2 * h + 1] = bf_hi(a[t][h]) * ATT_SCALE; }
                    else if (st == 2) a[t][h] = pack_bf16(x[t][2 * h], x[t][2 * h + 1]);
                    else { x[t][2 * h] = bf_lo(a[t][h]); x[t][2 * h + 1] = bf_hi(a[t][h]); }
                }
            SBAR();
        };
        logits(1, 0); logits(1, 1);
        sp[0] = MFMA16(kp[0], bf8(qfr[0]), mfirst); sp[2] = MFMA16(kp[1], bf8(qfr[2]), mfirst);
        SBAR();
        logits(1, 2); logits(1, 3);
        logits(0, 0); logits(0, 1); logits(0, 2); logits(0, 3);
        auto across = [&](float (&v)[4], auto op, auto fin) {
            const auto p = __builtin_amdgcn_permlane16_swap(__float_as_uint(v[0]), __float_as_uint(v[1]), false, false);
            const auto q = __builtin_amdgcn_permlane16_swap(__float_as_uint(v[2]), __float_as_uint(v[3]), false, false);
            const auto r = __builtin_amdgcn_permlane32_swap(__float_as_uint(op(__uint_as_float(p[0]), __uint_as_float(p[1]))),
                                                            __float_as_uint(op(__uint_as_float(q[0]), __uint_as_float(q[1]))), false, false);
            const unsigned z = __float_as_uint(fin(op(__uint_as_float(r[0]), __uint_as_float(r[1]))));
            const auto u = __builtin_amdgcn_permlane32_swap(z, z, false, false);
            const auto e = __builtin_amdgcn_permlane16_swap(u[0], u[0], false, false), f = __builtin_amdgcn_permlane16_swap(u[1], u[1], false, false);
            v[0] = __uint_as_float(e[0]); v[1] = __uint_as_float(e[1]); v[2] = __uint_as_float(f[0]); v[3] = __uint_as_float(f[1]);
        };
#pragma unroll
        for (int t = 0; t < 4; ++t) m[t] = amax3(x[t][0], x[t][1], x[t][2]);
        asm("v_max_f32 %0, %0, %4\n\tv_max_f32 %1, %1, %5\n\tv_max_f32 %2, %2, %6\n\tv_max_f32 %3, %3, %7\n\ts_nop 0"
            : "+v"(m[0]), "+v"(m[1]), "+v"(m[2]), "+v"(m[3]) : "v"(x[0][3]), "v"(x[1][3]), "v"(x[2][3]), "v"(x[3][3]));
        across(m, [](float p, float q) { float r; asm("v_max_f32 %0, %1, %2\n\ts_nop 0" : "=v"(r) : "v"(p), "v"(q)); return r; },
               [](float p) { return p * -LOG2E; });
        SBAR();
#pragma unroll
        for (int t = 0; t < 4; ++t)
#pragma unroll
            for (int r = 0; r < 4; ++r) x[t][r] = __builtin_fmaf(x[t][r], LOG2E, m[t]);
        SBAR();
#pragma unroll
        for (int t = 0; t < 4; ++t)
#pragma unroll
            for (int r = 0; r < 4; ++r) x[t][r] = __builtin_amdgcn_exp2f(x[t][r]);
        SBAR();
#pragma unroll
        for (int t = 0; t < 4; ++t) m[t] = (x[t][0] + x[t][1]) + (x[t][2] + x[t][3]);
        asm volatile("s_waitcnt lgkmcnt(0)" : "+v"(vp[0][0]), "+v"(vp[0][1]), "+v"(vp[1][0]), "+v"(vp[1][1]) :: "memory");
        u32x4 ve[2][2];
#pragma unroll
        for (int u = 0; u < 2; ++u)
#pragma unroll
            for (int f = 0; f < 2; ++f) ve[u][f] = u32x4{vp[u][f][0], vp[u][f][1], va[u][f][0], va[u][f][1]};
        across(m, [](float p, float q) { return p + q; }, [](float p) { return __builtin_amdgcn_rcpf(p); });
        asm volatile("" : "+v"(ve[0][0]), "+v"(ve[0][1]), "+v"(ve[1][0]), "+v"(ve[1][1]));
        SBAR();
#pragma unroll
        for (int t = 0; t < 4; ++t)
#pragma unroll
            for (int r = 0; r < 4; ++r) x[t][r] *= m[t];
        SBAR();
#pragma unroll
        for (int t = 0; t < 4; ++t)
#pragma unroll
            for (int h = 0; h < 2; ++h) a[t][h] = pack_bf16(x[t][2 * h], x[t][2 * h + 1]);
        SBAR();
        f32x4 o0[4], o1[4];
#pragma unroll
        for (int t = 0; t < 4; ++t) {
            const bf16x8 pb = bf8(u32x4{a[t][0] & ~cm01, a[t][1] & ~cm23, a[t][0] & cm01, a[t][1] & cm23});
            o0[t] = MFMA16(bf8((t & 1) ? va[t >> 1][0] : ve[t >> 1][0]), pb, zero4);
            o1[t] = MFMA16(bf8((t & 1) ? va[t >> 1][1] : ve[t >> 1][1]), pb, zero4);
        }
        SBAR();
#pragma unroll
        for (int t = 0; t < 4; ++t)
            st16(ofrag + t * 16384, qo, u32x4{pack_bf16(o0[t][0], o0[t][1]), pack_bf16(o0[t][2], o0[t][3]),
                                                 pack_bf16(o1[t][0], o1[t][1]), pack_bf16(o1[t][2], o1[t][3])});
    }
}

DI int out_row(int panel, bool localrows, int T, int a) {
    return localrows ? (panel * 128 + (T >> 2) * 64 + 2 * (16 * (T & 1) + a) + ((T >> 1) & 1)) : (panel * 128 + T * 16 + a);
}
DI int out_swz(int k) { return ((k >> 1) & 1) | (k & 4); }
DI unsigned wo_off(int w, int lane) {
    const int kl = lane >> 3;
    unsigned v = (unsigned)(w * 8 + kl) * 2048u + (unsigned)(ids().co * 128 + ((lane & 7) ^ out_swz(kl)) * 16);
    asm volatile("" : "+v"(v));
    return v;
}
DI void wo_load(const void* Wo, unsigned voff, int i0, int i1, unsigned lds0, int w) {
#pragma unroll
    for (int i = i0; i < i1; ++i) glds16((const char*)Wo + (size_t)i * 65536, voff, lds0 + (w + 4 * i) * 1024);
}
DI unsigned res_off(bool localrows, bool tiled, int lane) {
    const Ids I = ids();
    unsigned v = (unsigned)(I.po * 262144 + I.co * (tiled ? 16384 : 128) + out_row(0, localrows, 0, lane >> 3) * (tiled ? 128 : 2048) +
                            (((lane & 7) ^ (lane >> 3)) * 16));
    asm volatile("" : "+v"(v));
    return v;
}
DI void res_load(u32x4 (&res)[6], const void* Xin, bool localrows, bool tiled, unsigned off, int w) {
#pragma unroll
    for (int i = 0; i < 6; ++i) {
        const int j = min(w + 3 * i, 15);
        asm volatile("global_load_dwordx4 %0, %1, %2 sc1" : "=&v"(res[i]) : "v"(off), "s"((const char*)Xin + out_row(0, localrows, j >> 1, (j & 1) * 8) * (tiled ? 128 : 2048)) : "memory");
    }
}
DI void out_handoff(char* smem, int xoff, u32x4 (&res)[6], int w, char* ws, int set, u64 tag, int lane, u32x4 (&c)[8], int mat) {
    const Ids I = ids();
    u64 land;
    lds_ptr dst = (lds_ptr)smem + xoff + w * 1024 + lane * 16;
    asm volatile("" : "+v"(dst));
    if (w == 3) __builtin_amdgcn_s_setprio(3);
    signal_poll<false, false>(w - 3, ws, flag_off(set, I.pq, lane & 15, I.cq), flag_off(set, I.po, I.co, lane & 15), 0xffffull, tag, land);
    if (w < 3) {
        asm volatile("s_waitcnt vmcnt(0)" : "+v"(res[0]), "+v"(res[1]), "+v"(res[2]), "+v"(res[3]), "+v"(res[4]), "+v"(res[5]) :: "memory");
#pragma unroll
        for (int i = 0; i < 5; ++i) *(__attribute__((address_space(3))) u32x4*)(dst + i * 3072) = res[i];
        if (w == 0) *(__attribute__((address_space(3))) u32x4*)(dst + 15360) = res[5];
        if (mat >= 0) colsum_store(c, ws, mat, w, lane);
    }
    WAIT_LGKM_BAR();
    asm volatile("s_waitcnt vmcnt(0)" : "+v"(land) :: "memory");
    if (w < 3 && mat >= 0) flag_store(ws, (mat ? 10 : 13) + w, I.cq, I.pq, tag, lane);
    __builtin_amdgcn_s_setprio(0);
}

template <bool AFTER_LOCAL>
DI void out_compute(char* smem, int w, const __hip_bfloat16* __restrict__ ofrag,
                    __hip_bfloat16* __restrict__ Xout, float* __restrict__ statp SP_PARAM) {
    constexpr int XOFF = AFTER_LOCAL ? 147456 : 98304;
    STAMP(AFTER_LOCAL ? 3 : 10);
    VM_BAR(0);
    const int lane = lane_id();
    asm volatile("" : "+s"(w));
    const Ids I = ids();
    const int panel = I.po, ct = I.co;
    const int l15 = lane & 15, g = lane >> 4;
    const bool tiled = AFTER_LOCAL || statp != nullptr;
    const int rstr = tiled ? 128 : 2048;
    const int q4 = l15 >> 2, p4 = l15 & 3;
    const int krow = 4 * g + q4;
    const int sw = out_swz(krow);
    const lds_cptr sm = (lds_cptr)smem;
    lds_cptr bw[4], xo[4];
#pragma unroll
    for (int t = 0; t < 4; ++t) {
        bw[t] = sm + krow * 128 + ((((t >> 1) * 4 + (p4 >> 1) * 2 + (t & 1)) ^ sw) * 16) + (p4 & 1) * 8;
        xo[t] = sm + XOFF + (w * 32 + l15) * 128 + ((((t >> 1) * 4 + (g >> 1) * 2 + (t & 1)) ^ (l15 & 7)) * 16) + (g & 1) * 8;
    }
    unsigned so[2], sto, ao[2];
#pragma unroll
    for (int r2 = 0; r2 < 2; ++r2) {
        so[r2] = (unsigned)(panel * 262144 + ct * (tiled ? 16384 : 128) + out_row(0, AFTER_LOCAL, 2 * w + r2, l15 & 7) * rstr + (l15 >> 3) * 64 + g * 16);
        ao[r2] = (unsigned)((panel * 8 + 2 * w + r2) * 32768 + lane * 16);
    }
    sto = (unsigned)(((panel * 16 + ct) * 128 + 2 * w * 16 + l15) * 8);
    asm volatile("" : "+v"(bw[0]), "+v"(bw[1]), "+v"(bw[2]), "+v"(bw[3]), "+v"(xo[0]), "+v"(xo[1]), "+v"(xo[2]), "+v"(xo[3]),
                 "+v"(so[0]), "+v"(so[1]), "+v"(sto), "+v"(ao[0]), "+v"(ao[1]));
    bf16x8 ones;
    {
        unsigned o1; asm volatile("v_mov_b32 %0, 0x3f803f80" : "=v"(o1));
        ones = __builtin_bit_cast(bf16x8, (u32x4{o1, o1, o1, o1}));
        asm volatile("" : "+v"(ones));
    }
    const __amdgpu_buffer_rsrc_t ra = __builtin_amdgcn_make_buffer_rsrc((void*)ofrag, 0, 4 << 20, 0x00020000);
    asm volatile("" :: "s"(ra));
    u32x32 fr;
    BAR();
    STAMP(AFTER_LOCAL ? 4 : 11);
    u32x32 aw0, aw1, aw2, fr1;
    asm volatile(".irp j,0,1,2,3\n\tbuffer_load_dwordx4 v[96+4*\\j:99+4*\\j], %2, %4, 0 offen offset:1024*\\j sc1\n\t.endr\n\t"
                 ".irp j,0,1,2,3\n\tbuffer_load_dwordx4 v[112+4*\\j:115+4*\\j], %3, %4, 0 offen offset:1024*\\j sc1\n\t.endr\n\t"
                 ".irp j,0,1,2,3\n\tbuffer_load_dwordx4 v[128+4*\\j:131+4*\\j], %2, %4, %5 offen offset:1024*\\j sc1\n\t.endr"
                 : "=&{v[96:127]}"(aw0), "=&{v[128:159]}"(aw1) : "v"(ao[0]), "v"(ao[1]), "s"(ra), "s"(4096) : "memory");
    f32x16 acc[2];
#pragma unroll
    for (int r2 = 0; r2 < 2; ++r2)
#pragma unroll
        for (int c = 0; c < 4; ++c) {
            const u32x2 xr = *(const __attribute__((address_space(3))) u32x2*)(xo[c] + r2 * 2048);
            acc[r2][4 * c] = bf_lo(xr[0]); acc[r2][4 * c + 1] = bf_hi(xr[0]); acc[r2][4 * c + 2] = bf_lo(xr[1]); acc[r2][4 * c + 3] = bf_hi(xr[1]);
        }
    f32x2 cb;
#pragma unroll
    for (int r2 = 0; r2 < 2; ++r2) {
        float m[4];
#pragma unroll
        for (int c = 0; c < 4; ++c)
            asm("v_max3_f32 %0, |%1|, |%2|, |%3|\n\tv_max_f32 %0, %0, |%4|" : "=&v"(m[c])
                : "v"(acc[r2][4 * c]), "v"(acc[r2][4 * c + 1]), "v"(acc[r2][4 * c + 2]), "v"(acc[r2][4 * c + 3]));
        float mm;
        asm("v_min3_f32 %0, %1, %2, %3\n\tv_min3_f32 %0, %0, %4, %5" : "=&v"(mm) : "v"(m[0]), "v"(m[1]), "v"(m[2]), "v"(m[3]), "s"(1.0e30f));
        cb[r2] = mm * 256.f;
        acc[r2] += cb[r2];
    }
    asm volatile(
        ".macro OUT_MM r, t, j, s\n\t"
        "v_mfma_f32_16x16x32_bf16 v[16*\\r+4*\\t:16*\\r+4*\\t+3], v[32+16*\\t+4*\\j:35+16*\\t+4*\\j], v[96+32*\\s+16*\\r+4*\\j:99+32*\\s+16*\\r+4*\\j], v[16*\\r+4*\\t:16*\\r+4*\\t+3]\n\t"
        ".endm\n\t"
        ".macro OUT_RD t, i, g\n\t"
        "ds_read_b64_tr_b16 v[32+16*\\t+2*(\\i):33+16*\\t+2*(\\i)], v[192+\\t] offset:(\\g)*16384+(\\i)*2048\n\t"
        ".endm\n\t"
        ".macro OUT_LD i, s\n\t"
        ".if (\\i) < 4\n\tbuffer_load_dwordx4 v[96+32*(\\s)+4*(\\i):99+32*(\\s)+4*(\\i)], %[a0], %[ra], s96 offen offset:1024*(\\i) sc1\n\t"
        ".else\n\tbuffer_load_dwordx4 v[96+32*(\\s)+4*(\\i):99+32*(\\s)+4*(\\i)], %[a1], %[ra], s96 offen offset:1024*((\\i)-4) sc1\n\t.endif\n\t"
        ".endm\n\t"
        ".macro OUT_CH r, t, s, rt, g\n\t"
        ".irp j,0,1,2,3\n\tOUT_MM \\r, \\t, \\j, \\s\n\tOUT_RD \\rt, 4*\\r+\\j, \\g\n\t.endr\n\t"
        ".endm\n\t"
        ".macro OUT_GRP s, g, gn, ld, up, v0, v1\n\t"
        "OUT_CH 0, 0, \\s, 3, \\g\n\ts_waitcnt vmcnt(\\v0)\n\t.if \\ld\n\tOUT_LD 0, (\\s+2)%%3\n\t.endif\n\t"
        "OUT_CH 1, 0, \\s, 3, \\g\n\t.if \\up\n\t.irp t,0,1,2,3\n\tv_add_u32 v[192+\\t], s97, v[192+\\t]\n\t.endr\n\t.endif\n\t.if \\ld\n\tOUT_LD 1, (\\s+2)%%3\n\t.endif\n\t"
        "OUT_CH 0, 1, \\s, 0, \\gn\n\t.if \\ld\n\tOUT_LD 2, (\\s+2)%%3\n\t.endif\n\t"
        "OUT_CH 1, 1, \\s, 0, \\gn\n\t.if \\ld\n\tOUT_LD 3, (\\s+2)%%3\n\t.endif\n\ts_waitcnt lgkmcnt(8)\n\t"
        "OUT_CH 0, 2, \\s, 1, \\gn\n\t.if \\ld\n\tOUT_LD 4, (\\s+2)%%3\n\t.endif\n\t"
        "OUT_CH 1, 2, \\s, 1, \\gn\n\t.if \\ld\n\tOUT_LD 5, (\\s+2)%%3\n\t.endif\n\t"
        "OUT_CH 0, 3, \\s, 2, \\gn\n\t.if \\ld\n\tOUT_LD 6, (\\s+2)%%3\n\t.endif\n\t"
        "OUT_CH 1, 3, \\s, 2, \\gn\n\t.if \\ld\n\tOUT_LD 7, (\\s+2)%%3\n\ts_addk_i32 s96, 0x1000\n\t.endif\n\t"
        "s_waitcnt vmcnt(\\v1) lgkmcnt(8)\n\t"
        ".endm\n\t"
        ".macro OUT_PACK y, c, r\n\t"
        ".irp i,0,2,4,6,8,10,12,14\n\tv_pk_add_f32 v[\\c+\\i:\\c+\\i+1], v[\\c+\\i:\\c+\\i+1], %[cb] op_sel:[0,\\r] op_sel_hi:[1,\\r] neg_lo:[0,1] neg_hi:[0,1]\n\t.endr\n\t"
        ".irp i,0,1,2,3,4,5,6,7\n\tv_cvt_pk_bf16_f32 v[\\y+\\i], v[\\c+2*\\i], v[\\c+2*\\i+1]\n\t.endr\n\t"
        ".irp i,0,1,4,5\n\tv_permlane16_swap_b32 v[\\y+\\i], v[\\y+\\i+2]\n\t.endr\n\t"
        ".irp i,0,1,2,3\n\tv_mov_b32 v[\\y+8+\\i], v[\\y+\\i]\n\t.endr\n\t"
        "v_mfma_f32_16x16x32_bf16 v[\\y+12:\\y+15], v[\\y:\\y+3], v[\\y:\\y+3], 0\n\t"
        "v_mov_b32_dpp v[\\y+8], v[\\y+4] row_ror:8 row_mask:0xf bank_mask:0xc\n\tv_mov_b32_dpp v[\\y+9], v[\\y+5] row_ror:8 row_mask:0xf bank_mask:0xc\n\t"
        "v_mfma_f32_16x16x32_bf16 v[\\y+16:\\y+19], %[one], v[\\y:\\y+3], 0\n\t"
        "v_mov_b32_dpp v[\\y+10], v[\\y+6] row_ror:8 row_mask:0xf bank_mask:0xc\n\tv_mov_b32_dpp v[\\y+11], v[\\y+7] row_ror:8 row_mask:0xf bank_mask:0xc\n\t"
        "v_mfma_f32_16x16x32_bf16 v[\\y+12:\\y+15], v[\\y+4:\\y+7], v[\\y+4:\\y+7], v[\\y+12:\\y+15]\n\t"
        ".endm\n\t"
        ".macro OUT_REST y\n\t"
        "v_mfma_f32_16x16x32_bf16 v[\\y+16:\\y+19], %[one], v[\\y+4:\\y+7], v[\\y+16:\\y+19]\n\t"
        ".irp i,0,1,2,3\n\tv_mov_b32_dpp v[\\y+4+\\i], v[\\y+\\i] row_ror:8 row_mask:0xf bank_mask:0x3\n\t.endr\n\t"
        ".endm\n\t"
        ".macro OUT_DIAG y\n\t"
        "v_cndmask_b32 v[\\y+12], v[\\y+12], v[\\y+13], %[m0]\n\tv_cndmask_b32 v[\\y+14], v[\\y+14], v[\\y+15], %[m0]\n\t"
        "v_cndmask_b32 v[\\y+17], v[\\y+12], v[\\y+14], %[m1]\n\t"
        ".endm\n\t"
        "s_movk_i32 s96, 0x1000\n\t.irp i,4,5,6,7\n\tOUT_LD \\i, 1\n\t.endr\n\t"
        ".irp t,0,1,2\n\t.irp i,0,1,2,3,4,5,6,7\n\tOUT_RD \\t, \\i, 0\n\t.endr\n\ts_waitcnt lgkmcnt(4)\n\t.endr\n\t"
        "s_movk_i32 s96, 0x2000\n\ts_mov_b32 s97, 0xc000\n\t"
        "s_waitcnt vmcnt(12) lgkmcnt(0)\n"
        ".Lout_loop_%=:\n\t"
        "OUT_GRP 0, 0, 1, 1, 0, 8, 12\n\tOUT_GRP 1, 1, 2, 1, 0, 8, 12\n\tOUT_GRP 2, 2, 0, 1, 1, 8, 12\n\t"
        "s_mov_b32 s97, %[last]\n\t"
        "s_cmpk_lg_u32 s96, 0x8000\n\ts_cbranch_scc1 .Lout_loop_%=\n\t"
        "OUT_GRP 0, 0, 1, 0, 0, 8, 4\n\t"
        ".irp j,0,1,2,3\n\tOUT_MM 0, 0, \\j, 1\n\tOUT_RD 3, \\j, 1\n\t.endr\n\t.irp j,0,1,2,3\n\tOUT_MM 0, 1, \\j, 1\n\tOUT_RD 3, 4+\\j, 1\n\t.endr\n\t"
        "s_waitcnt lgkmcnt(8)\n\t.irp j,0,1,2,3\n\tOUT_MM 0, 2, \\j, 1\n\t.endr\n\t"
        "s_waitcnt lgkmcnt(0)\n\t.irp j,0,1,2,3\n\tOUT_MM 0, 3, \\j, 1\n\t.endr\n\t"
        "s_nop 1\n\t"
        "OUT_PACK 96, 0, 0\n\tglobal_store_dwordx4 %[s0], v[104:107], %[xa] sc1\n\tOUT_REST 96\n\tglobal_store_dwordx4 %[s0], v[100:103], %[xb] sc1\n\t"
        "s_waitcnt vmcnt(2)\n\t.irp t,0,1,2,3\n\t.irp j,0,1,2,3\n\tOUT_MM 1, \\t, \\j, 1\n\t.endr\n\t.endr\n\t"
        "s_nop 1\n\t"
        "OUT_PACK 160, 16, 1\n\tglobal_store_dwordx4 %[s1], v[168:171], %[xa] sc1\n\tOUT_REST 160\n\tglobal_store_dwordx4 %[s1], v[164:167], %[xb] sc1\n\t"
        "s_cmp_eq_u64 %[st], 0\n\ts_cbranch_scc1 .Lout_end_%=\n\t"
        "OUT_DIAG 96\n\tOUT_DIAG 160\n\t"
        "s_mov_b64 exec, %[md]\n\t"
        "global_store_dwordx2 %[so], v[112:113], %[st] sc1\n\tglobal_store_dwordx2 %[so], v[176:177], %[st] offset:128 sc1\n\t"
        "s_mov_b64 exec, -1\n"
        ".Lout_end_%=:\n\t"
        "s_nop 1\n\t"
        ".purgem OUT_MM\n\t.purgem OUT_RD\n\t.purgem OUT_LD\n\t.purgem OUT_CH\n\t.purgem OUT_GRP\n\t.purgem OUT_PACK\n\t.purgem OUT_REST\n\t.purgem OUT_DIAG"
        : "+{v[0:15]}"(acc[0]), "+{v[16:31]}"(acc[1]), "+{v[96:127]}"(aw0), "+{v[128:159]}"(aw1), "=&{v[160:191]}"(aw2),
          "+{v192}"(bw[0]), "+{v193}"(bw[1]), "+{v194}"(bw[2]), "+{v195}"(bw[3]), "=&{v[32:63]}"(fr), "=&{v[64:95]}"(fr1)
        : [a0] "v"(ao[0]), [a1] "v"(ao[1]), [ra] "s"(ra), [last] "s"(AFTER_LOCAL ? 49152 : 81920), [one] "v"(ones), [cb] "v"(cb), [s0] "v"(so[0]), [s1] "v"(so[1]),
          [xa] "s"(Xout), [xb] "s"((char*)Xout + (AFTER_LOCAL ? 16 : 8) * rstr), [st] "s"(statp), [so] "v"(sto),
          [m0] "s"(0xaaaaaaaaaaaaaaaaull), [m1] "s"(0xccccccccccccccccull), [md] "s"(0xf0000f0000f0000full)
        : "memory", "scc", "s96", "s97");
    STAMP(AFTER_LOCAL ? 5 : 12);
}

#define ROWSUM(p0, p1, c) __builtin_amdgcn_smfmac_f32_16x16x64_bf16(sel, __builtin_bit_cast(bf16v_t, __builtin_shufflevector(p0, p1, 0, 1, 2, 3, 4, 5, 6, 7)), c, sidx, 0, 0)
constexpr int ATT_STAGE = 32768;
typedef __attribute__((ext_vector_type(8))) unsigned u32x8;

DI void attn_loader(char* smem, int w, char* ws, int set, u64 tag, const __hip_bfloat16* __restrict__ kvf, const void* Wo, const void* Xin,
                    const void* Wcs SP_PARAM) {
    const int lane = lane_id();
    asm volatile("" : "+s"(w));
    const Ids I = ids();
    const int panel = I.pq, ct = I.cq;
    const unsigned lds0 = (unsigned)(uintptr_t)smem;
    const char* kvh = (const char*)kvf + (size_t)(ct * 2 + (w >> 1)) * 262144 + (w & 1) * 4096;
    const unsigned voff = (unsigned)(lane * 16);
    auto order = [&](int st) { return ((panel & 8) + st - ((st - 1 < (panel & 7)) ? 1 : 0)) & 15; };
    auto load = [&](int st) {
        const char* src = kvh + (size_t)order(st) * 16384;
        const unsigned dst = lds0 + (w >> 1) * 16384 + (w & 1) * 4096 + (st & 3) * ATT_STAGE;
#pragma unroll
        for (int u = 0; u < 2; ++u)
            asm volatile("s_mov_b32 m0, %0\n\ts_nop 0\n\tglobal_load_lds_dwordx4 %1, %2 sc1\n\tglobal_load_lds_dwordx4 %1, %2 offset:1024 sc1\n\t"
                         "global_load_lds_dwordx4 %1, %2 offset:2048 sc1\n\tglobal_load_lds_dwordx4 %1, %2 offset:3072 sc1"
                         :: "s"(dst + u * 8192), "v"(voff), "s"(src + u * 8192) : "memory");
    };
    const unsigned wv = wo_off(w, lane), rv = res_off(false, true, lane), cv = colsum_off(w, lane);
    const unsigned poff = flag_off(set, ct, panel, lane & 15);
    u64 land;
    __builtin_amdgcn_s_setprio(3);
    BAR();
    signal_poll<false, true>(w, ws, flag_off(set, ct, lane & 15, panel), poff, 1ull << order(1), tag, land);
    STAMP(9);
    load(1);
    asm volatile("global_load_dwordx2 %0, %1, %2 sc1\n\ts_waitcnt vmcnt(0)" : "+v"(land) : "v"(poff), "s"(ws + WS_FLAG) : "memory");
    STAMP(10);
    BAR();
    if (!__all(land == tag)) flag_poll(ws, poff, tag);
    load(2); load(3);
    __builtin_amdgcn_s_setprio(0);
    STAMP(11);
    VM_BAR(8); load(4);
    VM_BAR(0); load(5); load(6);
    VM_BAR(0); load(7); load(8);
    u32x4 res[6], ca[8];
    colsum_load(ca, w < 3 ? Wcs : nullptr, cv);
#pragma unroll 1
    for (int st = 6; st < 12; st += 2) {
        VM_BAR(0);
        load(st + 3); load(st + 4);
        if (st == 8) wo_load(Wo, wv, 24, 32, lds0 + 32768, w);
    }
    VM_BAR(0); load(15); wo_load(Wo, wv, 0, 8, lds0, w);
    res_load(res, Xin, false, true, rv, w);
    VM_BAR(0); wo_load(Wo, wv, 8, 24, lds0, w);
    STAMP(12);
    out_handoff(smem, 98304, res, w, ws, set + 1, tag, lane, ca, Wcs ? 0 : -1);
}

DI void attn_compute(char* smem, int w, const u32x4 (&q4)[4], const __hip_bfloat16* __restrict__ kvf, __hip_bfloat16* __restrict__ ofrag SP_PARAM) {
    const int lane = lane_id();
    asm volatile("" : "+s"(w));
    const int wm = w & 1, wn = w >> 1;
    const Ids I = ids();
    const int panel = I.pq, head = I.cq * 2 + wn;
    const int l15 = lane & 15;
    const unsigned se = ((l15 & 2) == 0 && (l15 & 4) == 0) ? 0x3f803f80u : 0u, so = ((l15 & 2) == 0 && (l15 & 4) != 0) ? 0x3f803f80u : 0u;
    const bf8_t sel = __builtin_bit_cast(bf8_t, u32x4{se, se, so, so});
    const int sidx = (l15 & 1) ? (int)0xEEEEEEEEu : 0x44444444;
    const lds_cptr sm = (lds_cptr)smem + wn * 16384 + lane * 16;
    const f32x16 z16 = {0, 0, 0, 0, 0, 0, 0, 0, 0, 0, 0, 0, 0, 0, 0, 0};
    const bf16x8 qA0 = __builtin_bit_cast(bf16x8, q4[0]), qA1 = __builtin_bit_cast(bf16x8, q4[1]);
    const bf16x8 qB0 = __builtin_bit_cast(bf16x8, q4[2]), qB1 = __builtin_bit_cast(bf16x8, q4[3]);
    f32x16 oA, oB, nmA, nmB, sA, sB;
    f32x4 lsA, lsB;
    u32x8 pB;
    bf16x8 v11;
    asm volatile(".irp r,106,108,110,112,134,136,138,140,170,172\n\tv_mov_b64 v[\\r:\\r+1], 0\n\t.endr"
                 : "={v[106:113]}"(pB), "={v[134:137]}"(lsA), "={v[138:141]}"(lsB), "={v[170:173]}"(v11));
    WAIT_LGKM_BAR();
    STAMP(8);
    bf16x8 k00 = lds_b128(sm), k10 = lds_b128(sm + 1024), k01 = lds_b128(sm + 4096), k11 = lds_b128(sm + 4096 + 1024);
    bf16x8 v00 = lds_b128(sm + 2048), v10 = lds_b128(sm + 3072), v01 = lds_b128(sm + 4096 + 2048);
    float ob;
    {
        const u32x4 vu = __builtin_bit_cast(u32x4, v00);
        const unsigned vm = max(max(vu[0] & 0x7fff0000u, vu[1] & 0x7fff0000u), max(vu[2] & 0x7fff0000u, vu[3] & 0x7fff0000u));
        ob = __uint_as_float(wave_min(vm)) * 0.015625f;
        sA = MFMA32(k00, qA0, z16); sB = MFMA32(k00, qB0, z16);
        sA = MFMA32(k10, qA1, sA); sB = MFMA32(k10, qB1, sB);
        SBAR(); VM_BAR(0); SBAR();
        float mA = sA[0], mB = sB[0];
#pragma unroll
        for (int r = 1; r < 16; ++r) { mA = fmaxf(mA, sA[r]); mB = fmaxf(mB, sB[r]); }
        mA = max_h(mA) + 16.f; mB = max_h(mB) + 16.f;
#pragma unroll
        for (int r = 0; r < 16; ++r) { nmA[r] = -mA; nmB[r] = -mB; sA[r] -= mA; oA[r] = ob; oB[r] = ob; }
    }
    const unsigned a0 = (unsigned)(uintptr_t)sm;
    unsigned dst = (unsigned)((((panel * 8 + wm * 4 + ((lane & 31) >> 4)) * 32 + head) * 64 + (lane & 15) + (lane >> 5) * 16) * 16);
    asm volatile("" : "+v"(dst));
    asm volatile(
        ".macro ATT_RD dst, y\n\t"
        "ds_read_b128 v[\\dst:\\dst+3], v[175+(((\\y)&0x1ffff)>>16)] offset:(\\y)&0xffff\n\t"
        ".endm\n\t"
        ".macro ATT_E4 c\n\t"
        "v_exp_f32 v[\\c], v[\\c]\n\tv_exp_f32 v[\\c+1], v[\\c+1]\n\tv_exp_f32 v[\\c+2], v[\\c+2]\n\tv_exp_f32 v[\\c+3], v[\\c+3]\n\t"
        ".endm\n\t"
        ".macro ATT_C4 p, c\n\t"
        "v_cvt_pk_bf16_f32 v[\\p], v[\\c], v[\\c+1]\n\tv_cvt_pk_bf16_f32 v[\\p+1], v[\\c+2], v[\\c+3]\n\t"
        "v_cvt_pk_bf16_f32 v[\\p+2], v[\\c+4], v[\\c+5]\n\tv_cvt_pk_bf16_f32 v[\\p+3], v[\\c+6], v[\\c+7]\n\t"
        ".endm\n\t"
        ".macro ATT_STEP cur, nxt, ka, kb, q, nm, oc, od, lsd, pc, pd, vd1, vc0, r0, y0, r1, y1, bar, rd\n\t"
        "v_mfma_f32_32x32x16_bf16 v[\\nxt:\\nxt+15], v[\\ka:\\ka+3], v[\\q:\\q+3], v[\\nm:\\nm+15]\n\t"
        "ATT_E4 \\cur\n\t"
        "v_mfma_f32_32x32x16_bf16 v[\\nxt:\\nxt+15], v[\\kb:\\kb+3], v[\\q+4:\\q+7], v[\\nxt:\\nxt+15]\n\t"
        "ATT_E4 \\cur+4\n\t"
        "v_mfma_f32_32x32x16_bf16 v[\\od:\\od+15], v[\\vd1:\\vd1+3], v[\\pd+4:\\pd+7], v[\\od:\\od+15]\n\t"
        "s_waitcnt lgkmcnt(0)\n\t"
        "ATT_C4 \\pc, \\cur\n\t"
        "v_exp_f32 v[\\cur+8], v[\\cur+8]\n\tv_exp_f32 v[\\cur+9], v[\\cur+9]\n\t"
        "v_mfma_f32_32x32x16_bf16 v[\\oc:\\oc+15], v[\\vc0:\\vc0+3], v[\\pc:\\pc+3], v[\\oc:\\oc+15]\n\t"
        ".if \\bar\n\ts_barrier\n\t.endif\n\t"
        "ATT_E4 \\cur+10\n\t"
        "v_smfmac_f32_16x16x64_bf16 v[\\lsd:\\lsd+3], v[130:133], v[\\pd:\\pd+7], v174\n\t"
        ".if \\rd & 4\n\tATT_RD 142, 32768\n\tATT_RD 150, 33792\n\tATT_RD 158, 34816\n\t.endif\n\t"
        ".if \\rd & 8\n\tATT_RD \\r1-8, \\y1-1024\n\t.endif\n\t"
        ".if \\rd & 1\n\tATT_RD \\r0, \\y0\n\t.endif\n\t"
        ".if \\rd & 2\n\tATT_RD \\r1, \\y1\n\t.endif\n\t"
        "v_exp_f32 v[\\cur+14], v[\\cur+14]\n\tv_exp_f32 v[\\cur+15], v[\\cur+15]\n\t"
        "ATT_C4 \\pc+4, \\cur+8\n\t"
        ".if \\rd & 4\n\ts_waitcnt lgkmcnt(3)\n\t.endif\n\t"
        ".endm\n\t"
        ".macro ATT_TILE p, y1, y2, bar, ra=3, rb=3\n\t"
        "ATT_STEP 66, 82, 142+\\p, 150+\\p, 122, 50, 2, 18, 138, 98, 106, 170-\\p, 158+\\p, 170-\\p, \\y1+3072, 142+\\p, \\y2, \\bar&1, \\ra\n\t"
        "ATT_STEP 82, 66, 146-\\p, 154-\\p, 114, 34, 18, 2, 134, 106, 98, 166+\\p, 158+\\p, 158+\\p, \\y2+2048, 150+\\p, \\y2+1024, \\bar&2, \\rb\n\t"
        ".endm\n\t"
        ".macro ATT_STAGE b, bar\n\t"
        "ATT_TILE 0, \\b+4096, \\b+8192, 0\n\t"
        "ATT_TILE 4, \\b+8192, \\b+12288, 0\n\t"
        ".if \\bar\n\tATT_TILE 0, \\b+12288, \\b+32768, 2, 1, 11\n\t.else\n\tATT_TILE 0, \\b+12288, \\b+32768, 0\n\t.endif\n\t"
        "ATT_TILE 4, \\b+32768, \\b+36864, 0\n\t"
        ".endm\n\t"
        "v_add_u32 v176, 0x10000, v175\n\t"
        "s_mov_b32 s96, 4\n\t"
        "s_waitcnt lgkmcnt(0)\n\t"
        "ATT_TILE 0, 4096, 8192, 0\n\tATT_TILE 4, 8192, 12288, 0\n\tATT_TILE 0, 12288, 0, 0, 1, 0\n\tATT_TILE 4, 32768, 36864, 1, 7, 3\n\t"
        "ATT_STAGE 32768, 1\n\t"
        "s_branch .Latt_entry_%=\n\t"
        ".p2align 6\n"
        ".Latt_loop_%=:\n\t"
        "ATT_STAGE 0, 1\n\tATT_STAGE 32768, 0\n"
        ".Latt_entry_%=:\n\t"
        "ATT_STAGE 65536, 1\n\t"
        "s_sub_u32 s96, s96, 1\n\t"
        "ATT_STAGE 98304, 0\n\t"
        "s_cmp_lg_u32 s96, 0\n\t"
        "s_cbranch_scc1 .Latt_loop_%=\n\t"
        "s_nop 1\n\t"
        "v_mfma_f32_32x32x16_bf16 v[18:33], v[170:173], v[110:113], v[18:33]\n\t"
        "v_smfmac_f32_16x16x64_bf16 v[138:141], v[130:133], v[106:113], v174\n\t"
        "s_waitcnt lgkmcnt(0)\n\ts_nop 10\n\t"
        ".purgem ATT_RD\n\t.purgem ATT_E4\n\t.purgem ATT_C4\n\t.purgem ATT_STEP\n\t.purgem ATT_TILE\n\t.purgem ATT_STAGE"
        : "+{v[2:17]}"(oA), "+{v[18:33]}"(oB), "+{v[66:81]}"(sA), "+{v[106:113]}"(pB), "+{v[134:137]}"(lsA), "+{v[138:141]}"(lsB),
          "+{v[142:145]}"(k00), "+{v[146:149]}"(k01), "+{v[150:153]}"(k10), "+{v[154:157]}"(k11),
          "+{v[158:161]}"(v00), "+{v[162:165]}"(v01), "+{v[166:169]}"(v10), "+{v[170:173]}"(v11)
        : "{v[34:49]}"(nmA), "{v[50:65]}"(nmB), "{v[114:117]}"(qA0), "{v[118:121]}"(qA1), "{v[122:125]}"(qB0), "{v[126:129]}"(qB1),
          "{v[130:133]}"(sel), "{v174}"(sidx), "{v175}"(a0)
        : "memory", "scc", "s96", "v176", "v82", "v83", "v84", "v85", "v86", "v87", "v88", "v89", "v90", "v91", "v92", "v93", "v94", "v95",
          "v96", "v97", "v98", "v99", "v100", "v101", "v102", "v103", "v104", "v105");
    STAMP(9);
    float lA = lsA[0] + lsA[1], lB = lsB[0] + lsB[1];
    auto store = [&] {
#pragma unroll
    for (int u = 0; u < 2; ++u) {
        const float inv = __builtin_amdgcn_rcpf(u ? lB : lA), sub = -ob * inv;
        const f32x16 e = (u ? oB : oA) * inv + sub;
        st16o<0>((char*)ofrag + u * 65536, dst, u32x4{pack_bf16(e[0], e[1]), pack_bf16(e[2], e[3]), pack_bf16(e[8], e[9]), pack_bf16(e[10], e[11])});
        st16o<512>((char*)ofrag + u * 65536, dst, u32x4{pack_bf16(e[4], e[5]), pack_bf16(e[6], e[7]), pack_bf16(e[12], e[13]), pack_bf16(e[14], e[15])});
    }
    };
    store();
    {
        const f32x16 os = oA + oB;
        const float osum = (((os[0] + os[1]) + (os[2] + os[3])) + ((os[4] + os[5]) + (os[6] + os[7]))) +
                           (((os[8] + os[9]) + (os[10] + os[11])) + ((os[12] + os[13]) + (os[14] + os[15])));
        const bool bad = !(fmaxf(lA, lB) < 1.0e24f) || !(lA > 1.0e-24f) || !(lB > 1.0e-24f) || !(fabsf(osum) < 3.0e38f);
                if (__any(bad)) {
            const __amdgpu_buffer_rsrc_t rk = __builtin_amdgcn_make_buffer_rsrc((char*)kvf + (size_t)head * 262144, 0, 262144, 0x00020000);
            oA = z16; oB = z16; lA = 0.f; lB = 0.f; ob = 0.f;
            float mA = -INFINITY, mB = -INFINITY;
#pragma unroll 1
            for (int t = 0; t < 64; ++t) {
                bf16x8 f[4];
#pragma unroll
                for (int i = 0; i < 4; ++i) f[i] = __builtin_bit_cast(bf16x8, __builtin_amdgcn_raw_buffer_load_b128(rk, lane * 16 + i * 1024, t * 4096, 16));
                auto upd = [&](bf16x8 q0, bf16x8 q1, f32x16& o, float& l, float& m) {
                    f32x16 s = MFMA32(f[0], q0, z16); s = MFMA32(f[1], q1, s);
                    float tm = s[0];
#pragma unroll
                    for (int r = 1; r < 16; ++r) tm = fmaxf(tm, s[r]);
                    tm = max_h(tm);
                    const float mn = fmaxf(m, tm);
                    const float alpha = __builtin_amdgcn_exp2f(m - mn);
                    m = mn;
#pragma unroll
                    for (int r = 0; r < 16; ++r) { s[r] = __builtin_amdgcn_exp2f(s[r] - mn); o[r] *= alpha; }
                    const u32x4 p0 = {pack_bf16(s[0], s[1]), pack_bf16(s[2], s[3]), pack_bf16(s[4], s[5]), pack_bf16(s[6], s[7])};
                    const u32x4 p1 = {pack_bf16(s[8], s[9]), pack_bf16(s[10], s[11]), pack_bf16(s[12], s[13]), pack_bf16(s[14], s[15])};
                    f32x4 t4 = {0, 0, 0, 0};
                    o = MFMA32(f[2], __builtin_bit_cast(bf16x8, p0), o);
                    o = MFMA32(f[3], __builtin_bit_cast(bf16x8, p1), o);
                    t4 = ROWSUM(p0, p1, t4);
                    l = l * alpha + (t4[0] + t4[1]);
                };
                upd(qA0, qA1, oA, lA, mA);
                upd(qB0, qB1, oB, lB, mB);
            }
            store();
        }
    }
}

DI void ln_row(const u32x4 (&v)[2], __hip_bfloat16* __restrict__ Y, unsigned off) {
    float s1 = 0.f, s2 = 0.f;
#pragma unroll
    for (int e = 0; e < 8; ++e) s1 = dot2bf(v[e >> 2][e & 3], 0x3f803f80u, s1);
    const float mean = wave_sum(s1) * (1.f / 1024.f);
#pragma unroll
    for (int e = 0; e < 8; ++e) {
        const float d0 = bf_lo(v[e >> 2][e & 3]) - mean, d1 = bf_hi(v[e >> 2][e & 3]) - mean;
        s2 += d0 * d0 + d1 * d1;
    }
    const float mb = round_bf16(mean);
    const float rden = __builtin_amdgcn_rcpf(round_bf16(__builtin_amdgcn_sqrtf(round_bf16(round_bf16(wave_sum(s2) * (1.f / 1024.f)) + 1e-5f))));
#pragma unroll
    for (int hh = 0; hh < 2; ++hh) {
        u32x4 y;
#pragma unroll
        for (int e = 0; e < 4; ++e) {
            const unsigned d = pack_bf16(bf_lo(v[hh][e]) - mb, bf_hi(v[hh][e]) - mb);
            y[e] = pack_bf16(bf_lo(d) * rden, bf_hi(d) * rden);
        }
        st16((char*)Y + hh * 131072, off, y);
    }
}

extern "C" __global__ __launch_bounds__(512, 2) void mega_kernel(const __hip_bfloat16* x, const __hip_bfloat16* wqkvA,
                                                            const __hip_bfloat16* woA, const __hip_bfloat16* wqkvB,
                                                            const __hip_bfloat16* woB, __hip_bfloat16* out,
                                                            char* ws) {
    char* const smem = smem_base;
    typedef __hip_bfloat16 bf;
    const int w8 = __builtin_amdgcn_readfirstlane(threadIdx.x >> 6);
    if (w8 == 7 && (blockIdx.x >> 3) < 10) code_touch(blockIdx.x >> 3, threadIdx.x & 63);
    asm volatile("" :: "s"(x), "s"(wqkvA), "s"(woA), "s"(wqkvB), "s"(woB), "s"(out), "s"(ws));
    STAMP_DECL; STAMPR(14); STAMP(0);
    u64 tag;
    {
        const int lane = threadIdx.x & 63;
        const Ids I = ids();
        const unsigned off = (unsigned)((I.po * 128 + I.co * 8 + w8) * 2048 + lane * 16);
        u32x4 v[2];
        u32x2 ep;
        asm volatile("global_load_dwordx4 %0, %3, %4\n\tglobal_load_dwordx4 %1, %3, %4 offset:1024\n\tglobal_load_dwordx2 %2, %5, %6 sc1"
                     : "=&v"(v[0]), "=&v"(v[1]), "=&v"(ep) : "v"(off), "s"(x), "v"(0), "s"(ws + WS_EPOCH) : "memory");
        if (w8 == 7) {
            const int l = lane & 15, k = l % 5;
            const unsigned o = (l >= 10 || k == 3) ? flag_off(l, I.cq, I.pq, 0) : (k == 0 || k == 2) ? flag_off(l, I.pq, I.cq, 0) : flag_off(l, I.po, I.co, 0);
            touch_at(ws + WS_FLAG, o);
        } else if (w8 >= 4) touch(wqkvA, (w8 - 4) * 256 + blockIdx.x, lane);
        if (w8 >= 4) asm volatile("s_waitcnt vmcnt(2)" ::: "memory"); else asm volatile("s_waitcnt vmcnt(1)" ::: "memory");
        asm volatile("" : "+v"(v[0]), "+v"(v[1]));
        ln_row(v, (bf*)(ws + WS_Y0), (unsigned)(I.po * 262144 + (lane >> 3) * 16384 + (I.co * 8 + w8) * 128 + (lane & 7) * 16));
        if (w8 >= 4) asm volatile("s_waitcnt vmcnt(3)" ::: "memory"); else asm volatile("s_waitcnt vmcnt(2)" ::: "memory");
        asm volatile("" : "+v"(ep));
        tag = (((u64)(unsigned)__builtin_amdgcn_readfirstlane(ep[1]) << 32) | (unsigned)__builtin_amdgcn_readfirstlane(ep[0])) + 1;
    }
    if (w8 >= 4) {
        int w = w8 & 3;
#pragma unroll 1
        for (int it = 0; it < 2; ++it) {
            const int lane = lane_id();
            asm volatile("" : "+s"(x), "+s"(wqkvA), "+s"(woA), "+s"(wqkvB), "+s"(woB), "+s"(ws), "+s"(w));
            const unsigned lds0 = (unsigned)(uintptr_t)smem;
            u32x4 cb[8];
            const unsigned wv = wo_off(w, lane), rv = res_off(true, it, lane), cv = colsum_off(w, lane);
            qkv_loader<true>(smem, w, ws, it * 5, tag, (const bf*)(ws + (it ? WS_X2 : WS_Y0)), wqkvA, (const float*)(ws + WS_ST + 262144),
                             ws + WS_CSP, it == 0, woA, [&] {
                if (w < 3) {
                    colsum_load(cb, wqkvB, cv);
                    asm volatile("s_waitcnt vmcnt(8)" ::: "memory");
                } else {
                    touch(woA, blockIdx.x, lane);
                    asm volatile("s_waitcnt vmcnt(1)" ::: "memory");
                }
            }, [&](int part) {
                if (part == 0) wo_load(woA, wv, 29, 32, lds0, w); else wo_load(woA, wv, 25, 29, lds0, w);
            } SP_ARG);
            STAMP(3);
            u32x4 res[6];
            res_load(res, it ? (const void*)(ws + WS_X2) : x, true, it, rv, w);
            if (it) __builtin_amdgcn_s_sleep(8);
            wo_load(woA, wv, 0, 7, lds0, w);
            BAR();
            wo_load(woA, wv, 7, 25, lds0, w);
            STAMP(4);
            out_handoff(smem, 147456, res, w, ws, it * 5 + 1, tag, lane, cb, it ? -1 : 1);
            STAMP(5);
            qkv_loader<false>(smem, w, ws, it * 5 + 2, tag, (const bf*)(ws + (it ? WS_X3 : WS_X1)), wqkvB, (const float*)(ws + WS_ST + it * 524288), ws + WS_CSP + 196608,
                              false, nullptr, [] {}, [](int) {} SP_ARG);
            STAMP(8);
            attn_loader(smem, w, ws, it * 5 + 3, tag, (const bf*)(ws + WS_KVF + it * 8 * MBy), woB, ws + (it ? WS_X3 : WS_X1),
                        it ? nullptr : wqkvA SP_ARG);
            STAMP(13); STAMPR(15); STAMP_FLUSH(it);
        }
        if (blockIdx.x == 0 && w == 0 && lane_id() == 0) __hip_atomic_store((g_u64ptr)(ws + WS_EPOCH), tag, __ATOMIC_RELAXED, __HIP_MEMORY_SCOPE_AGENT);
    } else {
        __builtin_amdgcn_s_setprio(2);
#pragma unroll 1
        for (int it = 0; it < 2; ++it) {
            u32x4 q4[4];
            asm volatile("" : "+s"(ws), "+s"(wqkvA), "+s"(wqkvB));
            qkv_compute<true>(smem, w8, wqkvA, q4, nullptr, (bf*)(ws + WS_OF + it * 8 * MBy), it == 0,
                              ws + WS_ST + 262144, ws + WS_WN + blockIdx.x * 16 SP_ARG);
            STAMP(2);
            out_compute<true>(smem, w8, (const bf*)(ws + WS_OF + it * 8 * MBy),
                              (bf*)(ws + (it ? WS_X3 : WS_X1)), (float*)(ws + WS_ST + it * 524288) SP_ARG);
            STAMP(6);
            qkv_compute<false>(smem, w8, wqkvB, q4, (bf*)(ws + WS_KVF + it * 8 * MBy), nullptr, false,
                               ws + WS_ST + it * 524288, ws + WS_WN + 4096 + blockIdx.x * 16 SP_ARG);
            attn_compute(smem, w8, q4, (const bf*)(ws + WS_KVF + it * 8 * MBy), (bf*)(ws + WS_OF + (it * 8 + 4) * MBy) SP_ARG);
            out_compute<false>(smem, w8, (const bf*)(ws + WS_OF + (it * 8 + 4) * MBy),
                               it ? out : (bf*)(ws + WS_X2), it ? nullptr : (float*)(ws + WS_ST + 262144) SP_ARG);
            STAMP(13); STAMPR(15); STAMP_FLUSH(it);
        }
    }
}

extern "C" __global__ void zz_code_end_kernel() {}

extern "C" void kernel_launch(void* const* d_in, const int* in_sizes, int n_in, void* d_out, int out_size, void* d_ws,
                              size_t ws_size, hipStream_t stream) {
    typedef __hip_bfloat16 bf;
    (void)hipFuncSetAttribute((const void*)mega_kernel, hipFuncAttributeMaxDynamicSharedMemorySize, CH_LDS);
    mega_kernel<<<256, 512, CH_LDS, stream>>>((const bf*)d_in[0], (const bf*)d_in[1], (const bf*)d_in[2], (const bf*)d_in[4], (const bf*)d_in[5],
                                              (bf*)d_out, (char*)d_ws);
}
```

```cpp
#include <hip/hip_runtime.h>
#include <hip/hip_bf16.h>
#include <stdint.h>

typedef __attribute__((ext_vector_type(8))) short bf16x8;
typedef __attribute__((ext_vector_type(4))) short s16x4;
typedef __attribute__((ext_vector_type(4))) float f32x4;
typedef __attribute__((ext_vector_type(2))) float f32x2;
typedef __attribute__((ext_vector_type(16))) float f32x16;
typedef __attribute__((ext_vector_type(4))) unsigned u32x4;
typedef __attribute__((ext_vector_type(2))) unsigned u32x2;
typedef __attribute__((ext_vector_type(2))) __bf16 bf16x2_t;
typedef __attribute__((ext_vector_type(8))) __bf16 bf8_t;
typedef __attribute__((ext_vector_type(16))) __bf16 bf16v_t;
typedef __attribute__((address_space(3))) const char* lds_cptr;
typedef __attribute__((address_space(3))) char* lds_ptr;

#define DI __device__ __forceinline__
#define MFMA16(a, b, c) __builtin_amdgcn_mfma_f32_16x16x32_bf16((a), (b), (c), 0, 0, 0)
#define MFMA32(a, b, c) __builtin_amdgcn_mfma_f32_32x32x16_bf16((a), (b), (c), 0, 0, 0)
#define SBAR() __builtin_amdgcn_sched_barrier(0)
#define STAMP(i)
#define STAMPR(i)
#define STAMP_DECL
#define STAMP_FLUSH(it)
#define SP_PARAM
#define SP_ARG
typedef unsigned long long u64;
typedef __attribute__((ext_vector_type(32))) unsigned u32x32;

constexpr float QSCALE = 0.17677669529663687f * 1.4426950408889634f;
constexpr float ATT_SCALE = 0.17677669529663687f;
constexpr float LOG2E = 1.4426950408889634f;
constexpr float NEG_MASK = -1e9f / ATT_SCALE;

DI int lane_id() { int l; asm volatile("v_mbcnt_lo_u32_b32 %0, -1, 0\n\tv_mbcnt_hi_u32_b32 %0, -1, %0" : "=v"(l)); return l; }
DI unsigned pack_bf16(float lo, float hi) {
    f32x2 v = {lo, hi};
    bf16x2_t r = __builtin_convertvector(v, bf16x2_t);
    return __builtin_bit_cast(unsigned, r);
}
DI float bf_lo(unsigned u) { return __uint_as_float(u << 16); }
DI float bf_hi(unsigned u) { return __uint_as_float(u & 0xffff0000u); }
DI float round_bf16(float x) { return bf_lo(pack_bf16(x, 0.f)); }
DI void ln_stats(float sum, float sumsq, float& mean_out, float& rstd_out) {
    float mean;
    asm("v_mul_f32 %0, 0x3a800000, %1" : "=v"(mean) : "v"(sum));
    const float var = fmaxf(__builtin_fmaf(-mean, mean, sumsq * (1.f / 1024.f)), 0.f);
    mean_out = round_bf16(mean);
    rstd_out = __builtin_amdgcn_rcpf(round_bf16(__builtin_amdgcn_sqrtf(round_bf16(round_bf16(var) + 1e-5f))));
}

DI void glds16(const void* sbase, unsigned voff, unsigned ldsaddr) {
    asm volatile("s_mov_b32 m0, %0\n\ts_nop 0\n\tglobal_load_lds_dwordx4 %1, %2"
                 :: "s"(ldsaddr), "v"(voff), "s"(sbase) : "memory");
}
DI void glds16c(const void* sbase, unsigned voff, unsigned ldsaddr) {
    asm volatile("s_mov_b32 m0, %0\n\ts_nop 0\n\tglobal_load_lds_dwordx4 %1, %2 sc1"
                 :: "s"(ldsaddr), "v"(voff), "s"(sbase) : "memory");
}
DI void st16(void* sbase, unsigned voff, u32x4 v) {
    asm volatile("global_store_dwordx4 %0, %1, %2 sc1\n\ts_nop 1" :: "v"(voff), "v"(v), "s"(sbase) : "memory");
}
template <int OFF> DI void st16o(void* sbase, unsigned voff, u32x4 v) {
    asm volatile("global_store_dwordx4 %0, %1, %2 offset:%3 sc1\n\ts_nop 1" :: "v"(voff), "v"(v), "s"(sbase), "n"(OFF) : "memory");
}
DI bf16x8 lds_b128(lds_cptr p) { return *(const __attribute__((address_space(3))) bf16x8*)p; }
DI s16x4 lds_tr(lds_cptr p) {
    return __builtin_amdgcn_ds_read_tr16_b64_v4i16((__attribute__((address_space(3))) s16x4*)p);
}
DI bf16x8 cat8(s16x4 a, s16x4 b) { return __builtin_shufflevector(a, b, 0, 1, 2, 3, 4, 5, 6, 7); }
DI float dot2bf(unsigned a, unsigned b, float c) {
    return __builtin_amdgcn_fdot2_f32_bf16(__builtin_bit_cast(bf16x2_t, a), __builtin_bit_cast(bf16x2_t, b), c, false);
}
DI float sum_g(float v) {
    auto a = __builtin_amdgcn_permlane16_swap(__float_as_uint(v), __float_as_uint(v), false, false);
    v = __uint_as_float(a[0]) + __uint_as_float(a[1]);
    auto b = __builtin_amdgcn_permlane32_swap(__float_as_uint(v), __float_as_uint(v), false, false);
    return __uint_as_float(b[0]) + __uint_as_float(b[1]);
}
DI float wave_sum(float v) {
    v += __builtin_bit_cast(float, __builtin_amdgcn_update_dpp(0, __builtin_bit_cast(int, v), 0xB1, 0xF, 0xF, true));
    v += __builtin_bit_cast(float, __builtin_amdgcn_update_dpp(0, __builtin_bit_cast(int, v), 0x4E, 0xF, 0xF, true));
    v += __builtin_bit_cast(float, __builtin_amdgcn_update_dpp(0, __builtin_bit_cast(int, v), 0x141, 0xF, 0xF, true));
    v += __builtin_bit_cast(float, __builtin_amdgcn_update_dpp(0, __builtin_bit_cast(int, v), 0x140, 0xF, 0xF, true));
    return sum_g(v);
}
DI unsigned wave_min(unsigned v) {
    v = min(v, (unsigned)__builtin_amdgcn_update_dpp(0, (int)v, 0xB1, 0xF, 0xF, true));
    v = min(v, (unsigned)__builtin_amdgcn_update_dpp(0, (int)v, 0x4E, 0xF, 0xF, true));
    v = min(v, (unsigned)__builtin_amdgcn_update_dpp(0, (int)v, 0x141, 0xF, 0xF, true));
    v = min(v, (unsigned)__builtin_amdgcn_update_dpp(0, (int)v, 0x140, 0xF, 0xF, true));
    const auto a = __builtin_amdgcn_permlane16_swap(v, v, false, false);
    v = min(a[0], a[1]);
    const auto b = __builtin_amdgcn_permlane32_swap(v, v, false, false);
    return min(b[0], b[1]);
}
DI float amax3(float a, float b, float c) { float r; asm("v_max3_f32 %0, %1, %2, %3" : "=v"(r) : "v"(a), "v"(b), "v"(c)); return r; }
DI float max2f(float a, float b) { return __builtin_amdgcn_fmed3f(a, b, INFINITY); }
DI float max_h(float v) {
    auto b = __builtin_amdgcn_permlane32_swap(__float_as_uint(v), __float_as_uint(v), false, false);
    return max2f(__uint_as_float(b[0]), __uint_as_float(b[1]));
}

constexpr unsigned OFF_DUMP = 145408;
extern __shared__ __attribute__((aligned(16))) char smem_base[];
DI void touch_at(const void* p, unsigned off) {
    asm volatile("s_mov_b32 m0, %0\n\ts_nop 0\n\tglobal_load_lds_dword %1, %2"
                 :: "s"((unsigned)(uintptr_t)smem_base + OFF_DUMP), "v"(off), "s"(p) : "memory");
}
DI void touch(const void* p, int slice, int lane) { touch_at(p, (unsigned)(slice * 64 + lane) * 128u); }
DI void code_touch(int slice, int lane) {
    unsigned long long c0, c1;
    asm volatile("s_getpc_b64 s[96:97]\n\ts_add_u32 s96, s96, mega_kernel@rel32@lo+4\n\ts_addc_u32 s97, s97, mega_kernel@rel32@hi+12\n\ts_mov_b64 %0, s[96:97]\n\t"
                 "s_getpc_b64 s[96:97]\n\ts_add_u32 s96, s96, zz_code_end_kernel@rel32@lo+4\n\ts_addc_u32 s97, s97, zz_code_end_kernel@rel32@hi+12\n\ts_mov_b64 %1, s[96:97]"
                 : "=s"(c0), "=s"(c1) :: "s96", "s97", "scc");
    touch_at((const void*)c0, min((unsigned)(slice * 64 + lane) * 128u, (unsigned)(c1 - c0)));
}

#define BAR() asm volatile("s_barrier" ::: "memory")
#define WAIT_LGKM() do { SBAR(); __builtin_amdgcn_s_waitcnt(0xC07F); SBAR(); } while (0)
#define WAIT_LGKM_BAR() do { SBAR(); __builtin_amdgcn_s_waitcnt(0xC07F); asm volatile("s_barrier" ::: "memory"); SBAR(); } while (0)
#define VM_BAR(n) asm volatile("s_waitcnt vmcnt(" #n ")\n\ts_barrier" ::: "memory")
constexpr int CH_LDS = 163840;
typedef __attribute__((address_space(1))) u64* g_u64ptr;

constexpr size_t MBy = 1 << 20;
constexpr size_t WS_X3 = 0, WS_X2 = 4 * MBy, WS_X1 = 8 * MBy, WS_OF = 12 * MBy  , WS_KVF = 36 * MBy  ,
                 WS_ST = 52 * MBy  , WS_CSP = 54 * MBy  , WS_FLAG = 55 * MBy  ,
                 WS_WN = WS_CSP + 458752  , WS_EPOCH = WS_CSP + 524288, WS_Y0 = 56 * MBy;
DI unsigned flag_off(int set, int group, int consumer, int producer) { return (unsigned)(set * 65536 + group * 4096 + consumer * 256 + producer * 8); }
DI void flag_store(char* ws, int set, int group, int producer, u64 tag, int lane) {
    asm volatile("" : "+s"(tag));
    if (lane < 16) __hip_atomic_store((g_u64ptr)(ws + WS_FLAG + flag_off(set, group, lane, producer)), tag, __ATOMIC_RELAXED, __HIP_MEMORY_SCOPE_AGENT);
}
DI void flag_poll(const char* ws, unsigned off, u64 tag) {
    asm volatile("" : "+s"(tag));
#pragma unroll 1
    for (int spin = 0; spin < (1 << 19); ++spin) {
        const u64 v = __hip_atomic_load((g_u64ptr)(ws + WS_FLAG + off), __ATOMIC_RELAXED, __HIP_MEMORY_SCOPE_AGENT);
        if (__all(v == tag)) break;
        __builtin_amdgcn_s_sleep(1);
    }
    asm volatile("" ::: "memory");
}
template <bool DRAIN, bool ALLPOLL>
DI void signal_poll(int follower, const char* ws, unsigned soff, unsigned poff, u64 pmask, u64 tag, u64& land) {
    u64 tagv, zero;
    asm volatile("s_not_b64 s[96:97], %[ts]\n\tv_mov_b64 %[tag], %[ts]\n\tv_mov_b64 %[zero], 0\n\tv_mov_b64 %[land], s[96:97]\n\t"
                 ".if %[dr]\n\ts_waitcnt vmcnt(0)\n\t.endif\n\ts_barrier\n\t"
                 "s_cmp_lg_u32 %[fl], 0\n\ts_cbranch_scc1 .Lsig_fol_%=\n\t"
                 "s_mov_b64 exec, 0xffff\n\t"
                 "global_store_dwordx2 %[so], %[tag], %[base] sc1\n"
                 ".if %[ap]\n.Lsig_fol_%=:\n.endif\n\t"
                 "s_mov_b64 exec, %[pm]\n\t"
                 "s_mov_b32 s96, 0x100000\n"
                 ".Lsig_loop_%=:\n\t"
                 "global_atomic_or_x2 %[land], %[po], %[zero], %[base] sc0 sc1\n\t"
                 "s_sleep 1\n\t"
                 "s_waitcnt vmcnt(3)\n\t"
                 "v_cmp_ne_u64 vcc, %[land], %[tag]\n\t"
                 "s_sub_u32 s96, s96, 1\n\t"
                 "s_cbranch_scc1 .Lsig_out_%=\n\t"
                 "s_cbranch_vccnz .Lsig_loop_%=\n"
                 ".Lsig_out_%=:\n\t"
                 "s_mov_b64 exec, -1\n"
                 ".if !%[ap]\n.Lsig_fol_%=:\n.endif"
                 : [land] "=&v"(land), [tag] "=&v"(tagv), [zero] "=&v"(zero)
                 : [fl] "s"(follower), [so] "v"(soff), [po] "v"(poff), [ts] "s"(tag), [base] "s"(ws + WS_FLAG), [pm] "s"(pmask), [ap] "n"(ALLPOLL ? 1 : 0), [dr] "n"(DRAIN ? 1 : 0)
                 : "memory", "scc", "vcc", "s96", "s97");
}
struct Ids { int pq, cq, po, co; };
DI Ids ids() {
    int bid = blockIdx.x; asm volatile("" : "+s"(bid));
    const int xcd = bid & 7, idx = bid >> 3;
    int pq, cq, po, co;
#define IDS_BODY pq = (xcd & 1) * 8 + (idx & 7); cq = (xcd >> 1) * 4 + (idx >> 3); po = (xcd & 3) * 4 + (idx & 3); co = (xcd >> 2) * 8 + (idx >> 2);
    IDS_BODY
    return Ids{pq, cq, po, co};
}

DI void slow_warm(unsigned dump, const void* W, int w, int lane) {
    int bid = blockIdx.x; asm volatile("" : "+s"(bid));
    const int xcd = bid & 7, idx = bid >> 3;
    const int sc = (3 - (int)((uintptr_t)W >> 7)) & 7;
    if ((sc >> 2) != ((xcd >> 1) & 1)) return;
    const char* src = (const char*)W + (idx + 32 * w) * 49152 + (sc + (xcd >> 2) * 8) * 128;
#pragma unroll
    for (int G = 0; G < 3; ++G) glds16(src + G * 2048, (unsigned)((lane >> 3) * 6144 + (lane & 7) * 16), dump);
}

template <int V> struct IC { static constexpr int value = V; };
template <bool LOCAL> struct QC {
    static constexpr int AROWS = LOCAL ? 144 : 128;
    static constexpr int ABYTES = AROWS * 128;
    static constexpr int STAGE = ABYTES + 24576;
    static constexpr int OFF_MEAN = 143360;
    static constexpr int OFF_RSTD = OFF_MEAN + 576;
    static constexpr int OFF_CS = OFF_RSTD + 576;
};
constexpr int OFF_EX = 131072;

DI unsigned colsum_off(int G, int lane) {
    const Ids I = ids();
    unsigned off = (unsigned)(I.pq * 64 + (lane >> 3)) * 6144u + (unsigned)(G * 2048 + I.cq * 128 + (lane & 7) * 16);
    asm volatile("" : "+v"(off));
    return off;
}
DI void colsum_load(u32x4 (&c)[8], const void* W, unsigned off) {
#define CS_NEXT "s_add_u32 s96, s96, 0xc000\n\ts_addc_u32 s97, s97, 0\n\t"
    asm volatile("s_cmp_eq_u64 %9, 0\n\ts_cbranch_scc1 .Lcs_skip_%=\n\ts_mov_b64 s[96:97], %9\n\t"
                 "global_load_dwordx4 %0, %8, s[96:97]\n\t" CS_NEXT "global_load_dwordx4 %1, %8, s[96:97]\n\t" CS_NEXT
                 "global_load_dwordx4 %2, %8, s[96:97]\n\t" CS_NEXT "global_load_dwordx4 %3, %8, s[96:97]\n\t" CS_NEXT
                 "global_load_dwordx4 %4, %8, s[96:97]\n\t" CS_NEXT "global_load_dwordx4 %5, %8, s[96:97]\n\t" CS_NEXT
                 "global_load_dwordx4 %6, %8, s[96:97]\n\t" CS_NEXT "global_load_dwordx4 %7, %8, s[96:97]\n"
                 ".Lcs_skip_%=:"
                 : "=&v"(c[0]), "=&v"(c[1]), "=&v"(c[2]), "=&v"(c[3]), "=&v"(c[4]), "=&v"(c[5]), "=&v"(c[6]), "=&v"(c[7])
                 : "v"(off), "s"(W) : "memory", "scc", "s96", "s97");
}
DI void colsum_store(u32x4 (&c)[8], char* ws, int mat, int G, int lane) {
    asm volatile("" : "+v"(c[0]), "+v"(c[1]), "+v"(c[2]), "+v"(c[3]), "+v"(c[4]), "+v"(c[5]), "+v"(c[6]), "+v"(c[7]));
    const Ids I = ids();
    const int m = lane & 15;
    const unsigned one = (m & 1) ? 0xbf800000u : 0xbf80u;
    const u32x4 sl = {(m >> 1) == 0 ? one : 0u, (m >> 1) == 1 ? one : 0u, (m >> 1) == 2 ? one : 0u, (m >> 1) == 3 ? one : 0u};
    f32x4 d = {0, 0, 0, 0};
#pragma unroll
    for (int i = 0; i < 8; ++i) d = MFMA16(__builtin_bit_cast(bf16x8, sl), __builtin_bit_cast(bf16x8, c[i]), d);
#pragma unroll
    for (int r = 0; r < 4; ++r)
        d[r] += __int_as_float(__builtin_amdgcn_update_dpp(0, __float_as_int(d[r]), 0x128, 0xF, 0xF, true));
    if (lane < 32 && (lane & 8) == 0)
        st16(ws + WS_CSP + mat * 196608, (unsigned)((I.cq * 16 + I.pq) * 768 + G * 256 + (lane & 7) * 32 + (lane >> 4) * 16), __builtin_bit_cast(u32x4, d));
    typedef __attribute__((ext_vector_type(2))) unsigned short u16x2;
    u16x2 mn = {0x7fff, 0x7fff};
#pragma unroll
    for (int e = 0; e < 4; ++e) {
        u16x2 mx = {0, 0};
#pragma unroll
        for (int i = 0; i < 8; ++i) { const unsigned u = c[i][e] & 0x7fff7fffu; mx = __builtin_elementwise_max(mx, __builtin_bit_cast(u16x2, u)); }
        mn = __builtin_elementwise_min(mn, mx);
    }
    const unsigned wn = wave_min((unsigned)min(mn[0], mn[1])) << 16;
    if (lane == 0) __hip_atomic_store((__attribute__((address_space(1))) unsigned*)(ws + WS_WN + mat * 4096 + blockIdx.x * 16 + G * 4), wn,
                                      __ATOMIC_RELAXED, __HIP_MEMORY_SCOPE_AGENT);
}

template <bool LOCAL>
struct WFirst {
    unsigned v[2];
    DI WFirst(int w, int ct, int lane) {
#pragma unroll
        for (int i = 0; i < 2; ++i) {
            const int k = 8 * i + (lane >> 3), pos = lane & 7;
            v[i] = (unsigned)k * 6144u + (unsigned)(w * 1024 + ct * 64 + ((pos >> 1) ^ (((k >> 1) & 1) | (i << 1))) * 16 + (pos & 1) * 8) * 2u;
        }
        asm volatile("" : "+v"(v[0]), "+v"(v[1]));
    }
    DI void issue(const void* W, unsigned lds0, int w, int i0, int i1) const {
#pragma unroll
        for (int i = i0; i < i1; ++i)
            glds16((const char*)W + (size_t)(i >> 1) * (16 * 6144), v[i & 1], lds0 + (i >> 3) * QC<LOCAL>::STAGE + QC<LOCAL>::ABYTES + (w * 8 + (i & 7)) * 1024);
    }
};

template <bool LOCAL, class F, class F2>
DI void qkv_loader(char* smem, int w, char* ws, int set, u64 tag, const __hip_bfloat16* __restrict__ X, const __hip_bfloat16* __restrict__ W,
                   const float* __restrict__ statp, const char* __restrict__ csp, bool ident, const void* nextW, F tail, F2 mid SP_PARAM) {
    typedef QC<LOCAL> C;
    constexpr int NT = 16;
    const int lane = lane_id();
    asm volatile("" : "+s"(w));
    const int idx = blockIdx.x >> 3;
    const Ids I = ids();
    const int panel = I.pq, ct = I.cq;
    const int R0 = panel * 128;
    const unsigned lds0 = (unsigned)(uintptr_t)smem;
    auto grow = [&](int i) -> int {
        if (!LOCAL) return R0 + i;
        if (i < 128) { const int t = (i >> 4) & 3, a = i & 15; return R0 + (i & 64) + 2 * (16 * (t & 1) + a) + (t >> 1); }
        const int a = i & 15; const int r = R0 - 16 + 2 * (a & 7) + (a >> 3);
        return r < 0 ? 0 : r;
    };
    unsigned voffA[5], voffB[6];
#pragma unroll
    for (int i = 0; i < 6; ++i) {
        const int j = w + 4 * i, G = j >> 3, k = 8 * (j & 7) + (lane >> 3);
        const int pos = lane & 7, f = ((k >> 1) & 1) | (((k >> 3) & 1) << 1);
        const int seg = (pos >> 1) ^ f;
        voffB[i] = (unsigned)k * 6144u + (unsigned)(G * 1024 + ct * 64 + seg * 16 + (pos & 1) * 8) * 2u;
    }
    auto issueW = [&](int t, int buf) {
        const char* wb = (const char*)W + (size_t)t * (64 * 6144);
        const unsigned sb = lds0 + buf * C::STAGE;
#pragma unroll
        for (int i = 0; i < 6; ++i) glds16(wb, voffB[i], sb + C::ABYTES + (w + 4 * i) * 1024);
    };
    asm volatile("" : "+v"(voffB[0]), "+v"(voffB[1]), "+v"(voffB[2]), "+v"(voffB[3]), "+v"(voffB[4]), "+v"(voffB[5]));
    const WFirst<LOCAL> first(w, ct, lane);
    const int g = lane >> 4, r = lane & 15, pos = 32 * w + 2 * r + (g >> 1);
    const int lr = !LOCAL ? (pos & 64) + 2 * (pos & 31) + ((pos >> 5) & 1)
                          : (w == 0 && (g & 1)) ? 128 + (r & 7) + 8 * (g >> 1) : (pos & 64) + ((pos & 1) * 2 + ((pos >> 5) & 1)) * 16 + ((pos >> 1) & 15);
    unsigned so = (unsigned)(panel * 16384 + g * 1024 + w * 256 + r * 16), ho = (unsigned)(g * 1024 + (panel ? panel * 16384 - 15488 + (r & 7) * 16 : 0));
    unsigned co = (unsigned)((ct * 16 + g) * 768 + w * 192 + r * 12);
    lds_ptr mp = (lds_ptr)smem + C::OFF_MEAN + lr * 4, cp = (lds_ptr)smem + C::OFF_CS + w * 192 + r * 12 + ((g & 1) ? 8 : g * 2);
    unsigned fo = flag_off((LOCAL ? 13 : 10) + min(g, 2), ct, panel, r);
    asm volatile("" : "+v"(so), "+v"(ho), "+v"(co), "+v"(mp), "+v"(cp), "+v"(fo));
#pragma unroll
    for (int i = 0; i < 5; ++i) {
        const int j = (i < 4) ? (w + 4 * i) : (16 + (w & 1));
        const int row = 8 * j + (lane >> 3);
        const int c = (lane & 7) ^ ((row >> 1) & 7);
        const int gr = grow(row < C::AROWS ? row : 0);
        voffA[i] = (unsigned)((gr >> 7) * 262144 + (gr & 127) * 128 + c * 16);
        asm volatile("" : "+v"(voffA[i]));
    }
    auto issueA = [&](int t, int buf) {
        const char* xa = (const char*)X + t * 16384;
        const unsigned sb = lds0 + buf * C::STAGE;
        if (LOCAL && w < 2) glds16c(xa, voffA[4], sb + (16 + w) * 1024);
#pragma unroll
        for (int i = 0; i < 4; ++i) glds16c(xa, voffA[i], sb + (w + 4 * i) * 1024);
    };
    u64 land;
    if (w == 3) __builtin_amdgcn_s_setprio(3);
    signal_poll<LOCAL, false>(w - 3, ws, flag_off(set, I.po, lane & 15, I.co),
                              flag_off(set, (lane & 16) && panel ? panel - 1 : panel, ct, lane & 15),
                              !LOCAL ? 0xffffull : ident ? 0xc000ffffull : 0xffffffffull, tag, land);
    if (w < 3) first.issue(W, lds0, w, 0, 12);
    BAR();
    STAMP(LOCAL ? 1 : 6);
    issueA(0, 0);
    issueA(1, 1);
    if (w < 3) { first.issue(W, lds0, w, 12, 16); asm volatile("s_waitcnt vmcnt(8)" ::: "memory"); }
    else asm volatile("s_waitcnt vmcnt(4)" ::: "memory");
    BAR();
    asm volatile("" :: "v"(land));
    __builtin_amdgcn_s_setprio(0);
    STAMP(LOCAL ? 2 : 7);
    issueW(2, 2);
    issueA(2, 2);
    VM_BAR(10);
    asm volatile("global_load_dwordx2 %0, %1, %2 sc1" : "=&v"(land) : "v"(fo), "s"(ws + WS_FLAG) : "memory");
    issueA(3, 0); issueW(3, 0);
    int buf = 1;
#pragma unroll 1
    for (int t = 1; t < NT - 3; ++t) {
        VM_BAR(10);
        issueA(t + 3, buf); issueW(t + 3, buf);
        if (t == 10 && idx < 3) code_touch(idx * 4 + w, lane);
        if (LOCAL && t == 11)
            glds16(nextW, (unsigned)((idx + 32 * w) * 8 + (lane >> 3)) * 2048u +
                   (unsigned)(((I.co & 8) + ((3 - (int)((uintptr_t)nextW >> 7)) & 7)) * 128 + (lane & 7) * 16), lds0 + OFF_DUMP);
        buf = (buf == 2) ? 0 : buf + 1;
    }
    typedef __attribute__((ext_vector_type(3))) float f32x3;
    const bool halo = LOCAL && w == 0;
    asm volatile("" : "+v"(land));
    if (!ident) {
        if (!__all(land == tag)) flag_poll(ws, fo, tag);
        f32x3 cs[4];
        f32x4 sv[4], hv[4];
        typedef __attribute__((address_space(3))) float* lds_f;
        auto add = [](float a, float b) { float r; asm("v_add_f32 %0, %1, %2" : "=v"(r) : "v"(a), "v"(b)); return r; };
        auto fold32 = [](float a, float b) { asm("s_nop 1\n\tv_permlane32_swap_b32 %0, %1\n\tv_add_f32 %0, %0, %1" : "+v"(a), "+v"(b)); return a; };
        auto fold16 = [](float a, float b) { asm("s_nop 1\n\tv_permlane16_swap_b32 %0, %1\n\tv_add_f32 %0, %0, %1" : "+v"(a), "+v"(b)); return a; };
        auto sum4 = [&](auto (&v)[4], int e) { return add(add(v[0][e], v[1][e]), add(v[2][e], v[3][e])); };
#pragma unroll
        for (int i = 0; i < 4; ++i) asm volatile("global_load_dwordx3 %0, %1, %2 sc1" : "=&v"(cs[i]) : "v"(co), "s"(csp + i * 3072) : "memory");
        VM_BAR(14);
#pragma unroll
        for (int i = 0; i < 4; ++i) asm volatile("global_load_dwordx4 %0, %1, %2 sc1" : "=&v"(sv[i]) : "v"(so), "s"((const char*)statp + i * 4096) : "memory");
        if (LOCAL)
            asm volatile("s_cmp_lg_u32 %9, 0\n\ts_cbranch_scc1 .Lhv_skip_%=\n\t"
                         "global_load_dwordx4 %0, %4, %5 sc1\n\tglobal_load_dwordx4 %1, %4, %6 sc1\n\t"
                         "global_load_dwordx4 %2, %4, %7 sc1\n\tglobal_load_dwordx4 %3, %4, %8 sc1\n"
                         ".Lhv_skip_%=:" : "=&v"(hv[0]), "=&v"(hv[1]), "=&v"(hv[2]), "=&v"(hv[3])
                         : "v"(ho), "s"(statp), "s"(statp + 1024), "s"(statp + 2048), "s"(statp + 3072), "s"(w) : "memory", "scc");
        if (halo) asm volatile("s_waitcnt vmcnt(8)" ::: "memory"); else asm volatile("s_waitcnt vmcnt(4)" ::: "memory");
        asm volatile("" : "+v"(cs[0]), "+v"(cs[1]), "+v"(cs[2]), "+v"(cs[3]));
        const float c2 = sum4(cs, 2);
        *(lds_f)cp = fold16(fold32(sum4(cs, 0), sum4(cs, 1)), fold32(c2, c2));
        BAR();
        asm volatile("s_waitcnt vmcnt(0)" : "+v"(sv[0]), "+v"(sv[1]), "+v"(sv[2]), "+v"(sv[3]) :: "memory");
        mid(0);
        const float s1 = fold32(sum4(sv, 0), sum4(sv, 2)), s2 = fold32(sum4(sv, 1), sum4(sv, 3));
        float h1, h2;
        asm("v_mov_b32 %0, %2\n\tv_mov_b32 %1, %3" : "=&v"(h1), "=&v"(h2) : "v"(s1), "v"(s2));
        if (halo) {
            asm volatile("" : "+v"(hv[0]), "+v"(hv[1]), "+v"(hv[2]), "+v"(hv[3]));
            const float u0 = sum4(hv, 0), u1 = sum4(hv, 1), u2 = sum4(hv, 2), u3 = sum4(hv, 3);
            h1 = fold32(u0, panel ? u2 : u0); h2 = fold32(u1, panel ? u3 : u1);
        }
        float mean, rstd;
        ln_stats(fold16(s1, h1), fold16(s2, h2), mean, rstd);
        *(lds_f)mp = mean; *(lds_f)(mp + (C::OFF_RSTD - C::OFF_MEAN)) = rstd;
    } else { VM_BAR(10); tail(); BAR(); mid(0); }
    mid(1);
    WAIT_LGKM_BAR();
}

template <bool LOCAL>
DI void qkv_compute(char* smem, int w, const void* W, u32x4 (&qo4)[4], __hip_bfloat16* __restrict__ kvf, __hip_bfloat16* __restrict__ ofrag, bool ident,
                    const char* statp, const char* wnp SP_PARAM) {
    typedef QC<LOCAL> C;
    constexpr int NT = 16, ABYTES = C::ABYTES, STAGE = C::STAGE;
    const int lane = lane_id();
    asm volatile("" : "+s"(w));
    const int wm = w & 1, wn = w >> 1;
    const Ids I = ids();
    const int panel = I.pq, ct = I.cq;
    const int l15 = lane & 15, g = lane >> 4;
    const lds_cptr sm = (lds_cptr)smem;
    lds_ptr smw = (lds_ptr)smem;

    f32x4 acc[3][4][2];
    f32x4 acch[2];
    float cb;
    f32x4 bias;

    bf16x8 A[2][5][2];
    s16x4 B[3][2][2];
    lds_cptr pa[2], pb[2];
    auto rdA = [&](auto WM, int p, int i) { A[p][i >> 1][i & 1] = lds_b128(pa[i & 1] + ((i >> 1) < 4 ? (i >> 1) * 2048 : 16384 - WM.value * 8192)); };
    auto rdB = [&](int c, int i) { B[c % 3][i >> 1][i & 1] = lds_tr(pb[c & 1] + ((c >> 1) + 1) % 3 * 8192 + (i >> 1) * 4096 + (i & 1) * 512); };
    auto column = [&](auto WM, int p, int c, bool first, auto gap) {
        const int G = ((c >> 1) + 1) % 3, f = c & 1;
        const bool halo = LOCAL && G == 1 + WM.value;
#pragma unroll
        for (int n = 0; n < (halo ? 10 : 8); ++n) {
            const bf16x8 x = A[p][n >> 1][n & 1], y = cat8(B[c % 3][n & 1][0], B[c % 3][n & 1][1]);
            const bf16x8 u = G < 2 ? y : x, v = G < 2 ? x : y;
            f32x4& d = n < 8 ? acc[G][n >> 1][f] : acch[f];
            const f32x4 c0 = bias;
            if (!first || (n & 1)) asm volatile("v_mfma_f32_16x16x32_bf16 %0, %1, %2, %0" : "+v"(d) : "v"(u), "v"(v));
            else if (n == 8) asm volatile("v_mfma_f32_16x16x32_bf16 %0, %1, %2, 0" : "=&v"(d) : "v"(u), "v"(v));
            else asm volatile("v_mfma_f32_16x16x32_bf16 %0, %1, %2, %3" : "=&v"(d) : "v"(u), "v"(v), "v"(c0));
            SBAR();
            gap(n);
            SBAR();
        }
    };

    f32x4 cq[2], ck[2], mu4[5], rs4[5]; float cv[2], mu[5], rs[5];
    u32x4 qfr[4], kfr[4];
    u32x4 va[2][2];
    f32x4 mwin, mfirst;
    typedef __attribute__((address_space(3))) u32x4* lds_u4;
    const bool negpos = (wm == 0) && (panel == 0);
    VM_BAR(0);
    unsigned cm01 = 0, cm23 = 0;
    lds_cptr ka[2] = {sm, sm}, vo2[2] = {sm, sm};
    lds_u4 ex = (lds_u4)(smw + OFF_EX + wn * 6144) + lane;
    lds_cptr s1 = sm + C::OFF_MEAN + wm * 256 + l15 * 4, s4 = sm + C::OFF_MEAN + wm * 256 + g * 16;
    lds_cptr c1 = sm + C::OFF_CS + wn * 128 + l15 * 4, c4 = sm + C::OFF_CS + wn * 128 + g * 16;
    asm volatile("" : "+v"(s1), "+v"(s4), "+v"(c1), "+v"(c4));
    if (LOCAL) {
        unsigned keepv = negpos ? __float_as_uint(NEG_MASK) : 0u;
        asm volatile("" : "+s"(keepv));
        u32x4 mw, mf;
        unsigned prv[4];
#pragma unroll
        for (int r = 0; r < 4; ++r) {
            const int d = l15 - (4 * g + r);
            unsigned out = (unsigned)(((d << 28) >> 31));
            prv[r] = (unsigned)(d >> 31);
            asm volatile("" : "+v"(out), "+v"(prv[r]));
            mw[r] = out & 0xff800000u;
            mf[r] = (out & 0xff800000u) | (~out & keepv);
        }
        const lds_cptr e0 = sm + OFF_EX + wn * 6144;
        ka[0] = e0 + (wm ? lane : 256 + (lane ^ 8)) * 16;         ka[1] = e0 + (wm ? 64 + lane : 256 + lane) * 16;
        vo2[0] = e0 + (wm ? 128 + lane : 320 + (lane ^ 32)) * 16; vo2[1] = e0 + (wm ? 192 + lane : 320 + lane) * 16;
        cm01 = ~((prv[0] >> 16) | (prv[1] << 16)); cm23 = ~((prv[2] >> 16) | (prv[3] << 16));
        mwin = __builtin_bit_cast(f32x4, mw); mfirst = __builtin_bit_cast(f32x4, mf);
        asm volatile("" : "+v"(mwin), "+v"(mfirst), "+v"(cm01), "+v"(cm23), "+v"(ka[0]), "+v"(ka[1]), "+v"(vo2[0]), "+v"(vo2[1]), "+v"(ex));
    }
    typedef const __attribute__((address_space(3))) f32x4* lds_f4; typedef const __attribute__((address_space(3))) float* lds_f1;
    auto mainloop = [&](auto WM) {
        int ty = lane_id();
        const int l15 = ty & 15, g = ty >> 4;
        const int swA = (l15 >> 1) & 7;
        const int q4 = l15 >> 2, p4 = l15 & 3, fk = ((q4 >> 1) & 1) | ((g & 1) << 1);
        lds_cptr pA0 = sm + wm * 8192 + l15 * 128 + ((0 + g) ^ swA) * 16, pA1 = sm + wm * 8192 + l15 * 128 + ((4 + g) ^ swA) * 16;
        lds_cptr pB0 = sm + ABYTES + (8 * g + q4) * 128 + (((2 * wn + 0) ^ fk) * 32) + p4 * 8;
        lds_cptr pB1 = sm + ABYTES + (8 * g + q4) * 128 + (((2 * wn + 1) ^ fk) * 32) + p4 * 8;
        asm volatile("" : "+v"(pA0), "+v"(pA1), "+v"(pB0), "+v"(pB1));
        slow_warm((unsigned)(uintptr_t)smem + OFF_DUMP, W, w, ty);
        unsigned so = (unsigned)(panel * 16384 + wm * 512 + ty * 8);
        const char *p0 = ident ? (const char*)W : statp, *p1 = ident ? (const char*)W : wnp;
        u32x4 wn;
        asm volatile("global_load_dwordx4 %0, %2, %3 sc1" : "=&v"(wn), "+v"(so) : "v"(0), "s"(p1) : "memory");
        BAR();
        STAMP(LOCAL ? 1 : 7);
        f32x2 sq;
        asm volatile("global_load_dwordx2 %0, %1, %2 sc1" : "=&v"(sq) : "v"(so), "s"(p0) : "memory");
        BAR();
        pa[0] = pA0; pa[1] = pA1; pb[0] = pB0; pb[1] = pB1;
#pragma unroll
        for (int i = 0; i < 4; ++i) rdB(0, i);
#pragma unroll
        for (int i = 0; i < 8; ++i) rdA(WM, 0, i);
#pragma unroll
        for (int i = 0; i < 4; ++i) if (LOCAL && WM.value == 0 && i >= 2) rdA(WM, 0, 6 + i); else rdB(1, i);
        SBAR();
        asm volatile("s_waitcnt vmcnt(0)" : "+v"(sq), "+v"(wn) :: "memory");
        const float d = fmaxf(__builtin_fmaf(sq[0] * (-1.f / 64.f), sq[0], sq[1]), 0.f);
        cb = fminf(__builtin_amdgcn_sqrtf(__uint_as_float(wave_min(__float_as_uint(d)))) * 64.f * __uint_as_float(min(min(wn[0], wn[1]), wn[2])), 1.0e30f);
        if (ident) cb = 0.f;
        bias = f32x4{cb, cb, cb, cb};
        asm volatile("s_nop 1" : "+v"(bias));
        WAIT_LGKM();
        int off = 0;
        constexpr bool HK = LOCAL && WM.value == 0;
        auto front = [&](int p, bool first) {
#pragma unroll
            for (int c = 0; c < 4; ++c)
                column(WM, p, c, first, [&](int n) {
                    if (HK && c == 0) { if (n < 2) rdB(1, 2 + n); else if (n < 6) rdB(2, n - 2); }
                    else if (n < 4) rdB(c + 2, n);
                    if (LOCAL && !HK && c == 0 && (n == 4 || n == 5)) rdA(WM, p, 4 + n);
                    if (c == 1 && n == 5) { off = (off == 2 * STAGE) ? 0 : off + STAGE; asm volatile("" : "+s"(off)); }
                    if (c == 2 && n == 4) { pa[0] = pA0 + off; asm volatile("" : "+v"(pa[0])); }
                    if (c == 2 && n == 5) { pa[1] = pA1 + off; asm volatile("" : "+v"(pa[1])); }
                    if (c == 3 && n == 4) { pb[0] = pB0 + off; asm volatile("" : "+v"(pb[0])); }
                    if (c == 3 && n == 5) { pb[1] = pB1 + off; asm volatile("" : "+v"(pb[1])); }
                    if ((c == 0 && n == 3) || ((c == 1 || c == 2 || (HK && c == 0)) && n == 7)) __builtin_amdgcn_s_waitcnt(0xC47F);
                });
            WAIT_LGKM_BAR();
        };
        auto back = [&](int p, bool first) {
            column(WM, p, 4, first, [&](int n) { if (n & 1) rdB(0, n >> 1); else rdA(WM, p ^ 1, n >> 1); });
            column(WM, p, 5, first, [&](int n) {
                if (!(n & 1)) rdA(WM, p ^ 1, 4 + (n >> 1)); else if (HK && n > 4) rdA(WM, p ^ 1, 6 + (n >> 1)); else rdB(1, n >> 1);
                if (n == 7) __builtin_amdgcn_s_waitcnt(0xC87F);
            });
        };
        auto stage = [&](int p, bool first) { front(p, first); back(p, first); };
        stage(0, true);
        front(1, false);
#pragma unroll 1
        for (int t = 1; t < NT - 1; t += 2) { back(1, false); stage(0, false); front(1, false); }
        column(WM, 1, 4, false, [&](int n) {
            constexpr int RS = C::OFF_RSTD - C::OFF_MEAN, H = 512 - WM.value * 256;
            if (n == 0) { ck[0] = *(lds_f4)(c4 + 256); ck[1] = *(lds_f4)(c4 + 320); cv[0] = *(lds_f1)(c1 + 512); cv[1] = *(lds_f1)(c1 + 512 + 64); }
            else if (n < 5) {
                mu[n - 1] = *(lds_f1)(s1 + (n - 1) * 64); rs[n - 1] = *(lds_f1)(s1 + RS + (n - 1) * 64);
                mu4[n - 1] = *(lds_f4)(s4 + (n - 1) * 64); rs4[n - 1] = *(lds_f4)(s4 + RS + (n - 1) * 64);
            } else if (n == 5) {
                if (LOCAL && WM.value == 0) { mu[4] = *(lds_f1)(s1 + H); rs[4] = *(lds_f1)(s1 + RS + H); }
                else if (LOCAL) { mu4[4] = *(lds_f4)(s4 + H); rs4[4] = *(lds_f4)(s4 + RS + H); }
            } else if (n == 6) { cq[0] = *(lds_f4)c4; cq[1] = *(lds_f4)(c4 + 64); }
        });
        if (LOCAL) column(WM, 1, 5, false, [](int n) { if (n == 6) __builtin_amdgcn_s_waitcnt(0xC07F); });
        if (LOCAL) {
            auto fix = [&](int a, int t) {
#pragma unroll
                for (int f = 0; f < 2; ++f)
                    acc[a][t][f] = (a == 2) ? rs4[t] * (acc[2][t][f] + mu4[t] * cv[f]) + rs4[t] * -cb
                                            : rs[t] * (acc[a][t][f] + mu[t] * (a ? ck[f] : cq[f])) + rs[t] * -cb;
            };
            auto pk = [&](const f32x4 (&v)[2]) { return u32x4{pack_bf16(v[0][0], v[0][1]), pack_bf16(v[0][2], v[0][3]), pack_bf16(v[1][0], v[1][1]), pack_bf16(v[1][2], v[1][3])}; };
            if (!ident) {
                fix(1, 1); fix(1, 3); fix(2, 1); fix(2, 3);
#pragma unroll
                for (int f = 0; f < 2; ++f)
                    acch[f] = (WM.value == 0) ? rs[4] * (acch[f] + mu[4] * ck[f]) : rs4[4] * (acch[f] + mu4[4] * cv[f]);
            }
            auto pkv = [&](int t) {
#pragma unroll
                for (int f = 0; f < 2; ++f) {
                    va[t >> 1][f][(t & 1) * 2] = pack_bf16(acc[2][t][f][0], acc[2][t][f][1]);
                    va[t >> 1][f][(t & 1) * 2 + 1] = pack_bf16(acc[2][t][f][2], acc[2][t][f][3]);
                }
            };
            kfr[1] = pk(acc[1][1]); kfr[3] = pk(acc[1][3]); pkv(1); pkv(3);
            u32x4 hf = pk(acch);
            if (WM.value == 0) {
                ex[0] = kfr[1]; ex[64] = kfr[3];
#pragma unroll
                for (int u = 0; u < 2; ++u)
                    asm volatile("ds_write_b64 %0, %1 offset:%3\n\tds_write_b64 %0, %2 offset:%3+8" :: "v"(ex), "v"(u32x2{va[u][0][2], va[u][0][3]}),
                                 "v"(u32x2{va[u][1][2], va[u][1][3]}), "n"(2048 + u * 1024) : "memory");
                if (negpos) hf = u32x4{0, 0, 0, 0};
            }
            ex[256 + WM.value * 64] = hf;
            SBAR();
            if (!ident) { fix(0, 0); fix(0, 1); fix(0, 2); fix(0, 3); fix(1, 0); fix(1, 2); fix(2, 0); fix(2, 2); }
#pragma unroll
            for (int t = 0; t < 4; ++t) qfr[t] = pk(acc[0][t]);
            kfr[0] = pk(acc[1][0]); kfr[2] = pk(acc[1][2]); pkv(0); pkv(2);
            asm volatile("" : "+v"(qfr[0]), "+v"(qfr[1]), "+v"(qfr[2]), "+v"(qfr[3]), "+v"(kfr[0]), "+v"(kfr[2]),
                         "+v"(va[0][0]), "+v"(va[0][1]), "+v"(va[1][0]), "+v"(va[1][1]));
        }
    };
    const int head = ct * 2 + wn;
    unsigned qo = 0, ko = 0, vo = 0;
    if (LOCAL) {
        qo = (unsigned)((((panel * 8 + wm * 4) * 32 + head) * 64 + lane) * 16);
        asm volatile("" : "+v"(qo));
    } else {
        ko = (unsigned)((wm * 8 + (g & 1)) * 1024 + ((l15 & 7) + ((l15 & 8) << 1) + 32 * (g >> 1)) * 16);
        vo = (unsigned)((wm * 8 + 2 + (g >> 1)) * 1024 + (l15 + 32 * (g & 1)) * 16);
        asm volatile("" : "+v"(ko), "+v"(vo));
    }
    if (LOCAL && wm) mainloop(IC<1>{}); else mainloop(IC<0>{});

    if (!LOCAL) {
        auto pk = [&](f32x4 a, f32x4 b) { return u32x4{pack_bf16(a[0], a[1]), pack_bf16(a[2], a[3]), pack_bf16(b[0], b[1]), pack_bf16(b[2], b[3])}; };
        char* kvp = (char*)kvf + (size_t)(head * 16 + panel) * 16384;
        lds_ptr own = smw + wn * 16384;
        SBAR();
#pragma unroll
        for (int t = 0; t < 4; ++t) {
            const u32x4 kk = pk(rs[t] * (acc[1][t][0] + mu[t] * ck[0]) + rs[t] * -cb, rs[t] * (acc[1][t][1] + mu[t] * ck[1]) + rs[t] * -cb);
            if (t & 1) st16o<128>(kvp + (t >> 1) * 4096, ko, kk); else st16o<0>(kvp + (t >> 1) * 4096, ko, kk);
            kfr[t] = kk;
            SBAR();
            const int u = t >> 1, f = t & 1;
            const u32x4 vv = pk(rs4[2 * u] * (acc[2][2 * u][f] + mu4[2 * u] * cv[f]) + rs4[2 * u] * -cb,
                                rs4[2 * u + 1] * (acc[2][2 * u + 1][f] + mu4[2 * u + 1] * cv[f]) + rs4[2 * u + 1] * -cb);
            if (f) st16o<256>(kvp + u * 4096, vo, vv); else st16o<0>(kvp + u * 4096, vo, vv);
            qfr[t] = vv;
            SBAR();
        }
        column(IC<0>{}, 1, 5, false, [&](int n) {
            const int t = n >> 1;
            if (n & 1) *(lds_u4)(own + vo + (t >> 1) * 4096 + (t & 1) * 256) = qfr[t];
            else       *(lds_u4)(own + ko + (t >> 1) * 4096 + (t & 1) * 128) = kfr[t];
        });
        asm volatile("s_nop 7");
        SBAR();
#pragma unroll
        for (int u = 0; u < 2; ++u) {
            const float r0 = rs[2 * u] * QSCALE, r1 = rs[2 * u + 1] * QSCALE;
            const u32x4 qa = pk(r0 * (acc[0][2 * u][0] + mu[2 * u] * cq[0]) + r0 * -cb, r0 * (acc[0][2 * u][1] + mu[2 * u] * cq[1]) + r0 * -cb);
            const u32x4 qb = pk(r1 * (acc[0][2 * u + 1][0] + mu[2 * u + 1] * cq[0]) + r1 * -cb, r1 * (acc[0][2 * u + 1][1] + mu[2 * u + 1] * cq[1]) + r1 * -cb);
#pragma unroll
            for (int e = 0; e < 4; ++e) {
                const auto x = __builtin_amdgcn_permlane16_swap(qa[e], qb[e], false, false);
                qo4[2 * u][e] = x[0]; qo4[2 * u + 1][e] = x[1];
            }
        }
        return;
    }
    {
        const f32x4 zero4 = {0, 0, 0, 0};
        auto bf8 = [](u32x4 v) { return __builtin_bit_cast(bf16x8, v); };
        f32x4 sp[4], sc[4];
        sc[1] = MFMA16(bf8(kfr[1]), bf8(qfr[1]), mwin); sp[1] = MFMA16(bf8(kfr[0]), bf8(qfr[1]), mwin);
        sc[3] = MFMA16(bf8(kfr[3]), bf8(qfr[3]), mwin); sp[3] = MFMA16(bf8(kfr[2]), bf8(qfr[3]), mwin);
        asm volatile("" : "+v"(sc[1]), "+v"(sp[1]), "+v"(sc[3]), "+v"(sp[3]));
        WAIT_LGKM_BAR();
        const bf16x8 kp[2] = {lds_b128(ka[0]), lds_b128(ka[1])};
        u32x2 vp[2][2];
#pragma unroll
        for (int u = 0; u < 2; ++u)
            asm volatile("ds_read_b64 %0, %2\n\tds_read_b64 %1, %2 offset:8" : "=&v"(vp[u][0]), "=&v"(vp[u][1]) : "v"(vo2[u]) : "memory");
        sc[0] = MFMA16(bf8(kfr[0]), bf8(qfr[0]), mwin); sc[2] = MFMA16(bf8(kfr[2]), bf8(qfr[2]), mwin);
        SBAR();
        float x[4][4], m[4];
        unsigned a[4][2];
        auto logits = [&](int t0, int st) {
#pragma unroll
            for (int t = t0; t < 4; t += 2)
#pragma unroll
                for (int h = 0; h < 2; ++h) {
                    if (st == 0) {
                        const unsigned uc = pack_bf16(sc[t][2 * h], sc[t][2 * h + 1]), up = pack_bf16(sp[t][2 * h], sp[t][2 * h + 1]);
                        asm("v_bfi_b32 %0, %1, %2, %3" : "=v"(a[t][h]) : "v"(h ? cm23 : cm01), "v"(uc), "v"(up));
                    } else if (st == 1) { x[t][2 * h] = bf_lo(a[t][h]) * ATT_SCALE; x[t][2 * h + 1] = bf_hi(a[t][h]) * ATT_SCALE; }
                    else if (st == 2) a[t][h] = pack_bf16(x[t][2 * h], x[t][2 * h + 1]);
                    else { x[t][2 * h] = bf_lo(a[t][h]); x[t][2 * h + 1] = bf_hi(a[t][h]); }
                }
            SBAR();
        };
        logits(1, 0); logits(1, 1);
        sp[0] = MFMA16(kp[0], bf8(qfr[0]), mfirst); sp[2] = MFMA16(kp[1], bf8(qfr[2]), mfirst);
        SBAR();
        logits(1, 2); logits(1, 3);
        logits(0, 0); logits(0, 1); logits(0, 2); logits(0, 3);
        auto across = [&](float (&v)[4], auto op, auto fin) {
            const auto p = __builtin_amdgcn_permlane16_swap(__float_as_uint(v[0]), __float_as_uint(v[1]), false, false);
            const auto q = __builtin_amdgcn_permlane16_swap(__float_as_uint(v[2]), __float_as_uint(v[3]), false, false);
            const auto r = __builtin_amdgcn_permlane32_swap(__float_as_uint(op(__uint_as_float(p[0]), __uint_as_float(p[1]))),
                                                            __float_as_uint(op(__uint_as_float(q[0]), __uint_as_float(q[1]))), false, false);
            const unsigned z = __float_as_uint(fin(op(__uint_as_float(r[0]), __uint_as_float(r[1]))));
            const auto u = __builtin_amdgcn_permlane32_swap(z, z, false, false);
            const auto e = __builtin_amdgcn_permlane16_swap(u[0], u[0], false, false), f = __builtin_amdgcn_permlane16_swap(u[1], u[1], false, false);
            v[0] = __uint_as_float(e[0]); v[1] = __uint_as_float(e[1]); v[2] = __uint_as_float(f[0]); v[3] = __uint_as_float(f[1]);
        };
#pragma unroll
        for (int t = 0; t < 4; ++t) m[t] = amax3(x[t][0], x[t][1], x[t][2]);
        asm("v_max_f32 %0, %0, %4\n\tv_max_f32 %1, %1, %5\n\tv_max_f32 %2, %2, %6\n\tv_max_f32 %3, %3, %7\n\ts_nop 0"
            : "+v"(m[0]), "+v"(m[1]), "+v"(m[2]), "+v"(m[3]) : "v"(x[0][3]), "v"(x[1][3]), "v"(x[2][3]), "v"(x[3][3]));
        across(m, [](float p, float q) { float r; asm("v_max_f32 %0, %1, %2\n\ts_nop 0" : "=v"(r) : "v"(p), "v"(q)); return r; },
               [](float p) { return p * -LOG2E; });
        SBAR();
#pragma unroll
        for (int t = 0; t < 4; ++t)
#pragma unroll
            for (int r = 0; r < 4; ++r) x[t][r] = __builtin_fmaf(x[t][r], LOG2E, m[t]);
        SBAR();
#pragma unroll
        for (int t = 0; t < 4; ++t)
#pragma unroll
            for (int r = 0; r < 4; ++r) x[t][r] = __builtin_amdgcn_exp2f(x[t][r]);
        SBAR();
#pragma unroll
        for (int t = 0; t < 4; ++t) m[t] = (x[t][0] + x[t][1]) + (x[t][2] + x[t][3]);
        asm volatile("s_waitcnt lgkmcnt(0)" : "+v"(vp[0][0]), "+v"(vp[0][1]), "+v"(vp[1][0]), "+v"(vp[1][1]) :: "memory");
        u32x4 ve[2][2];
#pragma unroll
        for (int u = 0; u < 2; ++u)
#pragma unroll
            for (int f = 0; f < 2; ++f) ve[u][f] = u32x4{vp[u][f][0], vp[u][f][1], va[u][f][0], va[u][f][1]};
        across(m, [](float p, float q) { return p + q; }, [](float p) { return __builtin_amdgcn_rcpf(p); });
        asm volatile("" : "+v"(ve[0][0]), "+v"(ve[0][1]), "+v"(ve[1][0]), "+v"(ve[1][1]));
        SBAR();
#pragma unroll
        for (int t = 0; t < 4; ++t)
#pragma unroll
            for (int r = 0; r < 4; ++r) x[t][r] *= m[t];
        SBAR();
#pragma unroll
        for (int t = 0; t < 4; ++t)
#pragma unroll
            for (int h = 0; h < 2; ++h) a[t][h] = pack_bf16(x[t][2 * h], x[t][2 * h + 1]);
        SBAR();
        f32x4 o0[4], o1[4];
#pragma unroll
        for (int t = 0; t < 4; ++t) {
            const bf16x8 pb = bf8(u32x4{a[t][0] & ~cm01, a[t][1] & ~cm23, a[t][0] & cm01, a[t][1] & cm23});
            o0[t] = MFMA16(bf8((t & 1) ? va[t >> 1][0] : ve[t >> 1][0]), pb, zero4);
            o1[t] = MFMA16(bf8((t & 1) ? va[t >> 1][1] : ve[t >> 1][1]), pb, zero4);
        }
        SBAR();
#pragma unroll
        for (int t = 0; t < 4; ++t)
            st16(ofrag + t * 16384, qo, u32x4{pack_bf16(o0[t][0], o0[t][1]), pack_bf16(o0[t][2], o0[t][3]),
                                                 pack_bf16(o1[t][0], o1[t][1]), pack_bf16(o1[t][2], o1[t][3])});
    }
}

DI int out_row(int panel, bool localrows, int T, int a) {
    return localrows ? (panel * 128 + (T >> 2) * 64 + 2 * (16 * (T & 1) + a) + ((T >> 1) & 1)) : (panel * 128 + T * 16 + a);
}
DI int out_swz(int k) { return ((k >> 1) & 1) | (k & 4); }
DI unsigned wo_off(int w, int lane) {
    const int kl = lane >> 3;
    unsigned v = (unsigned)(w * 8 + kl) * 2048u + (unsigned)(ids().co * 128 + ((lane & 7) ^ out_swz(kl)) * 16);
    asm volatile("" : "+v"(v));
    return v;
}
DI void wo_load(const void* Wo, unsigned voff, int i0, int i1, unsigned lds0, int w) {
#pragma unroll
    for (int i = i0; i < i1; ++i) glds16((const char*)Wo + (size_t)i * 65536, voff, lds0 + (w + 4 * i) * 1024);
}
DI unsigned res_off(bool localrows, bool tiled, int lane) {
    const Ids I = ids();
    unsigned v = (unsigned)(I.po * 262144 + I.co * (tiled ? 16384 : 128) + out_row(0, localrows, 0, lane >> 3) * (tiled ? 128 : 2048) +
                            (((lane & 7) ^ (lane >> 3)) * 16));
    asm volatile("" : "+v"(v));
    return v;
}
DI void res_load(u32x4 (&res)[6], const void* Xin, bool localrows, bool tiled, unsigned off, int w) {
#pragma unroll
    for (int i = 0; i < 6; ++i) {
        const int j = min(w + 3 * i, 15);
        asm volatile("global_load_dwordx4 %0, %1, %2 sc1" : "=&v"(res[i]) : "v"(off), "s"((const char*)Xin + out_row(0, localrows, j >> 1, (j & 1) * 8) * (tiled ? 128 : 2048)) : "memory");
    }
}
DI void out_handoff(char* smem, int xoff, u32x4 (&res)[6], int w, char* ws, int set, u64 tag, int lane, u32x4 (&c)[8], int mat) {
    const Ids I = ids();
    u64 land;
    lds_ptr dst = (lds_ptr)smem + xoff + w * 1024 + lane * 16;
    asm volatile("" : "+v"(dst));
    if (w == 3) __builtin_amdgcn_s_setprio(3);
    signal_poll<false, false>(w - 3, ws, flag_off(set, I.pq, lane & 15, I.cq), flag_off(set, I.po, I.co, lane & 15), 0xffffull, tag, land);
    if (w < 3) {
        asm volatile("s_waitcnt vmcnt(0)" : "+v"(res[0]), "+v"(res[1]), "+v"(res[2]), "+v"(res[3]), "+v"(res[4]), "+v"(res[5]) :: "memory");
#pragma unroll
        for (int i = 0; i < 5; ++i) *(__attribute__((address_space(3))) u32x4*)(dst + i * 3072) = res[i];
        if (w == 0) *(__attribute__((address_space(3))) u32x4*)(dst + 15360) = res[5];
        if (mat >= 0) colsum_store(c, ws, mat, w, lane);
    }
    WAIT_LGKM_BAR();
    asm volatile("s_waitcnt vmcnt(0)" : "+v"(land) :: "memory");
    if (w < 3 && mat >= 0) flag_store(ws, (mat ? 10 : 13) + w, I.cq, I.pq, tag, lane);
    __builtin_amdgcn_s_setprio(0);
}

template <bool AFTER_LOCAL>
DI void out_compute(char* smem, int w, const __hip_bfloat16* __restrict__ ofrag,
                    __hip_bfloat16* __restrict__ Xout, float* __restrict__ statp SP_PARAM) {
    constexpr int XOFF = AFTER_LOCAL ? 147456 : 98304;
    STAMP(AFTER_LOCAL ? 3 : 10);
    VM_BAR(0);
    const int lane = lane_id();
    asm volatile("" : "+s"(w));
    const Ids I = ids();
    const int panel = I.po, ct = I.co;
    const int l15 = lane & 15, g = lane >> 4;
    const bool tiled = AFTER_LOCAL || statp != nullptr;
    const int rstr = tiled ? 128 : 2048;
    const int q4 = l15 >> 2, p4 = l15 & 3;
    const int krow = 4 * g + q4;
    const int sw = out_swz(krow);
    const lds_cptr sm = (lds_cptr)smem;
    lds_cptr bw[4], xo[4];
#pragma unroll
    for (int t = 0; t < 4; ++t) {
        bw[t] = sm + krow * 128 + ((((t >> 1) * 4 + (p4 >> 1) * 2 + (t & 1)) ^ sw) * 16) + (p4 & 1) * 8;
        xo[t] = sm + XOFF + (w * 32 + l15) * 128 + ((((t >> 1) * 4 + (g >> 1) * 2 + (t & 1)) ^ (l15 & 7)) * 16) + (g & 1) * 8;
    }
    unsigned so[2], sto, ao[2];
#pragma unroll
    for (int r2 = 0; r2 < 2; ++r2) {
        so[r2] = (unsigned)(panel * 262144 + ct * (tiled ? 16384 : 128) + out_row(0, AFTER_LOCAL, 2 * w + r2, l15 & 7) * rstr + (l15 >> 3) * 64 + g * 16);
        ao[r2] = (unsigned)((panel * 8 + 2 * w + r2) * 32768 + lane * 16);
    }
    sto = (unsigned)(((panel * 16 + ct) * 128 + 2 * w * 16 + l15) * 8);
    asm volatile("" : "+v"(bw[0]), "+v"(bw[1]), "+v"(bw[2]), "+v"(bw[3]), "+v"(xo[0]), "+v"(xo[1]), "+v"(xo[2]), "+v"(xo[3]),
                 "+v"(so[0]), "+v"(so[1]), "+v"(sto), "+v"(ao[0]), "+v"(ao[1]));
    bf16x8 ones;
    {
        unsigned o1; asm volatile("v_mov_b32 %0, 0x3f803f80" : "=v"(o1));
        ones = __builtin_bit_cast(bf16x8, (u32x4{o1, o1, o1, o1}));
        asm volatile("" : "+v"(ones));
    }
    const __amdgpu_buffer_rsrc_t ra = __builtin_amdgcn_make_buffer_rsrc((void*)ofrag, 0, 4 << 20, 0x00020000);
    asm volatile("" :: "s"(ra));
    u32x32 fr;
    BAR();
    STAMP(AFTER_LOCAL ? 4 : 11);
    u32x32 aw0, aw1, aw2, fr1;
    asm volatile(".irp j,0,1,2,3\n\tbuffer_load_dwordx4 v[96+4*\\j:99+4*\\j], %2, %4, 0 offen offset:1024*\\j sc1\n\t.endr\n\t"
                 ".irp j,0,1,2,3\n\tbuffer_load_dwordx4 v[112+4*\\j:115+4*\\j], %3, %4, 0 offen offset:1024*\\j sc1\n\t.endr\n\t"
                 ".irp j,0,1,2,3\n\tbuffer_load_dwordx4 v[128+4*\\j:131+4*\\j], %2, %4, %5 offen offset:1024*\\j sc1\n\t.endr"
                 : "=&{v[96:127]}"(aw0), "=&{v[128:159]}"(aw1) : "v"(ao[0]), "v"(ao[1]), "s"(ra), "s"(4096) : "memory");
    f32x16 acc[2];
#pragma unroll
    for (int r2 = 0; r2 < 2; ++r2)
#pragma unroll
        for (int c = 0; c < 4; ++c) {
            const u32x2 xr = *(const __attribute__((address_space(3))) u32x2*)(xo[c] + r2 * 2048);
            acc[r2][4 * c] = bf_lo(xr[0]); acc[r2][4 * c + 1] = bf_hi(xr[0]); acc[r2][4 * c + 2] = bf_lo(xr[1]); acc[r2][4 * c + 3] = bf_hi(xr[1]);
        }
    asm volatile(
        ".macro OUT_MM r, t, j, s\n\t"
        "v_mfma_f32_16x16x32_bf16 v[16*\\r+4*\\t:16*\\r+4*\\t+3], v[32+16*\\t+4*\\j:35+16*\\t+4*\\j], v[96+32*\\s+16*\\r+4*\\j:99+32*\\s+16*\\r+4*\\j], v[16*\\r+4*\\t:16*\\r+4*\\t+3]\n\t"
        ".endm\n\t"
        ".macro OUT_RD t, i, g\n\t"
        "ds_read_b64_tr_b16 v[32+16*\\t+2*(\\i):33+16*\\t+2*(\\i)], v[192+\\t] offset:(\\g)*16384+(\\i)*2048\n\t"
        ".endm\n\t"
        ".macro OUT_LD i, s\n\t"
        ".if (\\i) < 4\n\tbuffer_load_dwordx4 v[96+32*(\\s)+4*(\\i):99+32*(\\s)+4*(\\i)], %[a0], %[ra], s96 offen offset:1024*(\\i) sc1\n\t"
        ".else\n\tbuffer_load_dwordx4 v[96+32*(\\s)+4*(\\i):99+32*(\\s)+4*(\\i)], %[a1], %[ra], s96 offen offset:1024*((\\i)-4) sc1\n\t.endif\n\t"
        ".endm\n\t"
        ".macro OUT_CH r, t, s, rt, g\n\t"
        ".irp j,0,1,2,3\n\tOUT_MM \\r, \\t, \\j, \\s\n\tOUT_RD \\rt, 4*\\r+\\j, \\g\n\t.endr\n\t"
        ".endm\n\t"
        ".macro OUT_GRP s, g, gn, ld, up, v0, v1\n\t"
        "OUT_CH 0, 0, \\s, 3, \\g\n\ts_waitcnt vmcnt(\\v0)\n\t.if \\ld\n\tOUT_LD 0, (\\s+2)%%3\n\t.endif\n\t"
        "OUT_CH 1, 0, \\s, 3, \\g\n\t.if \\up\n\t.irp t,0,1,2,3\n\tv_add_u32 v[192+\\t], s97, v[192+\\t]\n\t.endr\n\t.endif\n\t.if \\ld\n\tOUT_LD 1, (\\s+2)%%3\n\t.endif\n\t"
        "OUT_CH 0, 1, \\s, 0, \\gn\n\t.if \\ld\n\tOUT_LD 2, (\\s+2)%%3\n\t.endif\n\t"
        "OUT_CH 1, 1, \\s, 0, \\gn\n\t.if \\ld\n\tOUT_LD 3, (\\s+2)%%3\n\t.endif\n\ts_waitcnt lgkmcnt(8)\n\t"
        "OUT_CH 0, 2, \\s, 1, \\gn\n\t.if \\ld\n\tOUT_LD 4, (\\s+2)%%3\n\t.endif\n\t"
        "OUT_CH 1, 2, \\s, 1, \\gn\n\t.if \\ld\n\tOUT_LD 5, (\\s+2)%%3\n\t.endif\n\t"
        "OUT_CH 0, 3, \\s, 2, \\gn\n\t.if \\ld\n\tOUT_LD 6, (\\s+2)%%3\n\t.endif\n\t"
        "OUT_CH 1, 3, \\s, 2, \\gn\n\t.if \\ld\n\tOUT_LD 7, (\\s+2)%%3\n\ts_addk_i32 s96, 0x1000\n\t.endif\n\t"
        "s_waitcnt vmcnt(\\v1) lgkmcnt(8)\n\t"
        ".endm\n\t"
        ".macro OUT_PACK y, c\n\t"
        ".irp i,0,1,2,3,4,5,6,7\n\tv_cvt_pk_bf16_f32 v[\\y+\\i], v[\\c+2*\\i], v[\\c+2*\\i+1]\n\t.endr\n\t"
        ".irp i,0,1,4,5\n\tv_permlane16_swap_b32 v[\\y+\\i], v[\\y+\\i+2]\n\t.endr\n\t"
        ".irp i,0,1,2,3\n\tv_mov_b32 v[\\y+8+\\i], v[\\y+\\i]\n\t.endr\n\t"
        "v_mfma_f32_16x16x32_bf16 v[\\y+12:\\y+15], v[\\y:\\y+3], v[\\y:\\y+3], 0\n\t"
        "v_mov_b32_dpp v[\\y+8], v[\\y+4] row_ror:8 row_mask:0xf bank_mask:0xc\n\tv_mov_b32_dpp v[\\y+9], v[\\y+5] row_ror:8 row_mask:0xf bank_mask:0xc\n\t"
        "v_mfma_f32_16x16x32_bf16 v[\\y+16:\\y+19], %[one], v[\\y:\\y+3], 0\n\t"
        "v_mov_b32_dpp v[\\y+10], v[\\y+6] row_ror:8 row_mask:0xf bank_mask:0xc\n\tv_mov_b32_dpp v[\\y+11], v[\\y+7] row_ror:8 row_mask:0xf bank_mask:0xc\n\t"
        "v_mfma_f32_16x16x32_bf16 v[\\y+12:\\y+15], v[\\y+4:\\y+7], v[\\y+4:\\y+7], v[\\y+12:\\y+15]\n\t"
        ".endm\n\t"
        ".macro OUT_REST y\n\t"
        "v_mfma_f32_16x16x32_bf16 v[\\y+16:\\y+19], %[one], v[\\y+4:\\y+7], v[\\y+16:\\y+19]\n\t"
        ".irp i,0,1,2,3\n\tv_mov_b32_dpp v[\\y+4+\\i], v[\\y+\\i] row_ror:8 row_mask:0xf bank_mask:0x3\n\t.endr\n\t"
        ".endm\n\t"
        ".macro OUT_DIAG y\n\t"
        "v_cndmask_b32 v[\\y+12], v[\\y+12], v[\\y+13], %[m0]\n\tv_cndmask_b32 v[\\y+14], v[\\y+14], v[\\y+15], %[m0]\n\t"
        "v_cndmask_b32 v[\\y+17], v[\\y+12], v[\\y+14], %[m1]\n\t"
        ".endm\n\t"
        "s_movk_i32 s96, 0x1000\n\t.irp i,4,5,6,7\n\tOUT_LD \\i, 1\n\t.endr\n\t"
        ".irp t,0,1,2\n\t.irp i,0,1,2,3,4,5,6,7\n\tOUT_RD \\t, \\i, 0\n\t.endr\n\ts_waitcnt lgkmcnt(4)\n\t.endr\n\t"
        "s_movk_i32 s96, 0x2000\n\ts_mov_b32 s97, 0xc000\n\t"
        "s_waitcnt vmcnt(12) lgkmcnt(0)\n"
        ".Lout_loop_%=:\n\t"
        "OUT_GRP 0, 0, 1, 1, 0, 8, 12\n\tOUT_GRP 1, 1, 2, 1, 0, 8, 12\n\tOUT_GRP 2, 2, 0, 1, 1, 8, 12\n\t"
        "s_mov_b32 s97, %[last]\n\t"
        "s_cmpk_lg_u32 s96, 0x8000\n\ts_cbranch_scc1 .Lout_loop_%=\n\t"
        "OUT_GRP 0, 0, 1, 0, 0, 8, 4\n\t"
        ".irp j,0,1,2,3\n\tOUT_MM 0, 0, \\j, 1\n\tOUT_RD 3, \\j, 1\n\t.endr\n\t.irp j,0,1,2,3\n\tOUT_MM 0, 1, \\j, 1\n\tOUT_RD 3, 4+\\j, 1\n\t.endr\n\t"
        "s_waitcnt lgkmcnt(8)\n\t.irp j,0,1,2,3\n\tOUT_MM 0, 2, \\j, 1\n\t.endr\n\t"
        "s_waitcnt lgkmcnt(0)\n\t.irp j,0,1,2,3\n\tOUT_MM 0, 3, \\j, 1\n\t.endr\n\t"
        "s_nop 1\n\t"
        "OUT_PACK 96, 0\n\tglobal_store_dwordx4 %[s0], v[104:107], %[xa] sc1\n\tOUT_REST 96\n\tglobal_store_dwordx4 %[s0], v[100:103], %[xb] sc1\n\t"
        "s_waitcnt vmcnt(2)\n\t.irp t,0,1,2,3\n\t.irp j,0,1,2,3\n\tOUT_MM 1, \\t, \\j, 1\n\t.endr\n\t.endr\n\t"
        "s_nop 1\n\t"
        "OUT_PACK 160, 16\n\tglobal_store_dwordx4 %[s1], v[168:171], %[xa] sc1\n\tOUT_REST 160\n\tglobal_store_dwordx4 %[s1], v[164:167], %[xb] sc1\n\t"
        "s_cmp_eq_u64 %[st], 0\n\ts_cbranch_scc1 .Lout_end_%=\n\t"
        "OUT_DIAG 96\n\tOUT_DIAG 160\n\t"
        "s_mov_b64 exec, %[md]\n\t"
        "global_store_dwordx2 %[so], v[112:113], %[st] sc1\n\tglobal_store_dwordx2 %[so], v[176:177], %[st] offset:128 sc1\n\t"
        "s_mov_b64 exec, -1\n"
        ".Lout_end_%=:\n\t"
        "s_nop 1\n\t"
        ".purgem OUT_MM\n\t.purgem OUT_RD\n\t.purgem OUT_LD\n\t.purgem OUT_CH\n\t.purgem OUT_GRP\n\t.purgem OUT_PACK\n\t.purgem OUT_REST\n\t.purgem OUT_DIAG"
        : "+{v[0:15]}"(acc[0]), "+{v[16:31]}"(acc[1]), "+{v[96:127]}"(aw0), "+{v[128:159]}"(aw1), "=&{v[160:191]}"(aw2),
          "+{v192}"(bw[0]), "+{v193}"(bw[1]), "+{v194}"(bw[2]), "+{v195}"(bw[3]), "=&{v[32:63]}"(fr), "=&{v[64:95]}"(fr1)
        : [a0] "v"(ao[0]), [a1] "v"(ao[1]), [ra] "s"(ra), [last] "s"(AFTER_LOCAL ? 49152 : 81920), [one] "v"(ones), [s0] "v"(so[0]), [s1] "v"(so[1]),
          [xa] "s"(Xout), [xb] "s"((char*)Xout + (AFTER_LOCAL ? 16 : 8) * rstr), [st] "s"(statp), [so] "v"(sto),
          [m0] "s"(0xaaaaaaaaaaaaaaaaull), [m1] "s"(0xccccccccccccccccull), [md] "s"(0xf0000f0000f0000full)
        : "memory", "scc", "s96", "s97");
    STAMP(AFTER_LOCAL ? 5 : 12);
}

#define ROWSUM(p0, p1, c) __builtin_amdgcn_smfmac_f32_16x16x64_bf16(sel, __builtin_bit_cast(bf16v_t, __builtin_shufflevector(p0, p1, 0, 1, 2, 3, 4, 5, 6, 7)), c, sidx, 0, 0)
constexpr int ATT_STAGE = 32768;
typedef __attribute__((ext_vector_type(8))) unsigned u32x8;

DI void attn_loader(char* smem, int w, char* ws, int set, u64 tag, const __hip_bfloat16* __restrict__ kvf, const void* Wo, const void* Xin,
                    const void* Wcs SP_PARAM) {
    const int lane = lane_id();
    asm volatile("" : "+s"(w));
    const Ids I = ids();
    const int panel = I.pq, ct = I.cq;
    const unsigned lds0 = (unsigned)(uintptr_t)smem;
    const char* kvh = (const char*)kvf + (size_t)(ct * 2 + (w >> 1)) * 262144 + (w & 1) * 4096;
    const unsigned voff = (unsigned)(lane * 16);
    auto order = [&](int st) { return ((panel & 8) + st - ((st - 1 < (panel & 7)) ? 1 : 0)) & 15; };
    auto load = [&](int st) {
        const char* src = kvh + (size_t)order(st) * 16384;
        const unsigned dst = lds0 + (w >> 1) * 16384 + (w & 1) * 4096 + (st & 3) * ATT_STAGE;
#pragma unroll
        for (int u = 0; u < 2; ++u)
            asm volatile("s_mov_b32 m0, %0\n\ts_nop 0\n\tglobal_load_lds_dwordx4 %1, %2 sc1\n\tglobal_load_lds_dwordx4 %1, %2 offset:1024 sc1\n\t"
                         "global_load_lds_dwordx4 %1, %2 offset:2048 sc1\n\tglobal_load_lds_dwordx4 %1, %2 offset:3072 sc1"
                         :: "s"(dst + u * 8192), "v"(voff), "s"(src + u * 8192) : "memory");
    };
    const unsigned wv = wo_off(w, lane), rv = res_off(false, true, lane), cv = colsum_off(w, lane);
    const unsigned poff = flag_off(set, ct, panel, lane & 15);
    u64 land;
    __builtin_amdgcn_s_setprio(3);
    BAR();
    signal_poll<false, true>(w, ws, flag_off(set, ct, lane & 15, panel), poff, 1ull << order(1), tag, land);
    STAMP(9);
    load(1);
    asm volatile("global_load_dwordx2 %0, %1, %2 sc1\n\ts_waitcnt vmcnt(0)" : "+v"(land) : "v"(poff), "s"(ws + WS_FLAG) : "memory");
    STAMP(10);
    BAR();
    if (!__all(land == tag)) flag_poll(ws, poff, tag);
    load(2); load(3);
    __builtin_amdgcn_s_setprio(0);
    STAMP(11);
    VM_BAR(8); load(4);
    VM_BAR(0); load(5); load(6);
    VM_BAR(0); load(7); load(8);
    u32x4 res[6], ca[8];
    colsum_load(ca, w < 3 ? Wcs : nullptr, cv);
#pragma unroll 1
    for (int st = 6; st < 12; st += 2) {
        VM_BAR(0);
        load(st + 3); load(st + 4);
        if (st == 8) wo_load(Wo, wv, 24, 32, lds0 + 32768, w);
    }
    VM_BAR(0); load(15); wo_load(Wo, wv, 0, 8, lds0, w);
    res_load(res, Xin, false, true, rv, w);
    VM_BAR(0); wo_load(Wo, wv, 8, 24, lds0, w);
    STAMP(12);
    out_handoff(smem, 98304, res, w, ws, set + 1, tag, lane, ca, Wcs ? 0 : -1);
}

DI void attn_compute(char* smem, int w, const u32x4 (&q4)[4], const __hip_bfloat16* __restrict__ kvf, __hip_bfloat16* __restrict__ ofrag SP_PARAM) {
    const int lane = lane_id();
    asm volatile("" : "+s"(w));
    const int wm = w & 1, wn = w >> 1;
    const Ids I = ids();
    const int panel = I.pq, head = I.cq * 2 + wn;
    const int l15 = lane & 15;
    const unsigned se = (l15 & 7) == 0 ? 0x3f803f80u : 0u, so = (l15 & 7) == 4 ? 0x3f803f80u : 0u;
    const u32x4 sel1 = {se, se, so, so};
    const lds_cptr sm = (lds_cptr)smem + wn * 16384 + lane * 16;
    const lds_cptr smk = (lds_cptr)smem + wn * 16384 + ((lane & 0x33) | ((lane & 4) << 1) | ((lane & 8) >> 1)) * 16;
    const f32x16 z16 = {0, 0, 0, 0, 0, 0, 0, 0, 0, 0, 0, 0, 0, 0, 0, 0};
    const bf16x8 qA0 = __builtin_bit_cast(bf16x8, q4[0]), qA1 = __builtin_bit_cast(bf16x8, q4[1]);
    const bf16x8 qB0 = __builtin_bit_cast(bf16x8, q4[2]), qB1 = __builtin_bit_cast(bf16x8, q4[3]);
    f32x16 oA, oB, nmA, nmB, sA, sB;
    f32x4 lsA, lsB;
    u32x8 pB;
    bf16x8 v11;
    asm volatile(".irp r,106,108,110,112,134,136,138,140,170,172\n\tv_mov_b64 v[\\r:\\r+1], 0\n\t.endr"
                 : "={v[106:113]}"(pB), "={v[134:137]}"(lsA), "={v[138:141]}"(lsB), "={v[170:173]}"(v11));
    WAIT_LGKM_BAR();
    STAMP(8);
    bf16x8 k00 = lds_b128(smk), k10 = lds_b128(smk + 1024), k01 = lds_b128(smk + 4096), k11 = lds_b128(smk + 4096 + 1024);
    bf16x8 v00 = lds_b128(sm + 2048), v10 = lds_b128(sm + 3072), v01 = lds_b128(sm + 4096 + 2048);
    float ob;
    {
        const u32x4 vu = __builtin_bit_cast(u32x4, v00);
        const unsigned vm = max(max(vu[0] & 0x7fff0000u, vu[1] & 0x7fff0000u), max(vu[2] & 0x7fff0000u, vu[3] & 0x7fff0000u));
        ob = __uint_as_float(wave_min(vm)) * 0.015625f;
        sA = MFMA32(k00, qA0, z16); sB = MFMA32(k00, qB0, z16);
        sA = MFMA32(k10, qA1, sA); sB = MFMA32(k10, qB1, sB);
        SBAR(); VM_BAR(0); SBAR();
        float mA = sA[0], mB = sB[0];
#pragma unroll
        for (int r = 1; r < 16; ++r) { mA = fmaxf(mA, sA[r]); mB = fmaxf(mB, sB[r]); }
        mA = max_h(mA) + 16.f; mB = max_h(mB) + 16.f;
#pragma unroll
        for (int r = 0; r < 16; ++r) { nmA[r] = -mA; nmB[r] = -mB; sA[r] -= mA; oA[r] = ob; oB[r] = ob; }
    }
    const unsigned a0 = (unsigned)(uintptr_t)sm, a1 = (unsigned)(uintptr_t)smk;
    unsigned dst = (unsigned)((((panel * 8 + wm * 4 + ((lane & 31) >> 4)) * 32 + head) * 64 + (lane & 15) + (lane >> 5) * 16) * 16);
    asm volatile("" : "+v"(dst));
    asm volatile(
        ".macro ATT_RD dst, y\n\t"
        ".if (\\dst) < 158\n\tds_read_b128 v[\\dst:\\dst+3], v[177+(((\\y)&0x1ffff)>>16)] offset:(\\y)&0xffff\n\t"
        ".else\n\tds_read_b128 v[\\dst:\\dst+3], v[175+(((\\y)&0x1ffff)>>16)] offset:(\\y)&0xffff\n\t.endif\n\t"
        ".endm\n\t"
        ".macro ATT_E d, c, n\n\t"
        "v_exp_f32 v[\\d], v[\\c]\n\t.if \\n > 1\n\tv_exp_f32 v[\\d+1], v[\\c+1]\n\t.endif\n\t.if \\n > 2\n\tv_exp_f32 v[\\d+2], v[\\c+2]\n\t.endif\n\t"
        ".endm\n\t"
        ".macro ATT_STEP cur, nxt, ka, kb, q, nm, oc, od, lsc, lsd, pc, pd, vd1, vc0, r0, y0, r1, y1, bar, rd\n\t"
        "v_mfma_f32_32x32x16_bf16 v[\\nxt:\\nxt+15], v[\\ka:\\ka+3], v[\\q:\\q+3], v[\\nm:\\nm+15]\n\t"
        "ATT_E \\cur, \\cur, 3\n\t"
        "v_mfma_f32_32x32x16_bf16 v[\\nxt:\\nxt+15], v[\\kb:\\kb+3], v[\\q+4:\\q+7], v[\\nxt:\\nxt+15]\n\t"
        "ATT_E \\cur+3, \\cur+3, 3\n\t"
        "v_smfmac_f32_32x32x32_bf16 v[\\od:\\od+15], v[\\vd1:\\vd1+3], v[\\pd:\\pd+7], v174\n\t"
        "s_waitcnt lgkmcnt(0)\n\t"
        ".if \\bar & 2\n\ts_barrier\n\t.endif\n\t"
        "ATT_E \\cur+6, \\cur+6, 2\n\tATT_E \\pc, \\cur+8, 1\n\t"
        "v_smfmac_f32_16x16x64_bf16 v[\\lsd:\\lsd+3], v[130:133], v[\\pd:\\pd+7], v174\n\t"
        ".if \\rd & 8\n\tATT_RD \\r0-8, \\y0-1024\n\t.endif\n\t"
        ".if \\rd & 1\n\tATT_RD \\r0, \\y0\n\t.endif\n\t"
        "ATT_E \\pc+1, \\cur+9, 2\n\t"
        "v_smfmac_f32_32x32x32_bf16 v[\\oc:\\oc+15], v[\\vc0:\\vc0+3], v[\\cur:\\cur+7], v174\n\t"
        ".if \\bar & 1\n\ts_barrier\n\t.endif\n\t"
        "ATT_E \\pc+3, \\cur+11, 3\n\t"
        "v_smfmac_f32_16x16x64_bf16 v[\\lsc:\\lsc+3], v[130:133], v[\\cur:\\cur+7], v174\n\t"
        ".if \\rd & 4\n\tATT_RD 142, 32768\n\tATT_RD 150, 33792\n\tATT_RD 158, 34816\n\tATT_RD \\r0, \\y0\n\t.endif\n\t"
        ".if \\rd & 2\n\tATT_RD \\r1, \\y1\n\t.endif\n\t"
        "ATT_E \\pc+6, \\cur+14, 2\n\t"
        ".if \\rd & 4\n\ts_waitcnt lgkmcnt(3)\n\t.endif\n\t"
        ".endm\n\t"
        ".macro ATT_TILE p, y1, y2, bar, ra=3, rb=3\n\t"
        "ATT_STEP 66, 82, 142+\\p, 150+\\p, 122, 50, 2, 18, 134, 138, 98, 106, 170-\\p, 158+\\p, 170-\\p, \\y1+3072, 142+\\p, \\y2, \\bar&1, \\ra\n\t"
        "ATT_STEP 82, 66, 146-\\p, 154-\\p, 114, 34, 18, 2, 138, 134, 106, 98, 166+\\p, 158+\\p, 150+\\p, \\y2+1024, 158+\\p, \\y2+2048, \\bar&2, \\rb\n\t"
        ".endm\n\t"
        ".macro ATT_STAGE b, bar\n\t"
        "ATT_TILE 0, \\b+4096, \\b+8192, 0\n\t"
        "ATT_TILE 4, \\b+8192, \\b+12288, 0\n\t"
        ".if \\bar\n\tATT_TILE 0, \\b+12288, \\b+32768, 2, 1, 11\n\t.else\n\tATT_TILE 0, \\b+12288, \\b+32768, 0\n\t.endif\n\t"
        "ATT_TILE 4, \\b+32768, \\b+36864, 0\n\t"
        ".endm\n\t"
        "v_add_u32 v176, 0x10000, v175\n\tv_add_u32 v178, 0x10000, v177\n\tv_mov_b32 v174, 0xdddddddd\n\t"
        "s_mov_b32 s96, 4\n\t"
        "s_waitcnt lgkmcnt(0)\n\t"
        "ATT_TILE 0, 4096, 8192, 0\n\tATT_TILE 4, 8192, 12288, 0\n\tATT_TILE 0, 12288, 0, 0, 1, 0\n\tATT_TILE 4, 32768, 36864, 1, 6, 3\n\t"
        "ATT_STAGE 32768, 1\n\t"
        "s_branch .Latt_entry_%=\n\t"
        ".p2align 6\n"
        ".Latt_loop_%=:\n\t"
        "ATT_STAGE 0, 1\n\tATT_STAGE 32768, 0\n"
        ".Latt_entry_%=:\n\t"
        "ATT_STAGE 65536, 1\n\t"
        "s_sub_u32 s96, s96, 1\n\t"
        "ATT_STAGE 98304, 0\n\t"
        "s_cmp_lg_u32 s96, 0\n\t"
        "s_cbranch_scc1 .Latt_loop_%=\n\t"
        "s_nop 1\n\t"
        "v_smfmac_f32_32x32x32_bf16 v[18:33], v[170:173], v[106:113], v174\n\t"
        "v_smfmac_f32_16x16x64_bf16 v[138:141], v[130:133], v[106:113], v174\n\t"
        "s_waitcnt lgkmcnt(0)\n\ts_nop 10\n\t"
        ".purgem ATT_RD\n\t.purgem ATT_E\n\t.purgem ATT_STEP\n\t.purgem ATT_TILE\n\t.purgem ATT_STAGE"
        : "+{v[2:17]}"(oA), "+{v[18:33]}"(oB), "+{v[66:81]}"(sA), "+{v[106:113]}"(pB), "+{v[134:137]}"(lsA), "+{v[138:141]}"(lsB),
          "+{v[142:145]}"(k00), "+{v[146:149]}"(k01), "+{v[150:153]}"(k10), "+{v[154:157]}"(k11),
          "+{v[158:161]}"(v00), "+{v[162:165]}"(v01), "+{v[166:169]}"(v10), "+{v[170:173]}"(v11)
        : "{v[34:49]}"(nmA), "{v[50:65]}"(nmB), "{v[114:117]}"(qA0), "{v[118:121]}"(qA1), "{v[122:125]}"(qB0), "{v[126:129]}"(qB1),
          "{v[130:133]}"(sel1), "{v175}"(a0), "{v177}"(a1)
        : "memory", "scc", "s96", "v174", "v176", "v178", "v82", "v83", "v84", "v85", "v86", "v87", "v88", "v89", "v90", "v91", "v92", "v93", "v94", "v95",
          "v96", "v97", "v98", "v99", "v100", "v101", "v102", "v103", "v104", "v105");
    STAMP(9);
    float lA = lsA[0], lB = lsB[0];
    auto store = [&] {
#pragma unroll
    for (int u = 0; u < 2; ++u) {
        const float inv = __builtin_amdgcn_rcpf(u ? lB : lA), sub = -ob * inv;
        const f32x16 e = (u ? oB : oA) * inv + sub;
        st16o<0>((char*)ofrag + u * 65536, dst, u32x4{pack_bf16(e[0], e[1]), pack_bf16(e[2], e[3]), pack_bf16(e[8], e[9]), pack_bf16(e[10], e[11])});
        st16o<512>((char*)ofrag + u * 65536, dst, u32x4{pack_bf16(e[4], e[5]), pack_bf16(e[6], e[7]), pack_bf16(e[12], e[13]), pack_bf16(e[14], e[15])});
    }
    };
    store();
    {
        const f32x16 os = oA + oB;
        const float osum = (((os[0] + os[1]) + (os[2] + os[3])) + ((os[4] + os[5]) + (os[6] + os[7]))) +
                           (((os[8] + os[9]) + (os[10] + os[11])) + ((os[12] + os[13]) + (os[14] + os[15])));
        const bool bad = !(fmaxf(lA, lB) < 1.0e24f) || !(lA > 1.0e-24f) || !(lB > 1.0e-24f) || !(fabsf(osum) < 3.0e38f);
                if (__any(bad)) {
            const __amdgpu_buffer_rsrc_t rk = __builtin_amdgcn_make_buffer_rsrc((char*)kvf + (size_t)head * 262144, 0, 262144, 0x00020000);
            const unsigned s2 = (l15 & 6) == 0 ? 0x3f803f80u : 0u, s3 = (l15 & 6) == 4 ? 0x3f803f80u : 0u;
            const bf8_t sel = __builtin_bit_cast(bf8_t, u32x4{s2, s2, s3, s3});
            const int sidx = (l15 & 1) ? (int)0xEEEEEEEEu : 0x44444444;
            oA = z16; oB = z16; lA = 0.f; lB = 0.f; ob = 0.f;
            float mA = -INFINITY, mB = -INFINITY;
#pragma unroll 1
            for (int t = 0; t < 64; ++t) {
                bf16x8 f[4];
#pragma unroll
                for (int i = 0; i < 4; ++i) f[i] = __builtin_bit_cast(bf16x8, __builtin_amdgcn_raw_buffer_load_b128(rk, lane * 16 + i * 1024, t * 4096, 16));
                auto upd = [&](bf16x8 q0, bf16x8 q1, f32x16& o, float& l, float& m) {
                    f32x16 s = MFMA32(f[0], q0, z16); s = MFMA32(f[1], q1, s);
                    float tm = s[0];
#pragma unroll
                    for (int r = 1; r < 16; ++r) tm = fmaxf(tm, s[r]);
                    tm = max_h(tm);
                    const float mn = fmaxf(m, tm);
                    const float alpha = __builtin_amdgcn_exp2f(m - mn);
                    m = mn;
#pragma unroll
                    for (int r = 0; r < 16; ++r) { s[r] = __builtin_amdgcn_exp2f(s[r] - mn); o[r] *= alpha; }
                    const u32x4 p0 = {pack_bf16(s[0], s[1]), pack_bf16(s[2], s[3]), pack_bf16(s[4], s[5]), pack_bf16(s[6], s[7])};
                    const u32x4 p1 = {pack_bf16(s[8], s[9]), pack_bf16(s[10], s[11]), pack_bf16(s[12], s[13]), pack_bf16(s[14], s[15])};
                    f32x4 t4 = {0, 0, 0, 0};
                    o = MFMA32(f[2], __builtin_bit_cast(bf16x8, p0), o);
                    o = MFMA32(f[3], __builtin_bit_cast(bf16x8, p1), o);
                    t4 = ROWSUM(p0, p1, t4);
                    l = l * alpha + (t4[0] + t4[1]);
                };
                upd(qA0, qA1, oA, lA, mA);
                upd(qB0, qB1, oB, lB, mB);
            }
            store();
        }
    }
}

DI void ln_row(const u32x4 (&v)[2], __hip_bfloat16* __restrict__ Y, unsigned off) {
    float s1 = 0.f, s2 = 0.f;
#pragma unroll
    for (int e = 0; e < 8; ++e) s1 = dot2bf(v[e >> 2][e & 3], 0x3f803f80u, s1);
    const float mean = wave_sum(s1) * (1.f / 1024.f);
#pragma unroll
    for (int e = 0; e < 8; ++e) {
        const float d0 = bf_lo(v[e >> 2][e & 3]) - mean, d1 = bf_hi(v[e >> 2][e & 3]) - mean;
        s2 += d0 * d0 + d1 * d1;
    }
    const float mb = round_bf16(mean);
    const float rden = __builtin_amdgcn_rcpf(round_bf16(__builtin_amdgcn_sqrtf(round_bf16(round_bf16(wave_sum(s2) * (1.f / 1024.f)) + 1e-5f))));
#pragma unroll
    for (int hh = 0; hh < 2; ++hh) {
        u32x4 y;
#pragma unroll
        for (int e = 0; e < 4; ++e) {
            const unsigned d = pack_bf16(bf_lo(v[hh][e]) - mb, bf_hi(v[hh][e]) - mb);
            y[e] = pack_bf16(bf_lo(d) * rden, bf_hi(d) * rden);
        }
        st16((char*)Y + hh * 131072, off, y);
    }
}

extern "C" __global__ __launch_bounds__(512, 2) void mega_kernel(const __hip_bfloat16* x, const __hip_bfloat16* wqkvA,
                                                            const __hip_bfloat16* woA, const __hip_bfloat16* wqkvB,
                                                            const __hip_bfloat16* woB, __hip_bfloat16* out,
                                                            char* ws) {
    char* const smem = smem_base;
    typedef __hip_bfloat16 bf;
    const int w8 = __builtin_amdgcn_readfirstlane(threadIdx.x >> 6);
    if (w8 == 7 && (blockIdx.x >> 3) < 10) code_touch(blockIdx.x >> 3, threadIdx.x & 63);
    asm volatile("" :: "s"(x), "s"(wqkvA), "s"(woA), "s"(wqkvB), "s"(woB), "s"(out), "s"(ws));
    STAMP_DECL; STAMPR(14); STAMP(0);
    u64 tag;
    {
        const int lane = threadIdx.x & 63;
        const Ids I = ids();
        const unsigned off = (unsigned)((I.po * 128 + I.co * 8 + w8) * 2048 + lane * 16);
        u32x4 v[2];
        u32x2 ep;
        asm volatile("global_load_dwordx4 %0, %3, %4\n\tglobal_load_dwordx4 %1, %3, %4 offset:1024\n\tglobal_load_dwordx2 %2, %5, %6 sc1"
                     : "=&v"(v[0]), "=&v"(v[1]), "=&v"(ep) : "v"(off), "s"(x), "v"(0), "s"(ws + WS_EPOCH) : "memory");
        if (w8 == 7) {
            const int l = lane & 15, k = l % 5;
            const unsigned o = (l >= 10 || k == 3) ? flag_off(l, I.cq, I.pq, 0) : (k == 0 || k == 2) ? flag_off(l, I.pq, I.cq, 0) : flag_off(l, I.po, I.co, 0);
            touch_at(ws + WS_FLAG, o);
        } else if (w8 >= 4) touch(wqkvA, (w8 - 4) * 256 + blockIdx.x, lane);
        if (w8 >= 4) asm volatile("s_waitcnt vmcnt(2)" ::: "memory"); else asm volatile("s_waitcnt vmcnt(1)" ::: "memory");
        asm volatile("" : "+v"(v[0]), "+v"(v[1]));
        ln_row(v, (bf*)(ws + WS_Y0), (unsigned)(I.po * 262144 + (lane >> 3) * 16384 + (I.co * 8 + w8) * 128 + (lane & 7) * 16));
        if (w8 >= 4) asm volatile("s_waitcnt vmcnt(3)" ::: "memory"); else asm volatile("s_waitcnt vmcnt(2)" ::: "memory");
        asm volatile("" : "+v"(ep));
        tag = (((u64)(unsigned)__builtin_amdgcn_readfirstlane(ep[1]) << 32) | (unsigned)__builtin_amdgcn_readfirstlane(ep[0])) + 1;
    }
    if (w8 >= 4) {
        int w = w8 & 3;
#pragma unroll 1
        for (int it = 0; it < 2; ++it) {
            const int lane = lane_id();
            asm volatile("" : "+s"(x), "+s"(wqkvA), "+s"(woA), "+s"(wqkvB), "+s"(woB), "+s"(ws), "+s"(w));
            const unsigned lds0 = (unsigned)(uintptr_t)smem;
            u32x4 cb[8];
            const unsigned wv = wo_off(w, lane), rv = res_off(true, it, lane), cv = colsum_off(w, lane);
            qkv_loader<true>(smem, w, ws, it * 5, tag, (const bf*)(ws + (it ? WS_X2 : WS_Y0)), wqkvA, (const float*)(ws + WS_ST + 262144),
                             ws + WS_CSP, it == 0, woA, [&] {
                if (w < 3) {
                    colsum_load(cb, wqkvB, cv);
                    asm volatile("s_waitcnt vmcnt(8)" ::: "memory");
                } else {
                    touch(woA, blockIdx.x, lane);
                    asm volatile("s_waitcnt vmcnt(1)" ::: "memory");
                }
            }, [&](int part) {
                if (part == 0) wo_load(woA, wv, 29, 32, lds0, w); else wo_load(woA, wv, 25, 29, lds0, w);
            } SP_ARG);
            STAMP(3);
            u32x4 res[6];
            res_load(res, it ? (const void*)(ws + WS_X2) : x, true, it, rv, w);
            if (it) __builtin_amdgcn_s_sleep(8);
            wo_load(woA, wv, 0, 7, lds0, w);
            BAR();
            wo_load(woA, wv, 7, 25, lds0, w);
            STAMP(4);
            out_handoff(smem, 147456, res, w, ws, it * 5 + 1, tag, lane, cb, it ? -1 : 1);
            STAMP(5);
            qkv_loader<false>(smem, w, ws, it * 5 + 2, tag, (const bf*)(ws + (it ? WS_X3 : WS_X1)), wqkvB, (const float*)(ws + WS_ST + it * 524288), ws + WS_CSP + 196608,
                              false, nullptr, [] {}, [](int) {} SP_ARG);
            STAMP(8);
            attn_loader(smem, w, ws, it * 5 + 3, tag, (const bf*)(ws + WS_KVF + it * 8 * MBy), woB, ws + (it ? WS_X3 : WS_X1),
                        it ? nullptr : wqkvA SP_ARG);
            STAMP(13); STAMPR(15); STAMP_FLUSH(it);
        }
        if (blockIdx.x == 0 && w == 0 && lane_id() == 0) __hip_atomic_store((g_u64ptr)(ws + WS_EPOCH), tag, __ATOMIC_RELAXED, __HIP_MEMORY_SCOPE_AGENT);
    } else {
        __builtin_amdgcn_s_setprio(2);
#pragma unroll 1
        for (int it = 0; it < 2; ++it) {
            u32x4 q4[4];
            asm volatile("" : "+s"(ws), "+s"(wqkvA), "+s"(wqkvB));
            qkv_compute<true>(smem, w8, wqkvA, q4, nullptr, (bf*)(ws + WS_OF + it * 8 * MBy), it == 0,
                              ws + WS_ST + 262144, ws + WS_WN + blockIdx.x * 16 SP_ARG);
            STAMP(2);
            out_compute<true>(smem, w8, (const bf*)(ws + WS_OF + it * 8 * MBy),
                              (bf*)(ws + (it ? WS_X3 : WS_X1)), (float*)(ws + WS_ST + it * 524288) SP_ARG);
            STAMP(6);
            qkv_compute<false>(smem, w8, wqkvB, q4, (bf*)(ws + WS_KVF + it * 8 * MBy), nullptr, false,
                               ws + WS_ST + it * 524288, ws + WS_WN + 4096 + blockIdx.x * 16 SP_ARG);
            attn_compute(smem, w8, q4, (const bf*)(ws + WS_KVF + it * 8 * MBy), (bf*)(ws + WS_OF + (it * 8 + 4) * MBy) SP_ARG);
            out_compute<false>(smem, w8, (const bf*)(ws + WS_OF + (it * 8 + 4) * MBy),
                               it ? out : (bf*)(ws + WS_X2), it ? nullptr : (float*)(ws + WS_ST + 262144) SP_ARG);
            STAMP(13); STAMPR(15); STAMP_FLUSH(it);
        }
    }
}

extern "C" __global__ void zz_code_end_kernel() {}

extern "C" void kernel_launch(void* const* d_in, const int* in_sizes, int n_in, void* d_out, int out_size, void* d_ws,
                              size_t ws_size, hipStream_t stream) {
    typedef __hip_bfloat16 bf;
    (void)hipFuncSetAttribute((const void*)mega_kernel, hipFuncAttributeMaxDynamicSharedMemorySize, CH_LDS);
    mega_kernel<<<256, 512, CH_LDS, stream>>>((const bf*)d_in[0], (const bf*)d_in[1], (const bf*)d_in[2], (const bf*)d_in[4], (const bf*)d_in[5],
                                              (bf*)d_out, (char*)d_ws);
}
```

```cpp
#include <hip/hip_runtime.h>
#include <hip/hip_bf16.h>
#include <stdint.h>

typedef __attribute__((ext_vector_type(8))) short bf16x8;
typedef __attribute__((ext_vector_type(4))) short s16x4;
typedef __attribute__((ext_vector_type(4))) float f32x4;
typedef __attribute__((ext_vector_type(2))) float f32x2;
typedef __attribute__((ext_vector_type(16))) float f32x16;
typedef __attribute__((ext_vector_type(4))) unsigned u32x4;
typedef __attribute__((ext_vector_type(2))) unsigned u32x2;
typedef __attribute__((ext_vector_type(2))) __bf16 bf16x2_t;
typedef __attribute__((ext_vector_type(8))) __bf16 bf8_t;
typedef __attribute__((ext_vector_type(16))) __bf16 bf16v_t;
typedef __attribute__((address_space(3))) const char* lds_cptr;
typedef __attribute__((address_space(3))) char* lds_ptr;

#define DI __device__ __forceinline__
#define MFMA16(a, b, c) __builtin_amdgcn_mfma_f32_16x16x32_bf16((a), (b), (c), 0, 0, 0)
#define MFMA32(a, b, c) __builtin_amdgcn_mfma_f32_32x32x16_bf16((a), (b), (c), 0, 0, 0)
#define SBAR() __builtin_amdgcn_sched_barrier(0)
#define STAMP(i)
#define STAMPR(i)
#define STAMP_DECL
#define STAMP_FLUSH(it)
#define SP_PARAM
#define SP_ARG
typedef unsigned long long u64;
typedef __attribute__((ext_vector_type(32))) unsigned u32x32;

constexpr float QSCALE = 0.17677669529663687f * 1.4426950408889634f;
constexpr float ATT_SCALE = 0.17677669529663687f;
constexpr float LOG2E = 1.4426950408889634f;
constexpr float NEG_MASK = -1e9f / ATT_SCALE;

DI int lane_id() { int l; asm volatile("v_mbcnt_lo_u32_b32 %0, -1, 0\n\tv_mbcnt_hi_u32_b32 %0, -1, %0" : "=v"(l)); return l; }
DI unsigned pack_bf16(float lo, float hi) {
    f32x2 v = {lo, hi};
    bf16x2_t r = __builtin_convertvector(v, bf16x2_t);
    return __builtin_bit_cast(unsigned, r);
}
DI float bf_lo(unsigned u) { return __uint_as_float(u << 16); }
DI float bf_hi(unsigned u) { return __uint_as_float(u & 0xffff0000u); }
DI float round_bf16(float x) { return bf_lo(pack_bf16(x, 0.f)); }
DI void ln_stats(float sum, float sumsq, float& mean_out, float& rstd_out) {
    float mean;
    asm("v_mul_f32 %0, 0x3a800000, %1" : "=v"(mean) : "v"(sum));
    const float var = fmaxf(__builtin_fmaf(-mean, mean, sumsq * (1.f / 1024.f)), 0.f);
    mean_out = round_bf16(mean);
    rstd_out = __builtin_amdgcn_rcpf(round_bf16(__builtin_amdgcn_sqrtf(round_bf16(round_bf16(var) + 1e-5f))));
}

DI void glds16(const void* sbase, unsigned voff, unsigned ldsaddr) {
    asm volatile("s_mov_b32 m0, %0\n\ts_nop 0\n\tglobal_load_lds_dwordx4 %1, %2"
                 :: "s"(ldsaddr), "v"(voff), "s"(sbase) : "memory");
}
DI void glds16c(const void* sbase, unsigned voff, unsigned ldsaddr) {
    asm volatile("s_mov_b32 m0, %0\n\ts_nop 0\n\tglobal_load_lds_dwordx4 %1, %2 sc1"
                 :: "s"(ldsaddr), "v"(voff), "s"(sbase) : "memory");
}
DI void st16(void* sbase, unsigned voff, u32x4 v) {
    asm volatile("global_store_dwordx4 %0, %1, %2 sc1\n\ts_nop 1" :: "v"(voff), "v"(v), "s"(sbase) : "memory");
}
template <int OFF> DI void st16o(void* sbase, unsigned voff, u32x4 v) {
    asm volatile("global_store_dwordx4 %0, %1, %2 offset:%3 sc1\n\ts_nop 1" :: "v"(voff), "v"(v), "s"(sbase), "n"(OFF) : "memory");
}
template <int OFF> DI void st16k(void* sbase, unsigned voff, u32x4 v) {
    asm volatile("global_store_dwordx4 %0, %1, %2 offset:%3 sc1" :: "v"(voff), "v"(v), "s"(sbase), "n"(OFF) : "memory");
}
DI bf16x8 lds_b128(lds_cptr p) { return *(const __attribute__((address_space(3))) bf16x8*)p; }
DI s16x4 lds_tr(lds_cptr p) {
    return __builtin_amdgcn_ds_read_tr16_b64_v4i16((__attribute__((address_space(3))) s16x4*)p);
}
DI bf16x8 cat8(s16x4 a, s16x4 b) { return __builtin_shufflevector(a, b, 0, 1, 2, 3, 4, 5, 6, 7); }
DI float dot2bf(unsigned a, unsigned b, float c) {
    return __builtin_amdgcn_fdot2_f32_bf16(__builtin_bit_cast(bf16x2_t, a), __builtin_bit_cast(bf16x2_t, b), c, false);
}
DI float sum_g(float v) {
    auto a = __builtin_amdgcn_permlane16_swap(__float_as_uint(v), __float_as_uint(v), false, false);
    v = __uint_as_float(a[0]) + __uint_as_float(a[1]);
    auto b = __builtin_amdgcn_permlane32_swap(__float_as_uint(v), __float_as_uint(v), false, false);
    return __uint_as_float(b[0]) + __uint_as_float(b[1]);
}
DI float wave_sum(float v) {
    v += __builtin_bit_cast(float, __builtin_amdgcn_update_dpp(0, __builtin_bit_cast(int, v), 0xB1, 0xF, 0xF, true));
    v += __builtin_bit_cast(float, __builtin_amdgcn_update_dpp(0, __builtin_bit_cast(int, v), 0x4E, 0xF, 0xF, true));
    v += __builtin_bit_cast(float, __builtin_amdgcn_update_dpp(0, __builtin_bit_cast(int, v), 0x141, 0xF, 0xF, true));
    v += __builtin_bit_cast(float, __builtin_amdgcn_update_dpp(0, __builtin_bit_cast(int, v), 0x140, 0xF, 0xF, true));
    return sum_g(v);
}
DI unsigned wave_min(unsigned v) {
    v = min(v, (unsigned)__builtin_amdgcn_update_dpp(0, (int)v, 0xB1, 0xF, 0xF, true));
    v = min(v, (unsigned)__builtin_amdgcn_update_dpp(0, (int)v, 0x4E, 0xF, 0xF, true));
    v = min(v, (unsigned)__builtin_amdgcn_update_dpp(0, (int)v, 0x141, 0xF, 0xF, true));
    v = min(v, (unsigned)__builtin_amdgcn_update_dpp(0, (int)v, 0x140, 0xF, 0xF, true));
    const auto a = __builtin_amdgcn_permlane16_swap(v, v, false, false);
    v = min(a[0], a[1]);
    const auto b = __builtin_amdgcn_permlane32_swap(v, v, false, false);
    return min(b[0], b[1]);
}
DI float amax3(float a, float b, float c) { float r; asm("v_max3_f32 %0, %1, %2, %3" : "=v"(r) : "v"(a), "v"(b), "v"(c)); return r; }
DI float max2f(float a, float b) { return __builtin_amdgcn_fmed3f(a, b, INFINITY); }
DI float max_h(float v) {
    auto b = __builtin_amdgcn_permlane32_swap(__float_as_uint(v), __float_as_uint(v), false, false);
    return max2f(__uint_as_float(b[0]), __uint_as_float(b[1]));
}

constexpr unsigned OFF_DUMP = 145408;
extern __shared__ __attribute__((aligned(16))) char smem_base[];
DI void touch_at(const void* p, unsigned off) {
    asm volatile("s_mov_b32 m0, %0\n\ts_nop 0\n\tglobal_load_lds_dword %1, %2"
                 :: "s"((unsigned)(uintptr_t)smem_base + OFF_DUMP), "v"(off), "s"(p) : "memory");
}
DI void touch(const void* p, int slice, int lane) { touch_at(p, (unsigned)(slice * 64 + lane) * 128u); }
DI void code_touch(int slice, int lane) {
    unsigned long long c0, c1;
    asm volatile("s_getpc_b64 s[96:97]\n\ts_add_u32 s96, s96, mega_kernel@rel32@lo+4\n\ts_addc_u32 s97, s97, mega_kernel@rel32@hi+12\n\ts_mov_b64 %0, s[96:97]\n\t"
                 "s_getpc_b64 s[96:97]\n\ts_add_u32 s96, s96, zz_code_end_kernel@rel32@lo+4\n\ts_addc_u32 s97, s97, zz_code_end_kernel@rel32@hi+12\n\ts_mov_b64 %1, s[96:97]"
                 : "=s"(c0), "=s"(c1) :: "s96", "s97", "scc");
    touch_at((const void*)c0, min((unsigned)(slice * 64 + lane) * 128u, (unsigned)(c1 - c0)));
}

#define BAR() asm volatile("s_barrier" ::: "memory")
#define WAIT_LGKM() do { SBAR(); __builtin_amdgcn_s_waitcnt(0xC07F); SBAR(); } while (0)
#define WAIT_LGKM_BAR() do { SBAR(); __builtin_amdgcn_s_waitcnt(0xC07F); asm volatile("s_barrier" ::: "memory"); SBAR(); } while (0)
#define VM_BAR(n) asm volatile("s_waitcnt vmcnt(" #n ")\n\ts_barrier" ::: "memory")
constexpr int CH_LDS = 163840;
typedef __attribute__((address_space(1))) u64* g_u64ptr;

constexpr size_t MBy = 1 << 20;
constexpr size_t WS_X3 = 0, WS_X2 = 4 * MBy, WS_X1 = 8 * MBy, WS_OF = 12 * MBy  , WS_KVF = 36 * MBy  ,
                 WS_ST = 52 * MBy  , WS_CSP = 54 * MBy  , WS_FLAG = 55 * MBy  ,
                 WS_WN = WS_CSP + 458752  , WS_EPOCH = WS_CSP + 524288, WS_Y0 = 56 * MBy;
DI unsigned flag_off(int set, int group, int consumer, int producer) { return (unsigned)(set * 65536 + group * 4096 + consumer * 256 + producer * 8); }
DI void flag_store(char* ws, int set, int group, int producer, u64 tag, int lane) {
    asm volatile("" : "+s"(tag));
    if (lane < 16) __hip_atomic_store((g_u64ptr)(ws + WS_FLAG + flag_off(set, group, lane, producer)), tag, __ATOMIC_RELAXED, __HIP_MEMORY_SCOPE_AGENT);
}
DI void flag_poll(const char* ws, unsigned off, u64 tag) {
    asm volatile("" : "+s"(tag));
#pragma unroll 1
    for (int spin = 0; spin < (1 << 19); ++spin) {
        const u64 v = __hip_atomic_load((g_u64ptr)(ws + WS_FLAG + off), __ATOMIC_RELAXED, __HIP_MEMORY_SCOPE_AGENT);
        if (__all(v == tag)) break;
        __builtin_amdgcn_s_sleep(1);
    }
    asm volatile("" ::: "memory");
}
template <bool DRAIN, bool ALLPOLL>
DI void signal_poll(int follower, const char* ws, unsigned soff, unsigned poff, u64 pmask, u64 tag, u64& land) {
    u64 tagv, zero;
    asm volatile("s_not_b64 s[96:97], %[ts]\n\tv_mov_b64 %[tag], %[ts]\n\tv_mov_b64 %[zero], 0\n\tv_mov_b64 %[land], s[96:97]\n\t"
                 ".if %[dr]\n\ts_waitcnt vmcnt(0)\n\t.endif\n\ts_barrier\n\t"
                 "s_cmp_lg_u32 %[fl], 0\n\ts_cbranch_scc1 .Lsig_fol_%=\n\t"
                 "s_mov_b64 exec, 0xffff\n\t"
                 "global_store_dwordx2 %[so], %[tag], %[base] sc1\n"
                 ".if %[ap]\n.Lsig_fol_%=:\n.endif\n\t"
                 "s_mov_b64 exec, %[pm]\n\t"
                 "s_mov_b32 s96, 0x100000\n"
                 ".Lsig_loop_%=:\n\t"
                 "global_atomic_or_x2 %[land], %[po], %[zero], %[base] sc0 sc1\n\t"
                 "s_sleep 1\n\t"
                 "s_waitcnt vmcnt(3)\n\t"
                 "v_cmp_ne_u64 vcc, %[land], %[tag]\n\t"
                 "s_sub_u32 s96, s96, 1\n\t"
                 "s_cbranch_scc1 .Lsig_out_%=\n\t"
                 "s_cbranch_vccnz .Lsig_loop_%=\n"
                 ".Lsig_out_%=:\n\t"
                 "s_mov_b64 exec, -1\n"
                 ".if !%[ap]\n.Lsig_fol_%=:\n.endif"
                 : [land] "=&v"(land), [tag] "=&v"(tagv), [zero] "=&v"(zero)
                 : [fl] "s"(follower), [so] "v"(soff), [po] "v"(poff), [ts] "s"(tag), [base] "s"(ws + WS_FLAG), [pm] "s"(pmask), [ap] "n"(ALLPOLL ? 1 : 0), [dr] "n"(DRAIN ? 1 : 0)
                 : "memory", "scc", "vcc", "s96", "s97");
}
struct Ids { int pq, cq, po, co; };
DI Ids ids() {
    int bid = blockIdx.x; asm volatile("" : "+s"(bid));
    const int xcd = bid & 7, idx = bid >> 3;
    int pq, cq, po, co;
#define IDS_BODY pq = (xcd & 1) * 8 + (idx & 7); cq = (xcd >> 1) * 4 + (idx >> 3); po = (xcd & 3) * 4 + (idx & 3); co = (xcd >> 2) * 8 + (idx >> 2);
    IDS_BODY
    return Ids{pq, cq, po, co};
}

DI void slow_warm(unsigned dump, const void* W, int w, int lane) {
    int bid = blockIdx.x; asm volatile("" : "+s"(bid));
    const int xcd = bid & 7, idx = bid >> 3;
    const int sc = (3 - (int)((uintptr_t)W >> 7)) & 7;
    if ((sc >> 2) != ((xcd >> 1) & 1)) return;
    const char* src = (const char*)W + (idx + 32 * w) * 49152 + (sc + (xcd >> 2) * 8) * 128;
#pragma unroll
    for (int G = 0; G < 3; ++G) glds16(src + G * 2048, (unsigned)((lane >> 3) * 6144 + (lane & 7) * 16), dump);
}

template <int V> struct IC { static constexpr int value = V; };
template <bool LOCAL> struct QC {
    static constexpr int AROWS = LOCAL ? 144 : 128;
    static constexpr int ABYTES = AROWS * 128;
    static constexpr int STAGE = ABYTES + 24576;
    static constexpr int OFF_MEAN = 143360;
    static constexpr int OFF_RSTD = OFF_MEAN + 576;
    static constexpr int OFF_CS = OFF_RSTD + 576;
};
constexpr int OFF_EX = 131072;

DI unsigned colsum_off(int G, int lane) {
    const Ids I = ids();
    unsigned off = (unsigned)(I.pq * 64 + (lane >> 3)) * 6144u + (unsigned)(G * 2048 + I.cq * 128 + (lane & 7) * 16);
    asm volatile("" : "+v"(off));
    return off;
}
DI void colsum_load(u32x4 (&c)[8], const void* W, unsigned off) {
#define CS_NEXT "s_add_u32 s96, s96, 0xc000\n\ts_addc_u32 s97, s97, 0\n\t"
    asm volatile("s_cmp_eq_u64 %9, 0\n\ts_cbranch_scc1 .Lcs_skip_%=\n\ts_mov_b64 s[96:97], %9\n\t"
                 "global_load_dwordx4 %0, %8, s[96:97]\n\t" CS_NEXT "global_load_dwordx4 %1, %8, s[96:97]\n\t" CS_NEXT
                 "global_load_dwordx4 %2, %8, s[96:97]\n\t" CS_NEXT "global_load_dwordx4 %3, %8, s[96:97]\n\t" CS_NEXT
                 "global_load_dwordx4 %4, %8, s[96:97]\n\t" CS_NEXT "global_load_dwordx4 %5, %8, s[96:97]\n\t" CS_NEXT
                 "global_load_dwordx4 %6, %8, s[96:97]\n\t" CS_NEXT "global_load_dwordx4 %7, %8, s[96:97]\n"
                 ".Lcs_skip_%=:"
                 : "=&v"(c[0]), "=&v"(c[1]), "=&v"(c[2]), "=&v"(c[3]), "=&v"(c[4]), "=&v"(c[5]), "=&v"(c[6]), "=&v"(c[7])
                 : "v"(off), "s"(W) : "memory", "scc", "s96", "s97");
}
DI void colsum_store(u32x4 (&c)[8], char* ws, int mat, int G, int lane) {
    asm volatile("" : "+v"(c[0]), "+v"(c[1]), "+v"(c[2]), "+v"(c[3]), "+v"(c[4]), "+v"(c[5]), "+v"(c[6]), "+v"(c[7]));
    const Ids I = ids();
    const int m = lane & 15;
    const unsigned one = (m & 1) ? 0xbf800000u : 0xbf80u;
    const u32x4 sl = {(m >> 1) == 0 ? one : 0u, (m >> 1) == 1 ? one : 0u, (m >> 1) == 2 ? one : 0u, (m >> 1) == 3 ? one : 0u};
    f32x4 d = {0, 0, 0, 0};
#pragma unroll
    for (int i = 0; i < 8; ++i) d = MFMA16(__builtin_bit_cast(bf16x8, sl), __builtin_bit_cast(bf16x8, c[i]), d);
#pragma unroll
    for (int r = 0; r < 4; ++r)
        d[r] += __int_as_float(__builtin_amdgcn_update_dpp(0, __float_as_int(d[r]), 0x128, 0xF, 0xF, true));
    if (lane < 32 && (lane & 8) == 0)
        st16(ws + WS_CSP + mat * 196608, (unsigned)((I.cq * 16 + I.pq) * 768 + G * 256 + (lane & 7) * 32 + (lane >> 4) * 16), __builtin_bit_cast(u32x4, d));
    typedef __attribute__((ext_vector_type(2))) unsigned short u16x2;
    u16x2 mn = {0x7fff, 0x7fff};
#pragma unroll
    for (int e = 0; e < 4; ++e) {
        u16x2 mx = {0, 0};
#pragma unroll
        for (int i = 0; i < 8; ++i) { const unsigned u = c[i][e] & 0x7fff7fffu; mx = __builtin_elementwise_max(mx, __builtin_bit_cast(u16x2, u)); }
        mn = __builtin_elementwise_min(mn, mx);
    }
    const unsigned wn = wave_min((unsigned)min(mn[0], mn[1])) << 16;
    if (lane == 0) __hip_atomic_store((__attribute__((address_space(1))) unsigned*)(ws + WS_WN + mat * 4096 + blockIdx.x * 16 + G * 4), wn,
                                      __ATOMIC_RELAXED, __HIP_MEMORY_SCOPE_AGENT);
}

template <bool LOCAL>
struct WFirst {
    unsigned v[2];
    DI WFirst(int w, int ct, int lane) {
#pragma unroll
        for (int i = 0; i < 2; ++i) {
            const int k = 8 * i + (lane >> 3), pos = lane & 7;
            v[i] = (unsigned)k * 6144u + (unsigned)(w * 1024 + ct * 64 + ((pos >> 1) ^ (((k >> 1) & 1) | (i << 1))) * 16 + (pos & 1) * 8) * 2u;
        }
        asm volatile("" : "+v"(v[0]), "+v"(v[1]));
    }
    DI void issue(const void* W, unsigned lds0, int w, int i0, int i1) const {
#pragma unroll
        for (int i = i0; i < i1; ++i)
            glds16((const char*)W + (size_t)(i >> 1) * (16 * 6144), v[i & 1], lds0 + (i >> 3) * QC<LOCAL>::STAGE + QC<LOCAL>::ABYTES + (w * 8 + (i & 7)) * 1024);
    }
};

template <bool LOCAL, class F, class F2>
DI void qkv_loader(char* smem, int w, char* ws, int set, u64 tag, const __hip_bfloat16* __restrict__ X, const __hip_bfloat16* __restrict__ W,
                   const float* __restrict__ statp, const char* __restrict__ csp, bool ident, const void* nextW, F tail, F2 mid SP_PARAM) {
    typedef QC<LOCAL> C;
    constexpr int NT = 16;
    const int lane = lane_id();
    asm volatile("" : "+s"(w));
    const int idx = blockIdx.x >> 3;
    const Ids I = ids();
    const int panel = I.pq, ct = I.cq;
    const int R0 = panel * 128;
    const unsigned lds0 = (unsigned)(uintptr_t)smem;
    auto grow = [&](int i) -> int {
        if (!LOCAL) return R0 + i;
        if (i < 128) { const int t = (i >> 4) & 3, a = i & 15; return R0 + (i & 64) + 2 * (16 * (t & 1) + a) + (t >> 1); }
        const int a = i & 15; const int r = R0 - 16 + 2 * (a & 7) + (a >> 3);
        return r < 0 ? 0 : r;
    };
    unsigned voffA[5], voffB[6];
#pragma unroll
    for (int i = 0; i < 6; ++i) {
        const int j = w + 4 * i, G = j >> 3, k = 8 * (j & 7) + (lane >> 3);
        const int pos = lane & 7, f = ((k >> 1) & 1) | (((k >> 3) & 1) << 1);
        const int seg = (pos >> 1) ^ f;
        voffB[i] = (unsigned)k * 6144u + (unsigned)(G * 1024 + ct * 64 + seg * 16 + (pos & 1) * 8) * 2u;
    }
    auto issueW = [&](int t, int buf) {
        const char* wb = (const char*)W + (size_t)t * (64 * 6144);
        const unsigned sb = lds0 + buf * C::STAGE;
#pragma unroll
        for (int i = 0; i < 6; ++i) glds16(wb, voffB[i], sb + C::ABYTES + (w + 4 * i) * 1024);
    };
    asm volatile("" : "+v"(voffB[0]), "+v"(voffB[1]), "+v"(voffB[2]), "+v"(voffB[3]), "+v"(voffB[4]), "+v"(voffB[5]));
    const WFirst<LOCAL> first(w, ct, lane);
    const int g = lane >> 4, r = lane & 15, pos = 32 * w + 2 * r + (g >> 1);
    const int lr = !LOCAL ? (pos & 64) + 2 * (pos & 31) + ((pos >> 5) & 1)
                          : (w == 0 && (g & 1)) ? 128 + (r & 7) + 8 * (g >> 1) : (pos & 64) + ((pos & 1) * 2 + ((pos >> 5) & 1)) * 16 + ((pos >> 1) & 15);
    unsigned so = (unsigned)(panel * 16384 + g * 1024 + w * 256 + r * 16), ho = (unsigned)(g * 1024 + (panel ? panel * 16384 - 15488 + (r & 7) * 16 : 0));
    unsigned co = (unsigned)((ct * 16 + g) * 768 + w * 192 + r * 12);
    lds_ptr mp = (lds_ptr)smem + C::OFF_MEAN + lr * 4, cp = (lds_ptr)smem + C::OFF_CS + w * 192 + r * 12 + ((g & 1) ? 8 : g * 2);
    unsigned fo = flag_off((LOCAL ? 13 : 10) + min(g, 2), ct, panel, r);
    asm volatile("" : "+v"(so), "+v"(ho), "+v"(co), "+v"(mp), "+v"(cp), "+v"(fo));
#pragma unroll
    for (int i = 0; i < 5; ++i) {
        const int j = (i < 4) ? (w + 4 * i) : (16 + (w & 1));
        const int row = 8 * j + (lane >> 3);
        const int c = (lane & 7) ^ ((row >> 1) & 7);
        const int gr = grow(row < C::AROWS ? row : 0);
        voffA[i] = (unsigned)((gr >> 7) * 262144 + (gr & 127) * 128 + c * 16);
        asm volatile("" : "+v"(voffA[i]));
    }
    auto issueA = [&](int t, int buf) {
        const char* xa = (const char*)X + t * 16384;
        const unsigned sb = lds0 + buf * C::STAGE;
        if (LOCAL && w < 2) glds16c(xa, voffA[4], sb + (16 + w) * 1024);
#pragma unroll
        for (int i = 0; i < 4; ++i) glds16c(xa, voffA[i], sb + (w + 4 * i) * 1024);
    };
    u64 land;
    if (w == 3) __builtin_amdgcn_s_setprio(3);
    signal_poll<LOCAL, false>(w - 3, ws, flag_off(set, I.po, lane & 15, I.co),
                              flag_off(set, (lane & 16) && panel ? panel - 1 : panel, ct, lane & 15),
                              !LOCAL ? 0xffffull : ident ? 0xc000ffffull : 0xffffffffull, tag, land);
    if (w < 3) first.issue(W, lds0, w, 0, 12);
    BAR();
    STAMP(LOCAL ? 1 : 6);
    issueA(0, 0);
    issueA(1, 1);
    if (w < 3) { first.issue(W, lds0, w, 12, 16); asm volatile("s_waitcnt vmcnt(8)" ::: "memory"); }
    else asm volatile("s_waitcnt vmcnt(4)" ::: "memory");
    BAR();
    asm volatile("" :: "v"(land));
    __builtin_amdgcn_s_setprio(0);
    STAMP(LOCAL ? 2 : 7);
    issueW(2, 2);
    issueA(2, 2);
    VM_BAR(10);
    asm volatile("global_load_dwordx2 %0, %1, %2 sc1" : "=&v"(land) : "v"(fo), "s"(ws + WS_FLAG) : "memory");
    issueA(3, 0); issueW(3, 0);
    int buf = 1;
#pragma unroll 1
    for (int t = 1; t < NT - 3; ++t) {
        VM_BAR(10);
        issueA(t + 3, buf); issueW(t + 3, buf);
        if (t == 10 && idx < 3) code_touch(idx * 4 + w, lane);
        if (LOCAL && t == 11)
            glds16(nextW, (unsigned)((idx + 32 * w) * 8 + (lane >> 3)) * 2048u +
                   (unsigned)(((I.co & 8) + ((3 - (int)((uintptr_t)nextW >> 7)) & 7)) * 128 + (lane & 7) * 16), lds0 + OFF_DUMP);
        buf = (buf == 2) ? 0 : buf + 1;
    }
    typedef __attribute__((ext_vector_type(3))) float f32x3;
    const bool halo = LOCAL && w == 0;
    asm volatile("" : "+v"(land));
    if (!ident) {
        if (!__all(land == tag)) flag_poll(ws, fo, tag);
        f32x3 cs[4];
        f32x4 sv[4], hv[4];
        typedef __attribute__((address_space(3))) float* lds_f;
        auto add = [](float a, float b) { float r; asm("v_add_f32 %0, %1, %2" : "=v"(r) : "v"(a), "v"(b)); return r; };
        auto fold32 = [](float a, float b) { asm("s_nop 1\n\tv_permlane32_swap_b32 %0, %1\n\tv_add_f32 %0, %0, %1" : "+v"(a), "+v"(b)); return a; };
        auto fold16 = [](float a, float b) { asm("s_nop 1\n\tv_permlane16_swap_b32 %0, %1\n\tv_add_f32 %0, %0, %1" : "+v"(a), "+v"(b)); return a; };
        auto sum4 = [&](auto (&v)[4], int e) { return add(add(v[0][e], v[1][e]), add(v[2][e], v[3][e])); };
#pragma unroll
        for (int i = 0; i < 4; ++i) asm volatile("global_load_dwordx3 %0, %1, %2 sc1" : "=&v"(cs[i]) : "v"(co), "s"(csp + i * 3072) : "memory");
        VM_BAR(14);
#pragma unroll
        for (int i = 0; i < 4; ++i) asm volatile("global_load_dwordx4 %0, %1, %2 sc1" : "=&v"(sv[i]) : "v"(so), "s"((const char*)statp + i * 4096) : "memory");
        if (LOCAL)
            asm volatile("s_cmp_lg_u32 %9, 0\n\ts_cbranch_scc1 .Lhv_skip_%=\n\t"
                         "global_load_dwordx4 %0, %4, %5 sc1\n\tglobal_load_dwordx4 %1, %4, %6 sc1\n\t"
                         "global_load_dwordx4 %2, %4, %7 sc1\n\tglobal_load_dwordx4 %3, %4, %8 sc1\n"
                         ".Lhv_skip_%=:" : "=&v"(hv[0]), "=&v"(hv[1]), "=&v"(hv[2]), "=&v"(hv[3])
                         : "v"(ho), "s"(statp), "s"(statp + 1024), "s"(statp + 2048), "s"(statp + 3072), "s"(w) : "memory", "scc");
        if (halo) asm volatile("s_waitcnt vmcnt(8)" ::: "memory"); else asm volatile("s_waitcnt vmcnt(4)" ::: "memory");
        asm volatile("" : "+v"(cs[0]), "+v"(cs[1]), "+v"(cs[2]), "+v"(cs[3]));
        const float c2 = sum4(cs, 2);
        *(lds_f)cp = fold16(fold32(sum4(cs, 0), sum4(cs, 1)), fold32(c2, c2));
        BAR();
        asm volatile("s_waitcnt vmcnt(0)" : "+v"(sv[0]), "+v"(sv[1]), "+v"(sv[2]), "+v"(sv[3]) :: "memory");
        mid(0);
        const float s1 = fold32(sum4(sv, 0), sum4(sv, 2)), s2 = fold32(sum4(sv, 1), sum4(sv, 3));
        float h1, h2;
        asm("v_mov_b32 %0, %2\n\tv_mov_b32 %1, %3" : "=&v"(h1), "=&v"(h2) : "v"(s1), "v"(s2));
        if (halo) {
            asm volatile("" : "+v"(hv[0]), "+v"(hv[1]), "+v"(hv[2]), "+v"(hv[3]));
            const float u0 = sum4(hv, 0), u1 = sum4(hv, 1), u2 = sum4(hv, 2), u3 = sum4(hv, 3);
            h1 = fold32(u0, panel ? u2 : u0); h2 = fold32(u1, panel ? u3 : u1);
        }
        float mean, rstd;
        ln_stats(fold16(s1, h1), fold16(s2, h2), mean, rstd);
        *(lds_f)mp = mean; *(lds_f)(mp + (C::OFF_RSTD - C::OFF_MEAN)) = rstd;
    } else { VM_BAR(10); tail(); BAR(); mid(0); }
    mid(1);
    WAIT_LGKM_BAR();
}

template <bool LOCAL>
DI void qkv_compute(char* smem, int w, const void* W, u32x4 (&qo4)[4], __hip_bfloat16* __restrict__ kvf, __hip_bfloat16* __restrict__ ofrag, bool ident,
                    const char* statp, const char* wnp SP_PARAM) {
    typedef QC<LOCAL> C;
    constexpr int NT = 16, ABYTES = C::ABYTES, STAGE = C::STAGE;
    const int lane = lane_id();
    asm volatile("" : "+s"(w));
    const int wm = w & 1, wn = w >> 1;
    const Ids I = ids();
    const int panel = I.pq, ct = I.cq;
    const int l15 = lane & 15, g = lane >> 4;
    const lds_cptr sm = (lds_cptr)smem;
    lds_ptr smw = (lds_ptr)smem;

    f32x4 acc[3][4][2];
    f32x4 acch[2];
    float cb;
    f32x4 bias;

    bf16x8 A[2][5][2];
    s16x4 B[3][2][2];
    lds_cptr pa[2], pb[2];
    auto rdA = [&](auto WM, int p, int i) { A[p][i >> 1][i & 1] = lds_b128(pa[i & 1] + ((i >> 1) < 4 ? (i >> 1) * 2048 : 16384 - WM.value * 8192)); };
    auto rdB = [&](int c, int i) { B[c % 3][i >> 1][i & 1] = lds_tr(pb[c & 1] + ((c >> 1) + 1) % 3 * 8192 + (i >> 1) * 4096 + (i & 1) * 512); };
    auto column = [&](auto WM, int p, int c, bool first, auto gap) {
        const int G = ((c >> 1) + 1) % 3, f = c & 1;
        const bool halo = LOCAL && G == 1 + WM.value;
#pragma unroll
        for (int n = 0; n < (halo ? 10 : 8); ++n) {
            const bf16x8 x = A[p][n >> 1][n & 1], y = cat8(B[c % 3][n & 1][0], B[c % 3][n & 1][1]);
            const bf16x8 u = G < 2 ? y : x, v = G < 2 ? x : y;
            f32x4& d = n < 8 ? acc[G][n >> 1][f] : acch[f];
            const f32x4 c0 = bias;
            if (!first || (n & 1)) asm volatile("v_mfma_f32_16x16x32_bf16 %0, %1, %2, %0" : "+v"(d) : "v"(u), "v"(v));
            else if (n == 8) asm volatile("v_mfma_f32_16x16x32_bf16 %0, %1, %2, 0" : "=&v"(d) : "v"(u), "v"(v));
            else asm volatile("v_mfma_f32_16x16x32_bf16 %0, %1, %2, %3" : "=&v"(d) : "v"(u), "v"(v), "v"(c0));
            SBAR();
            gap(n);
            SBAR();
        }
    };

    f32x4 cq[2], ck[2], mu4[5], rs4[5]; float cv[2], mu[5], rs[5];
    u32x4 qfr[4], kfr[4];
    u32x4 va[2][2];
    f32x4 mwin, mfirst;
    typedef __attribute__((address_space(3))) u32x4* lds_u4;
    const bool negpos = (wm == 0) && (panel == 0);
    VM_BAR(0);
    unsigned cm01 = 0, cm23 = 0;
    lds_cptr ka[2] = {sm, sm}, vo2[2] = {sm, sm};
    lds_u4 ex = (lds_u4)(smw + OFF_EX + wn * 6144) + lane;
    lds_cptr s1 = sm + C::OFF_MEAN + wm * 256 + l15 * 4, s4 = sm + C::OFF_MEAN + wm * 256 + g * 16;
    lds_cptr c1 = sm + C::OFF_CS + wn * 128 + l15 * 4, c4 = sm + C::OFF_CS + wn * 128 + g * 16;
    asm volatile("" : "+v"(s1), "+v"(s4), "+v"(c1), "+v"(c4));
    if (LOCAL) {
        unsigned keepv = negpos ? __float_as_uint(NEG_MASK) : 0u;
        asm volatile("" : "+s"(keepv));
        u32x4 mw, mf;
        unsigned prv[4];
#pragma unroll
        for (int r = 0; r < 4; ++r) {
            const int d = l15 - (4 * g + r);
            unsigned out = (unsigned)(((d << 28) >> 31));
            prv[r] = (unsigned)(d >> 31);
            asm volatile("" : "+v"(out), "+v"(prv[r]));
            mw[r] = out & 0xff800000u;
            mf[r] = (out & 0xff800000u) | (~out & keepv);
        }
        const lds_cptr e0 = sm + OFF_EX + wn * 6144;
        ka[0] = e0 + (wm ? lane : 256 + (lane ^ 8)) * 16;         ka[1] = e0 + (wm ? 64 + lane : 256 + lane) * 16;
        vo2[0] = e0 + (wm ? 128 + lane : 320 + (lane ^ 32)) * 16; vo2[1] = e0 + (wm ? 192 + lane : 320 + lane) * 16;
        cm01 = ~((prv[0] >> 16) | (prv[1] << 16)); cm23 = ~((prv[2] >> 16) | (prv[3] << 16));
        mwin = __builtin_bit_cast(f32x4, mw); mfirst = __builtin_bit_cast(f32x4, mf);
        asm volatile("" : "+v"(mwin), "+v"(mfirst), "+v"(cm01), "+v"(cm23), "+v"(ka[0]), "+v"(ka[1]), "+v"(vo2[0]), "+v"(vo2[1]), "+v"(ex));
    }
    typedef const __attribute__((address_space(3))) f32x4* lds_f4; typedef const __attribute__((address_space(3))) float* lds_f1;
    auto mainloop = [&](auto WM) {
        int ty = lane_id();
        const int l15 = ty & 15, g = ty >> 4;
        const int swA = (l15 >> 1) & 7;
        const int q4 = l15 >> 2, p4 = l15 & 3, fk = ((q4 >> 1) & 1) | ((g & 1) << 1);
        lds_cptr pA0 = sm + wm * 8192 + l15 * 128 + ((0 + g) ^ swA) * 16, pA1 = sm + wm * 8192 + l15 * 128 + ((4 + g) ^ swA) * 16;
        lds_cptr pB0 = sm + ABYTES + (8 * g + q4) * 128 + (((2 * wn + 0) ^ fk) * 32) + p4 * 8;
        lds_cptr pB1 = sm + ABYTES + (8 * g + q4) * 128 + (((2 * wn + 1) ^ fk) * 32) + p4 * 8;
        asm volatile("" : "+v"(pA0), "+v"(pA1), "+v"(pB0), "+v"(pB1));
        slow_warm((unsigned)(uintptr_t)smem + OFF_DUMP, W, w, ty);
        unsigned so = (unsigned)(panel * 16384 + wm * 512 + ty * 8);
        const char *p0 = ident ? (const char*)W : statp, *p1 = ident ? (const char*)W : wnp;
        u32x4 wn;
        asm volatile("global_load_dwordx4 %0, %2, %3 sc1" : "=&v"(wn), "+v"(so) : "v"(0), "s"(p1) : "memory");
        BAR();
        STAMP(LOCAL ? 1 : 7);
        f32x2 sq;
        asm volatile("global_load_dwordx2 %0, %1, %2 sc1" : "=&v"(sq) : "v"(so), "s"(p0) : "memory");
        BAR();
        pa[0] = pA0; pa[1] = pA1; pb[0] = pB0; pb[1] = pB1;
#pragma unroll
        for (int i = 0; i < 4; ++i) rdB(0, i);
#pragma unroll
        for (int i = 0; i < 8; ++i) rdA(WM, 0, i);
#pragma unroll
        for (int i = 0; i < 4; ++i) if (LOCAL && WM.value == 0 && i >= 2) rdA(WM, 0, 6 + i); else rdB(1, i);
        SBAR();
        asm volatile("s_waitcnt vmcnt(0)" : "+v"(sq), "+v"(wn) :: "memory");
        const float d = fmaxf(__builtin_fmaf(sq[0] * (-1.f / 64.f), sq[0], sq[1]), 0.f);
        cb = fminf(__builtin_amdgcn_sqrtf(__uint_as_float(wave_min(__float_as_uint(d)))) * 64.f * __uint_as_float(min(min(wn[0], wn[1]), wn[2])), 1.0e30f);
        if (ident) cb = 12.f;
        bias = f32x4{cb, cb, cb, cb};
        asm volatile("s_nop 1" : "+v"(bias));
        WAIT_LGKM();
        int off = 0;
        constexpr bool HK = LOCAL && WM.value == 0;
        auto front = [&](int p, bool first) {
#pragma unroll
            for (int c = 0; c < 4; ++c)
                column(WM, p, c, first, [&](int n) {
                    if (HK && c == 0) { if (n < 2) rdB(1, 2 + n); else if (n < 6) rdB(2, n - 2); }
                    else if (n < 4) rdB(c + 2, n);
                    if (LOCAL && !HK && c == 0 && (n == 4 || n == 5)) rdA(WM, p, 4 + n);
                    if (c == 1 && n == 5) { off = (off == 2 * STAGE) ? 0 : off + STAGE; asm volatile("" : "+s"(off)); }
                    if (c == 2 && n == 4) { pa[0] = pA0 + off; asm volatile("" : "+v"(pa[0])); }
                    if (c == 2 && n == 5) { pa[1] = pA1 + off; asm volatile("" : "+v"(pa[1])); }
                    if (c == 3 && n == 4) { pb[0] = pB0 + off; asm volatile("" : "+v"(pb[0])); }
                    if (c == 3 && n == 5) { pb[1] = pB1 + off; asm volatile("" : "+v"(pb[1])); }
                    if ((c == 0 && n == 3) || ((c == 1 || c == 2 || (HK && c == 0)) && n == 7)) __builtin_amdgcn_s_waitcnt(0xC47F);
                });
            WAIT_LGKM_BAR();
        };
        auto back = [&](int p, bool first) {
            column(WM, p, 4, first, [&](int n) { if (n & 1) rdB(0, n >> 1); else rdA(WM, p ^ 1, n >> 1); });
            column(WM, p, 5, first, [&](int n) {
                if (!(n & 1)) rdA(WM, p ^ 1, 4 + (n >> 1)); else if (HK && n > 4) rdA(WM, p ^ 1, 6 + (n >> 1)); else rdB(1, n >> 1);
                if (n == 7) __builtin_amdgcn_s_waitcnt(0xC87F);
            });
        };
        auto stage = [&](int p, bool first) { front(p, first); back(p, first); };
        stage(0, true);
        front(1, false);
#pragma unroll 1
        for (int t = 1; t < NT - 1; t += 2) { back(1, false); stage(0, false); front(1, false); }
        column(WM, 1, 4, false, [&](int n) {
            constexpr int RS = C::OFF_RSTD - C::OFF_MEAN, H = 512 - WM.value * 256;
            if (n == 0) { ck[0] = *(lds_f4)(c4 + 256); ck[1] = *(lds_f4)(c4 + 320); cv[0] = *(lds_f1)(c1 + 512); cv[1] = *(lds_f1)(c1 + 512 + 64); }
            else if (n < 5) {
                mu[n - 1] = *(lds_f1)(s1 + (n - 1) * 64); rs[n - 1] = *(lds_f1)(s1 + RS + (n - 1) * 64);
                mu4[n - 1] = *(lds_f4)(s4 + (n - 1) * 64); rs4[n - 1] = *(lds_f4)(s4 + RS + (n - 1) * 64);
            } else if (n == 5) {
                if (LOCAL && WM.value == 0) { mu[4] = *(lds_f1)(s1 + H); rs[4] = *(lds_f1)(s1 + RS + H); }
                else if (LOCAL) { mu4[4] = *(lds_f4)(s4 + H); rs4[4] = *(lds_f4)(s4 + RS + H); }
            } else if (n == 6) { cq[0] = *(lds_f4)c4; cq[1] = *(lds_f4)(c4 + 64); }
        });
        if (LOCAL) column(WM, 1, 5, false, [](int n) { if (n == 6) __builtin_amdgcn_s_waitcnt(0xC07F); });
        if (LOCAL) {
            auto fix = [&](int a, int t) {
#pragma unroll
                for (int f = 0; f < 2; ++f)
                    acc[a][t][f] = (a == 2) ? rs4[t] * (acc[2][t][f] + mu4[t] * cv[f]) + rs4[t] * -cb
                                            : rs[t] * (acc[a][t][f] + mu[t] * (a ? ck[f] : cq[f])) + rs[t] * -cb;
            };
            auto pk = [&](const f32x4 (&v)[2]) { return u32x4{pack_bf16(v[0][0], v[0][1]), pack_bf16(v[0][2], v[0][3]), pack_bf16(v[1][0], v[1][1]), pack_bf16(v[1][2], v[1][3])}; };
            auto sub = [&](int a, int t) { acc[a][t][0] -= cb; acc[a][t][1] -= cb; };
            if (ident) { sub(1, 1); sub(1, 3); sub(2, 1); sub(2, 3); }
            else {
                fix(1, 1); fix(1, 3); fix(2, 1); fix(2, 3);
#pragma unroll
                for (int f = 0; f < 2; ++f)
                    acch[f] = (WM.value == 0) ? rs[4] * (acch[f] + mu[4] * ck[f]) : rs4[4] * (acch[f] + mu4[4] * cv[f]);
            }
            auto pkv = [&](int t) {
#pragma unroll
                for (int f = 0; f < 2; ++f) {
                    va[t >> 1][f][(t & 1) * 2] = pack_bf16(acc[2][t][f][0], acc[2][t][f][1]);
                    va[t >> 1][f][(t & 1) * 2 + 1] = pack_bf16(acc[2][t][f][2], acc[2][t][f][3]);
                }
            };
            kfr[1] = pk(acc[1][1]); kfr[3] = pk(acc[1][3]); pkv(1); pkv(3);
            u32x4 hf = pk(acch);
            if (WM.value == 0) {
                ex[0] = kfr[1]; ex[64] = kfr[3];
#pragma unroll
                for (int u = 0; u < 2; ++u)
                    asm volatile("ds_write_b64 %0, %1 offset:%3\n\tds_write_b64 %0, %2 offset:%3+8" :: "v"(ex), "v"(u32x2{va[u][0][2], va[u][0][3]}),
                                 "v"(u32x2{va[u][1][2], va[u][1][3]}), "n"(2048 + u * 1024) : "memory");
                if (negpos) hf = u32x4{0, 0, 0, 0};
            }
            ex[256 + WM.value * 64] = hf;
            SBAR();
            if (ident) { sub(0, 0); sub(0, 1); sub(0, 2); sub(0, 3); sub(1, 0); sub(1, 2); sub(2, 0); sub(2, 2); }
            else { fix(0, 0); fix(0, 1); fix(0, 2); fix(0, 3); fix(1, 0); fix(1, 2); fix(2, 0); fix(2, 2); }
#pragma unroll
            for (int t = 0; t < 4; ++t) qfr[t] = pk(acc[0][t]);
            kfr[0] = pk(acc[1][0]); kfr[2] = pk(acc[1][2]); pkv(0); pkv(2);
            asm volatile("" : "+v"(qfr[0]), "+v"(qfr[1]), "+v"(qfr[2]), "+v"(qfr[3]), "+v"(kfr[0]), "+v"(kfr[2]),
                         "+v"(va[0][0]), "+v"(va[0][1]), "+v"(va[1][0]), "+v"(va[1][1]));
        }
    };
    const int head = ct * 2 + wn;
    unsigned qo = 0, ko = 0, vo = 0;
    if (LOCAL) {
        qo = (unsigned)((((panel * 8 + wm * 4) * 32 + head) * 64 + lane) * 16);
        asm volatile("" : "+v"(qo));
    } else {
        ko = (unsigned)((wm * 8 + (g & 1)) * 1024 + ((l15 & 7) + ((l15 & 8) << 1) + 32 * (g >> 1)) * 16);
        vo = (unsigned)((wm * 8 + 2 + (g >> 1)) * 1024 + (l15 + 32 * (g & 1)) * 16);
        asm volatile("" : "+v"(ko), "+v"(vo));
    }
    char* sb[4];
#pragma unroll
    for (int t = 0; t < 4; ++t) sb[t] = LOCAL ? (char*)ofrag + t * 32768 : (char*)kvf + (size_t)(head * 16 + panel) * 16384 + (t & 1) * 4096;
    lds_ptr ownk = smw + wn * 16384 + ko, ownv = smw + wn * 16384 + vo;
    if (LOCAL) asm volatile("" : "+s"(sb[0]), "+s"(sb[1]), "+s"(sb[2]), "+s"(sb[3]));
    else asm volatile("" : "+s"(sb[0]), "+s"(sb[1]), "+v"(ownk), "+v"(ownv));
    if (LOCAL && wm) mainloop(IC<1>{}); else mainloop(IC<0>{});

    if (!LOCAL) {
        auto pk = [&](f32x4 a, f32x4 b) { return u32x4{pack_bf16(a[0], a[1]), pack_bf16(a[2], a[3]), pack_bf16(b[0], b[1]), pack_bf16(b[2], b[3])}; };
        SBAR();
#pragma unroll
        for (int t = 0; t < 4; ++t) {
            const u32x4 kk = pk(rs[t] * (acc[1][t][0] + mu[t] * ck[0]) + rs[t] * -cb, rs[t] * (acc[1][t][1] + mu[t] * ck[1]) + rs[t] * -cb);
            if (t & 1) st16k<128>(sb[t >> 1], ko, kk); else st16k<0>(sb[t >> 1], ko, kk);
            kfr[t] = kk;
            SBAR();
            const int u = t >> 1, f = t & 1;
            const u32x4 vv = pk(rs4[2 * u] * (acc[2][2 * u][f] + mu4[2 * u] * cv[f]) + rs4[2 * u] * -cb,
                                rs4[2 * u + 1] * (acc[2][2 * u + 1][f] + mu4[2 * u + 1] * cv[f]) + rs4[2 * u + 1] * -cb);
            if (f) st16k<256>(sb[u], vo, vv); else st16k<0>(sb[u], vo, vv);
            qfr[t] = vv;
            SBAR();
        }
        column(IC<0>{}, 1, 5, false, [&](int n) {
            const int t = n >> 1;
            if (n & 1) *(lds_u4)(ownv + (t >> 1) * 4096 + (t & 1) * 256) = qfr[t];
            else       *(lds_u4)(ownk + (t >> 1) * 4096 + (t & 1) * 128) = kfr[t];
        });
        asm volatile("s_nop 7");
        SBAR();
#pragma unroll
        for (int u = 0; u < 2; ++u) {
            const float r0 = rs[2 * u] * QSCALE, r1 = rs[2 * u + 1] * QSCALE;
            const u32x4 qa = pk(r0 * (acc[0][2 * u][0] + mu[2 * u] * cq[0]) + r0 * -cb, r0 * (acc[0][2 * u][1] + mu[2 * u] * cq[1]) + r0 * -cb);
            const u32x4 qb = pk(r1 * (acc[0][2 * u + 1][0] + mu[2 * u + 1] * cq[0]) + r1 * -cb, r1 * (acc[0][2 * u + 1][1] + mu[2 * u + 1] * cq[1]) + r1 * -cb);
#pragma unroll
            for (int e = 0; e < 4; ++e) {
                const auto x = __builtin_amdgcn_permlane16_swap(qa[e], qb[e], false, false);
                qo4[2 * u][e] = x[0]; qo4[2 * u + 1][e] = x[1];
            }
        }
        return;
    }
    {
        const f32x4 zero4 = {0, 0, 0, 0};
        auto bf8 = [](u32x4 v) { return __builtin_bit_cast(bf16x8, v); };
        f32x4 sp[4], sc[4];
        sc[1] = MFMA16(bf8(kfr[1]), bf8(qfr[1]), mwin); sp[1] = MFMA16(bf8(kfr[0]), bf8(qfr[1]), mwin);
        sc[3] = MFMA16(bf8(kfr[3]), bf8(qfr[3]), mwin); sp[3] = MFMA16(bf8(kfr[2]), bf8(qfr[3]), mwin);
        asm volatile("" : "+v"(sc[1]), "+v"(sp[1]), "+v"(sc[3]), "+v"(sp[3]));
        WAIT_LGKM_BAR();
        const bf16x8 kp[2] = {lds_b128(ka[0]), lds_b128(ka[1])};
        u32x2 vp[2][2];
#pragma unroll
        for (int u = 0; u < 2; ++u)
            asm volatile("ds_read_b64 %0, %2\n\tds_read_b64 %1, %2 offset:8" : "=&v"(vp[u][0]), "=&v"(vp[u][1]) : "v"(vo2[u]) : "memory");
        sc[0] = MFMA16(bf8(kfr[0]), bf8(qfr[0]), mwin); sc[2] = MFMA16(bf8(kfr[2]), bf8(qfr[2]), mwin);
        SBAR();
        float x[4][4], m[4];
        unsigned a[4][2];
        auto logits = [&](int t0, int st) {
#pragma unroll
            for (int t = t0; t < 4; t += 2)
#pragma unroll
                for (int h = 0; h < 2; ++h) {
                    if (st == 0) {
                        const unsigned uc = pack_bf16(sc[t][2 * h], sc[t][2 * h + 1]), up = pack_bf16(sp[t][2 * h], sp[t][2 * h + 1]);
                        asm("v_bfi_b32 %0, %1, %2, %3" : "=v"(a[t][h]) : "v"(h ? cm23 : cm01), "v"(uc), "v"(up));
                    } else if (st == 1) { x[t][2 * h] = bf_lo(a[t][h]) * ATT_SCALE; x[t][2 * h + 1] = bf_hi(a[t][h]) * ATT_SCALE; }
                    else if (st == 2) a[t][h] = pack_bf16(x[t][2 * h], x[t][2 * h + 1]);
                    else { x[t][2 * h] = bf_lo(a[t][h]); x[t][2 * h + 1] = bf_hi(a[t][h]); }
                }
            SBAR();
        };
        logits(1, 0); logits(1, 1);
        sp[0] = MFMA16(kp[0], bf8(qfr[0]), mfirst); sp[2] = MFMA16(kp[1], bf8(qfr[2]), mfirst);
        SBAR();
        logits(1, 2); logits(1, 3);
        logits(0, 0); logits(0, 1); logits(0, 2); logits(0, 3);
        auto across = [&](float (&v)[4], auto op, auto fin) {
            const auto p = __builtin_amdgcn_permlane16_swap(__float_as_uint(v[0]), __float_as_uint(v[1]), false, false);
            const auto q = __builtin_amdgcn_permlane16_swap(__float_as_uint(v[2]), __float_as_uint(v[3]), false, false);
            const auto r = __builtin_amdgcn_permlane32_swap(__float_as_uint(op(__uint_as_float(p[0]), __uint_as_float(p[1]))),
                                                            __float_as_uint(op(__uint_as_float(q[0]), __uint_as_float(q[1]))), false, false);
            const unsigned z = __float_as_uint(fin(op(__uint_as_float(r[0]), __uint_as_float(r[1]))));
            const auto u = __builtin_amdgcn_permlane32_swap(z, z, false, false);
            const auto e = __builtin_amdgcn_permlane16_swap(u[0], u[0], false, false), f = __builtin_amdgcn_permlane16_swap(u[1], u[1], false, false);
            v[0] = __uint_as_float(e[0]); v[1] = __uint_as_float(e[1]); v[2] = __uint_as_float(f[0]); v[3] = __uint_as_float(f[1]);
        };
#pragma unroll
        for (int t = 0; t < 4; ++t) m[t] = amax3(x[t][0], x[t][1], x[t][2]);
        asm("v_max_f32 %0, %0, %4\n\tv_max_f32 %1, %1, %5\n\tv_max_f32 %2, %2, %6\n\tv_max_f32 %3, %3, %7\n\ts_nop 0"
            : "+v"(m[0]), "+v"(m[1]), "+v"(m[2]), "+v"(m[3]) : "v"(x[0][3]), "v"(x[1][3]), "v"(x[2][3]), "v"(x[3][3]));
        across(m, [](float p, float q) { float r; asm("v_max_f32 %0, %1, %2\n\ts_nop 0" : "=v"(r) : "v"(p), "v"(q)); return r; },
               [](float p) { return p * -LOG2E; });
        SBAR();
#pragma unroll
        for (int t = 0; t < 4; ++t)
#pragma unroll
            for (int r = 0; r < 4; ++r) x[t][r] = __builtin_fmaf(x[t][r], LOG2E, m[t]);
        SBAR();
#pragma unroll
        for (int t = 0; t < 4; ++t)
#pragma unroll
            for (int r = 0; r < 4; ++r) x[t][r] = __builtin_amdgcn_exp2f(x[t][r]);
        SBAR();
#pragma unroll
        for (int t = 0; t < 4; ++t) m[t] = (x[t][0] + x[t][1]) + (x[t][2] + x[t][3]);
        asm volatile("s_waitcnt lgkmcnt(0)" : "+v"(vp[0][0]), "+v"(vp[0][1]), "+v"(vp[1][0]), "+v"(vp[1][1]) :: "memory");
        u32x4 ve[2][2];
#pragma unroll
        for (int u = 0; u < 2; ++u)
#pragma unroll
            for (int f = 0; f < 2; ++f) ve[u][f] = u32x4{vp[u][f][0], vp[u][f][1], va[u][f][0], va[u][f][1]};
        across(m, [](float p, float q) { return p + q; }, [](float p) { return __builtin_amdgcn_rcpf(p); });
        asm volatile("" : "+v"(ve[0][0]), "+v"(ve[0][1]), "+v"(ve[1][0]), "+v"(ve[1][1]));
        SBAR();
#pragma unroll
        for (int t = 0; t < 4; ++t)
#pragma unroll
            for (int r = 0; r < 4; ++r) x[t][r] *= m[t];
        SBAR();
#pragma unroll
        for (int t = 0; t < 4; ++t)
#pragma unroll
            for (int h = 0; h < 2; ++h) a[t][h] = pack_bf16(x[t][2 * h], x[t][2 * h + 1]);
        SBAR();
        f32x4 o0[4], o1[4];
#pragma unroll
        for (int t = 0; t < 4; ++t) {
            const bf16x8 pb = bf8(u32x4{a[t][0] & ~cm01, a[t][1] & ~cm23, a[t][0] & cm01, a[t][1] & cm23});
            o0[t] = MFMA16(bf8((t & 1) ? va[t >> 1][0] : ve[t >> 1][0]), pb, zero4);
            o1[t] = MFMA16(bf8((t & 1) ? va[t >> 1][1] : ve[t >> 1][1]), pb, zero4);
        }
        SBAR();
#pragma unroll
        for (int t = 0; t < 4; ++t)
            st16(sb[t], qo, u32x4{pack_bf16(o0[t][0], o0[t][1]), pack_bf16(o0[t][2], o0[t][3]),
                                                 pack_bf16(o1[t][0], o1[t][1]), pack_bf16(o1[t][2], o1[t][3])});
    }
}

DI int out_row(int panel, bool localrows, int T, int a) {
    return localrows ? (panel * 128 + (T >> 2) * 64 + 2 * (16 * (T & 1) + a) + ((T >> 1) & 1)) : (panel * 128 + T * 16 + a);
}
DI int out_swz(int k) { return ((k >> 1) & 1) | (k & 4); }
DI unsigned wo_off(int w, int lane) {
    const int kl = lane >> 3;
    unsigned v = (unsigned)(w * 8 + kl) * 2048u + (unsigned)(ids().co * 128 + ((lane & 7) ^ out_swz(kl)) * 16);
    asm volatile("" : "+v"(v));
    return v;
}
DI void wo_load(const void* Wo, unsigned voff, int i0, int i1, unsigned lds0, int w) {
#pragma unroll
    for (int i = i0; i < i1; ++i) glds16((const char*)Wo + (size_t)i * 65536, voff, lds0 + (w + 4 * i) * 1024);
}
DI unsigned res_off(bool localrows, bool tiled, int lane) {
    const Ids I = ids();
    unsigned v = (unsigned)(I.po * 262144 + I.co * (tiled ? 16384 : 128) + out_row(0, localrows, 0, lane >> 3) * (tiled ? 128 : 2048) +
                            (((lane & 7) ^ (lane >> 3)) * 16));
    asm volatile("" : "+v"(v));
    return v;
}
DI void res_load(u32x4 (&res)[6], const void* Xin, bool localrows, bool tiled, unsigned off, int w) {
#pragma unroll
    for (int i = 0; i < 6; ++i) {
        const int j = min(w + 3 * i, 15);
        asm volatile("global_load_dwordx4 %0, %1, %2 sc1" : "=&v"(res[i]) : "v"(off), "s"((const char*)Xin + out_row(0, localrows, j >> 1, (j & 1) * 8) * (tiled ? 128 : 2048)) : "memory");
    }
}
DI void out_handoff(char* smem, int xoff, u32x4 (&res)[6], int w, char* ws, int set, u64 tag, int lane, u32x4 (&c)[8], int mat) {
    const Ids I = ids();
    u64 land;
    lds_ptr dst = (lds_ptr)smem + xoff + w * 1024 + lane * 16;
    asm volatile("" : "+v"(dst));
    if (w == 3) __builtin_amdgcn_s_setprio(3);
    signal_poll<false, false>(w - 3, ws, flag_off(set, I.pq, lane & 15, I.cq), flag_off(set, I.po, I.co, lane & 15), 0xffffull, tag, land);
    if (w < 3) {
        asm volatile("s_waitcnt vmcnt(0)" : "+v"(res[0]), "+v"(res[1]), "+v"(res[2]), "+v"(res[3]), "+v"(res[4]), "+v"(res[5]) :: "memory");
#pragma unroll
        for (int i = 0; i < 5; ++i) *(__attribute__((address_space(3))) u32x4*)(dst + i * 3072) = res[i];
        if (w == 0) *(__attribute__((address_space(3))) u32x4*)(dst + 15360) = res[5];
        if (mat >= 0) colsum_store(c, ws, mat, w, lane);
    }
    WAIT_LGKM_BAR();
    asm volatile("s_waitcnt vmcnt(0)" : "+v"(land) :: "memory");
    if (w < 3 && mat >= 0) flag_store(ws, (mat ? 10 : 13) + w, I.cq, I.pq, tag, lane);
    __builtin_amdgcn_s_setprio(0);
}

template <bool AFTER_LOCAL>
DI void out_compute(char* smem, int w, const __hip_bfloat16* __restrict__ ofrag,
                    __hip_bfloat16* __restrict__ Xout, float* __restrict__ statp SP_PARAM) {
    constexpr int XOFF = AFTER_LOCAL ? 147456 : 98304;
    STAMP(AFTER_LOCAL ? 3 : 10);
    VM_BAR(0);
    const int lane = lane_id();
    asm volatile("" : "+s"(w));
    const Ids I = ids();
    const int panel = I.po, ct = I.co;
    const int l15 = lane & 15, g = lane >> 4;
    const bool tiled = AFTER_LOCAL || statp != nullptr;
    const int rstr = tiled ? 128 : 2048;
    const int q4 = l15 >> 2, p4 = l15 & 3;
    const int krow = 4 * g + q4;
    const int sw = out_swz(krow);
    const lds_cptr sm = (lds_cptr)smem;
    lds_cptr bw[4], xo[4];
#pragma unroll
    for (int t = 0; t < 4; ++t) {
        bw[t] = sm + krow * 128 + ((((t >> 1) * 4 + (p4 >> 1) * 2 + (t & 1)) ^ sw) * 16) + (p4 & 1) * 8;
        xo[t] = sm + XOFF + (w * 32 + l15) * 128 + ((((t >> 1) * 4 + (g >> 1) * 2 + (t & 1)) ^ (l15 & 7)) * 16) + (g & 1) * 8;
    }
    unsigned so[2], sto, ao[2];
#pragma unroll
    for (int r2 = 0; r2 < 2; ++r2) {
        so[r2] = (unsigned)(panel * 262144 + ct * (tiled ? 16384 : 128) + out_row(0, AFTER_LOCAL, 2 * w + r2, l15 & 7) * rstr + (l15 >> 3) * 64 + g * 16);
        ao[r2] = (unsigned)((panel * 8 + 2 * w + r2) * 32768 + lane * 16);
    }
    sto = (unsigned)(((panel * 16 + ct) * 128 + 2 * w * 16 + l15) * 8);
    asm volatile("" : "+v"(bw[0]), "+v"(bw[1]), "+v"(bw[2]), "+v"(bw[3]), "+v"(xo[0]), "+v"(xo[1]), "+v"(xo[2]), "+v"(xo[3]),
                 "+v"(so[0]), "+v"(so[1]), "+v"(sto), "+v"(ao[0]), "+v"(ao[1]));
    bf16x8 ones;
    {
        unsigned o1; asm volatile("v_mov_b32 %0, 0x3f803f80" : "=v"(o1));
        ones = __builtin_bit_cast(bf16x8, (u32x4{o1, o1, o1, o1}));
        asm volatile("" : "+v"(ones));
    }
    const __amdgpu_buffer_rsrc_t ra = __builtin_amdgcn_make_buffer_rsrc((void*)ofrag, 0, 4 << 20, 0x00020000);
    asm volatile("" :: "s"(ra));
    u32x32 fr;
    BAR();
    STAMP(AFTER_LOCAL ? 4 : 11);
    u32x32 aw0, aw1, aw2, fr1;
    asm volatile(".irp j,0,1,2,3\n\tbuffer_load_dwordx4 v[96+4*\\j:99+4*\\j], %2, %4, 0 offen offset:1024*\\j sc1\n\t.endr\n\t"
                 ".irp j,0,1,2,3\n\tbuffer_load_dwordx4 v[112+4*\\j:115+4*\\j], %3, %4, 0 offen offset:1024*\\j sc1\n\t.endr\n\t"
                 ".irp j,0,1,2,3\n\tbuffer_load_dwordx4 v[128+4*\\j:131+4*\\j], %2, %4, %5 offen offset:1024*\\j sc1\n\t.endr"
                 : "=&{v[96:127]}"(aw0), "=&{v[128:159]}"(aw1) : "v"(ao[0]), "v"(ao[1]), "s"(ra), "s"(4096) : "memory");
    f32x16 acc[2];
#pragma unroll
    for (int r2 = 0; r2 < 2; ++r2)
#pragma unroll
        for (int c = 0; c < 4; ++c) {
            const u32x2 xr = *(const __attribute__((address_space(3))) u32x2*)(xo[c] + r2 * 2048);
            acc[r2][4 * c] = bf_lo(xr[0]); acc[r2][4 * c + 1] = bf_hi(xr[0]); acc[r2][4 * c + 2] = bf_lo(xr[1]); acc[r2][4 * c + 3] = bf_hi(xr[1]);
        }
    asm volatile(
        ".macro OUT_MM r, t, j, s\n\t"
        "v_mfma_f32_16x16x32_bf16 v[16*\\r+4*\\t:16*\\r+4*\\t+3], v[32+16*\\t+4*\\j:35+16*\\t+4*\\j], v[96+32*\\s+16*\\r+4*\\j:99+32*\\s+16*\\r+4*\\j], v[16*\\r+4*\\t:16*\\r+4*\\t+3]\n\t"
        ".endm\n\t"
        ".macro OUT_RD t, i, g\n\t"
        "ds_read_b64_tr_b16 v[32+16*\\t+2*(\\i):33+16*\\t+2*(\\i)], v[192+\\t] offset:(\\g)*16384+(\\i)*2048\n\t"
        ".endm\n\t"
        ".macro OUT_LD i, s\n\t"
        ".if (\\i) < 4\n\tbuffer_load_dwordx4 v[96+32*(\\s)+4*(\\i):99+32*(\\s)+4*(\\i)], %[a0], %[ra], s96 offen offset:1024*(\\i) sc1\n\t"
        ".else\n\tbuffer_load_dwordx4 v[96+32*(\\s)+4*(\\i):99+32*(\\s)+4*(\\i)], %[a1], %[ra], s96 offen offset:1024*((\\i)-4) sc1\n\t.endif\n\t"
        ".endm\n\t"
        ".macro OUT_CH r, t, s, rt, g\n\t"
        ".irp j,0,1,2,3\n\tOUT_MM \\r, \\t, \\j, \\s\n\tOUT_RD \\rt, 4*\\r+\\j, \\g\n\t.endr\n\t"
        ".endm\n\t"
        ".macro OUT_GRP s, g, gn, ld, up, v0, v1\n\t"
        "OUT_CH 0, 0, \\s, 3, \\g\n\ts_waitcnt vmcnt(\\v0)\n\t.if \\ld\n\tOUT_LD 0, (\\s+2)%%3\n\t.endif\n\t"
        "OUT_CH 1, 0, \\s, 3, \\g\n\t.if \\up\n\t.irp t,0,1,2,3\n\tv_add_u32 v[192+\\t], s97, v[192+\\t]\n\t.endr\n\t.endif\n\t.if \\ld\n\tOUT_LD 1, (\\s+2)%%3\n\t.endif\n\t"
        "OUT_CH 0, 1, \\s, 0, \\gn\n\t.if \\ld\n\tOUT_LD 2, (\\s+2)%%3\n\t.endif\n\t"
        "OUT_CH 1, 1, \\s, 0, \\gn\n\t.if \\ld\n\tOUT_LD 3, (\\s+2)%%3\n\t.endif\n\ts_waitcnt lgkmcnt(8)\n\t"
        "OUT_CH 0, 2, \\s, 1, \\gn\n\t.if \\ld\n\tOUT_LD 4, (\\s+2)%%3\n\t.endif\n\t"
        "OUT_CH 1, 2, \\s, 1, \\gn\n\t.if \\ld\n\tOUT_LD 5, (\\s+2)%%3\n\t.endif\n\t"
        "OUT_CH 0, 3, \\s, 2, \\gn\n\t.if \\ld\n\tOUT_LD 6, (\\s+2)%%3\n\t.endif\n\t"
        "OUT_CH 1, 3, \\s, 2, \\gn\n\t.if \\ld\n\tOUT_LD 7, (\\s+2)%%3\n\ts_addk_i32 s96, 0x1000\n\t.endif\n\t"
        "s_waitcnt vmcnt(\\v1) lgkmcnt(8)\n\t"
        ".endm\n\t"
        ".macro OUT_PACK y, c\n\t"
        ".irp i,0,1,2,3,4,5,6,7\n\tv_cvt_pk_bf16_f32 v[\\y+\\i], v[\\c+2*\\i], v[\\c+2*\\i+1]\n\t.endr\n\t"
        ".irp i,0,1,4,5\n\tv_permlane16_swap_b32 v[\\y+\\i], v[\\y+\\i+2]\n\t.endr\n\t"
        ".irp i,0,1,2,3\n\tv_mov_b32 v[\\y+8+\\i], v[\\y+\\i]\n\t.endr\n\t"
        "v_mfma_f32_16x16x32_bf16 v[\\y+12:\\y+15], v[\\y:\\y+3], v[\\y:\\y+3], 0\n\t"
        "v_mov_b32_dpp v[\\y+8], v[\\y+4] row_ror:8 row_mask:0xf bank_mask:0xc\n\tv_mov_b32_dpp v[\\y+9], v[\\y+5] row_ror:8 row_mask:0xf bank_mask:0xc\n\t"
        "v_mfma_f32_16x16x32_bf16 v[\\y+16:\\y+19], %[one], v[\\y:\\y+3], 0\n\t"
        "v_mov_b32_dpp v[\\y+10], v[\\y+6] row_ror:8 row_mask:0xf bank_mask:0xc\n\tv_mov_b32_dpp v[\\y+11], v[\\y+7] row_ror:8 row_mask:0xf bank_mask:0xc\n\t"
        "v_mfma_f32_16x16x32_bf16 v[\\y+12:\\y+15], v[\\y+4:\\y+7], v[\\y+4:\\y+7], v[\\y+12:\\y+15]\n\t"
        ".endm\n\t"
        ".macro OUT_REST y\n\t"
        "v_mfma_f32_16x16x32_bf16 v[\\y+16:\\y+19], %[one], v[\\y+4:\\y+7], v[\\y+16:\\y+19]\n\t"
        ".irp i,0,1,2,3\n\tv_mov_b32_dpp v[\\y+4+\\i], v[\\y+\\i] row_ror:8 row_mask:0xf bank_mask:0x3\n\t.endr\n\t"
        ".endm\n\t"
        ".macro OUT_DIAG y\n\t"
        "v_cndmask_b32 v[\\y+12], v[\\y+12], v[\\y+13], %[m0]\n\tv_cndmask_b32 v[\\y+14], v[\\y+14], v[\\y+15], %[m0]\n\t"
        "v_cndmask_b32 v[\\y+17], v[\\y+12], v[\\y+14], %[m1]\n\t"
        ".endm\n\t"
        "s_movk_i32 s96, 0x1000\n\t.irp i,4,5,6,7\n\tOUT_LD \\i, 1\n\t.endr\n\t"
        ".irp t,0,1,2\n\t.irp i,0,1,2,3,4,5,6,7\n\tOUT_RD \\t, \\i, 0\n\t.endr\n\ts_waitcnt lgkmcnt(4)\n\t.endr\n\t"
        "s_movk_i32 s96, 0x2000\n\ts_mov_b32 s97, 0xc000\n\t"
        "s_waitcnt vmcnt(12) lgkmcnt(0)\n"
        ".Lout_loop_%=:\n\t"
        "OUT_GRP 0, 0, 1, 1, 0, 8, 12\n\tOUT_GRP 1, 1, 2, 1, 0, 8, 12\n\tOUT_GRP 2, 2, 0, 1, 1, 8, 12\n\t"
        "s_mov_b32 s97, %[last]\n\t"
        "s_cmpk_lg_u32 s96, 0x8000\n\ts_cbranch_scc1 .Lout_loop_%=\n\t"
        "OUT_GRP 0, 0, 1, 0, 0, 8, 4\n\t"
        ".irp j,0,1,2,3\n\tOUT_MM 0, 0, \\j, 1\n\tOUT_RD 3, \\j, 1\n\t.endr\n\t.irp j,0,1,2,3\n\tOUT_MM 0, 1, \\j, 1\n\tOUT_RD 3, 4+\\j, 1\n\t.endr\n\t"
        "s_waitcnt lgkmcnt(8)\n\t.irp j,0,1,2,3\n\tOUT_MM 0, 2, \\j, 1\n\t.endr\n\t"
        "s_waitcnt lgkmcnt(0)\n\t.irp j,0,1,2,3\n\tOUT_MM 0, 3, \\j, 1\n\t.endr\n\t"
        "s_nop 1\n\t"
        "OUT_PACK 96, 0\n\tglobal_store_dwordx4 %[s0], v[104:107], %[xa] sc1\n\tOUT_REST 96\n\tglobal_store_dwordx4 %[s0], v[100:103], %[xb] sc1\n\t"
        "s_waitcnt vmcnt(2)\n\t.irp t,0,1,2,3\n\t.irp j,0,1,2,3\n\tOUT_MM 1, \\t, \\j, 1\n\t.endr\n\t.endr\n\t"
        "s_nop 1\n\t"
        "OUT_PACK 160, 16\n\tglobal_store_dwordx4 %[s1], v[168:171], %[xa] sc1\n\tOUT_REST 160\n\tglobal_store_dwordx4 %[s1], v[164:167], %[xb] sc1\n\t"
        "s_cmp_eq_u64 %[st], 0\n\ts_cbranch_scc1 .Lout_end_%=\n\t"
        "OUT_DIAG 96\n\tOUT_DIAG 160\n\t"
        "s_mov_b64 exec, %[md]\n\t"
        "global_store_dwordx2 %[so], v[112:113], %[st] sc1\n\tglobal_store_dwordx2 %[so], v[176:177], %[st] offset:128 sc1\n\t"
        "s_mov_b64 exec, -1\n"
        ".Lout_end_%=:\n\t"
        "s_nop 1\n\t"
        ".purgem OUT_MM\n\t.purgem OUT_RD\n\t.purgem OUT_LD\n\t.purgem OUT_CH\n\t.purgem OUT_GRP\n\t.purgem OUT_PACK\n\t.purgem OUT_REST\n\t.purgem OUT_DIAG"
        : "+{v[0:15]}"(acc[0]), "+{v[16:31]}"(acc[1]), "+{v[96:127]}"(aw0), "+{v[128:159]}"(aw1), "=&{v[160:191]}"(aw2),
          "+{v192}"(bw[0]), "+{v193}"(bw[1]), "+{v194}"(bw[2]), "+{v195}"(bw[3]), "=&{v[32:63]}"(fr), "=&{v[64:95]}"(fr1)
        : [a0] "v"(ao[0]), [a1] "v"(ao[1]), [ra] "s"(ra), [last] "s"(AFTER_LOCAL ? 49152 : 81920), [one] "v"(ones), [s0] "v"(so[0]), [s1] "v"(so[1]),
          [xa] "s"(Xout), [xb] "s"((char*)Xout + (AFTER_LOCAL ? 16 : 8) * rstr), [st] "s"(statp), [so] "v"(sto),
          [m0] "s"(0xaaaaaaaaaaaaaaaaull), [m1] "s"(0xccccccccccccccccull), [md] "s"(0xf0000f0000f0000full)
        : "memory", "scc", "s96", "s97");
    STAMP(AFTER_LOCAL ? 5 : 12);
}

#define ROWSUM(p0, p1, c) __builtin_amdgcn_smfmac_f32_16x16x64_bf16(sel, __builtin_bit_cast(bf16v_t, __builtin_shufflevector(p0, p1, 0, 1, 2, 3, 4, 5, 6, 7)), c, sidx, 0, 0)
constexpr int ATT_STAGE = 32768;
typedef __attribute__((ext_vector_type(8))) unsigned u32x8;

DI void attn_loader(char* smem, int w, char* ws, int set, u64 tag, const __hip_bfloat16* __restrict__ kvf, const void* Wo, const void* Xin,
                    const void* Wcs SP_PARAM) {
    const int lane = lane_id();
    asm volatile("" : "+s"(w));
    const Ids I = ids();
    const int panel = I.pq, ct = I.cq;
    const unsigned lds0 = (unsigned)(uintptr_t)smem;
    const char* kvh = (const char*)kvf + (size_t)(ct * 2 + (w >> 1)) * 262144 + (w & 1) * 4096;
    const unsigned voff = (unsigned)(lane * 16);
    auto order = [&](int st) { return ((panel & 8) + st - ((st - 1 < (panel & 7)) ? 1 : 0)) & 15; };
    auto load = [&](int st) {
        const char* src = kvh + (size_t)order(st) * 16384;
        const unsigned dst = lds0 + (w >> 1) * 16384 + (w & 1) * 4096 + (st & 3) * ATT_STAGE;
#pragma unroll
        for (int u = 0; u < 2; ++u)
            asm volatile("s_mov_b32 m0, %0\n\ts_nop 0\n\tglobal_load_lds_dwordx4 %1, %2 sc1\n\tglobal_load_lds_dwordx4 %1, %2 offset:1024 sc1\n\t"
                         "global_load_lds_dwordx4 %1, %2 offset:2048 sc1\n\tglobal_load_lds_dwordx4 %1, %2 offset:3072 sc1"
                         :: "s"(dst + u * 8192), "v"(voff), "s"(src + u * 8192) : "memory");
    };
    const unsigned wv = wo_off(w, lane), rv = res_off(false, true, lane), cv = colsum_off(w, lane);
    const unsigned poff = flag_off(set, ct, panel, lane & 15);
    u64 land;
    __builtin_amdgcn_s_setprio(3);
    BAR();
    signal_poll<false, true>(w, ws, flag_off(set, ct, lane & 15, panel), poff, 1ull << order(1), tag, land);
    STAMP(9);
    load(1);
    asm volatile("global_load_dwordx2 %0, %1, %2 sc1\n\ts_waitcnt vmcnt(0)" : "+v"(land) : "v"(poff), "s"(ws + WS_FLAG) : "memory");
    STAMP(10);
    BAR();
    if (!__all(land == tag)) flag_poll(ws, poff, tag);
    load(2); load(3);
    __builtin_amdgcn_s_setprio(0);
    STAMP(11);
    VM_BAR(8); load(4);
    VM_BAR(0); load(5); load(6);
    VM_BAR(0); load(7); load(8);
    u32x4 res[6], ca[8];
    colsum_load(ca, w < 3 ? Wcs : nullptr, cv);
#pragma unroll 1
    for (int st = 6; st < 12; st += 2) {
        VM_BAR(0);
        load(st + 3); load(st + 4);
        if (st == 8) wo_load(Wo, wv, 24, 32, lds0 + 32768, w);
    }
    VM_BAR(0); load(15); wo_load(Wo, wv, 0, 8, lds0, w);
    res_load(res, Xin, false, true, rv, w);
    VM_BAR(0); wo_load(Wo, wv, 8, 24, lds0, w);
    STAMP(12);
    out_handoff(smem, 98304, res, w, ws, set + 1, tag, lane, ca, Wcs ? 0 : -1);
}

DI void attn_compute(char* smem, int w, const u32x4 (&q4)[4], const __hip_bfloat16* __restrict__ kvf, __hip_bfloat16* __restrict__ ofrag SP_PARAM) {
    const int lane = lane_id();
    asm volatile("" : "+s"(w));
    const int wm = w & 1, wn = w >> 1;
    const Ids I = ids();
    const int panel = I.pq, head = I.cq * 2 + wn;
    const int l15 = lane & 15;
    const unsigned se = (l15 & 7) == 0 ? 0x3f803f80u : 0u, so = (l15 & 7) == 4 ? 0x3f803f80u : 0u;
    const u32x4 sel1 = {se, se, so, so};
    const lds_cptr sm = (lds_cptr)smem + wn * 16384 + lane * 16;
    const lds_cptr smk = (lds_cptr)smem + wn * 16384 + ((lane & 0x33) | ((lane & 4) << 1) | ((lane & 8) >> 1)) * 16;
    const f32x16 z16 = {0, 0, 0, 0, 0, 0, 0, 0, 0, 0, 0, 0, 0, 0, 0, 0};
    const bf16x8 qA0 = __builtin_bit_cast(bf16x8, q4[0]), qA1 = __builtin_bit_cast(bf16x8, q4[1]);
    const bf16x8 qB0 = __builtin_bit_cast(bf16x8, q4[2]), qB1 = __builtin_bit_cast(bf16x8, q4[3]);
    f32x16 oA, oB, nmA, nmB, sA, sB;
    f32x4 lsA, lsB;
    u32x8 pB;
    bf16x8 v11;
    asm volatile(".irp r,106,108,110,112,134,136,138,140,170,172\n\tv_mov_b64 v[\\r:\\r+1], 0\n\t.endr"
                 : "={v[106:113]}"(pB), "={v[134:137]}"(lsA), "={v[138:141]}"(lsB), "={v[170:173]}"(v11));
    WAIT_LGKM_BAR();
    STAMP(8);
    bf16x8 k00 = lds_b128(smk), k10 = lds_b128(smk + 1024), k01 = lds_b128(smk + 4096), k11 = lds_b128(smk + 4096 + 1024);
    bf16x8 v00 = lds_b128(sm + 2048), v10 = lds_b128(sm + 3072), v01 = lds_b128(sm + 4096 + 2048);
    float ob;
    {
        const u32x4 vu = __builtin_bit_cast(u32x4, v00);
        const unsigned vm = max(max(vu[0] & 0x7fff0000u, vu[1] & 0x7fff0000u), max(vu[2] & 0x7fff0000u, vu[3] & 0x7fff0000u));
        ob = __uint_as_float(wave_min(vm)) * 0.015625f;
        sA = MFMA32(k00, qA0, z16); sB = MFMA32(k00, qB0, z16);
        sA = MFMA32(k10, qA1, sA); sB = MFMA32(k10, qB1, sB);
        SBAR(); VM_BAR(0); SBAR();
        float mA = sA[0], mB = sB[0];
#pragma unroll
        for (int r = 1; r < 16; ++r) { mA = fmaxf(mA, sA[r]); mB = fmaxf(mB, sB[r]); }
        mA = max_h(mA) + 16.f; mB = max_h(mB) + 16.f;
#pragma unroll
        for (int r = 0; r < 16; ++r) { nmA[r] = -mA; nmB[r] = -mB; sA[r] -= mA; oA[r] = ob; oB[r] = ob; }
    }
    const unsigned a0 = (unsigned)(uintptr_t)sm, a1 = (unsigned)(uintptr_t)smk;
    unsigned dst = (unsigned)((((panel * 8 + wm * 4 + ((lane & 31) >> 4)) * 32 + head) * 64 + (lane & 15) + (lane >> 5) * 16) * 16);
    asm volatile("" : "+v"(dst));
    asm volatile(
        ".macro ATT_RD dst, y\n\t"
        ".if (\\dst) < 158\n\tds_read_b128 v[\\dst:\\dst+3], v[177+(((\\y)&0x1ffff)>>16)] offset:(\\y)&0xffff\n\t"
        ".else\n\tds_read_b128 v[\\dst:\\dst+3], v[175+(((\\y)&0x1ffff)>>16)] offset:(\\y)&0xffff\n\t.endif\n\t"
        ".endm\n\t"
        ".macro ATT_E d, c, n\n\t"
        "v_exp_f32 v[\\d], v[\\c]\n\t.if \\n > 1\n\tv_exp_f32 v[\\d+1], v[\\c+1]\n\t.endif\n\t.if \\n > 2\n\tv_exp_f32 v[\\d+2], v[\\c+2]\n\t.endif\n\t"
        ".endm\n\t"
        ".macro ATT_STEP cur, nxt, ka, kb, q, nm, oc, od, lsc, lsd, pc, pd, vd1, vc0, r0, y0, r1, y1, bar, rd\n\t"
        "v_mfma_f32_32x32x16_bf16 v[\\nxt:\\nxt+15], v[\\ka:\\ka+3], v[\\q:\\q+3], v[\\nm:\\nm+15]\n\t"
        "ATT_E \\cur, \\cur, 3\n\t"
        "v_mfma_f32_32x32x16_bf16 v[\\nxt:\\nxt+15], v[\\kb:\\kb+3], v[\\q+4:\\q+7], v[\\nxt:\\nxt+15]\n\t"
        "ATT_E \\cur+3, \\cur+3, 3\n\t"
        "v_smfmac_f32_32x32x32_bf16 v[\\od:\\od+15], v[\\vd1:\\vd1+3], v[\\pd:\\pd+7], v174\n\t"
        "s_waitcnt lgkmcnt(0)\n\t"
        ".if \\bar & 2\n\ts_barrier\n\t.endif\n\t"
        "ATT_E \\cur+6, \\cur+6, 2\n\tATT_E \\pc, \\cur+8, 1\n\t"
        "v_smfmac_f32_16x16x64_bf16 v[\\lsd:\\lsd+3], v[130:133], v[\\pd:\\pd+7], v174\n\t"
        ".if \\rd & 8\n\tATT_RD \\r0-8, \\y0-1024\n\t.endif\n\t"
        ".if \\rd & 1\n\tATT_RD \\r0, \\y0\n\t.endif\n\t"
        "ATT_E \\pc+1, \\cur+9, 2\n\t"
        "v_smfmac_f32_32x32x32_bf16 v[\\oc:\\oc+15], v[\\vc0:\\vc0+3], v[\\cur:\\cur+7], v174\n\t"
        ".if \\bar & 1\n\ts_barrier\n\t.endif\n\t"
        "ATT_E \\pc+3, \\cur+11, 3\n\t"
        "v_smfmac_f32_16x16x64_bf16 v[\\lsc:\\lsc+3], v[130:133], v[\\cur:\\cur+7], v174\n\t"
        ".if \\rd & 4\n\tATT_RD 142, 32768\n\tATT_RD 150, 33792\n\tATT_RD 158, 34816\n\tATT_RD \\r0, \\y0\n\t.endif\n\t"
        ".if \\rd & 2\n\tATT_RD \\r1, \\y1\n\t.endif\n\t"
        "ATT_E \\pc+6, \\cur+14, 2\n\t"
        ".if \\rd & 4\n\ts_waitcnt lgkmcnt(3)\n\t.endif\n\t"
        ".endm\n\t"
        ".macro ATT_OUT o, l, y, base\n\t"
        "v_rcp_f32 v34, v[\\l]\n\ts_nop 0\n\tv_mul_f32 v35, -%[ob], v34\n\ts_nop 0\n\t"
        ".irp i,0,2,8,10,4,6,12,14\n\tv_pk_fma_f32 v[36+\\i:37+\\i], v[\\o+\\i:\\o+1+\\i], v[34:35], v[34:35] op_sel:[0,0,1] op_sel_hi:[1,0,1]\n\t.endr\n\t"
        "v_cvt_pk_bf16_f32 v[\\y], v36, v37\n\tv_cvt_pk_bf16_f32 v[\\y+1], v38, v39\n\tv_cvt_pk_bf16_f32 v[\\y+2], v44, v45\n\tv_cvt_pk_bf16_f32 v[\\y+3], v46, v47\n\t"
        "global_store_dwordx4 %[dst], v[\\y:\\y+3], \\base sc1\n\t"
        "v_cvt_pk_bf16_f32 v[\\y+4], v40, v41\n\tv_cvt_pk_bf16_f32 v[\\y+5], v42, v43\n\tv_cvt_pk_bf16_f32 v[\\y+6], v48, v49\n\tv_cvt_pk_bf16_f32 v[\\y+7], v50, v51\n\t"
        "global_store_dwordx4 %[dst], v[\\y+4:\\y+7], \\base offset:512 sc1\n\t"
        ".endm\n\t"
        ".macro ATT_TILE p, y1, y2, bar, ra=3, rb=3\n\t"
        "ATT_STEP 66, 82, 142+\\p, 150+\\p, 122, 50, 2, 18, 134, 138, 98, 106, 170-\\p, 158+\\p, 170-\\p, \\y1+3072, 142+\\p, \\y2, \\bar&1, \\ra\n\t"
        "ATT_STEP 82, 66, 146-\\p, 154-\\p, 114, 34, 18, 2, 138, 134, 106, 98, 166+\\p, 158+\\p, 150+\\p, \\y2+1024, 158+\\p, \\y2+2048, \\bar&2, \\rb\n\t"
        ".endm\n\t"
        ".macro ATT_STAGE b, bar\n\t"
        "ATT_TILE 0, \\b+4096, \\b+8192, 0\n\t"
        "ATT_TILE 4, \\b+8192, \\b+12288, 0\n\t"
        ".if \\bar\n\tATT_TILE 0, \\b+12288, \\b+32768, 2, 1, 11\n\t.else\n\tATT_TILE 0, \\b+12288, \\b+32768, 0\n\t.endif\n\t"
        "ATT_TILE 4, \\b+32768, \\b+36864, 0\n\t"
        ".endm\n\t"
        "v_add_u32 v176, 0x10000, v175\n\tv_add_u32 v178, 0x10000, v177\n\tv_mov_b32 v174, 0xdddddddd\n\t"
        "s_mov_b32 s96, 4\n\t"
        "s_waitcnt lgkmcnt(0)\n\t"
        "ATT_TILE 0, 4096, 8192, 0\n\tATT_TILE 4, 8192, 12288, 0\n\tATT_TILE 0, 12288, 0, 0, 1, 0\n\tATT_TILE 4, 32768, 36864, 1, 6, 3\n\t"
        "ATT_STAGE 32768, 1\n\t"
        "s_branch .Latt_entry_%=\n\t"
        ".p2align 6\n"
        ".Latt_loop_%=:\n\t"
        "ATT_STAGE 0, 1\n\tATT_STAGE 32768, 0\n"
        ".Latt_entry_%=:\n\t"
        "ATT_STAGE 65536, 1\n\t"
        "s_sub_u32 s96, s96, 1\n\t"
        "ATT_TILE 0, 102400, 106496, 0\n\t"
        "s_cmp_eq_u32 s96, 0\n\t"
        "ATT_TILE 4, 106496, 110592, 0\n\t"
        "s_cbranch_scc1 .Latt_tail_%=\n\t"
        "ATT_TILE 0, 110592, 131072, 0\n\tATT_TILE 4, 131072, 135168, 0\n\t"
        "s_branch .Latt_loop_%=\n"
        ".Latt_tail_%=:\n\t"
        "s_waitcnt lgkmcnt(0)\n\t"
        "ATT_STEP 66, 82, 146, 154, 114, 34, 2, 18, 134, 138, 98, 106, 170, 158, 170, 113664, 0, 0, 0, 1\n\t"
        "ATT_STEP 82, 66, 142, 150, 122, 50, 2, 2, 134, 134, 106, 98, 166, 162, 0, 0, 0, 0, 0, 0\n\t"
        "ATT_STEP 66, 82, 146, 154, 122, 50, 18, 2, 138, 134, 98, 106, 170, 158, 0, 0, 0, 0, 0, 0\n\t"
        "ATT_OUT 2, 134, 52, %[of]\n\t"
        "v_smfmac_f32_32x32x32_bf16 v[18:33], v[166:169], v[98:105], v174\n\tATT_E 82, 82, 3\n\tATT_E 85, 85, 2\n\t"
        "v_smfmac_f32_16x16x64_bf16 v[138:141], v[130:133], v[98:105], v174\n\tATT_E 87, 87, 3\n\tATT_E 90, 90, 2\n\t"
        "v_smfmac_f32_32x32x32_bf16 v[18:33], v[162:165], v[82:89], v174\n\tATT_E 92, 92, 3\n\t"
        "v_smfmac_f32_16x16x64_bf16 v[138:141], v[130:133], v[82:89], v174\n\tATT_E 95, 95, 3\n\ts_nop 1\n\t"
        "v_smfmac_f32_16x16x64_bf16 v[138:141], v[130:133], v[90:97], v174\n\t"
        "v_smfmac_f32_32x32x32_bf16 v[18:33], v[170:173], v[90:97], v174\n\t"
        "s_nop 7\n\tATT_OUT 18, 138, 68, %[og]\n\ts_nop 1\n\t"
        ".purgem ATT_RD\n\t.purgem ATT_E\n\t.purgem ATT_OUT\n\t.purgem ATT_STEP\n\t.purgem ATT_TILE\n\t.purgem ATT_STAGE"
        : "+{v[2:17]}"(oA), "+{v[18:33]}"(oB), "+{v[66:81]}"(sA), "+{v[106:113]}"(pB), "+{v[134:137]}"(lsA), "+{v[138:141]}"(lsB),
          "+{v[142:145]}"(k00), "+{v[146:149]}"(k01), "+{v[150:153]}"(k10), "+{v[154:157]}"(k11),
          "+{v[158:161]}"(v00), "+{v[162:165]}"(v01), "+{v[166:169]}"(v10), "+{v[170:173]}"(v11), "+{v[34:49]}"(nmA), "+{v[50:65]}"(nmB),
          [ob] "+v"(ob)
        : [dst] "v"(dst), [of] "s"(ofrag), [og] "s"((char*)ofrag + 65536), "{v[114:117]}"(qA0), "{v[118:121]}"(qA1), "{v[122:125]}"(qB0), "{v[126:129]}"(qB1),
          "{v[130:133]}"(sel1), "{v175}"(a0), "{v177}"(a1)
        : "memory", "scc", "s96", "v174", "v176", "v178", "v82", "v83", "v84", "v85", "v86", "v87", "v88", "v89", "v90", "v91", "v92", "v93", "v94", "v95",
          "v96", "v97", "v98", "v99", "v100", "v101", "v102", "v103", "v104", "v105");
    STAMP(9);
    float lA = lsA[0], lB = lsB[0];
    {
        const f32x16 os = oA + oB;
        const float osum = (((os[0] + os[1]) + (os[2] + os[3])) + ((os[4] + os[5]) + (os[6] + os[7]))) +
                           (((os[8] + os[9]) + (os[10] + os[11])) + ((os[12] + os[13]) + (os[14] + os[15])));
        const bool bad = !(fmaxf(lA, lB) < 1.0e24f) || !(lA > 1.0e-24f) || !(lB > 1.0e-24f) || !(fabsf(osum) < 3.0e38f);
                if (__any(bad)) {
            const __amdgpu_buffer_rsrc_t rk = __builtin_amdgcn_make_buffer_rsrc((char*)kvf + (size_t)head * 262144, 0, 262144, 0x00020000);
            const unsigned s2 = (l15 & 6) == 0 ? 0x3f803f80u : 0u, s3 = (l15 & 6) == 4 ? 0x3f803f80u : 0u;
            const bf8_t sel = __builtin_bit_cast(bf8_t, u32x4{s2, s2, s3, s3});
            const int sidx = (l15 & 1) ? (int)0xEEEEEEEEu : 0x44444444;
            oA = z16; oB = z16; lA = 0.f; lB = 0.f;
            float mA = -INFINITY, mB = -INFINITY;
#pragma unroll 1
            for (int t = 0; t < 64; ++t) {
                bf16x8 f[4];
#pragma unroll
                for (int i = 0; i < 4; ++i) f[i] = __builtin_bit_cast(bf16x8, __builtin_amdgcn_raw_buffer_load_b128(rk, lane * 16 + i * 1024, t * 4096, 16));
                auto upd = [&](bf16x8 q0, bf16x8 q1, f32x16& o, float& l, float& m) {
                    f32x16 s = MFMA32(f[0], q0, z16); s = MFMA32(f[1], q1, s);
                    float tm = s[0];
#pragma unroll
                    for (int r = 1; r < 16; ++r) tm = fmaxf(tm, s[r]);
                    tm = max_h(tm);
                    const float mn = fmaxf(m, tm);
                    const float alpha = __builtin_amdgcn_exp2f(m - mn);
                    m = mn;
#pragma unroll
                    for (int r = 0; r < 16; ++r) { s[r] = __builtin_amdgcn_exp2f(s[r] - mn); o[r] *= alpha; }
                    const u32x4 p0 = {pack_bf16(s[0], s[1]), pack_bf16(s[2], s[3]), pack_bf16(s[4], s[5]), pack_bf16(s[6], s[7])};
                    const u32x4 p1 = {pack_bf16(s[8], s[9]), pack_bf16(s[10], s[11]), pack_bf16(s[12], s[13]), pack_bf16(s[14], s[15])};
                    f32x4 t4 = {0, 0, 0, 0};
                    o = MFMA32(f[2], __builtin_bit_cast(bf16x8, p0), o);
                    o = MFMA32(f[3], __builtin_bit_cast(bf16x8, p1), o);
                    t4 = ROWSUM(p0, p1, t4);
                    l = l * alpha + (t4[0] + t4[1]);
                };
                upd(qA0, qA1, oA, lA, mA);
                upd(qB0, qB1, oB, lB, mB);
            }
#pragma unroll
            for (int u = 0; u < 2; ++u) {
                const f32x16 e = (u ? oB : oA) * __builtin_amdgcn_rcpf(u ? lB : lA);
                st16o<0>((char*)ofrag + u * 65536, dst, u32x4{pack_bf16(e[0], e[1]), pack_bf16(e[2], e[3]), pack_bf16(e[8], e[9]), pack_bf16(e[10], e[11])});
                st16o<512>((char*)ofrag + u * 65536, dst, u32x4{pack_bf16(e[4], e[5]), pack_bf16(e[6], e[7]), pack_bf16(e[12], e[13]), pack_bf16(e[14], e[15])});
            }
        }
    }
}

DI void ln_row(const u32x4 (&v)[2], __hip_bfloat16* __restrict__ Y, unsigned off) {
    float s1 = 0.f, s2 = 0.f;
#pragma unroll
    for (int e = 0; e < 8; ++e) s1 = dot2bf(v[e >> 2][e & 3], 0x3f803f80u, s1);
    const float mean = wave_sum(s1) * (1.f / 1024.f);
#pragma unroll
    for (int e = 0; e < 8; ++e) {
        const float d0 = bf_lo(v[e >> 2][e & 3]) - mean, d1 = bf_hi(v[e >> 2][e & 3]) - mean;
        s2 += d0 * d0 + d1 * d1;
    }
    const float mb = round_bf16(mean);
    const float rden = __builtin_amdgcn_rcpf(round_bf16(__builtin_amdgcn_sqrtf(round_bf16(round_bf16(wave_sum(s2) * (1.f / 1024.f)) + 1e-5f))));
#pragma unroll
    for (int hh = 0; hh < 2; ++hh) {
        u32x4 y;
#pragma unroll
        for (int e = 0; e < 4; ++e) {
            const unsigned d = pack_bf16(bf_lo(v[hh][e]) - mb, bf_hi(v[hh][e]) - mb);
            y[e] = pack_bf16(bf_lo(d) * rden, bf_hi(d) * rden);
        }
        st16((char*)Y + hh * 131072, off, y);
    }
}

extern "C" __global__ __launch_bounds__(512, 2) void mega_kernel(const __hip_bfloat16* x, const __hip_bfloat16* wqkvA,
                                                            const __hip_bfloat16* woA, const __hip_bfloat16* wqkvB,
                                                            const __hip_bfloat16* woB, __hip_bfloat16* out,
                                                            char* ws) {
    char* const smem = smem_base;
    typedef __hip_bfloat16 bf;
    const int w8 = __builtin_amdgcn_readfirstlane(threadIdx.x >> 6);
    if (w8 == 7 && (blockIdx.x >> 3) < 10) code_touch(blockIdx.x >> 3, threadIdx.x & 63);
    asm volatile("" :: "s"(x), "s"(wqkvA), "s"(woA), "s"(wqkvB), "s"(woB), "s"(out), "s"(ws));
    STAMP_DECL; STAMPR(14); STAMP(0);
    u64 tag;
    {
        const int lane = threadIdx.x & 63;
        const Ids I = ids();
        const unsigned off = (unsigned)((I.po * 128 + I.co * 8 + w8) * 2048 + lane * 16);
        u32x4 v[2];
        u32x2 ep;
        asm volatile("global_load_dwordx4 %0, %3, %4\n\tglobal_load_dwordx4 %1, %3, %4 offset:1024\n\tglobal_load_dwordx2 %2, %5, %6 sc1"
                     : "=&v"(v[0]), "=&v"(v[1]), "=&v"(ep) : "v"(off), "s"(x), "v"(0), "s"(ws + WS_EPOCH) : "memory");
        if (w8 == 7) {
            const int l = lane & 15, k = l % 5;
            const unsigned o = (l >= 10 || k == 3) ? flag_off(l, I.cq, I.pq, 0) : (k == 0 || k == 2) ? flag_off(l, I.pq, I.cq, 0) : flag_off(l, I.po, I.co, 0);
            touch_at(ws + WS_FLAG, o);
        } else if (w8 >= 4) touch(wqkvA, (w8 - 4) * 256 + blockIdx.x, lane);
        if (w8 >= 4) asm volatile("s_waitcnt vmcnt(2)" ::: "memory"); else asm volatile("s_waitcnt vmcnt(1)" ::: "memory");
        asm volatile("" : "+v"(v[0]), "+v"(v[1]));
        ln_row(v, (bf*)(ws + WS_Y0), (unsigned)(I.po * 262144 + (lane >> 3) * 16384 + (I.co * 8 + w8) * 128 + (lane & 7) * 16));
        if (w8 >= 4) asm volatile("s_waitcnt vmcnt(3)" ::: "memory"); else asm volatile("s_waitcnt vmcnt(2)" ::: "memory");
        asm volatile("" : "+v"(ep));
        tag = (((u64)(unsigned)__builtin_amdgcn_readfirstlane(ep[1]) << 32) | (unsigned)__builtin_amdgcn_readfirstlane(ep[0])) + 1;
    }
    if (w8 >= 4) {
        int w = w8 & 3;
#pragma unroll 1
        for (int it = 0; it < 2; ++it) {
            const int lane = lane_id();
            asm volatile("" : "+s"(x), "+s"(wqkvA), "+s"(woA), "+s"(wqkvB), "+s"(woB), "+s"(ws), "+s"(w));
            const unsigned lds0 = (unsigned)(uintptr_t)smem;
            u32x4 cb[8];
            const unsigned wv = wo_off(w, lane), rv = res_off(true, it, lane), cv = colsum_off(w, lane);
            qkv_loader<true>(smem, w, ws, it * 5, tag, (const bf*)(ws + (it ? WS_X2 : WS_Y0)), wqkvA, (const float*)(ws + WS_ST + 262144),
                             ws + WS_CSP, it == 0, woA, [&] {
                if (w < 3) {
                    colsum_load(cb, wqkvB, cv);
                    asm volatile("s_waitcnt vmcnt(8)" ::: "memory");
                } else {
                    touch(woA, blockIdx.x, lane);
                    asm volatile("s_waitcnt vmcnt(1)" ::: "memory");
                }
            }, [&](int part) {
                if (part == 0) wo_load(woA, wv, 29, 32, lds0, w); else wo_load(woA, wv, 25, 29, lds0, w);
            } SP_ARG);
            STAMP(3);
            u32x4 res[6];
            res_load(res, it ? (const void*)(ws + WS_X2) : x, true, it, rv, w);
            if (it) __builtin_amdgcn_s_sleep(8);
            wo_load(woA, wv, 0, 7, lds0, w);
            BAR();
            wo_load(woA, wv, 7, 25, lds0, w);
            STAMP(4);
            out_handoff(smem, 147456, res, w, ws, it * 5 + 1, tag, lane, cb, it ? -1 : 1);
            STAMP(5);
            qkv_loader<false>(smem, w, ws, it * 5 + 2, tag, (const bf*)(ws + (it ? WS_X3 : WS_X1)), wqkvB, (const float*)(ws + WS_ST + it * 524288), ws + WS_CSP + 196608,
                              false, nullptr, [] {}, [](int) {} SP_ARG);
            STAMP(8);
            attn_loader(smem, w, ws, it * 5 + 3, tag, (const bf*)(ws + WS_KVF + it * 8 * MBy), woB, ws + (it ? WS_X3 : WS_X1),
                        it ? nullptr : wqkvA SP_ARG);
            STAMP(13); STAMPR(15); STAMP_FLUSH(it);
        }
        if (blockIdx.x == 0 && w == 0 && lane_id() == 0) __hip_atomic_store((g_u64ptr)(ws + WS_EPOCH), tag, __ATOMIC_RELAXED, __HIP_MEMORY_SCOPE_AGENT);
    } else {
        __builtin_amdgcn_s_setprio(2);
#pragma unroll 1
        for (int it = 0; it < 2; ++it) {
            u32x4 q4[4];
            asm volatile("" : "+s"(ws), "+s"(wqkvA), "+s"(wqkvB));
            qkv_compute<true>(smem, w8, wqkvA, q4, nullptr, (bf*)(ws + WS_OF + it * 8 * MBy), it == 0,
                              ws + WS_ST + 262144, ws + WS_WN + blockIdx.x * 16 SP_ARG);
            STAMP(2);
            out_compute<true>(smem, w8, (const bf*)(ws + WS_OF + it * 8 * MBy),
                              (bf*)(ws + (it ? WS_X3 : WS_X1)), (float*)(ws + WS_ST + it * 524288) SP_ARG);
            STAMP(6);
            qkv_compute<false>(smem, w8, wqkvB, q4, (bf*)(ws + WS_KVF + it * 8 * MBy), nullptr, false,
                               ws + WS_ST + it * 524288, ws + WS_WN + 4096 + blockIdx.x * 16 SP_ARG);
            attn_compute(smem, w8, q4, (const bf*)(ws + WS_KVF + it * 8 * MBy), (bf*)(ws + WS_OF + (it * 8 + 4) * MBy) SP_ARG);
            out_compute<false>(smem, w8, (const bf*)(ws + WS_OF + (it * 8 + 4) * MBy),
                               it ? out : (bf*)(ws + WS_X2), it ? nullptr : (float*)(ws + WS_ST + 262144) SP_ARG);
            STAMP(13); STAMPR(15); STAMP_FLUSH(it);
        }
    }
}

extern "C" __global__ void zz_code_end_kernel() {}

extern "C" void kernel_launch(void* const* d_in, const int* in_sizes, int n_in, void* d_out, int out_size, void* d_ws,
                              size_t ws_size, hipStream_t stream) {
    typedef __hip_bfloat16 bf;
    (void)hipFuncSetAttribute((const void*)mega_kernel, hipFuncAttributeMaxDynamicSharedMemorySize, CH_LDS);
    mega_kernel<<<256, 512, CH_LDS, stream>>>((const bf*)d_in[0], (const bf*)d_in[1], (const bf*)d_in[2], (const bf*)d_in[4], (const bf*)d_in[5],
                                              (bf*)d_out, (char*)d_ws);
}
```

```cpp
#include <hip/hip_runtime.h>
#include <hip/hip_bf16.h>
#include <stdint.h>

typedef __attribute__((ext_vector_type(8))) short bf16x8;
typedef __attribute__((ext_vector_type(4))) short s16x4;
typedef __attribute__((ext_vector_type(4))) float f32x4;
typedef __attribute__((ext_vector_type(2))) float f32x2;
typedef __attribute__((ext_vector_type(16))) float f32x16;
typedef __attribute__((ext_vector_type(4))) unsigned u32x4;
typedef __attribute__((ext_vector_type(2))) unsigned u32x2;
typedef __attribute__((ext_vector_type(2))) __bf16 bf16x2_t;
typedef __attribute__((ext_vector_type(8))) __bf16 bf8_t;
typedef __attribute__((ext_vector_type(16))) __bf16 bf16v_t;
typedef __attribute__((address_space(3))) const char* lds_cptr;
typedef __attribute__((address_space(3))) char* lds_ptr;

#define DI __device__ __forceinline__
#define MFMA16(a, b, c) __builtin_amdgcn_mfma_f32_16x16x32_bf16((a), (b), (c), 0, 0, 0)
#define MFMA32(a, b, c) __builtin_amdgcn_mfma_f32_32x32x16_bf16((a), (b), (c), 0, 0, 0)
#define SBAR() __builtin_amdgcn_sched_barrier(0)
#define STAMP(i)
#define STAMPR(i)
#define STAMP_DECL
#define STAMP_FLUSH(it)
#define SP_PARAM
#define SP_ARG
typedef unsigned long long u64;
typedef __attribute__((ext_vector_type(32))) unsigned u32x32;

constexpr float QSCALE = 0.17677669529663687f * 1.4426950408889634f;
constexpr float ATT_SCALE = 0.17677669529663687f;
constexpr float LOG2E = 1.4426950408889634f;
constexpr float NEG_MASK = -1e9f / ATT_SCALE;

DI int lane_id() { int l; asm volatile("v_mbcnt_lo_u32_b32 %0, -1, 0\n\tv_mbcnt_hi_u32_b32 %0, -1, %0" : "=v"(l)); return l; }
DI unsigned pack_bf16(float lo, float hi) {
    f32x2 v = {lo, hi};
    bf16x2_t r = __builtin_convertvector(v, bf16x2_t);
    return __builtin_bit_cast(unsigned, r);
}
DI float bf_lo(unsigned u) { return __uint_as_float(u << 16); }
DI float bf_hi(unsigned u) { return __uint_as_float(u & 0xffff0000u); }
DI float round_bf16(float x) { return bf_lo(pack_bf16(x, 0.f)); }
DI void ln_stats(float sum, float sumsq, float& mean_out, float& rstd_out) {
    float mean;
    asm("v_mul_f32 %0, 0x3a800000, %1" : "=v"(mean) : "v"(sum));
    const float var = fmaxf(__builtin_fmaf(-mean, mean, sumsq * (1.f / 1024.f)), 0.f);
    mean_out = round_bf16(mean);
    rstd_out = __builtin_amdgcn_rcpf(round_bf16(__builtin_amdgcn_sqrtf(round_bf16(round_bf16(var) + 1e-5f))));
}

DI void glds16(const void* sbase, unsigned voff, unsigned ldsaddr) {
    asm volatile("s_mov_b32 m0, %0\n\ts_nop 0\n\tglobal_load_lds_dwordx4 %1, %2"
                 :: "s"(ldsaddr), "v"(voff), "s"(sbase) : "memory");
}
DI void glds16c(const void* sbase, unsigned voff, unsigned ldsaddr) {
    asm volatile("s_mov_b32 m0, %0\n\ts_nop 0\n\tglobal_load_lds_dwordx4 %1, %2 sc1"
                 :: "s"(ldsaddr), "v"(voff), "s"(sbase) : "memory");
}
DI void st16(void* sbase, unsigned voff, u32x4 v) {
    asm volatile("global_store_dwordx4 %0, %1, %2 sc1\n\ts_nop 1" :: "v"(voff), "v"(v), "s"(sbase) : "memory");
}
template <int OFF> DI void st16o(void* sbase, unsigned voff, u32x4 v) {
    asm volatile("global_store_dwordx4 %0, %1, %2 offset:%3 sc1\n\ts_nop 1" :: "v"(voff), "v"(v), "s"(sbase), "n"(OFF) : "memory");
}
template <int OFF> DI void st16k(void* sbase, unsigned voff, u32x4 v) {
    asm volatile("global_store_dwordx4 %0, %1, %2 offset:%3 sc1" :: "v"(voff), "v"(v), "s"(sbase), "n"(OFF) : "memory");
}
DI bf16x8 lds_b128(lds_cptr p) { return *(const __attribute__((address_space(3))) bf16x8*)p; }
DI s16x4 lds_tr(lds_cptr p) {
    return __builtin_amdgcn_ds_read_tr16_b64_v4i16((__attribute__((address_space(3))) s16x4*)p);
}
DI bf16x8 cat8(s16x4 a, s16x4 b) { return __builtin_shufflevector(a, b, 0, 1, 2, 3, 4, 5, 6, 7); }
DI float dot2bf(unsigned a, unsigned b, float c) {
    return __builtin_amdgcn_fdot2_f32_bf16(__builtin_bit_cast(bf16x2_t, a), __builtin_bit_cast(bf16x2_t, b), c, false);
}
DI float sum_g(float v) {
    auto a = __builtin_amdgcn_permlane16_swap(__float_as_uint(v), __float_as_uint(v), false, false);
    v = __uint_as_float(a[0]) + __uint_as_float(a[1]);
    auto b = __builtin_amdgcn_permlane32_swap(__float_as_uint(v), __float_as_uint(v), false, false);
    return __uint_as_float(b[0]) + __uint_as_float(b[1]);
}
DI float wave_sum(float v) {
    v += __builtin_bit_cast(float, __builtin_amdgcn_update_dpp(0, __builtin_bit_cast(int, v), 0xB1, 0xF, 0xF, true));
    v += __builtin_bit_cast(float, __builtin_amdgcn_update_dpp(0, __builtin_bit_cast(int, v), 0x4E, 0xF, 0xF, true));
    v += __builtin_bit_cast(float, __builtin_amdgcn_update_dpp(0, __builtin_bit_cast(int, v), 0x141, 0xF, 0xF, true));
    v += __builtin_bit_cast(float, __builtin_amdgcn_update_dpp(0, __builtin_bit_cast(int, v), 0x140, 0xF, 0xF, true));
    return sum_g(v);
}
DI unsigned wave_min(unsigned v) {
    v = min(v, (unsigned)__builtin_amdgcn_update_dpp(0, (int)v, 0xB1, 0xF, 0xF, true));
    v = min(v, (unsigned)__builtin_amdgcn_update_dpp(0, (int)v, 0x4E, 0xF, 0xF, true));
    v = min(v, (unsigned)__builtin_amdgcn_update_dpp(0, (int)v, 0x141, 0xF, 0xF, true));
    v = min(v, (unsigned)__builtin_amdgcn_update_dpp(0, (int)v, 0x140, 0xF, 0xF, true));
    const auto a = __builtin_amdgcn_permlane16_swap(v, v, false, false);
    v = min(a[0], a[1]);
    const auto b = __builtin_amdgcn_permlane32_swap(v, v, false, false);
    return min(b[0], b[1]);
}
DI float amax3(float a, float b, float c) { float r; asm("v_max3_f32 %0, %1, %2, %3" : "=v"(r) : "v"(a), "v"(b), "v"(c)); return r; }
DI float max2f(float a, float b) { return __builtin_amdgcn_fmed3f(a, b, INFINITY); }
DI float max_h(float v) {
    auto b = __builtin_amdgcn_permlane32_swap(__float_as_uint(v), __float_as_uint(v), false, false);
    return max2f(__uint_as_float(b[0]), __uint_as_float(b[1]));
}

constexpr unsigned OFF_DUMP = 145408;
extern __shared__ __attribute__((aligned(16))) char smem_base[];
DI void touch_at(const void* p, unsigned off) {
    asm volatile("s_mov_b32 m0, %0\n\ts_nop 0\n\tglobal_load_lds_dword %1, %2"
                 :: "s"((unsigned)(uintptr_t)smem_base + OFF_DUMP), "v"(off), "s"(p) : "memory");
}
DI void touch(const void* p, int slice, int lane) { touch_at(p, (unsigned)(slice * 64 + lane) * 128u); }
DI void code_touch(int slice, int lane) {
    unsigned long long c0, c1;
    asm volatile("s_getpc_b64 s[96:97]\n\ts_add_u32 s96, s96, mega_kernel@rel32@lo+4\n\ts_addc_u32 s97, s97, mega_kernel@rel32@hi+12\n\ts_mov_b64 %0, s[96:97]\n\t"
                 "s_getpc_b64 s[96:97]\n\ts_add_u32 s96, s96, zz_code_end_kernel@rel32@lo+4\n\ts_addc_u32 s97, s97, zz_code_end_kernel@rel32@hi+12\n\ts_mov_b64 %1, s[96:97]"
                 : "=s"(c0), "=s"(c1) :: "s96", "s97", "scc");
    touch_at((const void*)c0, min((unsigned)(slice * 64 + lane) * 128u, (unsigned)(c1 - c0)));
}

#define BAR() asm volatile("s_barrier" ::: "memory")
#define WAIT_LGKM() do { SBAR(); __builtin_amdgcn_s_waitcnt(0xC07F); SBAR(); } while (0)
#define WAIT_LGKM_BAR() do { SBAR(); __builtin_amdgcn_s_waitcnt(0xC07F); asm volatile("s_barrier" ::: "memory"); SBAR(); } while (0)
#define VM_BAR(n) asm volatile("s_waitcnt vmcnt(" #n ")\n\ts_barrier" ::: "memory")
constexpr int CH_LDS = 163840;
typedef __attribute__((address_space(1))) u64* g_u64ptr;

constexpr size_t MBy = 1 << 20;
constexpr size_t WS_X3 = 0, WS_X2 = 4 * MBy, WS_X1 = 8 * MBy, WS_OF = 12 * MBy  , WS_KVF = 36 * MBy  ,
                 WS_ST = 52 * MBy  , WS_CSP = 54 * MBy  , WS_FLAG = 55 * MBy  ,
                 WS_WN = WS_CSP + 458752  , WS_EPOCH = WS_CSP + 524288, WS_Y0 = 56 * MBy;
DI unsigned flag_off(int set, int group, int consumer, int producer) { return (unsigned)(set * 65536 + group * 4096 + consumer * 256 + producer * 8); }
DI void flag_store(char* ws, int set, int group, int producer, u64 tag, int lane) {
    asm volatile("" : "+s"(tag));
    if (lane < 16) __hip_atomic_store((g_u64ptr)(ws + WS_FLAG + flag_off(set, group, lane, producer)), tag, __ATOMIC_RELAXED, __HIP_MEMORY_SCOPE_AGENT);
}
DI void flag_poll(const char* ws, unsigned off, u64 tag) {
    asm volatile("" : "+s"(tag));
#pragma unroll 1
    for (int spin = 0; spin < (1 << 19); ++spin) {
        const u64 v = __hip_atomic_load((g_u64ptr)(ws + WS_FLAG + off), __ATOMIC_RELAXED, __HIP_MEMORY_SCOPE_AGENT);
        if (__all(v == tag)) break;
        __builtin_amdgcn_s_sleep(1);
    }
    asm volatile("" ::: "memory");
}
template <bool DRAIN, bool ALLPOLL>
DI void signal_poll(int follower, const char* ws, unsigned soff, unsigned poff, u64 pmask, u64 tag, u64& land) {
    u64 tagv, zero;
    asm volatile("s_not_b64 s[96:97], %[ts]\n\tv_mov_b64 %[tag], %[ts]\n\tv_mov_b64 %[zero], 0\n\tv_mov_b64 %[land], s[96:97]\n\t"
                 ".if %[dr]\n\ts_waitcnt vmcnt(0)\n\t.endif\n\ts_barrier\n\t"
                 "s_cmp_lg_u32 %[fl], 0\n\ts_cbranch_scc1 .Lsig_fol_%=\n\t"
                 "s_mov_b64 exec, 0xffff\n\t"
                 "global_store_dwordx2 %[so], %[tag], %[base] sc1\n"
                 ".if %[ap]\n.Lsig_fol_%=:\n.endif\n\t"
                 "s_mov_b64 exec, %[pm]\n\t"
                 "s_mov_b32 s96, 0x100000\n"
                 ".Lsig_loop_%=:\n\t"
                 "global_atomic_or_x2 %[land], %[po], %[zero], %[base] sc0 sc1\n\t"
                 "s_sleep 1\n\t"
                 "s_waitcnt vmcnt(3)\n\t"
                 "v_cmp_ne_u64 vcc, %[land], %[tag]\n\t"
                 "s_sub_u32 s96, s96, 1\n\t"
                 "s_cbranch_scc1 .Lsig_out_%=\n\t"
                 "s_cbranch_vccnz .Lsig_loop_%=\n"
                 ".Lsig_out_%=:\n\t"
                 "s_mov_b64 exec, -1\n"
                 ".if !%[ap]\n.Lsig_fol_%=:\n.endif"
                 : [land] "=&v"(land), [tag] "=&v"(tagv), [zero] "=&v"(zero)
                 : [fl] "s"(follower), [so] "v"(soff), [po] "v"(poff), [ts] "s"(tag), [base] "s"(ws + WS_FLAG), [pm] "s"(pmask), [ap] "n"(ALLPOLL ? 1 : 0), [dr] "n"(DRAIN ? 1 : 0)
                 : "memory", "scc", "vcc", "s96", "s97");
}
struct Ids { int pq, cq, po, co; };
DI Ids ids() {
    int bid = blockIdx.x; asm volatile("" : "+s"(bid));
    const int xcd = bid & 7, idx = bid >> 3;
    int pq, cq, po, co;
#define IDS_BODY pq = (xcd & 1) * 8 + (idx & 7); cq = (xcd >> 1) * 4 + (idx >> 3); po = (xcd & 3) * 4 + (idx & 3); co = (xcd >> 2) * 8 + (idx >> 2);
    IDS_BODY
    return Ids{pq, cq, po, co};
}

DI void slow_warm(unsigned dump, const void* W, int w, int lane) {
    int bid = blockIdx.x; asm volatile("" : "+s"(bid));
    const int xcd = bid & 7, idx = bid >> 3;
    const int sc = (3 - (int)((uintptr_t)W >> 7)) & 7;
    if ((sc >> 2) != ((xcd >> 1) & 1)) return;
    const char* src = (const char*)W + (idx + 32 * w) * 49152 + (sc + (xcd >> 2) * 8) * 128;
#pragma unroll
    for (int G = 0; G < 3; ++G) glds16(src + G * 2048, (unsigned)((lane >> 3) * 6144 + (lane & 7) * 16), dump);
}

template <int V> struct IC { static constexpr int value = V; };
template <bool LOCAL> struct QC {
    static constexpr int AROWS = LOCAL ? 144 : 128;
    static constexpr int ABYTES = AROWS * 128;
    static constexpr int STAGE = ABYTES + 24576;
    static constexpr int OFF_MEAN = 143360;
    static constexpr int OFF_RSTD = OFF_MEAN + 576;
    static constexpr int OFF_CS = OFF_RSTD + 576;
};
constexpr int OFF_EX = 131072;

DI unsigned colsum_off(int G, int lane) {
    const Ids I = ids();
    unsigned off = (unsigned)(I.pq * 64 + (lane >> 3)) * 6144u + (unsigned)(G * 2048 + I.cq * 128 + (lane & 7) * 16);
    asm volatile("" : "+v"(off));
    return off;
}
DI void colsum_load(u32x4 (&c)[8], const void* W, unsigned off) {
#define CS_NEXT "s_add_u32 s96, s96, 0xc000\n\ts_addc_u32 s97, s97, 0\n\t"
    asm volatile("s_cmp_eq_u64 %9, 0\n\ts_cbranch_scc1 .Lcs_skip_%=\n\ts_mov_b64 s[96:97], %9\n\t"
                 "global_load_dwordx4 %0, %8, s[96:97]\n\t" CS_NEXT "global_load_dwordx4 %1, %8, s[96:97]\n\t" CS_NEXT
                 "global_load_dwordx4 %2, %8, s[96:97]\n\t" CS_NEXT "global_load_dwordx4 %3, %8, s[96:97]\n\t" CS_NEXT
                 "global_load_dwordx4 %4, %8, s[96:97]\n\t" CS_NEXT "global_load_dwordx4 %5, %8, s[96:97]\n\t" CS_NEXT
                 "global_load_dwordx4 %6, %8, s[96:97]\n\t" CS_NEXT "global_load_dwordx4 %7, %8, s[96:97]\n"
                 ".Lcs_skip_%=:"
                 : "=&v"(c[0]), "=&v"(c[1]), "=&v"(c[2]), "=&v"(c[3]), "=&v"(c[4]), "=&v"(c[5]), "=&v"(c[6]), "=&v"(c[7])
                 : "v"(off), "s"(W) : "memory", "scc", "s96", "s97");
}
DI void colsum_store(u32x4 (&c)[8], char* ws, int mat, int G, int lane) {
    asm volatile("" : "+v"(c[0]), "+v"(c[1]), "+v"(c[2]), "+v"(c[3]), "+v"(c[4]), "+v"(c[5]), "+v"(c[6]), "+v"(c[7]));
    const Ids I = ids();
    const int m = lane & 15;
    const unsigned one = (m & 1) ? 0xbf800000u : 0xbf80u;
    const u32x4 sl = {(m >> 1) == 0 ? one : 0u, (m >> 1) == 1 ? one : 0u, (m >> 1) == 2 ? one : 0u, (m >> 1) == 3 ? one : 0u};
    f32x4 d = {0, 0, 0, 0};
#pragma unroll
    for (int i = 0; i < 8; ++i) d = MFMA16(__builtin_bit_cast(bf16x8, sl), __builtin_bit_cast(bf16x8, c[i]), d);
#pragma unroll
    for (int r = 0; r < 4; ++r)
        d[r] += __int_as_float(__builtin_amdgcn_update_dpp(0, __float_as_int(d[r]), 0x128, 0xF, 0xF, true));
    if (lane < 32 && (lane & 8) == 0)
        st16(ws + WS_CSP + mat * 196608, (unsigned)((I.cq * 16 + I.pq) * 768 + G * 256 + (lane & 7) * 32 + (lane >> 4) * 16), __builtin_bit_cast(u32x4, d));
    typedef __attribute__((ext_vector_type(2))) unsigned short u16x2;
    u16x2 mn = {0x7fff, 0x7fff};
#pragma unroll
    for (int e = 0; e < 4; ++e) {
        u16x2 mx = {0, 0};
#pragma unroll
        for (int i = 0; i < 8; ++i) { const unsigned u = c[i][e] & 0x7fff7fffu; mx = __builtin_elementwise_max(mx, __builtin_bit_cast(u16x2, u)); }
        mn = __builtin_elementwise_min(mn, mx);
    }
    const unsigned wn = wave_min((unsigned)min(mn[0], mn[1])) << 16;
    if (lane == 0) __hip_atomic_store((__attribute__((address_space(1))) unsigned*)(ws + WS_WN + mat * 4096 + blockIdx.x * 16 + G * 4), wn,
                                      __ATOMIC_RELAXED, __HIP_MEMORY_SCOPE_AGENT);
}

template <bool LOCAL>
struct WFirst {
    unsigned v[2];
    DI WFirst(int w, int ct, int lane) {
#pragma unroll
        for (int i = 0; i < 2; ++i) {
            const int k = 8 * i + (lane >> 3), pos = lane & 7;
            v[i] = (unsigned)k * 6144u + (unsigned)(w * 1024 + ct * 64 + ((pos >> 1) ^ (((k >> 1) & 1) | (i << 1))) * 16 + (pos & 1) * 8) * 2u;
        }
        asm volatile("" : "+v"(v[0]), "+v"(v[1]));
    }
    DI void issue(const void* W, unsigned lds0, int w, int i0, int i1) const {
#pragma unroll
        for (int i = i0; i < i1; ++i)
            glds16((const char*)W + (size_t)(i >> 1) * (16 * 6144), v[i & 1], lds0 + (i >> 3) * QC<LOCAL>::STAGE + QC<LOCAL>::ABYTES + (w * 8 + (i & 7)) * 1024);
    }
};

template <bool LOCAL, class F, class F2>
DI void qkv_loader(char* smem, int w, char* ws, int set, u64 tag, const __hip_bfloat16* __restrict__ X, const __hip_bfloat16* __restrict__ W,
                   const float* __restrict__ statp, const char* __restrict__ csp, bool ident, const void* nextW, F tail, F2 mid SP_PARAM) {
    typedef QC<LOCAL> C;
    constexpr int NT = 16;
    const int lane = lane_id();
    asm volatile("" : "+s"(w));
    const int idx = blockIdx.x >> 3;
    const Ids I = ids();
    const int panel = I.pq, ct = I.cq;
    const int R0 = panel * 128;
    const unsigned lds0 = (unsigned)(uintptr_t)smem;
    auto grow = [&](int i) -> int {
        if (!LOCAL) return R0 + i;
        if (i < 128) { const int t = (i >> 4) & 3, a = i & 15; return R0 + (i & 64) + 2 * (16 * (t & 1) + a) + (t >> 1); }
        const int a = i & 15; const int r = R0 - 16 + 2 * (a & 7) + (a >> 3);
        return r < 0 ? 0 : r;
    };
    unsigned voffA[5], voffB[6];
#pragma unroll
    for (int i = 0; i < 6; ++i) {
        const int j = w + 4 * i, G = j >> 3, k = 8 * (j & 7) + (lane >> 3);
        const int pos = lane & 7, f = ((k >> 1) & 1) | (((k >> 3) & 1) << 1);
        const int seg = (pos >> 1) ^ f;
        voffB[i] = (unsigned)k * 6144u + (unsigned)(G * 1024 + ct * 64 + seg * 16 + (pos & 1) * 8) * 2u;
    }
    auto issueW = [&](int t, int buf) {
        const char* wb = (const char*)W + (size_t)t * (64 * 6144);
        const unsigned sb = lds0 + buf * C::STAGE;
#pragma unroll
        for (int i = 0; i < 6; ++i) glds16(wb, voffB[i], sb + C::ABYTES + (w + 4 * i) * 1024);
    };
    asm volatile("" : "+v"(voffB[0]), "+v"(voffB[1]), "+v"(voffB[2]), "+v"(voffB[3]), "+v"(voffB[4]), "+v"(voffB[5]));
    const WFirst<LOCAL> first(w, ct, lane);
    const int g = lane >> 4, r = lane & 15, pos = 32 * w + 2 * r + (g >> 1);
    const int lr = !LOCAL ? (pos & 64) + 2 * (pos & 31) + ((pos >> 5) & 1)
                          : (w == 0 && (g & 1)) ? 128 + (r & 7) + 8 * (g >> 1) : (pos & 64) + ((pos & 1) * 2 + ((pos >> 5) & 1)) * 16 + ((pos >> 1) & 15);
    unsigned so = (unsigned)(panel * 16384 + g * 1024 + w * 256 + r * 16), ho = (unsigned)(g * 1024 + (panel ? panel * 16384 - 15488 + (r & 7) * 16 : 0));
    unsigned co = (unsigned)((ct * 16 + g) * 768 + w * 192 + r * 12);
    lds_ptr mp = (lds_ptr)smem + C::OFF_MEAN + lr * 4, cp = (lds_ptr)smem + C::OFF_CS + w * 192 + r * 12 + ((g & 1) ? 8 : g * 2);
    unsigned fo = flag_off((LOCAL ? 13 : 10) + min(g, 2), ct, panel, r);
    asm volatile("" : "+v"(so), "+v"(ho), "+v"(co), "+v"(mp), "+v"(cp), "+v"(fo));
#pragma unroll
    for (int i = 0; i < 5; ++i) {
        const int j = (i < 4) ? (w + 4 * i) : (16 + (w & 1));
        const int row = 8 * j + (lane >> 3);
        const int c = (lane & 7) ^ ((row >> 1) & 7);
        const int gr = grow(row < C::AROWS ? row : 0);
        voffA[i] = (unsigned)((gr >> 7) * 262144 + (gr & 127) * 128 + c * 16);
        asm volatile("" : "+v"(voffA[i]));
    }
    auto issueA = [&](int t, int buf) {
        const char* xa = (const char*)X + t * 16384;
        const unsigned sb = lds0 + buf * C::STAGE;
        if (LOCAL && w < 2) glds16c(xa, voffA[4], sb + (16 + w) * 1024);
#pragma unroll
        for (int i = 0; i < 4; ++i) glds16c(xa, voffA[i], sb + (w + 4 * i) * 1024);
    };
    u64 land;
    if (w == 3) __builtin_amdgcn_s_setprio(3);
    signal_poll<LOCAL, false>(w - 3, ws, flag_off(set, I.po, lane & 15, I.co),
                              flag_off(set, (lane & 16) && panel ? panel - 1 : panel, ct, lane & 15),
                              !LOCAL ? 0xffffull : ident ? 0xc000ffffull : 0xffffffffull, tag, land);
    if (w < 3) first.issue(W, lds0, w, 0, 12);
    BAR();
    STAMP(LOCAL ? 1 : 6);
    issueA(0, 0);
    issueA(1, 1);
    if (w < 3) { first.issue(W, lds0, w, 12, 16); asm volatile("s_waitcnt vmcnt(8)" ::: "memory"); }
    else asm volatile("s_waitcnt vmcnt(4)" ::: "memory");
    BAR();
    asm volatile("" :: "v"(land));
    __builtin_amdgcn_s_setprio(0);
    STAMP(LOCAL ? 2 : 7);
    issueW(2, 2);
    issueA(2, 2);
    VM_BAR(10);
    asm volatile("global_load_dwordx2 %0, %1, %2 sc1" : "=&v"(land) : "v"(fo), "s"(ws + WS_FLAG) : "memory");
    issueA(3, 0); issueW(3, 0);
    int buf = 1;
#pragma unroll 1
    for (int t = 1; t < NT - 3; ++t) {
        VM_BAR(10);
        issueA(t + 3, buf); issueW(t + 3, buf);
        if (t == 10 && idx < 3) code_touch(idx * 4 + w, lane);
        if (LOCAL && t == 11)
            glds16(nextW, (unsigned)((idx + 32 * w) * 8 + (lane >> 3)) * 2048u +
                   (unsigned)(((I.co & 8) + ((3 - (int)((uintptr_t)nextW >> 7)) & 7)) * 128 + (lane & 7) * 16), lds0 + OFF_DUMP);
        buf = (buf == 2) ? 0 : buf + 1;
    }
    typedef __attribute__((ext_vector_type(3))) float f32x3;
    const bool halo = LOCAL && w == 0;
    asm volatile("" : "+v"(land));
    if (!ident) {
        if (!__all(land == tag)) flag_poll(ws, fo, tag);
        f32x3 cs[4];
        f32x4 sv[4], hv[4];
        typedef __attribute__((address_space(3))) float* lds_f;
        auto add = [](float a, float b) { float r; asm("v_add_f32 %0, %1, %2" : "=v"(r) : "v"(a), "v"(b)); return r; };
        auto fold32 = [](float a, float b) { asm("s_nop 1\n\tv_permlane32_swap_b32 %0, %1\n\tv_add_f32 %0, %0, %1" : "+v"(a), "+v"(b)); return a; };
        auto fold16 = [](float a, float b) { asm("s_nop 1\n\tv_permlane16_swap_b32 %0, %1\n\tv_add_f32 %0, %0, %1" : "+v"(a), "+v"(b)); return a; };
        auto sum4 = [&](auto (&v)[4], int e) { return add(add(v[0][e], v[1][e]), add(v[2][e], v[3][e])); };
#pragma unroll
        for (int i = 0; i < 4; ++i) asm volatile("global_load_dwordx3 %0, %1, %2 sc1" : "=&v"(cs[i]) : "v"(co), "s"(csp + i * 3072) : "memory");
        VM_BAR(14);
#pragma unroll
        for (int i = 0; i < 4; ++i) asm volatile("global_load_dwordx4 %0, %1, %2 sc1" : "=&v"(sv[i]) : "v"(so), "s"((const char*)statp + i * 4096) : "memory");
        if (LOCAL)
            asm volatile("s_cmp_lg_u32 %9, 0\n\ts_cbranch_scc1 .Lhv_skip_%=\n\t"
                         "global_load_dwordx4 %0, %4, %5 sc1\n\tglobal_load_dwordx4 %1, %4, %6 sc1\n\t"
                         "global_load_dwordx4 %2, %4, %7 sc1\n\tglobal_load_dwordx4 %3, %4, %8 sc1\n"
                         ".Lhv_skip_%=:" : "=&v"(hv[0]), "=&v"(hv[1]), "=&v"(hv[2]), "=&v"(hv[3])
                         : "v"(ho), "s"(statp), "s"(statp + 1024), "s"(statp + 2048), "s"(statp + 3072), "s"(w) : "memory", "scc");
        if (halo) asm volatile("s_waitcnt vmcnt(8)" ::: "memory"); else asm volatile("s_waitcnt vmcnt(4)" ::: "memory");
        asm volatile("" : "+v"(cs[0]), "+v"(cs[1]), "+v"(cs[2]), "+v"(cs[3]));
        const float c2 = sum4(cs, 2);
        *(lds_f)cp = fold16(fold32(sum4(cs, 0), sum4(cs, 1)), fold32(c2, c2));
        BAR();
        asm volatile("s_waitcnt vmcnt(0)" : "+v"(sv[0]), "+v"(sv[1]), "+v"(sv[2]), "+v"(sv[3]) :: "memory");
        mid(0);
        const float s1 = fold32(sum4(sv, 0), sum4(sv, 2)), s2 = fold32(sum4(sv, 1), sum4(sv, 3));
        float h1, h2;
        asm("v_mov_b32 %0, %2\n\tv_mov_b32 %1, %3" : "=&v"(h1), "=&v"(h2) : "v"(s1), "v"(s2));
        if (halo) {
            asm volatile("" : "+v"(hv[0]), "+v"(hv[1]), "+v"(hv[2]), "+v"(hv[3]));
            const float u0 = sum4(hv, 0), u1 = sum4(hv, 1), u2 = sum4(hv, 2), u3 = sum4(hv, 3);
            h1 = fold32(u0, panel ? u2 : u0); h2 = fold32(u1, panel ? u3 : u1);
        }
        float mean, rstd;
        ln_stats(fold16(s1, h1), fold16(s2, h2), mean, rstd);
        *(lds_f)mp = mean; *(lds_f)(mp + (C::OFF_RSTD - C::OFF_MEAN)) = rstd;
    } else { VM_BAR(10); tail(); BAR(); mid(0); }
    mid(1);
    WAIT_LGKM_BAR();
}

template <bool LOCAL>
DI void qkv_compute(char* smem, int w, const void* W, u32x4 (&qo4)[4], __hip_bfloat16* __restrict__ kvf, __hip_bfloat16* __restrict__ ofrag, bool ident,
                    const char* statp, const char* wnp SP_PARAM) {
    typedef QC<LOCAL> C;
    constexpr int NT = 16, ABYTES = C::ABYTES, STAGE = C::STAGE;
    const int lane = lane_id();
    asm volatile("" : "+s"(w));
    const int wm = w & 1, wn = w >> 1;
    const Ids I = ids();
    const int panel = I.pq, ct = I.cq;
    const int l15 = lane & 15, g = lane >> 4;
    const lds_cptr sm = (lds_cptr)smem;
    lds_ptr smw = (lds_ptr)smem;

    f32x4 acc[3][4][2];
    f32x4 acch[2];
    float cb;
    f32x4 bias;

    bf16x8 A[2][5][2];
    s16x4 B[3][2][2];
    lds_cptr pa[2], pb[2];
    auto rdA = [&](auto WM, int p, int i) { A[p][i >> 1][i & 1] = lds_b128(pa[i & 1] + ((i >> 1) < 4 ? (i >> 1) * 2048 : 16384 - WM.value * 8192)); };
    auto rdB = [&](int c, int i) { B[c % 3][i >> 1][i & 1] = lds_tr(pb[c & 1] + ((c >> 1) + 1) % 3 * 8192 + (i >> 1) * 4096 + (i & 1) * 512); };
    auto column = [&](auto WM, int p, int c, bool first, auto gap) {
        const int G = ((c >> 1) + 1) % 3, f = c & 1;
        const bool halo = LOCAL && G == 1 + WM.value;
#pragma unroll
        for (int n = 0; n < (halo ? 10 : 8); ++n) {
            const bf16x8 x = A[p][n >> 1][n & 1], y = cat8(B[c % 3][n & 1][0], B[c % 3][n & 1][1]);
            const bf16x8 u = G < 2 ? y : x, v = G < 2 ? x : y;
            f32x4& d = n < 8 ? acc[G][n >> 1][f] : acch[f];
            const f32x4 c0 = bias;
            if (!first || (n & 1)) asm volatile("v_mfma_f32_16x16x32_bf16 %0, %1, %2, %0" : "+v"(d) : "v"(u), "v"(v));
            else if (n == 8) asm volatile("v_mfma_f32_16x16x32_bf16 %0, %1, %2, 0" : "=&v"(d) : "v"(u), "v"(v));
            else asm volatile("v_mfma_f32_16x16x32_bf16 %0, %1, %2, %3" : "=&v"(d) : "v"(u), "v"(v), "v"(c0));
            SBAR();
            gap(n);
            SBAR();
        }
    };

    f32x4 cq[2], ck[2], mu4[5], rs4[5]; float cv[2], mu[5], rs[5];
    u32x4 qfr[4], kfr[4];
    u32x4 va[2][2];
    f32x4 mwin, mfirst;
    typedef __attribute__((address_space(3))) u32x4* lds_u4;
    const bool negpos = (wm == 0) && (panel == 0);
    VM_BAR(0);
    unsigned cm01 = 0, cm23 = 0;
    lds_cptr ka[2] = {sm, sm}, vo2[2] = {sm, sm};
    lds_u4 ex = (lds_u4)(smw + OFF_EX + wn * 6144) + lane;
    lds_cptr s1 = sm + C::OFF_MEAN + wm * 256 + l15 * 4, s4 = sm + C::OFF_MEAN + wm * 256 + g * 16;
    lds_cptr c1 = sm + C::OFF_CS + wn * 128 + l15 * 4, c4 = sm + C::OFF_CS + wn * 128 + g * 16;
    asm volatile("" : "+v"(s1), "+v"(s4), "+v"(c1), "+v"(c4));
    if (LOCAL) {
        unsigned keepv = negpos ? __float_as_uint(NEG_MASK) : 0u;
        asm volatile("" : "+s"(keepv));
        u32x4 mw, mf;
        unsigned prv[4];
#pragma unroll
        for (int r = 0; r < 4; ++r) {
            const int d = l15 - (4 * g + r);
            unsigned out = (unsigned)(((d << 28) >> 31));
            prv[r] = (unsigned)(d >> 31);
            asm volatile("" : "+v"(out), "+v"(prv[r]));
            mw[r] = out & 0xff800000u;
            mf[r] = (out & 0xff800000u) | (~out & keepv);
        }
        const lds_cptr e0 = sm + OFF_EX + wn * 6144;
        ka[0] = e0 + (wm ? lane : 256 + (lane ^ 8)) * 16;         ka[1] = e0 + (wm ? 64 + lane : 256 + lane) * 16;
        vo2[0] = e0 + (wm ? 128 + lane : 320 + (lane ^ 32)) * 16; vo2[1] = e0 + (wm ? 192 + lane : 320 + lane) * 16;
        cm01 = ~((prv[0] >> 16) | (prv[1] << 16)); cm23 = ~((prv[2] >> 16) | (prv[3] << 16));
        mwin = __builtin_bit_cast(f32x4, mw); mfirst = __builtin_bit_cast(f32x4, mf);
        asm volatile("" : "+v"(mwin), "+v"(mfirst), "+v"(cm01), "+v"(cm23), "+v"(ka[0]), "+v"(ka[1]), "+v"(vo2[0]), "+v"(vo2[1]), "+v"(ex));
    }
    typedef const __attribute__((address_space(3))) f32x4* lds_f4; typedef const __attribute__((address_space(3))) float* lds_f1;
    auto mainloop = [&](auto WM) {
        int ty = lane_id();
        const int l15 = ty & 15, g = ty >> 4;
        const int swA = (l15 >> 1) & 7;
        const int q4 = l15 >> 2, p4 = l15 & 3, fk = ((q4 >> 1) & 1) | ((g & 1) << 1);
        lds_cptr pA0 = sm + wm * 8192 + l15 * 128 + ((0 + g) ^ swA) * 16, pA1 = sm + wm * 8192 + l15 * 128 + ((4 + g) ^ swA) * 16;
        lds_cptr pB0 = sm + ABYTES + (8 * g + q4) * 128 + (((2 * wn + 0) ^ fk) * 32) + p4 * 8;
        lds_cptr pB1 = sm + ABYTES + (8 * g + q4) * 128 + (((2 * wn + 1) ^ fk) * 32) + p4 * 8;
        asm volatile("" : "+v"(pA0), "+v"(pA1), "+v"(pB0), "+v"(pB1));
        slow_warm((unsigned)(uintptr_t)smem + OFF_DUMP, W, w, ty);
        unsigned so = (unsigned)(panel * 16384 + wm * 512 + ty * 8);
        const char *p0 = ident ? (const char*)W : statp, *p1 = ident ? (const char*)W : wnp;
        u32x4 wn;
        asm volatile("global_load_dwordx4 %0, %2, %3 sc1" : "=&v"(wn), "+v"(so) : "v"(0), "s"(p1) : "memory");
        BAR();
        STAMP(LOCAL ? 1 : 7);
        f32x2 sq;
        asm volatile("global_load_dwordx2 %0, %1, %2 sc1" : "=&v"(sq) : "v"(so), "s"(p0) : "memory");
        BAR();
        pa[0] = pA0; pa[1] = pA1; pb[0] = pB0; pb[1] = pB1;
#pragma unroll
        for (int i = 0; i < 4; ++i) rdB(0, i);
#pragma unroll
        for (int i = 0; i < 8; ++i) rdA(WM, 0, i);
#pragma unroll
        for (int i = 0; i < 4; ++i) if (LOCAL && WM.value == 0 && i >= 2) rdA(WM, 0, 6 + i); else rdB(1, i);
        SBAR();
        asm volatile("s_waitcnt vmcnt(0)" : "+v"(sq), "+v"(wn) :: "memory");
        const float d = fmaxf(__builtin_fmaf(sq[0] * (-1.f / 64.f), sq[0], sq[1]), 0.f);
        cb = fminf(__builtin_amdgcn_sqrtf(__uint_as_float(wave_min(__float_as_uint(d)))) * 64.f * __uint_as_float(min(min(wn[0], wn[1]), wn[2])), 1.0e30f);
        if (ident) cb = 0.f;
        bias = f32x4{cb, cb, cb, cb};
        asm volatile("s_nop 1" : "+v"(bias));
        WAIT_LGKM();
        int off = 0;
        constexpr bool HK = LOCAL && WM.value == 0;
        auto front = [&](int p, bool first) {
#pragma unroll
            for (int c = 0; c < 4; ++c)
                column(WM, p, c, first, [&](int n) {
                    if (HK && c == 0) { if (n < 2) rdB(1, 2 + n); else if (n < 6) rdB(2, n - 2); }
                    else if (n < 4) rdB(c + 2, n);
                    if (LOCAL && !HK && c == 0 && (n == 4 || n == 5)) rdA(WM, p, 4 + n);
                    if (c == 1 && n == 5) { off = (off == 2 * STAGE) ? 0 : off + STAGE; asm volatile("" : "+s"(off)); }
                    if (c == 2 && n == 4) { pa[0] = pA0 + off; asm volatile("" : "+v"(pa[0])); }
                    if (c == 2 && n == 5) { pa[1] = pA1 + off; asm volatile("" : "+v"(pa[1])); }
                    if (c == 3 && n == 4) { pb[0] = pB0 + off; asm volatile("" : "+v"(pb[0])); }
                    if (c == 3 && n == 5) { pb[1] = pB1 + off; asm volatile("" : "+v"(pb[1])); }
                    if ((c == 0 && n == 3) || ((c == 1 || c == 2 || (HK && c == 0)) && n == 7)) __builtin_amdgcn_s_waitcnt(0xC47F);
                });
            WAIT_LGKM_BAR();
        };
        auto back = [&](int p, bool first) {
            column(WM, p, 4, first, [&](int n) { if (n & 1) rdB(0, n >> 1); else rdA(WM, p ^ 1, n >> 1); });
            column(WM, p, 5, first, [&](int n) {
                if (!(n & 1)) rdA(WM, p ^ 1, 4 + (n >> 1)); else if (HK && n > 4) rdA(WM, p ^ 1, 6 + (n >> 1)); else rdB(1, n >> 1);
                if (n == 7) __builtin_amdgcn_s_waitcnt(0xC87F);
            });
        };
        auto stage = [&](int p, bool first) { front(p, first); back(p, first); };
        stage(0, true);
        front(1, false);
#pragma unroll 1
        for (int t = 1; t < NT - 1; t += 2) { back(1, false); stage(0, false); front(1, false); }
        column(WM, 1, 4, false, [&](int n) {
            constexpr int RS = C::OFF_RSTD - C::OFF_MEAN, H = 512 - WM.value * 256;
            if (n == 0) { ck[0] = *(lds_f4)(c4 + 256); ck[1] = *(lds_f4)(c4 + 320); cv[0] = *(lds_f1)(c1 + 512); cv[1] = *(lds_f1)(c1 + 512 + 64); }
            else if (n < 5) {
                mu[n - 1] = *(lds_f1)(s1 + (n - 1) * 64); rs[n - 1] = *(lds_f1)(s1 + RS + (n - 1) * 64);
                mu4[n - 1] = *(lds_f4)(s4 + (n - 1) * 64); rs4[n - 1] = *(lds_f4)(s4 + RS + (n - 1) * 64);
            } else if (n == 5) {
                if (LOCAL && WM.value == 0) { mu[4] = *(lds_f1)(s1 + H); rs[4] = *(lds_f1)(s1 + RS + H); }
                else if (LOCAL) { mu4[4] = *(lds_f4)(s4 + H); rs4[4] = *(lds_f4)(s4 + RS + H); }
            } else if (n == 6) { cq[0] = *(lds_f4)c4; cq[1] = *(lds_f4)(c4 + 64); }
        });
        if (LOCAL) column(WM, 1, 5, false, [](int n) { if (n == 6) __builtin_amdgcn_s_waitcnt(0xC07F); });
        if (LOCAL) {
            auto fix = [&](int a, int t) {
#pragma unroll
                for (int f = 0; f < 2; ++f)
                    acc[a][t][f] = (a == 2) ? rs4[t] * (acc[2][t][f] + mu4[t] * cv[f]) + rs4[t] * -cb
                                            : rs[t] * (acc[a][t][f] + mu[t] * (a ? ck[f] : cq[f])) + rs[t] * -cb;
            };
            auto pk = [&](const f32x4 (&v)[2]) { return u32x4{pack_bf16(v[0][0], v[0][1]), pack_bf16(v[0][2], v[0][3]), pack_bf16(v[1][0], v[1][1]), pack_bf16(v[1][2], v[1][3])}; };
            if (!ident) {
                fix(1, 1); fix(1, 3); fix(2, 1); fix(2, 3);
#pragma unroll
                for (int f = 0; f < 2; ++f)
                    acch[f] = (WM.value == 0) ? rs[4] * (acch[f] + mu[4] * ck[f]) : rs4[4] * (acch[f] + mu4[4] * cv[f]);
            }
            auto pkv = [&](int t) {
#pragma unroll
                for (int f = 0; f < 2; ++f) {
                    va[t >> 1][f][(t & 1) * 2] = pack_bf16(acc[2][t][f][0], acc[2][t][f][1]);
                    va[t >> 1][f][(t & 1) * 2 + 1] = pack_bf16(acc[2][t][f][2], acc[2][t][f][3]);
                }
            };
            kfr[1] = pk(acc[1][1]); kfr[3] = pk(acc[1][3]); pkv(1); pkv(3);
            u32x4 hf = pk(acch);
            if (WM.value == 0) {
                ex[0] = kfr[1]; ex[64] = kfr[3];
#pragma unroll
                for (int u = 0; u < 2; ++u)
                    asm volatile("ds_write_b64 %0, %1 offset:%3\n\tds_write_b64 %0, %2 offset:%3+8" :: "v"(ex), "v"(u32x2{va[u][0][2], va[u][0][3]}),
                                 "v"(u32x2{va[u][1][2], va[u][1][3]}), "n"(2048 + u * 1024) : "memory");
                if (negpos) hf = u32x4{0, 0, 0, 0};
            }
            ex[256 + WM.value * 64] = hf;
            SBAR();
            if (!ident) { fix(0, 0); fix(0, 1); fix(0, 2); fix(0, 3); fix(1, 0); fix(1, 2); fix(2, 0); fix(2, 2); }
#pragma unroll
            for (int t = 0; t < 4; ++t) qfr[t] = pk(acc[0][t]);
            kfr[0] = pk(acc[1][0]); kfr[2] = pk(acc[1][2]); pkv(0); pkv(2);
            asm volatile("" : "+v"(qfr[0]), "+v"(qfr[1]), "+v"(qfr[2]), "+v"(qfr[3]), "+v"(kfr[0]), "+v"(kfr[2]),
                         "+v"(va[0][0]), "+v"(va[0][1]), "+v"(va[1][0]), "+v"(va[1][1]));
        }
    };
    const int head = ct * 2 + wn;
    unsigned qo = 0, ko = 0, vo = 0;
    if (LOCAL) {
        qo = (unsigned)((((panel * 8 + wm * 4) * 32 + head) * 64 + lane) * 16);
        asm volatile("" : "+v"(qo));
    } else {
        ko = (unsigned)((wm * 8 + (g & 1)) * 1024 + ((l15 & 7) + ((l15 & 8) << 1) + 32 * (g >> 1)) * 16);
        vo = (unsigned)((wm * 8 + 2 + (g >> 1)) * 1024 + (l15 + 32 * (g & 1)) * 16);
        asm volatile("" : "+v"(ko), "+v"(vo));
    }
    char* sb[4];
#pragma unroll
    for (int t = 0; t < 4; ++t) sb[t] = LOCAL ? (char*)ofrag + t * 32768 : (char*)kvf + (size_t)(head * 16 + panel) * 16384 + (t & 1) * 4096;
    lds_ptr ownk = smw + wn * 16384 + ko, ownv = smw + wn * 16384 + vo;
    if (LOCAL) asm volatile("" : "+s"(sb[0]), "+s"(sb[1]), "+s"(sb[2]), "+s"(sb[3]));
    else asm volatile("" : "+s"(sb[0]), "+s"(sb[1]), "+v"(ownk), "+v"(ownv));
    if (LOCAL && wm) mainloop(IC<1>{}); else mainloop(IC<0>{});

    if (!LOCAL) {
        auto pk = [&](f32x4 a, f32x4 b) { return u32x4{pack_bf16(a[0], a[1]), pack_bf16(a[2], a[3]), pack_bf16(b[0], b[1]), pack_bf16(b[2], b[3])}; };
        SBAR();
#pragma unroll
        for (int t = 0; t < 4; ++t) {
            const u32x4 kk = pk(rs[t] * (acc[1][t][0] + mu[t] * ck[0]) + rs[t] * -cb, rs[t] * (acc[1][t][1] + mu[t] * ck[1]) + rs[t] * -cb);
            if (t & 1) st16k<128>(sb[t >> 1], ko, kk); else st16k<0>(sb[t >> 1], ko, kk);
            kfr[t] = kk;
            SBAR();
            const int u = t >> 1, f = t & 1;
            const u32x4 vv = pk(rs4[2 * u] * (acc[2][2 * u][f] + mu4[2 * u] * cv[f]) + rs4[2 * u] * -cb,
                                rs4[2 * u + 1] * (acc[2][2 * u + 1][f] + mu4[2 * u + 1] * cv[f]) + rs4[2 * u + 1] * -cb);
            if (f) st16k<256>(sb[u], vo, vv); else st16k<0>(sb[u], vo, vv);
            qfr[t] = vv;
            SBAR();
        }
        column(IC<0>{}, 1, 5, false, [&](int n) {
            const int t = n >> 1;
            if (n & 1) *(lds_u4)(ownv + (t >> 1) * 4096 + (t & 1) * 256) = qfr[t];
            else       *(lds_u4)(ownk + (t >> 1) * 4096 + (t & 1) * 128) = kfr[t];
        });
        asm volatile("s_nop 7");
        SBAR();
#pragma unroll
        for (int u = 0; u < 2; ++u) {
            const float r0 = rs[2 * u] * QSCALE, r1 = rs[2 * u + 1] * QSCALE;
            const u32x4 qa = pk(r0 * (acc[0][2 * u][0] + mu[2 * u] * cq[0]) + r0 * -cb, r0 * (acc[0][2 * u][1] + mu[2 * u] * cq[1]) + r0 * -cb);
            const u32x4 qb = pk(r1 * (acc[0][2 * u + 1][0] + mu[2 * u + 1] * cq[0]) + r1 * -cb, r1 * (acc[0][2 * u + 1][1] + mu[2 * u + 1] * cq[1]) + r1 * -cb);
#pragma unroll
            for (int e = 0; e < 4; ++e) {
                const auto x = __builtin_amdgcn_permlane16_swap(qa[e], qb[e], false, false);
                qo4[2 * u][e] = x[0]; qo4[2 * u + 1][e] = x[1];
            }
        }
        return;
    }
    {
        const f32x4 zero4 = {0, 0, 0, 0};
        auto bf8 = [](u32x4 v) { return __builtin_bit_cast(bf16x8, v); };
        f32x4 sp[4], sc[4];
        sc[1] = MFMA16(bf8(kfr[1]), bf8(qfr[1]), mwin); sp[1] = MFMA16(bf8(kfr[0]), bf8(qfr[1]), mwin);
        sc[3] = MFMA16(bf8(kfr[3]), bf8(qfr[3]), mwin); sp[3] = MFMA16(bf8(kfr[2]), bf8(qfr[3]), mwin);
        asm volatile("" : "+v"(sc[1]), "+v"(sp[1]), "+v"(sc[3]), "+v"(sp[3]));
        WAIT_LGKM_BAR();
        const bf16x8 kp[2] = {lds_b128(ka[0]), lds_b128(ka[1])};
        u32x2 vp[2][2];
#pragma unroll
        for (int u = 0; u < 2; ++u)
            asm volatile("ds_read_b64 %0, %2\n\tds_read_b64 %1, %2 offset:8" : "=&v"(vp[u][0]), "=&v"(vp[u][1]) : "v"(vo2[u]) : "memory");
        sc[0] = MFMA16(bf8(kfr[0]), bf8(qfr[0]), mwin); sc[2] = MFMA16(bf8(kfr[2]), bf8(qfr[2]), mwin);
        SBAR();
        float x[4][4], m[4];
        unsigned a[4][2];
        auto logits = [&](int t0, int st) {
#pragma unroll
            for (int t = t0; t < 4; t += 2)
#pragma unroll
                for (int h = 0; h < 2; ++h) {
                    if (st == 0) {
                        const unsigned uc = pack_bf16(sc[t][2 * h], sc[t][2 * h + 1]), up = pack_bf16(sp[t][2 * h], sp[t][2 * h + 1]);
                        asm("v_bfi_b32 %0, %1, %2, %3" : "=v"(a[t][h]) : "v"(h ? cm23 : cm01), "v"(uc), "v"(up));
                    } else if (st == 1) { x[t][2 * h] = bf_lo(a[t][h]) * ATT_SCALE; x[t][2 * h + 1] = bf_hi(a[t][h]) * ATT_SCALE; }
                    else if (st == 2) a[t][h] = pack_bf16(x[t][2 * h], x[t][2 * h + 1]);
                    else { x[t][2 * h] = bf_lo(a[t][h]); x[t][2 * h + 1] = bf_hi(a[t][h]); }
                }
            SBAR();
        };
        logits(1, 0); logits(1, 1);
        sp[0] = MFMA16(kp[0], bf8(qfr[0]), mfirst); sp[2] = MFMA16(kp[1], bf8(qfr[2]), mfirst);
        SBAR();
        logits(1, 2); logits(1, 3);
        logits(0, 0); logits(0, 1); logits(0, 2); logits(0, 3);
        auto across = [&](float (&v)[4], auto op, auto fin) {
            const auto p = __builtin_amdgcn_permlane16_swap(__float_as_uint(v[0]), __float_as_uint(v[1]), false, false);
            const auto q = __builtin_amdgcn_permlane16_swap(__float_as_uint(v[2]), __float_as_uint(v[3]), false, false);
            const auto r = __builtin_amdgcn_permlane32_swap(__float_as_uint(op(__uint_as_float(p[0]), __uint_as_float(p[1]))),
                                                            __float_as_uint(op(__uint_as_float(q[0]), __uint_as_float(q[1]))), false, false);
            const unsigned z = __float_as_uint(fin(op(__uint_as_float(r[0]), __uint_as_float(r[1]))));
            const auto u = __builtin_amdgcn_permlane32_swap(z, z, false, false);
            const auto e = __builtin_amdgcn_permlane16_swap(u[0], u[0], false, false), f = __builtin_amdgcn_permlane16_swap(u[1], u[1], false, false);
            v[0] = __uint_as_float(e[0]); v[1] = __uint_as_float(e[1]); v[2] = __uint_as_float(f[0]); v[3] = __uint_as_float(f[1]);
        };
#pragma unroll
        for (int t = 0; t < 4; ++t) m[t] = amax3(x[t][0], x[t][1], x[t][2]);
        asm("v_max_f32 %0, %0, %4\n\tv_max_f32 %1, %1, %5\n\tv_max_f32 %2, %2, %6\n\tv_max_f32 %3, %3, %7\n\ts_nop 0"
            : "+v"(m[0]), "+v"(m[1]), "+v"(m[2]), "+v"(m[3]) : "v"(x[0][3]), "v"(x[1][3]), "v"(x[2][3]), "v"(x[3][3]));
        across(m, [](float p, float q) { float r; asm("v_max_f32 %0, %1, %2\n\ts_nop 0" : "=v"(r) : "v"(p), "v"(q)); return r; },
               [](float p) { return p * -LOG2E; });
        SBAR();
#pragma unroll
        for (int t = 0; t < 4; ++t)
#pragma unroll
            for (int r = 0; r < 4; ++r) x[t][r] = __builtin_fmaf(x[t][r], LOG2E, m[t]);
        SBAR();
#pragma unroll
        for (int t = 0; t < 4; ++t)
#pragma unroll
            for (int r = 0; r < 4; ++r) x[t][r] = __builtin_amdgcn_exp2f(x[t][r]);
        SBAR();
#pragma unroll
        for (int t = 0; t < 4; ++t) m[t] = (x[t][0] + x[t][1]) + (x[t][2] + x[t][3]);
        asm volatile("s_waitcnt lgkmcnt(0)" : "+v"(vp[0][0]), "+v"(vp[0][1]), "+v"(vp[1][0]), "+v"(vp[1][1]) :: "memory");
        u32x4 ve[2][2];
#pragma unroll
        for (int u = 0; u < 2; ++u)
#pragma unroll
            for (int f = 0; f < 2; ++f) ve[u][f] = u32x4{vp[u][f][0], vp[u][f][1], va[u][f][0], va[u][f][1]};
        across(m, [](float p, float q) { return p + q; }, [](float p) { return __builtin_amdgcn_rcpf(p); });
        asm volatile("" : "+v"(ve[0][0]), "+v"(ve[0][1]), "+v"(ve[1][0]), "+v"(ve[1][1]));
        SBAR();
#pragma unroll
        for (int t = 0; t < 4; ++t)
#pragma unroll
            for (int r = 0; r < 4; ++r) x[t][r] *= m[t];
        SBAR();
#pragma unroll
        for (int t = 0; t < 4; ++t)
#pragma unroll
            for (int h = 0; h < 2; ++h) a[t][h] = pack_bf16(x[t][2 * h], x[t][2 * h + 1]);
        SBAR();
        f32x4 o0[4], o1[4];
#pragma unroll
        for (int t = 0; t < 4; ++t) {
            const bf16x8 pb = bf8(u32x4{a[t][0] & ~cm01, a[t][1] & ~cm23, a[t][0] & cm01, a[t][1] & cm23});
            o0[t] = MFMA16(bf8((t & 1) ? va[t >> 1][0] : ve[t >> 1][0]), pb, zero4);
            o1[t] = MFMA16(bf8((t & 1) ? va[t >> 1][1] : ve[t >> 1][1]), pb, zero4);
        }
        SBAR();
#pragma unroll
        for (int t = 0; t < 4; ++t)
            st16(sb[t], qo, u32x4{pack_bf16(o0[t][0], o0[t][1]), pack_bf16(o0[t][2], o0[t][3]),
                                                 pack_bf16(o1[t][0], o1[t][1]), pack_bf16(o1[t][2], o1[t][3])});
    }
}

DI int out_row(int panel, bool localrows, int T, int a) {
    return localrows ? (panel * 128 + (T >> 2) * 64 + 2 * (16 * (T & 1) + a) + ((T >> 1) & 1)) : (panel * 128 + T * 16 + a);
}
DI int out_swz(int k) { return ((k >> 1) & 1) | (k & 4); }
DI unsigned wo_off(int w, int lane) {
    const int kl = lane >> 3;
    unsigned v = (unsigned)(w * 8 + kl) * 2048u + (unsigned)(ids().co * 128 + ((lane & 7) ^ out_swz(kl)) * 16);
    asm volatile("" : "+v"(v));
    return v;
}
DI void wo_load(const void* Wo, unsigned voff, int i0, int i1, unsigned lds0, int w) {
#pragma unroll
    for (int i = i0; i < i1; ++i) glds16((const char*)Wo + (size_t)i * 65536, voff, lds0 + (w + 4 * i) * 1024);
}
DI unsigned res_off(bool localrows, bool tiled, int lane) {
    const Ids I = ids();
    unsigned v = (unsigned)(I.po * 262144 + I.co * (tiled ? 16384 : 128) + out_row(0, localrows, 0, lane >> 3) * (tiled ? 128 : 2048) +
                            (((lane & 7) ^ (lane >> 3)) * 16));
    asm volatile("" : "+v"(v));
    return v;
}
DI void res_load(u32x4 (&res)[6], const void* Xin, bool localrows, bool tiled, unsigned off, int w) {
#pragma unroll
    for (int i = 0; i < 6; ++i) {
        const int j = min(w + 3 * i, 15);
        asm volatile("global_load_dwordx4 %0, %1, %2 sc1" : "=&v"(res[i]) : "v"(off), "s"((const char*)Xin + out_row(0, localrows, j >> 1, (j & 1) * 8) * (tiled ? 128 : 2048)) : "memory");
    }
}
DI void out_handoff(char* smem, int xoff, u32x4 (&res)[6], int w, char* ws, int set, u64 tag, int lane, u32x4 (&c)[8], int mat) {
    const Ids I = ids();
    u64 land;
    lds_ptr dst = (lds_ptr)smem + xoff + w * 1024 + lane * 16;
    asm volatile("" : "+v"(dst));
    if (w == 3) __builtin_amdgcn_s_setprio(3);
    signal_poll<false, false>(w - 3, ws, flag_off(set, I.pq, lane & 15, I.cq), flag_off(set, I.po, I.co, lane & 15), 0xffffull, tag, land);
    if (w < 3) {
        asm volatile("s_waitcnt vmcnt(0)" : "+v"(res[0]), "+v"(res[1]), "+v"(res[2]), "+v"(res[3]), "+v"(res[4]), "+v"(res[5]) :: "memory");
#pragma unroll
        for (int i = 0; i < 5; ++i) *(__attribute__((address_space(3))) u32x4*)(dst + i * 3072) = res[i];
        if (w == 0) *(__attribute__((address_space(3))) u32x4*)(dst + 15360) = res[5];
        if (mat >= 0) colsum_store(c, ws, mat, w, lane);
    }
    WAIT_LGKM_BAR();
    asm volatile("s_waitcnt vmcnt(0)" : "+v"(land) :: "memory");
    if (w < 3 && mat >= 0) flag_store(ws, (mat ? 10 : 13) + w, I.cq, I.pq, tag, lane);
    __builtin_amdgcn_s_setprio(0);
}

template <bool AFTER_LOCAL>
DI void out_compute(char* smem, int w, const __hip_bfloat16* __restrict__ ofrag,
                    __hip_bfloat16* __restrict__ Xout, float* __restrict__ statp SP_PARAM) {
    constexpr int XOFF = AFTER_LOCAL ? 147456 : 98304;
    STAMP(AFTER_LOCAL ? 3 : 10);
    VM_BAR(0);
    const int lane = lane_id();
    asm volatile("" : "+s"(w));
    const Ids I = ids();
    const int panel = I.po, ct = I.co;
    const int l15 = lane & 15, g = lane >> 4;
    const bool tiled = AFTER_LOCAL || statp != nullptr;
    const int rstr = tiled ? 128 : 2048;
    const int q4 = l15 >> 2, p4 = l15 & 3;
    const int krow = 4 * g + q4;
    const int sw = out_swz(krow);
    const lds_cptr sm = (lds_cptr)smem;
    lds_cptr bw[4], xo[4];
#pragma unroll
    for (int t = 0; t < 4; ++t) {
        bw[t] = sm + krow * 128 + ((((t >> 1) * 4 + (p4 >> 1) * 2 + (t & 1)) ^ sw) * 16) + (p4 & 1) * 8;
        xo[t] = sm + XOFF + (w * 32 + l15) * 128 + ((((t >> 1) * 4 + (g >> 1) * 2 + (t & 1)) ^ (l15 & 7)) * 16) + (g & 1) * 8;
    }
    unsigned so[2], sto, ao[2];
#pragma unroll
    for (int r2 = 0; r2 < 2; ++r2) {
        so[r2] = (unsigned)(panel * 262144 + ct * (tiled ? 16384 : 128) + out_row(0, AFTER_LOCAL, 2 * w + r2, l15 & 7) * rstr + (l15 >> 3) * 64 + g * 16);
        ao[r2] = (unsigned)((panel * 8 + 2 * w + r2) * 32768 + lane * 16);
    }
    sto = (unsigned)(((panel * 16 + ct) * 128 + 2 * w * 16 + l15) * 8);
    asm volatile("" : "+v"(bw[0]), "+v"(bw[1]), "+v"(bw[2]), "+v"(bw[3]), "+v"(xo[0]), "+v"(xo[1]), "+v"(xo[2]), "+v"(xo[3]),
                 "+v"(so[0]), "+v"(so[1]), "+v"(sto), "+v"(ao[0]), "+v"(ao[1]));
    bf16x8 ones;
    {
        unsigned o1; asm volatile("v_mov_b32 %0, 0x3f803f80" : "=v"(o1));
        ones = __builtin_bit_cast(bf16x8, (u32x4{o1, o1, o1, o1}));
        asm volatile("" : "+v"(ones));
    }
    const __amdgpu_buffer_rsrc_t ra = __builtin_amdgcn_make_buffer_rsrc((void*)ofrag, 0, 4 << 20, 0x00020000);
    asm volatile("" :: "s"(ra));
    u32x32 fr;
    BAR();
    STAMP(AFTER_LOCAL ? 4 : 11);
    u32x32 aw0, aw1, aw2, fr1;
    asm volatile(".irp j,0,1,2,3\n\tbuffer_load_dwordx4 v[96+4*\\j:99+4*\\j], %2, %4, 0 offen offset:1024*\\j sc1\n\t.endr\n\t"
                 ".irp j,0,1,2,3\n\tbuffer_load_dwordx4 v[112+4*\\j:115+4*\\j], %3, %4, 0 offen offset:1024*\\j sc1\n\t.endr\n\t"
                 ".irp j,0,1,2,3\n\tbuffer_load_dwordx4 v[128+4*\\j:131+4*\\j], %2, %4, %5 offen offset:1024*\\j sc1\n\t.endr"
                 : "=&{v[96:127]}"(aw0), "=&{v[128:159]}"(aw1) : "v"(ao[0]), "v"(ao[1]), "s"(ra), "s"(4096) : "memory");
    f32x16 acc[2];
#pragma unroll
    for (int r2 = 0; r2 < 2; ++r2)
#pragma unroll
        for (int c = 0; c < 4; ++c) {
            const u32x2 xr = *(const __attribute__((address_space(3))) u32x2*)(xo[c] + r2 * 2048);
            acc[r2][4 * c] = bf_lo(xr[0]); acc[r2][4 * c + 1] = bf_hi(xr[0]); acc[r2][4 * c + 2] = bf_lo(xr[1]); acc[r2][4 * c + 3] = bf_hi(xr[1]);
        }
    asm volatile(
        ".macro OUT_MM r, t, j, s\n\t"
        "v_mfma_f32_16x16x32_bf16 v[16*\\r+4*\\t:16*\\r+4*\\t+3], v[32+16*\\t+4*\\j:35+16*\\t+4*\\j], v[96+32*\\s+16*\\r+4*\\j:99+32*\\s+16*\\r+4*\\j], v[16*\\r+4*\\t:16*\\r+4*\\t+3]\n\t"
        ".endm\n\t"
        ".macro OUT_RD t, i, g\n\t"
        "ds_read_b64_tr_b16 v[32+16*\\t+2*(\\i):33+16*\\t+2*(\\i)], v[192+\\t] offset:(\\g)*16384+(\\i)*2048\n\t"
        ".endm\n\t"
        ".macro OUT_LD i, s\n\t"
        ".if (\\i) < 4\n\tbuffer_load_dwordx4 v[96+32*(\\s)+4*(\\i):99+32*(\\s)+4*(\\i)], %[a0], %[ra], s96 offen offset:1024*(\\i) sc1\n\t"
        ".else\n\tbuffer_load_dwordx4 v[96+32*(\\s)+4*(\\i):99+32*(\\s)+4*(\\i)], %[a1], %[ra], s96 offen offset:1024*((\\i)-4) sc1\n\t.endif\n\t"
        ".endm\n\t"
        ".macro OUT_CH r, t, s, rt, g\n\t"
        ".irp j,0,1,2,3\n\tOUT_MM \\r, \\t, \\j, \\s\n\tOUT_RD \\rt, 4*\\r+\\j, \\g\n\t.endr\n\t"
        ".endm\n\t"
        ".macro OUT_GRP s, g, gn, ld, up, v0, v1\n\t"
        "OUT_CH 0, 0, \\s, 3, \\g\n\ts_waitcnt vmcnt(\\v0)\n\t.if \\ld\n\tOUT_LD 0, (\\s+2)%%3\n\t.endif\n\t"
        "OUT_CH 1, 0, \\s, 3, \\g\n\t.if \\up\n\t.irp t,0,1,2,3\n\tv_add_u32 v[192+\\t], s97, v[192+\\t]\n\t.endr\n\t.endif\n\t.if \\ld\n\tOUT_LD 1, (\\s+2)%%3\n\t.endif\n\t"
        "OUT_CH 0, 1, \\s, 0, \\gn\n\t.if \\ld\n\tOUT_LD 2, (\\s+2)%%3\n\t.endif\n\t"
        "OUT_CH 1, 1, \\s, 0, \\gn\n\t.if \\ld\n\tOUT_LD 3, (\\s+2)%%3\n\t.endif\n\ts_waitcnt lgkmcnt(8)\n\t"
        "OUT_CH 0, 2, \\s, 1, \\gn\n\t.if \\ld\n\tOUT_LD 4, (\\s+2)%%3\n\t.endif\n\t"
        "OUT_CH 1, 2, \\s, 1, \\gn\n\t.if \\ld\n\tOUT_LD 5, (\\s+2)%%3\n\t.endif\n\t"
        "OUT_CH 0, 3, \\s, 2, \\gn\n\t.if \\ld\n\tOUT_LD 6, (\\s+2)%%3\n\t.endif\n\t"
        "OUT_CH 1, 3, \\s, 2, \\gn\n\t.if \\ld\n\tOUT_LD 7, (\\s+2)%%3\n\ts_addk_i32 s96, 0x1000\n\t.endif\n\t"
        "s_waitcnt vmcnt(\\v1) lgkmcnt(8)\n\t"
        ".endm\n\t"
        ".macro OUT_PACK y, c\n\t"
        ".irp i,0,1,2,3,4,5,6,7\n\tv_cvt_pk_bf16_f32 v[\\y+\\i], v[\\c+2*\\i], v[\\c+2*\\i+1]\n\t.endr\n\t"
        ".irp i,0,1,4,5\n\tv_permlane16_swap_b32 v[\\y+\\i], v[\\y+\\i+2]\n\t.endr\n\t"
        ".irp i,0,1,2,3\n\tv_mov_b32 v[\\y+8+\\i], v[\\y+\\i]\n\t.endr\n\t"
        "v_mfma_f32_16x16x32_bf16 v[\\y+12:\\y+15], v[\\y:\\y+3], v[\\y:\\y+3], 0\n\t"
        "v_mov_b32_dpp v[\\y+8], v[\\y+4] row_ror:8 row_mask:0xf bank_mask:0xc\n\tv_mov_b32_dpp v[\\y+9], v[\\y+5] row_ror:8 row_mask:0xf bank_mask:0xc\n\t"
        "v_mfma_f32_16x16x32_bf16 v[\\y+16:\\y+19], %[one], v[\\y:\\y+3], 0\n\t"
        "v_mov_b32_dpp v[\\y+10], v[\\y+6] row_ror:8 row_mask:0xf bank_mask:0xc\n\tv_mov_b32_dpp v[\\y+11], v[\\y+7] row_ror:8 row_mask:0xf bank_mask:0xc\n\t"
        "v_mfma_f32_16x16x32_bf16 v[\\y+12:\\y+15], v[\\y+4:\\y+7], v[\\y+4:\\y+7], v[\\y+12:\\y+15]\n\t"
        ".endm\n\t"
        ".macro OUT_REST y\n\t"
        "v_mfma_f32_16x16x32_bf16 v[\\y+16:\\y+19], %[one], v[\\y+4:\\y+7], v[\\y+16:\\y+19]\n\t"
        ".irp i,0,1,2,3\n\tv_mov_b32_dpp v[\\y+4+\\i], v[\\y+\\i] row_ror:8 row_mask:0xf bank_mask:0x3\n\t.endr\n\t"
        ".endm\n\t"
        ".macro OUT_DIAG y\n\t"
        "v_cndmask_b32 v[\\y+12], v[\\y+12], v[\\y+13], %[m0]\n\tv_cndmask_b32 v[\\y+14], v[\\y+14], v[\\y+15], %[m0]\n\t"
        "v_cndmask_b32 v[\\y+17], v[\\y+12], v[\\y+14], %[m1]\n\t"
        ".endm\n\t"
        "s_movk_i32 s96, 0x1000\n\t.irp i,4,5,6,7\n\tOUT_LD \\i, 1\n\t.endr\n\t"
        ".irp t,0,1,2\n\t.irp i,0,1,2,3,4,5,6,7\n\tOUT_RD \\t, \\i, 0\n\t.endr\n\ts_waitcnt lgkmcnt(4)\n\t.endr\n\t"
        "s_movk_i32 s96, 0x2000\n\ts_mov_b32 s97, 0xc000\n\t"
        "s_waitcnt vmcnt(12) lgkmcnt(0)\n"
        ".Lout_loop_%=:\n\t"
        "OUT_GRP 0, 0, 1, 1, 0, 8, 12\n\tOUT_GRP 1, 1, 2, 1, 0, 8, 12\n\tOUT_GRP 2, 2, 0, 1, 1, 8, 12\n\t"
        "s_mov_b32 s97, %[last]\n\t"
        "s_cmpk_lg_u32 s96, 0x8000\n\ts_cbranch_scc1 .Lout_loop_%=\n\t"
        "OUT_GRP 0, 0, 1, 0, 0, 8, 4\n\t"
        ".irp j,0,1,2,3\n\tOUT_MM 0, 0, \\j, 1\n\tOUT_RD 3, \\j, 1\n\t.endr\n\t.irp j,0,1,2,3\n\tOUT_MM 0, 1, \\j, 1\n\tOUT_RD 3, 4+\\j, 1\n\t.endr\n\t"
        "s_waitcnt lgkmcnt(8)\n\t.irp j,0,1,2,3\n\tOUT_MM 0, 2, \\j, 1\n\t.endr\n\t"
        "s_waitcnt lgkmcnt(0)\n\t.irp j,0,1,2,3\n\tOUT_MM 0, 3, \\j, 1\n\t.endr\n\t"
        "s_nop 1\n\t"
        "OUT_PACK 96, 0\n\tglobal_store_dwordx4 %[s0], v[104:107], %[xa] sc1\n\tOUT_REST 96\n\tglobal_store_dwordx4 %[s0], v[100:103], %[xb] sc1\n\t"
        "s_waitcnt vmcnt(2)\n\t.irp t,0,1,2,3\n\t.irp j,0,1,2,3\n\tOUT_MM 1, \\t, \\j, 1\n\t.endr\n\t.endr\n\t"
        "s_nop 1\n\t"
        "OUT_PACK 160, 16\n\tglobal_store_dwordx4 %[s1], v[168:171], %[xa] sc1\n\tOUT_REST 160\n\tglobal_store_dwordx4 %[s1], v[164:167], %[xb] sc1\n\t"
        "s_cmp_eq_u64 %[st], 0\n\ts_cbranch_scc1 .Lout_end_%=\n\t"
        "OUT_DIAG 96\n\tOUT_DIAG 160\n\t"
        "s_mov_b64 exec, %[md]\n\t"
        "global_store_dwordx2 %[so], v[112:113], %[st] sc1\n\tglobal_store_dwordx2 %[so], v[176:177], %[st] offset:128 sc1\n\t"
        "s_mov_b64 exec, -1\n"
        ".Lout_end_%=:\n\t"
        "s_nop 1\n\t"
        ".purgem OUT_MM\n\t.purgem OUT_RD\n\t.purgem OUT_LD\n\t.purgem OUT_CH\n\t.purgem OUT_GRP\n\t.purgem OUT_PACK\n\t.purgem OUT_REST\n\t.purgem OUT_DIAG"
        : "+{v[0:15]}"(acc[0]), "+{v[16:31]}"(acc[1]), "+{v[96:127]}"(aw0), "+{v[128:159]}"(aw1), "=&{v[160:191]}"(aw2),
          "+{v192}"(bw[0]), "+{v193}"(bw[1]), "+{v194}"(bw[2]), "+{v195}"(bw[3]), "=&{v[32:63]}"(fr), "=&{v[64:95]}"(fr1)
        : [a0] "v"(ao[0]), [a1] "v"(ao[1]), [ra] "s"(ra), [last] "s"(AFTER_LOCAL ? 49152 : 81920), [one] "v"(ones), [s0] "v"(so[0]), [s1] "v"(so[1]),
          [xa] "s"(Xout), [xb] "s"((char*)Xout + (AFTER_LOCAL ? 16 : 8) * rstr), [st] "s"(statp), [so] "v"(sto),
          [m0] "s"(0xaaaaaaaaaaaaaaaaull), [m1] "s"(0xccccccccccccccccull), [md] "s"(0xf0000f0000f0000full)
        : "memory", "scc", "s96", "s97");
    STAMP(AFTER_LOCAL ? 5 : 12);
}

#define ROWSUM(p0, p1, c) __builtin_amdgcn_smfmac_f32_16x16x64_bf16(sel, __builtin_bit_cast(bf16v_t, __builtin_shufflevector(p0, p1, 0, 1, 2, 3, 4, 5, 6, 7)), c, sidx, 0, 0)
constexpr int ATT_STAGE = 32768;
typedef __attribute__((ext_vector_type(8))) unsigned u32x8;

DI void attn_loader(char* smem, int w, char* ws, int set, u64 tag, const __hip_bfloat16* __restrict__ kvf, const void* Wo, const void* Xin,
                    const void* Wcs SP_PARAM) {
    const int lane = lane_id();
    asm volatile("" : "+s"(w));
    const Ids I = ids();
    const int panel = I.pq, ct = I.cq;
    const unsigned lds0 = (unsigned)(uintptr_t)smem;
    const char* kvh = (const char*)kvf + (size_t)(ct * 2 + (w >> 1)) * 262144 + (w & 1) * 4096;
    const unsigned voff = (unsigned)(lane * 16);
    auto order = [&](int st) { return ((panel & 8) + st - ((st - 1 < (panel & 7)) ? 1 : 0)) & 15; };
    auto load = [&](int st) {
        const char* src = kvh + (size_t)order(st) * 16384;
        const unsigned dst = lds0 + (w >> 1) * 16384 + (w & 1) * 4096 + (st & 3) * ATT_STAGE;
#pragma unroll
        for (int u = 0; u < 2; ++u)
            asm volatile("s_mov_b32 m0, %0\n\ts_nop 0\n\tglobal_load_lds_dwordx4 %1, %2 sc1\n\tglobal_load_lds_dwordx4 %1, %2 offset:1024 sc1\n\t"
                         "global_load_lds_dwordx4 %1, %2 offset:2048 sc1\n\tglobal_load_lds_dwordx4 %1, %2 offset:3072 sc1"
                         :: "s"(dst + u * 8192), "v"(voff), "s"(src + u * 8192) : "memory");
    };
    const unsigned wv = wo_off(w, lane), rv = res_off(false, true, lane), cv = colsum_off(w, lane);
    const unsigned poff = flag_off(set, ct, panel, lane & 15);
    u64 land;
    __builtin_amdgcn_s_setprio(3);
    BAR();
    signal_poll<false, true>(w, ws, flag_off(set, ct, lane & 15, panel), poff, 1ull << order(1), tag, land);
    STAMP(9);
    load(1);
    asm volatile("global_load_dwordx2 %0, %1, %2 sc1\n\ts_waitcnt vmcnt(0)" : "+v"(land) : "v"(poff), "s"(ws + WS_FLAG) : "memory");
    STAMP(10);
    BAR();
    if (!__all(land == tag)) flag_poll(ws, poff, tag);
    load(2); load(3);
    __builtin_amdgcn_s_setprio(0);
    STAMP(11);
    VM_BAR(8); load(4);
    VM_BAR(0); load(5); load(6);
    VM_BAR(0); load(7); load(8);
    u32x4 res[6], ca[8];
    colsum_load(ca, w < 3 ? Wcs : nullptr, cv);
#pragma unroll 1
    for (int st = 6; st < 12; st += 2) {
        VM_BAR(0);
        load(st + 3); load(st + 4);
        if (st == 8) wo_load(Wo, wv, 24, 32, lds0 + 32768, w);
    }
    VM_BAR(0); load(15); wo_load(Wo, wv, 0, 8, lds0, w);
    res_load(res, Xin, false, true, rv, w);
    VM_BAR(0); wo_load(Wo, wv, 8, 24, lds0, w);
    STAMP(12);
    out_handoff(smem, 98304, res, w, ws, set + 1, tag, lane, ca, Wcs ? 0 : -1);
}

DI void attn_compute(char* smem, int w, const u32x4 (&q4)[4], const __hip_bfloat16* __restrict__ kvf, __hip_bfloat16* __restrict__ ofrag SP_PARAM) {
    const int lane = lane_id();
    asm volatile("" : "+s"(w));
    const int wm = w & 1, wn = w >> 1;
    const Ids I = ids();
    const int panel = I.pq, head = I.cq * 2 + wn;
    const int l15 = lane & 15;
    const unsigned se = (l15 & 7) == 0 ? 0x3f803f80u : 0u, so = (l15 & 7) == 4 ? 0x3f803f80u : 0u;
    const u32x4 sel1 = {se, se, so, so};
    const lds_cptr sm = (lds_cptr)smem + wn * 16384 + lane * 16;
    const lds_cptr smk = (lds_cptr)smem + wn * 16384 + ((lane & 0x33) | ((lane & 4) << 1) | ((lane & 8) >> 1)) * 16;
    const f32x16 z16 = {0, 0, 0, 0, 0, 0, 0, 0, 0, 0, 0, 0, 0, 0, 0, 0};
    const bf16x8 qA0 = __builtin_bit_cast(bf16x8, q4[0]), qA1 = __builtin_bit_cast(bf16x8, q4[1]);
    const bf16x8 qB0 = __builtin_bit_cast(bf16x8, q4[2]), qB1 = __builtin_bit_cast(bf16x8, q4[3]);
    f32x16 oA, oB, nmA, nmB, sA, sB;
    f32x4 lsA, lsB;
    u32x8 pB;
    bf16x8 v11;
    asm volatile(".irp r,106,108,110,112,134,136,138,140,170,172\n\tv_mov_b64 v[\\r:\\r+1], 0\n\t.endr"
                 : "={v[106:113]}"(pB), "={v[134:137]}"(lsA), "={v[138:141]}"(lsB), "={v[170:173]}"(v11));
    WAIT_LGKM_BAR();
    STAMP(8);
    bf16x8 k00 = lds_b128(smk), k10 = lds_b128(smk + 1024), k01 = lds_b128(smk + 4096), k11 = lds_b128(smk + 4096 + 1024);
    bf16x8 v00 = lds_b128(sm + 2048), v10 = lds_b128(sm + 3072), v01 = lds_b128(sm + 4096 + 2048);
    float ob;
    {
        const u32x4 vu = __builtin_bit_cast(u32x4, v00);
        const unsigned vm = max(max(vu[0] & 0x7fff0000u, vu[1] & 0x7fff0000u), max(vu[2] & 0x7fff0000u, vu[3] & 0x7fff0000u));
        ob = __uint_as_float(wave_min(vm)) * 0.015625f;
        sA = MFMA32(k00, qA0, z16); sB = MFMA32(k00, qB0, z16);
        sA = MFMA32(k10, qA1, sA); sB = MFMA32(k10, qB1, sB);
        SBAR(); VM_BAR(0); SBAR();
        float mA = sA[0], mB = sB[0];
#pragma unroll
        for (int r = 1; r < 16; ++r) { mA = fmaxf(mA, sA[r]); mB = fmaxf(mB, sB[r]); }
        mA = max_h(mA) + 16.f; mB = max_h(mB) + 16.f;
#pragma unroll
        for (int r = 0; r < 16; ++r) { nmA[r] = -mA; nmB[r] = -mB; sA[r] -= mA; oA[r] = ob; oB[r] = ob; }
    }
    const unsigned a0 = (unsigned)(uintptr_t)sm, a1 = (unsigned)(uintptr_t)smk;
    unsigned dst = (unsigned)((((panel * 8 + wm * 4 + ((lane & 31) >> 4)) * 32 + head) * 64 + (lane & 15) + (lane >> 5) * 16) * 16);
    asm volatile("" : "+v"(dst));
    asm volatile(
        ".macro ATT_RD dst, y\n\t"
        ".if (\\dst) < 158\n\tds_read_b128 v[\\dst:\\dst+3], v[177+(((\\y)&0x1ffff)>>16)] offset:(\\y)&0xffff\n\t"
        ".else\n\tds_read_b128 v[\\dst:\\dst+3], v[175+(((\\y)&0x1ffff)>>16)] offset:(\\y)&0xffff\n\t.endif\n\t"
        ".endm\n\t"
        ".macro ATT_E d, c, n\n\t"
        "v_exp_f32 v[\\d], v[\\c]\n\t.if \\n > 1\n\tv_exp_f32 v[\\d+1], v[\\c+1]\n\t.endif\n\t.if \\n > 2\n\tv_exp_f32 v[\\d+2], v[\\c+2]\n\t.endif\n\t"
        ".endm\n\t"
        ".macro ATT_STEP cur, nxt, ka, kb, q, nm, oc, od, lsc, lsd, pc, pd, vd1, vc0, r0, y0, r1, y1, bar, rd\n\t"
        "v_mfma_f32_32x32x16_bf16 v[\\nxt:\\nxt+15], v[\\ka:\\ka+3], v[\\q:\\q+3], v[\\nm:\\nm+15]\n\t"
        "ATT_E \\cur, \\cur, 3\n\t"
        "v_mfma_f32_32x32x16_bf16 v[\\nxt:\\nxt+15], v[\\kb:\\kb+3], v[\\q+4:\\q+7], v[\\nxt:\\nxt+15]\n\t"
        "ATT_E \\cur+3, \\cur+3, 3\n\t"
        "v_smfmac_f32_32x32x32_bf16 v[\\od:\\od+15], v[\\vd1:\\vd1+3], v[\\pd:\\pd+7], v174\n\t"
        "s_waitcnt lgkmcnt(0)\n\t"
        ".if \\bar & 2\n\ts_barrier\n\t.endif\n\t"
        "ATT_E \\cur+6, \\cur+6, 2\n\tATT_E \\pc, \\cur+8, 1\n\t"
        "v_smfmac_f32_16x16x64_bf16 v[\\lsd:\\lsd+3], v[130:133], v[\\pd:\\pd+7], v174\n\t"
        ".if \\rd & 8\n\tATT_RD \\r0-8, \\y0-1024\n\t.endif\n\t"
        ".if \\rd & 1\n\tATT_RD \\r0, \\y0\n\t.endif\n\t"
        "ATT_E \\pc+1, \\cur+9, 2\n\t"
        "v_smfmac_f32_32x32x32_bf16 v[\\oc:\\oc+15], v[\\vc0:\\vc0+3], v[\\cur:\\cur+7], v174\n\t"
        ".if \\bar & 1\n\ts_barrier\n\t.endif\n\t"
        "ATT_E \\pc+3, \\cur+11, 3\n\t"
        "v_smfmac_f32_16x16x64_bf16 v[\\lsc:\\lsc+3], v[130:133], v[\\cur:\\cur+7], v174\n\t"
        ".if \\rd & 4\n\tATT_RD 142, 32768\n\tATT_RD 150, 33792\n\tATT_RD 158, 34816\n\tATT_RD \\r0, \\y0\n\t.endif\n\t"
        ".if \\rd & 2\n\tATT_RD \\r1, \\y1\n\t.endif\n\t"
        "ATT_E \\pc+6, \\cur+14, 2\n\t"
        ".if \\rd & 4\n\ts_waitcnt lgkmcnt(3)\n\t.endif\n\t"
        ".endm\n\t"
        ".macro ATT_OUT o, l, y, base\n\t"
        "v_rcp_f32 v34, v[\\l]\n\ts_nop 0\n\tv_mul_f32 v35, -%[ob], v34\n\ts_nop 0\n\t"
        ".irp i,0,2,8,10,4,6,12,14\n\tv_pk_fma_f32 v[36+\\i:37+\\i], v[\\o+\\i:\\o+1+\\i], v[34:35], v[34:35] op_sel:[0,0,1] op_sel_hi:[1,0,1]\n\t.endr\n\t"
        "v_cvt_pk_bf16_f32 v[\\y], v36, v37\n\tv_cvt_pk_bf16_f32 v[\\y+1], v38, v39\n\tv_cvt_pk_bf16_f32 v[\\y+2], v44, v45\n\tv_cvt_pk_bf16_f32 v[\\y+3], v46, v47\n\t"
        "global_store_dwordx4 %[dst], v[\\y:\\y+3], \\base sc1\n\t"
        "v_cvt_pk_bf16_f32 v[\\y+4], v40, v41\n\tv_cvt_pk_bf16_f32 v[\\y+5], v42, v43\n\tv_cvt_pk_bf16_f32 v[\\y+6], v48, v49\n\tv_cvt_pk_bf16_f32 v[\\y+7], v50, v51\n\t"
        "global_store_dwordx4 %[dst], v[\\y+4:\\y+7], \\base offset:512 sc1\n\t"
        ".endm\n\t"
        ".macro ATT_TILE p, y1, y2, bar, ra=3, rb=3\n\t"
        "ATT_STEP 66, 82, 142+\\p, 150+\\p, 122, 50, 2, 18, 134, 138, 98, 106, 170-\\p, 158+\\p, 170-\\p, \\y1+3072, 142+\\p, \\y2, \\bar&1, \\ra\n\t"
        "ATT_STEP 82, 66, 146-\\p, 154-\\p, 114, 34, 18, 2, 138, 134, 106, 98, 166+\\p, 158+\\p, 150+\\p, \\y2+1024, 158+\\p, \\y2+2048, \\bar&2, \\rb\n\t"
        ".endm\n\t"
        ".macro ATT_STAGE b, bar\n\t"
        "ATT_TILE 0, \\b+4096, \\b+8192, 0\n\t"
        "ATT_TILE 4, \\b+8192, \\b+12288, 0\n\t"
        ".if \\bar\n\tATT_TILE 0, \\b+12288, \\b+32768, 2, 1, 11\n\t.else\n\tATT_TILE 0, \\b+12288, \\b+32768, 0\n\t.endif\n\t"
        "ATT_TILE 4, \\b+32768, \\b+36864, 0\n\t"
        ".endm\n\t"
        "v_add_u32 v176, 0x10000, v175\n\tv_add_u32 v178, 0x10000, v177\n\tv_mov_b32 v174, 0xdddddddd\n\t"
        "s_mov_b32 s96, 4\n\t"
        "s_waitcnt lgkmcnt(0)\n\t"
        "ATT_TILE 0, 4096, 8192, 0\n\tATT_TILE 4, 8192, 12288, 0\n\tATT_TILE 0, 12288, 0, 0, 1, 0\n\tATT_TILE 4, 32768, 36864, 1, 6, 3\n\t"
        "ATT_STAGE 32768, 1\n\t"
        "s_branch .Latt_entry_%=\n\t"
        ".p2align 6\n"
        ".Latt_loop_%=:\n\t"
        "ATT_STAGE 0, 1\n\tATT_STAGE 32768, 0\n"
        ".Latt_entry_%=:\n\t"
        "ATT_STAGE 65536, 1\n\t"
        "s_sub_u32 s96, s96, 1\n\t"
        "ATT_TILE 0, 102400, 106496, 0\n\t"
        "s_cmp_eq_u32 s96, 0\n\t"
        "ATT_TILE 4, 106496, 110592, 0\n\t"
        "s_cbranch_scc1 .Latt_tail_%=\n\t"
        "ATT_TILE 0, 110592, 131072, 0\n\tATT_TILE 4, 131072, 135168, 0\n\t"
        "s_branch .Latt_loop_%=\n"
        ".Latt_tail_%=:\n\t"
        "s_waitcnt lgkmcnt(0)\n\t"
        "ATT_STEP 66, 82, 146, 154, 114, 34, 2, 18, 134, 138, 98, 106, 170, 158, 170, 113664, 0, 0, 0, 1\n\t"
        "ATT_STEP 82, 66, 142, 150, 122, 50, 2, 2, 134, 134, 106, 98, 166, 162, 0, 0, 0, 0, 0, 0\n\t"
        "ATT_STEP 66, 82, 146, 154, 122, 50, 18, 2, 138, 134, 98, 106, 170, 158, 0, 0, 0, 0, 0, 0\n\t"
        "ATT_OUT 2, 134, 52, %[of]\n\t"
        "v_smfmac_f32_32x32x32_bf16 v[18:33], v[166:169], v[98:105], v174\n\tATT_E 82, 82, 3\n\tATT_E 85, 85, 2\n\t"
        "v_smfmac_f32_16x16x64_bf16 v[138:141], v[130:133], v[98:105], v174\n\tATT_E 87, 87, 3\n\tATT_E 90, 90, 2\n\t"
        "v_smfmac_f32_32x32x32_bf16 v[18:33], v[162:165], v[82:89], v174\n\tATT_E 92, 92, 3\n\t"
        "v_smfmac_f32_16x16x64_bf16 v[138:141], v[130:133], v[82:89], v174\n\tATT_E 95, 95, 3\n\ts_nop 1\n\t"
        "v_smfmac_f32_16x16x64_bf16 v[138:141], v[130:133], v[90:97], v174\n\t"
        "v_smfmac_f32_32x32x32_bf16 v[18:33], v[170:173], v[90:97], v174\n\t"
        "s_nop 7\n\tATT_OUT 18, 138, 68, %[og]\n\ts_nop 1\n\t"
        ".purgem ATT_RD\n\t.purgem ATT_E\n\t.purgem ATT_OUT\n\t.purgem ATT_STEP\n\t.purgem ATT_TILE\n\t.purgem ATT_STAGE"
        : "+{v[2:17]}"(oA), "+{v[18:33]}"(oB), "+{v[66:81]}"(sA), "+{v[106:113]}"(pB), "+{v[134:137]}"(lsA), "+{v[138:141]}"(lsB),
          "+{v[142:145]}"(k00), "+{v[146:149]}"(k01), "+{v[150:153]}"(k10), "+{v[154:157]}"(k11),
          "+{v[158:161]}"(v00), "+{v[162:165]}"(v01), "+{v[166:169]}"(v10), "+{v[170:173]}"(v11), "+{v[34:49]}"(nmA), "+{v[50:65]}"(nmB),
          [ob] "+v"(ob)
        : [dst] "v"(dst), [of] "s"(ofrag), [og] "s"((char*)ofrag + 65536), "{v[114:117]}"(qA0), "{v[118:121]}"(qA1), "{v[122:125]}"(qB0), "{v[126:129]}"(qB1),
          "{v[130:133]}"(sel1), "{v175}"(a0), "{v177}"(a1)
        : "memory", "scc", "s96", "v174", "v176", "v178", "v82", "v83", "v84", "v85", "v86", "v87", "v88", "v89", "v90", "v91", "v92", "v93", "v94", "v95",
          "v96", "v97", "v98", "v99", "v100", "v101", "v102", "v103", "v104", "v105");
    STAMP(9);
    float lA = lsA[0], lB = lsB[0];
    {
        const f32x16 os = oA + oB;
        const float osum = (((os[0] + os[1]) + (os[2] + os[3])) + ((os[4] + os[5]) + (os[6] + os[7]))) +
                           (((os[8] + os[9]) + (os[10] + os[11])) + ((os[12] + os[13]) + (os[14] + os[15])));
        const bool bad = !(fmaxf(lA, lB) < 1.0e24f) || !(lA > 1.0e-24f) || !(lB > 1.0e-24f) || !(fabsf(osum) < 3.0e38f);
                if (__any(bad)) {
            const __amdgpu_buffer_rsrc_t rk = __builtin_amdgcn_make_buffer_rsrc((char*)kvf + (size_t)head * 262144, 0, 262144, 0x00020000);
            const unsigned s2 = (l15 & 6) == 0 ? 0x3f803f80u : 0u, s3 = (l15 & 6) == 4 ? 0x3f803f80u : 0u;
            const bf8_t sel = __builtin_bit_cast(bf8_t, u32x4{s2, s2, s3, s3});
            const int sidx = (l15 & 1) ? (int)0xEEEEEEEEu : 0x44444444;
            oA = z16; oB = z16; lA = 0.f; lB = 0.f;
            float mA = -INFINITY, mB = -INFINITY;
#pragma unroll 1
            for (int t = 0; t < 64; ++t) {
                bf16x8 f[4];
#pragma unroll
                for (int i = 0; i < 4; ++i) f[i] = __builtin_bit_cast(bf16x8, __builtin_amdgcn_raw_buffer_load_b128(rk, lane * 16 + i * 1024, t * 4096, 16));
                auto upd = [&](bf16x8 q0, bf16x8 q1, f32x16& o, float& l, float& m) {
                    f32x16 s = MFMA32(f[0], q0, z16); s = MFMA32(f[1], q1, s);
                    float tm = s[0];
#pragma unroll
                    for (int r = 1; r < 16; ++r) tm = fmaxf(tm, s[r]);
                    tm = max_h(tm);
                    const float mn = fmaxf(m, tm);
                    const float alpha = __builtin_amdgcn_exp2f(m - mn);
                    m = mn;
#pragma unroll
                    for (int r = 0; r < 16; ++r) { s[r] = __builtin_amdgcn_exp2f(s[r] - mn); o[r] *= alpha; }
                    const u32x4 p0 = {pack_bf16(s[0], s[1]), pack_bf16(s[2], s[3]), pack_bf16(s[4], s[5]), pack_bf16(s[6], s[7])};
                    const u32x4 p1 = {pack_bf16(s[8], s[9]), pack_bf16(s[10], s[11]), pack_bf16(s[12], s[13]), pack_bf16(s[14], s[15])};
                    f32x4 t4 = {0, 0, 0, 0};
                    o = MFMA32(f[2], __builtin_bit_cast(bf16x8, p0), o);
                    o = MFMA32(f[3], __builtin_bit_cast(bf16x8, p1), o);
                    t4 = ROWSUM(p0, p1, t4);
                    l = l * alpha + (t4[0] + t4[1]);
                };
                upd(qA0, qA1, oA, lA, mA);
                upd(qB0, qB1, oB, lB, mB);
            }
#pragma unroll
            for (int u = 0; u < 2; ++u) {
                const f32x16 e = (u ? oB : oA) * __builtin_amdgcn_rcpf(u ? lB : lA);
                st16o<0>((char*)ofrag + u * 65536, dst, u32x4{pack_bf16(e[0], e[1]), pack_bf16(e[2], e[3]), pack_bf16(e[8], e[9]), pack_bf16(e[10], e[11])});
                st16o<512>((char*)ofrag + u * 65536, dst, u32x4{pack_bf16(e[4], e[5]), pack_bf16(e[6], e[7]), pack_bf16(e[12], e[13]), pack_bf16(e[14], e[15])});
            }
        }
    }
}

DI void ln_row(const u32x4 (&v)[2], __hip_bfloat16* __restrict__ Y, unsigned off) {
    float s1 = 0.f, s2 = 0.f;
#pragma unroll
    for (int e = 0; e < 8; ++e) s1 = dot2bf(v[e >> 2][e & 3], 0x3f803f80u, s1);
    const float mean = wave_sum(s1) * (1.f / 1024.f);
#pragma unroll
    for (int e = 0; e < 8; ++e) {
        const float d0 = bf_lo(v[e >> 2][e & 3]) - mean, d1 = bf_hi(v[e >> 2][e & 3]) - mean;
        s2 += d0 * d0 + d1 * d1;
    }
    const float mb = round_bf16(mean);
    const float rden = __builtin_amdgcn_rcpf(round_bf16(__builtin_amdgcn_sqrtf(round_bf16(round_bf16(wave_sum(s2) * (1.f / 1024.f)) + 1e-5f))));
#pragma unroll
    for (int hh = 0; hh < 2; ++hh) {
        u32x4 y;
#pragma unroll
        for (int e = 0; e < 4; ++e) {
            const unsigned d = pack_bf16(bf_lo(v[hh][e]) - mb, bf_hi(v[hh][e]) - mb);
            y[e] = pack_bf16(bf_lo(d) * rden, bf_hi(d) * rden);
        }
        st16((char*)Y + hh * 131072, off, y);
    }
}

extern "C" __global__ __launch_bounds__(512, 2) void mega_kernel(const __hip_bfloat16* x, const __hip_bfloat16* wqkvA,
                                                            const __hip_bfloat16* woA, const __hip_bfloat16* wqkvB,
                                                            const __hip_bfloat16* woB, __hip_bfloat16* out,
                                                            char* ws) {
    char* const smem = smem_base;
    typedef __hip_bfloat16 bf;
    const int w8 = __builtin_amdgcn_readfirstlane(threadIdx.x >> 6);
    if (w8 == 7 && (blockIdx.x >> 3) < 10) code_touch(blockIdx.x >> 3, threadIdx.x & 63);
    asm volatile("" :: "s"(x), "s"(wqkvA), "s"(woA), "s"(wqkvB), "s"(woB), "s"(out), "s"(ws));
    STAMP_DECL; STAMPR(14); STAMP(0);
    u64 tag;
    {
        const int lane = threadIdx.x & 63;
        const Ids I = ids();
        const unsigned off = (unsigned)((I.po * 128 + I.co * 8 + w8) * 2048 + lane * 16);
        u32x4 v[2];
        u32x2 ep;
        asm volatile("global_load_dwordx4 %0, %3, %4\n\tglobal_load_dwordx4 %1, %3, %4 offset:1024\n\tglobal_load_dwordx2 %2, %5, %6 sc1"
                     : "=&v"(v[0]), "=&v"(v[1]), "=&v"(ep) : "v"(off), "s"(x), "v"(0), "s"(ws + WS_EPOCH) : "memory");
        if (w8 == 7) {
            const int l = lane & 15, k = l % 5;
            const unsigned o = (l >= 10 || k == 3) ? flag_off(l, I.cq, I.pq, 0) : (k == 0 || k == 2) ? flag_off(l, I.pq, I.cq, 0) : flag_off(l, I.po, I.co, 0);
            touch_at(ws + WS_FLAG, o);
        } else if (w8 >= 4) touch(wqkvA, (w8 - 4) * 256 + blockIdx.x, lane);
        if (w8 >= 4) asm volatile("s_waitcnt vmcnt(2)" ::: "memory"); else asm volatile("s_waitcnt vmcnt(1)" ::: "memory");
        asm volatile("" : "+v"(v[0]), "+v"(v[1]));
        ln_row(v, (bf*)(ws + WS_Y0), (unsigned)(I.po * 262144 + (lane >> 3) * 16384 + (I.co * 8 + w8) * 128 + (lane & 7) * 16));
        if (w8 >= 4) asm volatile("s_waitcnt vmcnt(3)" ::: "memory"); else asm volatile("s_waitcnt vmcnt(2)" ::: "memory");
        asm volatile("" : "+v"(ep));
        tag = (((u64)(unsigned)__builtin_amdgcn_readfirstlane(ep[1]) << 32) | (unsigned)__builtin_amdgcn_readfirstlane(ep[0])) + 1;
    }
    if (w8 >= 4) {
        int w = w8 & 3;
#pragma unroll 1
        for (int it = 0; it < 2; ++it) {
            const int lane = lane_id();
            asm volatile("" : "+s"(x), "+s"(wqkvA), "+s"(woA), "+s"(wqkvB), "+s"(woB), "+s"(ws), "+s"(w));
            const unsigned lds0 = (unsigned)(uintptr_t)smem;
            u32x4 cb[8];
            const unsigned wv = wo_off(w, lane), rv = res_off(true, it, lane), cv = colsum_off(w, lane);
            qkv_loader<true>(smem, w, ws, it * 5, tag, (const bf*)(ws + (it ? WS_X2 : WS_Y0)), wqkvA, (const float*)(ws + WS_ST + 262144),
                             ws + WS_CSP, it == 0, woA, [&] {
                if (w < 3) {
                    colsum_load(cb, wqkvB, cv);
                    asm volatile("s_waitcnt vmcnt(8)" ::: "memory");
                } else {
                    touch(woA, blockIdx.x, lane);
                    asm volatile("s_waitcnt vmcnt(1)" ::: "memory");
                }
            }, [&](int part) {
                if (part == 0) wo_load(woA, wv, 29, 32, lds0, w); else wo_load(woA, wv, 25, 29, lds0, w);
            } SP_ARG);
            STAMP(3);
            u32x4 res[6];
            res_load(res, it ? (const void*)(ws + WS_X2) : x, true, it, rv, w);
            if (it) __builtin_amdgcn_s_sleep(8);
            wo_load(woA, wv, 0, 7, lds0, w);
            BAR();
            wo_load(woA, wv, 7, 25, lds0, w);
            STAMP(4);
            out_handoff(smem, 147456, res, w, ws, it * 5 + 1, tag, lane, cb, it ? -1 : 1);
            STAMP(5);
            qkv_loader<false>(smem, w, ws, it * 5 + 2, tag, (const bf*)(ws + (it ? WS_X3 : WS_X1)), wqkvB, (const float*)(ws + WS_ST + it * 524288), ws + WS_CSP + 196608,
                              false, nullptr, [] {}, [](int) {} SP_ARG);
            STAMP(8);
            attn_loader(smem, w, ws, it * 5 + 3, tag, (const bf*)(ws + WS_KVF + it * 8 * MBy), woB, ws + (it ? WS_X3 : WS_X1),
                        it ? nullptr : wqkvA SP_ARG);
            STAMP(13); STAMPR(15); STAMP_FLUSH(it);
        }
        if (blockIdx.x == 0 && w == 0 && lane_id() == 0) __hip_atomic_store((g_u64ptr)(ws + WS_EPOCH), tag, __ATOMIC_RELAXED, __HIP_MEMORY_SCOPE_AGENT);
    } else {
        __builtin_amdgcn_s_setprio(2);
#pragma unroll 1
        for (int it = 0; it < 2; ++it) {
            u32x4 q4[4];
            asm volatile("" : "+s"(ws), "+s"(wqkvA), "+s"(wqkvB));
            qkv_compute<true>(smem, w8, wqkvA, q4, nullptr, (bf*)(ws + WS_OF + it * 8 * MBy), it == 0,
                              ws + WS_ST + 262144, ws + WS_WN + blockIdx.x * 16 SP_ARG);
            STAMP(2);
            out_compute<true>(smem, w8, (const bf*)(ws + WS_OF + it * 8 * MBy),
                              (bf*)(ws + (it ? WS_X3 : WS_X1)), (float*)(ws + WS_ST + it * 524288) SP_ARG);
            STAMP(6);
            qkv_compute<false>(smem, w8, wqkvB, q4, (bf*)(ws + WS_KVF + it * 8 * MBy), nullptr, false,
                               ws + WS_ST + it * 524288, ws + WS_WN + 4096 + blockIdx.x * 16 SP_ARG);
            attn_compute(smem, w8, q4, (const bf*)(ws + WS_KVF + it * 8 * MBy), (bf*)(ws + WS_OF + (it * 8 + 4) * MBy) SP_ARG);
            out_compute<false>(smem, w8, (const bf*)(ws + WS_OF + (it * 8 + 4) * MBy),
                               it ? out : (bf*)(ws + WS_X2), it ? nullptr : (float*)(ws + WS_ST + 262144) SP_ARG);
            STAMP(13); STAMPR(15); STAMP_FLUSH(it);
        }
    }
}

extern "C" __global__ void zz_code_end_kernel() {}

extern "C" void kernel_launch(void* const* d_in, const int* in_sizes, int n_in, void* d_out, int out_size, void* d_ws,
                              size_t ws_size, hipStream_t stream) {
    typedef __hip_bfloat16 bf;
    (void)hipFuncSetAttribute((const void*)mega_kernel, hipFuncAttributeMaxDynamicSharedMemorySize, CH_LDS);
    mega_kernel<<<256, 512, CH_LDS, stream>>>((const bf*)d_in[0], (const bf*)d_in[1], (const bf*)d_in[2], (const bf*)d_in[4], (const bf*)d_in[5],
                                              (bf*)d_out, (char*)d_ws);
}
```
